```python
import torch

def _softmax(x, axis=-1):
    return torch.softmax(x, dim=axis)

def _gelu(x):
    return torch.nn.functional.gelu(x, approximate='tanh')

def _silu(x):
    return torch.nn.functional.silu(x)

def _sigmoid(x):
    return torch.sigmoid(x)

def _layernorm(x, axis=-1, eps=1e-05):
    m = x.mean(dim=axis, keepdim=True)
    v = x.var(dim=axis, keepdim=True, unbiased=False)
    return (x - m) / torch.sqrt(v + eps)

def _scatter_add(tgt, idx, src, axis=0):
    return tgt.clone().index_add_(axis, idx, src)

def _segment_sum(src, seg, n):
    out = torch.zeros((n,) + src.shape[1:], dtype=src.dtype)
    return out.index_add_(0, seg, src)

def _pad(x, pw):
    flat = tuple((v for pair in reversed(pw) for v in pair))
    return torch.nn.functional.pad(x, flat)
kernel_name = 'adaLN_cond__compressed_kv_cla__glu_gated_bilinear__prompt'

def setup_inputs():

    _w = lambda a: (lambda t: t.to(torch.bfloat16) if t.is_floating_point() else t)(torch.as_tensor(a))

    def _A_setup():
        B, H, Sq, Sk, D, M = (1, 32, 2048, 2048, 128, 512)
        Dc = 4 * D
        return {'x': _w((lambda *s: torch.randn(*s, dtype=torch.bfloat16) * 0.05)(B, Sq, M)), 'c_kv': _w((lambda *s: torch.randn(*s, dtype=torch.bfloat16) * 0.8)(B, Sk, Dc)), 'w_q': _w((lambda *s: torch.randn(*s, dtype=torch.bfloat16) * 1.6)(M, H, D) * M ** (-0.5)), 'w_uk': _w((lambda *s: torch.randn(*s, dtype=torch.bfloat16) * 1.6)(Dc, H, D) * Dc ** (-0.5)), 'w_uv': _w((lambda *s: torch.randn(*s, dtype=torch.bfloat16) * 0.8)(Dc, H, D) * Dc ** (-0.5)), 'w_o': _w((lambda *s: torch.randn(*s, dtype=torch.bfloat16) * 0.8)(H, D, M) * (H * D) ** (-0.5))}
    _A_in = _A_setup()

    def _B_setup():
        B, S, D, F = (1, 2048, 512, 1792)
        return {'x': _w((lambda *s: torch.randn(*s, dtype=torch.bfloat16) * 0.05)(B, S, D)), 'w_gate': _w((lambda *s: torch.randn(*s, dtype=torch.bfloat16) * 0.4)(D, F) * D ** (-0.5)), 'w_up': _w((lambda *s: torch.randn(*s, dtype=torch.bfloat16) * 0.4)(D, F) * D ** (-0.5)), 'w_down': _w((lambda *s: torch.randn(*s, dtype=torch.bfloat16) * 0.4)(F, D) * F ** (-0.5))}
    _B_in = _B_setup()
    return {'A_x': _A_in['x'], 'A_c_kv': _A_in['c_kv'], 'A_w_q': _A_in['w_q'], 'A_w_uk': _A_in['w_uk'], 'A_w_uv': _A_in['w_uv'], 'A_w_o': _A_in['w_o'], 'B_x': _B_in['x'], 'B_w_gate': _B_in['w_gate'], 'B_w_up': _B_in['w_up'], 'B_w_down': _B_in['w_down'], 'c': _w((lambda *s: torch.randn(*s, dtype=torch.bfloat16) * 0.2)(1, 512) * 8.0), 'w_mod': _w((lambda *s: torch.randn(*s, dtype=torch.bfloat16) * 0.2)(512, 6 * 512) * 512 ** (-0.5) * 8.0)}

def reference(A_x, A_c_kv, A_w_q, A_w_uk, A_w_uv, A_w_o, B_x, B_w_gate, B_w_up, B_w_down, c, w_mod):

    def _A_ref(x, c_kv, w_q, w_uk, w_uv, w_o):
        B, Sq, M = (x.shape[0], x.shape[1], x.shape[2])
        Sk = c_kv.shape[1]
        H, D = (w_q.shape[1], w_q.shape[2])
        q = torch.einsum('bqm,mhd->bhqd', x, w_q)
        k = torch.einsum('bkc,chd->bhkd', c_kv, w_uk)
        v = torch.einsum('bkc,chd->bhkd', c_kv, w_uv)
        scores = torch.einsum('bhqd,bhkd->bhqk', q, k) * D ** (-0.5)
        off = Sk - Sq
        qi = torch.arange(Sq)[:, None]
        kj = torch.arange(Sk)[None, :]
        cmask = kj <= qi + off
        scores = torch.where(cmask[None, None], scores, -1000000000.0)
        p = _softmax(scores, axis=-1)
        ctx = torch.einsum('bhqk,bhkd->bhqd', p, v)
        return torch.einsum('bhqd,hdm->bqm', ctx, w_o)

    def _B_ref(x, w_gate, w_up, w_down):
        g = torch.einsum('bsd,df->bsf', x, w_gate)
        u = torch.einsum('bsd,df->bsf', x, w_up)
        h = g * u
        return torch.einsum('bsf,fd->bsd', h, w_down)
    x = A_x
    mod = (_silu(c) @ w_mod).reshape(c.shape[0], 6, 1, -1)
    sa, ca, ga, sb, cb, gb = (mod[:, 0].clone(), mod[:, 1].clone(), mod[:, 2].clone(), mod[:, 3].clone(), mod[:, 4].clone(), mod[:, 5].clone())
    h = _layernorm(x, axis=-1) * (1 + ca) + sa
    x = x + ga * _A_ref(h, A_c_kv, A_w_q, A_w_uk, A_w_uv, A_w_o)
    h = _layernorm(x, axis=-1) * (1 + cb) + sb
    x = x + gb * _B_ref(h, B_w_gate, B_w_up, B_w_down)
    return x
```

```cpp
#include <hip/hip_runtime.h>
#include <hip/hip_bf16.h>
typedef unsigned short u16;
typedef short bf16x8 __attribute__((ext_vector_type(8)));
typedef float f32x16 __attribute__((ext_vector_type(16)));
typedef float f32x4 __attribute__((ext_vector_type(4)));
typedef unsigned u32x4 __attribute__((ext_vector_type(4)));
typedef unsigned u32x2 __attribute__((ext_vector_type(2)));
#define DI __device__ __forceinline__
#define MFMA(a, b, c) __builtin_amdgcn_mfma_f32_32x32x16_bf16((a), (b), (c), 0, 0, 0)
DI float bf2f(u16 v) { return __uint_as_float((unsigned)v << 16); }
DI unsigned cvtpk(float lo, float hi) { unsigned r; asm("v_cvt_pk_bf16_f32 %0, %1, %2" : "=v"(r) : "v"(lo), "v"(hi)); return r; }
DI float rbf(float x) { return __uint_as_float(cvtpk(x, 0.f) << 16); }
DI int corder(int h, int j) { return 8 * (j >> 2) + 4 * h + (j & 3); }
DI bf16x8 pack8(const f32x16& x, int s, float sc) {
  u32x4 p = {cvtpk(x[8*s]*sc, x[8*s+1]*sc), cvtpk(x[8*s+2]*sc, x[8*s+3]*sc), cvtpk(x[8*s+4]*sc, x[8*s+5]*sc), cvtpk(x[8*s+6]*sc, x[8*s+7]*sc)};
  return __builtin_bit_cast(bf16x8, p);
}
constexpr int S = 2048, M = 512, H = 32, D = 128, F = 1792;
constexpr size_t WS_MOD = 0, WS_XH = 65536, WS_KI = WS_XH + (size_t)S * M * 2, WS_VI = WS_KI + (size_t)S * H * D * 2, WS_CI = WS_VI + (size_t)S * H * D * 2,
  WS_X1 = WS_CI + (size_t)S * H * D * 2, WS_H2 = WS_X1 + (size_t)S * M * 4, WS_HH = WS_H2 + (size_t)S * M * 2, WS_END = WS_HH + (size_t)S * F * 2;

template <bool CORD> DI bf16x8 wfrag(const u16* W, int ldw, int k0, int n0, int r, int h) {
  bf16x8 f;
#pragma unroll
  for (int j = 0; j < 8; ++j) f[j] = (short)W[(size_t)(k0 + (CORD ? corder(h, j) : 8 * h + j)) * ldw + n0 + r];
  return f;
}
__global__ void k_mod(const u16* c, const u16* wmod, float* mod) {
  int n = blockIdx.x * 64 + threadIdx.x; float acc = 0.f;
  for (int k = 0; k < M; ++k) { float cv = bf2f(c[k]); float s = rbf(cv / (1.f + __expf(-cv))); acc += s * bf2f(wmod[(size_t)k * 3072 + n]); }
  float v = rbf(acc); int which = n / M; if (which == 1 || which == 4) v = rbf(1.f + v);
  mod[n] = v;
}
__global__ void k_xhat(const u16* x, u16* XH) {
  int row = blockIdx.x, lane = threadIdx.x; float v[8]; float s = 0.f;
  bf16x8 xv = *(const bf16x8*)(x + (size_t)row * M + lane * 8);
  for (int j = 0; j < 8; ++j) { v[j] = bf2f((u16)xv[j]); s += v[j]; }
  for (int o = 32; o; o >>= 1) s += __shfl_xor(s, o);
  float mean = s / M, q = 0.f; for (int j = 0; j < 8; ++j) { v[j] -= mean; q += v[j] * v[j]; }
  for (int o = 32; o; o >>= 1) q += __shfl_xor(q, o);
  float rstd = rsqrtf(q / M + 1e-5f);
  u32x4 p = {cvtpk(v[0]*rstd, v[1]*rstd), cvtpk(v[2]*rstd, v[3]*rstd), cvtpk(v[4]*rstd, v[5]*rstd), cvtpk(v[6]*rstd, v[7]*rstd)};
  int ks = lane >> 1, h = lane & 1;
  *(u32x4*)(XH + ((size_t)((row >> 5) * 32 + ks) * 64 + h * 32 + (row & 31)) * 8) = p;
}
__global__ __launch_bounds__(64) void k_kv(const u16* ckv, const u16* wuk, const u16* wuv, u16* KI, u16* VI) {
  int lane = threadIdx.x, r = lane & 31, h = lane >> 5; int kb32 = blockIdx.x, head = blockIdx.y, isv = blockIdx.z;
  f32x16 acc[4] = {};
  const u16* W = isv ? wuv : wuk;
  for (int ks = 0; ks < 32; ++ks) {
    bf16x8 a = *(const bf16x8*)(ckv + (size_t)(kb32 * 32 + r) * M + ks * 16 + 8 * h);
#pragma unroll
    for (int db = 0; db < 4; ++db) { bf16x8 w = wfrag<false>(W, H * D, ks * 16, head * D + db * 32, r, h);
      acc[db] = isv ? MFMA(a, w, acc[db]) : MFMA(w, a, acc[db]); }
  }
  int t = kb32 >> 1, kb = kb32 & 1;
#pragma unroll
  for (int db = 0; db < 4; ++db)
#pragma unroll
    for (int s = 0; s < 2; ++s) { bf16x8 f = pack8(acc[db], s, 1.f);
      if (isv) *(bf16x8*)(VI + (((size_t)(head * 32 + t) * 4 + db) * 4 + 2 * kb + s) * 512 + lane * 8) = f;
      else     *(bf16x8*)(KI + (((size_t)(head * 32 + t) * 2 + kb) * 8 + 2 * db + s) * 512 + lane * 8) = f; }
}
__global__ __launch_bounds__(64) void k_attn(const u16* XH, const float* mod, const u16* wq, const u16* KI, const u16* VI, u16* CI) {
  int lane = threadIdx.x, r = lane & 31, h = lane >> 5; int qb = 63 - blockIdx.x, head = blockIdx.y;
  f32x16 o[4] = {};
  for (int ks = 0; ks < 32; ++ks) {
    bf16x8 xh = *(const bf16x8*)(XH + ((size_t)(qb * 32 + ks) * 64 + lane) * 8);
    float y[8];
#pragma unroll
    for (int j = 0; j < 8; ++j) { int k = ks * 16 + 8 * h + j; y[j] = bf2f((u16)xh[j]) * mod[512 + k] + mod[k]; }
    u32x4 p = {cvtpk(y[0], y[1]), cvtpk(y[2], y[3]), cvtpk(y[4], y[5]), cvtpk(y[6], y[7])};
    bf16x8 hf = __builtin_bit_cast(bf16x8, p);
#pragma unroll
    for (int db = 0; db < 4; ++db) { bf16x8 w = wfrag<false>(wq, H * D, ks * 16, head * D + db * 32, r, h); o[db] = MFMA(w, hf, o[db]); }
  }
  const float C = 0.08838834764831845f * 1.4426950408889634f;
  bf16x8 q[8];
#pragma unroll
  for (int db = 0; db < 4; ++db) { q[2*db] = pack8(o[db], 0, C); q[2*db+1] = pack8(o[db], 1, C); o[db] = f32x16{}; }
  float m = -1e30f, l = 0.f;
  int nt = (qb * 32 + 31) / 64 + 1; int qrow = qb * 32 + r;
  for (int t = 0; t < nt; ++t) {
    f32x16 s[2] = {};
#pragma unroll
    for (int kb = 0; kb < 2; ++kb)
#pragma unroll
      for (int d0 = 0; d0 < 8; ++d0) { bf16x8 kf = *(const bf16x8*)(KI + (((size_t)(head * 32 + t) * 2 + kb) * 8 + d0) * 512 + lane * 8); s[kb] = MFMA(kf, q[d0], s[kb]); }
    float mx = -1e30f;
#pragma unroll
    for (int kb = 0; kb < 2; ++kb)
#pragma unroll
      for (int i = 0; i < 16; ++i) { int key = t * 64 + kb * 32 + (i & 3) + 8 * (i >> 2) + 4 * h; if (key > qrow) s[kb][i] = -__builtin_inff(); mx = fmaxf(mx, s[kb][i]); }
    mx = fmaxf(mx, __shfl_xor(mx, 32));
    float mn = fmaxf(m, mx), al = __builtin_amdgcn_exp2f(m - mn); m = mn; l *= al;
#pragma unroll
    for (int db = 0; db < 4; ++db) o[db] *= al;
#pragma unroll
    for (int kb = 0; kb < 2; ++kb)
#pragma unroll
      for (int i = 0; i < 16; ++i) { s[kb][i] = __builtin_amdgcn_exp2f(s[kb][i] - m); l += s[kb][i]; }
#pragma unroll
    for (int ks = 0; ks < 4; ++ks) { bf16x8 p = pack8(s[ks >> 1], ks & 1, 1.f);
#pragma unroll
      for (int db = 0; db < 4; ++db) { bf16x8 vf = *(const bf16x8*)(VI + (((size_t)(head * 32 + t) * 4 + db) * 4 + ks) * 512 + lane * 8); o[db] = MFMA(vf, p, o[db]); } }
  }
  l += __shfl_xor(l, 32); float il = 1.f / l;
#pragma unroll
  for (int db = 0; db < 4; ++db)
#pragma unroll
    for (int s = 0; s < 2; ++s) *(bf16x8*)(CI + (((size_t)qb * 32 + head) * 8 + 2 * db + s) * 512 + lane * 8) = pack8(o[db], s, il);
}
__global__ __launch_bounds__(64) void k_oproj(const u16* CI, const u16* wo, const u16* x, const float* mod, float* X1) {
  int lane = threadIdx.x, r = lane & 31, h = lane >> 5; int qb = blockIdx.x, mb = blockIdx.y;
  f32x16 acc = {};
  for (int kk = 0; kk < 256; ++kk) { bf16x8 cf = *(const bf16x8*)(CI + ((size_t)qb * 256 + kk) * 512 + lane * 8);
    bf16x8 w = wfrag<true>(wo, M, kk * 16, mb * 32, r, h); acc = MFMA(w, cf, acc); }
#pragma unroll
  for (int i = 0; i < 16; ++i) { int mcol = mb * 32 + (i & 3) + 8 * (i >> 2) + 4 * h; size_t o = (size_t)(qb * 32 + r) * M + mcol; X1[o] = bf2f(x[o]) + mod[2 * 512 + mcol] * acc[i]; }
}
__global__ void k_ln2(const float* X1, const float* mod, u16* H2) {
  int row = blockIdx.x, lane = threadIdx.x; int ks = lane >> 1, h = lane & 1; float v[8]; float s = 0.f;
  for (int j = 0; j < 8; ++j) { v[j] = X1[(size_t)row * M + ks * 16 + corder(h, j)]; s += v[j]; }
  for (int o = 32; o; o >>= 1) s += __shfl_xor(s, o);
  float mean = s / M, q = 0.f; for (int j = 0; j < 8; ++j) { v[j] -= mean; q += v[j] * v[j]; }
  for (int o = 32; o; o >>= 1) q += __shfl_xor(q, o);
  float rstd = rsqrtf(q / M + 1e-5f);
  for (int j = 0; j < 8; ++j) { int k = ks * 16 + corder(h, j); v[j] = v[j] * rstd * mod[4 * 512 + k] + mod[3 * 512 + k]; }
  u32x4 p = {cvtpk(v[0], v[1]), cvtpk(v[2], v[3]), cvtpk(v[4], v[5]), cvtpk(v[6], v[7])};
  *(u32x4*)(H2 + ((size_t)((row >> 5) * 32 + ks) * 64 + h * 32 + (row & 31)) * 8) = p;
}
__global__ __launch_bounds__(64) void k_gateup(const u16* H2, const u16* wg, const u16* wu, u16* HH) {
  int lane = threadIdx.x, r = lane & 31, h = lane >> 5; int qb = blockIdx.x, fb = blockIdx.y;
  f32x16 g = {}, u = {};
  for (int ks = 0; ks < 32; ++ks) { bf16x8 a = *(const bf16x8*)(H2 + ((size_t)qb * 32 + ks) * 512 + lane * 8);
    g = MFMA(wfrag<true>(wg, F, ks * 16, fb * 32, r, h), a, g); u = MFMA(wfrag<true>(wu, F, ks * 16, fb * 32, r, h), a, u); }
#pragma unroll
  for (int i = 0; i < 16; ++i) g[i] = rbf(g[i]) * rbf(u[i]);
#pragma unroll
  for (int s = 0; s < 2; ++s) *(bf16x8*)(HH + ((size_t)qb * 112 + 2 * fb + s) * 512 + lane * 8) = pack8(g, s, 1.f);
}
__global__ __launch_bounds__(64) void k_down(const u16* HH, const u16* wd, const float* X1, const float* mod, u16* out) {
  int lane = threadIdx.x, r = lane & 31, h = lane >> 5; int qb = blockIdx.x, mb = blockIdx.y;
  f32x16 acc = {};
  for (int kk = 0; kk < 112; ++kk) { bf16x8 cf = *(const bf16x8*)(HH + ((size_t)qb * 112 + kk) * 512 + lane * 8);
    acc = MFMA(wfrag<true>(wd, M, kk * 16, mb * 32, r, h), cf, acc); }
#pragma unroll
  for (int i = 0; i < 16; ++i) { int mcol = mb * 32 + (i & 3) + 8 * (i >> 2) + 4 * h; size_t o = (size_t)(qb * 32 + r) * M + mcol;
    out[o] = (u16)(cvtpk(X1[o] + mod[5 * 512 + mcol] * acc[i], 0.f) & 0xffff); }
}
extern "C" void kernel_launch(void* const* d_in, const int* in_sizes, int n_in, void* d_out, int out_size, void* d_ws, size_t ws_size, hipStream_t stream) {
  const u16* x = (const u16*)d_in[0]; const u16* ckv = (const u16*)d_in[1]; const u16* wq = (const u16*)d_in[2]; const u16* wuk = (const u16*)d_in[3];
  const u16* wuv = (const u16*)d_in[4]; const u16* wo = (const u16*)d_in[5]; const u16* wg = (const u16*)d_in[7]; const u16* wu = (const u16*)d_in[8];
  const u16* wd = (const u16*)d_in[9]; const u16* c = (const u16*)d_in[10]; const u16* wmod = (const u16*)d_in[11];
  char* ws = (char*)d_ws; float* mod = (float*)(ws + WS_MOD); u16* XH = (u16*)(ws + WS_XH); u16* KI = (u16*)(ws + WS_KI); u16* VI = (u16*)(ws + WS_VI);
  u16* CI = (u16*)(ws + WS_CI); float* X1 = (float*)(ws + WS_X1); u16* H2 = (u16*)(ws + WS_H2); u16* HH = (u16*)(ws + WS_HH);
  k_mod<<<48, 64, 0, stream>>>(c, wmod, mod);
  k_xhat<<<S, 64, 0, stream>>>(x, XH);
  k_kv<<<dim3(64, 32, 2), 64, 0, stream>>>(ckv, wuk, wuv, KI, VI);
  k_attn<<<dim3(64, 32), 64, 0, stream>>>(XH, mod, wq, KI, VI, CI);
  k_oproj<<<dim3(64, 16), 64, 0, stream>>>(CI, wo, x, mod, X1);
  k_ln2<<<S, 64, 0, stream>>>(X1, mod, H2);
  k_gateup<<<dim3(64, 56), 64, 0, stream>>>(H2, wg, wu, HH);
  k_down<<<dim3(64, 16), 64, 0, stream>>>(HH, wd, X1, mod, (u16*)d_out);
}
```

```cpp
#include <hip/hip_runtime.h>
#include <hip/hip_bf16.h>
typedef unsigned short u16;
constexpr size_t WS_SYNC = 0x0;
constexpr size_t WS_H1 = 0x80000;
constexpr size_t WS_KI = 0x280000;
constexpr size_t WS_VI = 0x1280000;
constexpr size_t WS_CI = 0x2280000;
constexpr size_t WS_X1 = 0x3280000;
constexpr size_t WS_H2 = 0x3680000;
constexpr size_t WS_HH = 0x3880000;
constexpr size_t WS_PT = 0x3f80000;
constexpr size_t WS_END = 0x4f80000;
constexpr size_t WS_DBG = 0x4f80000;
__global__ __launch_bounds__(256, 1) void k_all(const u16* x, const u16* ckv, const u16* wq, const u16* wuk, const u16* wuv, const u16* wo, const u16* wg, const u16* wu,
                                               const u16* wd, const u16* c, const u16* wmod, u16* out, char* ws) {
  const unsigned wave = __builtin_amdgcn_readfirstlane(threadIdx.x >> 6);
  const unsigned b = blockIdx.x;
  const unsigned long long karg = (unsigned long long)__builtin_amdgcn_kernarg_segment_ptr();
  asm volatile(
"s_load_dwordx16 s[56:71], %[karg], 0x0\n"
"s_load_dwordx8 s[72:79], %[karg], 0x40\n"
"s_load_dwordx2 s[80:81], %[karg], 0x60\n"
"s_mov_b32 s83, %[wave]\n"
"s_mov_b32 s84, %[bid]\n"
"v_mbcnt_lo_u32_b32 v255, -1, 0\n"
"v_mbcnt_hi_u32_b32 v255, -1, v255\n"
"v_lshlrev_b32 v248, 4, v255\n"
"s_and_b32 s92, s84, 7\n"
"s_lshl_b32 s92, s92, 2\n"
"s_bfe_u32 s85, s84, 0x20003\n"
"s_add_u32 s85, s85, s92\n"
"s_lshr_b32 s86, s84, 5\n"
"s_lshl_b32 s45, s83, 13\n"
"s_lshl_b32 s46, s83, 11\n"
"s_lshl_b32 s54, s83, 12\n"
"s_bfe_u32 s49, s84, 0x20003\n"
"s_lshl_b32 s49, s49, 1\n"
"s_add_u32 s49, s49, s86\n"
"s_and_b32 s49, s49, 7\n"
"s_lshl_b32 s44, s49, 7\n"
"s_lshl_b32 s49, s49, 19\n"
"v_lshrrev_b32 v253, 3, v255\n"
"v_and_b32 v252, 7, v255\n"
"v_lshrrev_b32 v1, 4, v255\n"
"v_xor_b32 v1, v1, v252\n"
"v_lshlrev_b32 v1, 4, v1\n"
"s_lshl_b32 s92, s83, 16\n"
"v_lshl_add_u32 v236, v253, 10, v1\n"
"v_add_u32 v236, s92, v236\n"
"v_xor_b32 v237, 64, v236\n"
"v_add_u32 v237, 7168, v237\n"
"v_add_u32 v250, 14336, v236\n"
"v_add_u32 v251, 14336, v237\n"
"v_bfe_u32 v1, v255, 4, 1\n"
"v_lshlrev_b32 v1, 1, v1\n"
"v_xor_b32 v1, v1, v252\n"
"v_lshlrev_b32 v1, 4, v1\n"
"s_lshl_b32 s92, s83, 17\n"
"v_lshl_add_u32 v238, v253, 13, v1\n"
"v_add_u32 v238, s92, v238\n"
"v_xor_b32 v239, 64, v238\n"
"v_add_u32 v239, 64512, v239\n"
"v_mul_u32_u24 v232, 0x1800, v253\n"
"v_lshl_add_u32 v232, v252, 4, v232\n"
"v_lshlrev_b32 v233, 1, v253\n"
"v_lshlrev_b32 v234, 10, v252\n"
"v_lshl_add_u32 v234, v253, 4, v234\n"
"s_waitcnt lgkmcnt(0)\n"
"s_lshl_b32 s92, s86, 18\n"
"s_add_u32 s16, s58, s92\n"
"s_addc_u32 s17, s59, 0\n"
"s_and_b32 s17, s17, 0xffff\n"
"s_mov_b32 s18, 0x40000\n"
"s_mov_b32 s19, 0x20000\n"
"s_lshl_b32 s92, s85, 8\n"
"s_add_u32 s20, s62, s92\n"
"s_addc_u32 s21, s63, 0\n"
"s_and_b32 s21, s21, 0xffff\n"
"s_mov_b32 s22, 0x400000\n"
"s_mov_b32 s23, 0x20000\n"
"s_sub_u32 s22, s22, s92\n"
"s_add_u32 s88, s80, 0x0\n"
"s_addc_u32 s89, s81, 0\n"
"s_and_b32 s89, s89, 0xffff\n"
"s_mov_b32 s90, 0x68000\n"
"s_mov_b32 s91, 0x20000\n"
"s_add_u32 s40, s56, 0\n"
"s_addc_u32 s41, s57, 0\n"
"s_and_b32 s41, s41, 0xffff\n"
"s_mov_b32 s42, 0x200000\n"
"s_mov_b32 s43, 0x20000\n"
"s_lshl_b32 s95, s83, 8\n"
"s_lshl_b32 s94, s84, 13\n"
"s_add_u32 s94, s94, s95\n"
"buffer_load_dwordx4 v[216:219], v234, s[40:43], s94 offen\n"
"buffer_load_dwordx4 v[220:223], v234, s[40:43], s94 offen offset:128\n"
"buffer_load_dword v247, off, s[88:91], 0 sc1\n"
"s_mov_b32 s95, 0x7ffffff0\n"
"s_cmp_eq_u32 s84, 0\n"
"s_cselect_b32 s95, 0x400, s95\n"
"v_mov_b32 v2, 0\n"
"s_mov_b64 exec, 1\n"
"buffer_store_dword v2, off, s[88:91], s95 sc1\n"
"s_mov_b64 exec, -1\n"
"s_and_b32 s87, s84, 7\n"
"s_mul_i32 s87, s87, 24\n"
"s_lshr_b32 s94, s84, 3\n"
"s_add_u32 s87, s87, s94\n"
"s_cmp_lt_u32 s94, 24\n"
"s_cselect_b32 s87, s87, 0\n"
"s_cselect_b32 s51, 0, 0x1000000\n"
"s_cselect_b32 s52, 0x300000, 0\n"
"s_add_u32 s28, s76, 0\n"
"s_addc_u32 s29, s77, 0\n"
"s_and_b32 s29, s29, 0xffff\n"
"s_mov_b32 s30, s52\n"
"s_mov_b32 s31, 0x20000\n"
"s_add_u32 s36, s74, 0\n"
"s_addc_u32 s37, s75, 0\n"
"s_and_b32 s37, s37, 0xffff\n"
"s_mov_b32 s38, 0x400\n"
"s_mov_b32 s39, 0x20000\n"
"s_and_b32 s94, s87, 3\n"
"s_lshl_b32 s94, s94, 2\n"
"s_add_u32 s94, s94, s83\n"
"s_lshl_b32 s95, s94, 6\n"
"s_mul_i32 s55, s94, 196608\n"
"s_lshr_b32 s94, s87, 2\n"
"s_lshl_b32 s94, s94, 7\n"
"s_add_u32 s55, s55, s94\n"
"buffer_load_ushort v228, v233, s[36:39], s95 offen offset:0\n"
"buffer_load_ushort v229, v233, s[36:39], s95 offen offset:16\n"
"buffer_load_ushort v230, v233, s[36:39], s95 offen offset:32\n"
"buffer_load_ushort v231, v233, s[36:39], s95 offen offset:48\n"
"buffer_load_dwordx4 v[192:195], v232, s[28:31], s55 offen\n"
"s_add_u32 s55, s55, 49152\n"
"buffer_load_dwordx4 v[196:199], v232, s[28:31], s55 offen\n"
"s_add_u32 s55, s55, 49152\n"
"buffer_load_dwordx4 v[200:203], v232, s[28:31], s55 offen\n"
"s_add_u32 s55, s55, 49152\n"
"buffer_load_dwordx4 v[204:207], v232, s[28:31], s55 offen\n"
"s_add_u32 m0, s46, 98304\n"
"s_add_u32 s92, s49, 0\n"
"s_and_b32 s92, s92, 0x380000\n"
"buffer_load_dwordx4 v238, s[20:23], s92 offen offset:0 lds\n"
"buffer_load_dwordx4 v239, s[20:23], s92 offen offset:1024 lds\n"
"s_add_u32 s50, s44, 0\n"
"s_and_b32 s50, s50, 0x380\n"
"s_add_u32 m0, s45, 0\n"
"s_nop 0\n"
"buffer_load_dwordx4 v236, s[16:19], s50 offen offset:0 lds\n"
"buffer_load_dwordx4 v237, s[16:19], s50 offen offset:1024 lds\n"
"buffer_load_dwordx4 v250, s[16:19], s50 offen offset:2048 lds\n"
"buffer_load_dwordx4 v251, s[16:19], s50 offen offset:3072 lds\n"
"s_add_u32 m0, s45, 4096\n"
"s_add_u32 s50, s50, 32768\n"
"buffer_load_dwordx4 v236, s[16:19], s50 offen offset:0 lds\n"
"buffer_load_dwordx4 v237, s[16:19], s50 offen offset:1024 lds\n"
"buffer_load_dwordx4 v250, s[16:19], s50 offen offset:2048 lds\n"
"buffer_load_dwordx4 v251, s[16:19], s50 offen offset:3072 lds\n"
"s_add_u32 m0, s46, 106368\n"
"s_add_u32 s92, s49, 0\n"
"s_and_b32 s92, s92, 0x380000\n"
"buffer_load_dwordx4 v238, s[20:23], s92 offen offset:128 lds\n"
"buffer_load_dwordx4 v239, s[20:23], s92 offen offset:1152 lds\n"
"s_getpc_b64 s[92:93]\n"
"LPC_%=:\n"
"s_lshr_b32 s94, s84, 3\n"
"s_lshl_b32 s95, s83, 5\n"
"s_add_u32 s95, s95, s94\n"
"s_lshl_b32 s94, s94, 13\n"
"s_min_u32 s94, s94, LHOTEND_%=-LPC_%=-8192\n"
"s_cmp_lt_u32 s95, 16\n"
"s_cselect_b32 s95, 128, 0\n"
"v_mul_u32_u24 v235, s95, v255\n"
"v_add_u32 v235, s94, v235\n"
"global_load_dword v249, v235, s[92:93]\n"
"s_lshl_b32 s94, s85, 8\n"
"s_add_u32 s24, s64, s94\n"
"s_addc_u32 s25, s65, 0\n"
"s_and_b32 s25, s25, 0xffff\n"
"s_mov_b32 s26, 0x400000\n"
"s_mov_b32 s27, 0x20000\n"
"s_sub_u32 s26, s26, s94\n"
"v_and_b32 v0, 3, v255\n"
"v_bfe_u32 v253, v255, 2, 1\n"
"v_lshl_add_u32 v0, v253, 3, v0\n"
"v_bfe_u32 v253, v255, 3, 1\n"
"v_lshl_add_u32 v0, v253, 2, v0\n"
"v_and_b32 v253, 48, v255\n"
"v_add_u32 v253, v253, v0\n"
"v_lshlrev_b32 v246, 4, v253\n"
"v_lshrrev_b32 v253, 3, v0\n"
"v_lshlrev_b32 v240, 10, v253\n"
"v_and_b32 v253, 7, v0\n"
"v_lshl_add_u32 v240, v253, 7, v240\n"
"v_lshrrev_b32 v253, 1, v0\n"
"v_lshrrev_b32 v252, 4, v255\n"
"s_add_u32 m0, s46, 114688\n"
"s_add_u32 s92, s49, 524288\n"
"s_and_b32 s92, s92, 0x380000\n"
"buffer_load_dwordx4 v238, s[20:23], s92 offen offset:0 lds\n"
"v_xor_b32 v253, v253, v252\n"
"v_lshl_add_u32 v240, v253, 4, v240\n"
"v_add_u32 v240, s45, v240\n"
"v_xor_b32 v241, 64, v240\n"
"v_lshlrev_b32 v242, 10, v252\n"
"v_bfe_u32 v253, v255, 2, 2\n"
"v_lshl_add_u32 v242, v253, 7, v242\n"
"v_bfe_u32 v253, v255, 3, 2\n"
"v_lshlrev_b32 v253, 1, v253\n"
"v_bfe_u32 v1, v255, 1, 1\n"
"v_xor_b32 v253, v253, v1\n"
"v_lshl_add_u32 v242, v253, 4, v242\n"
"v_and_b32 v1, 1, v255\n"
"v_lshl_add_u32 v242, v1, 3, v242\n"
"v_add_u32 v242, 0x10000, v242\n"
"v_xor_b32 v243, 32, v242\n"
"v_xor_b32 v244, 64, v242\n"
"v_xor_b32 v245, 96, v242\n"
"s_lshl_b32 s94, s84, 2\n"
"s_add_u32 s94, s94, s83\n"
"buffer_load_dwordx4 v239, s[20:23], s92 offen offset:1024 lds\n"
"s_lshl_b32 s95, s94, 12\n"
"v_and_b32 v252, 31, v255\n"
"v_lshlrev_b32 v252, 7, v252\n"
"v_add_u32 v252, s95, v252\n"
"s_mul_i32 s95, s94, 1792\n"
"v_min_u32 v253, 13, v255\n"
"v_lshlrev_b32 v253, 7, v253\n"
"v_add_u32 v253, s95, v253\n"
"s_lshr_b32 s94, s84, 3\n"
"s_lshl_b32 s94, s94, 2\n"
"s_add_u32 s94, s94, s83\n"
"s_lshl_b32 s94, s94, 15\n"
"s_and_b32 s95, s84, 7\n"
"s_lshl_b32 s95, s95, 10\n"
"s_add_u32 s94, s94, s95\n"
"v_bfe_u32 v252, v255, 3, 2\n"
"v_lshlrev_b32 v252, 13, v252\n"
"v_and_b32 v253, 7, v255\n"
"v_lshl_add_u32 v252, v253, 7, v252\n"
"v_add_u32 v252, s94, v252\n"
"s_add_u32 s50, s44, 128\n"
"s_and_b32 s50, s50, 0x380\n"
"s_add_u32 m0, s45, 32768\n"
"s_nop 0\n"
"buffer_load_dwordx4 v236, s[16:19], s50 offen offset:0 lds\n"
"s_lshl_b32 s48, s86, 2\n"
"s_add_u32 s48, s48, s83\n"
"s_lshl_b32 s48, s48, 14\n"
"s_waitcnt vmcnt(25)\n"
"v_lshlrev_b32 v208, 16, v216\n"
"v_and_b32 v209, 0xffff0000, v216\n"
"v_lshlrev_b32 v210, 16, v217\n"
"v_and_b32 v211, 0xffff0000, v217\n"
"v_lshlrev_b32 v212, 16, v218\n"
"v_and_b32 v213, 0xffff0000, v218\n"
"v_lshlrev_b32 v214, 16, v219\n"
"v_and_b32 v215, 0xffff0000, v219\n"
"v_lshlrev_b32 v216, 16, v220\n"
"v_and_b32 v217, 0xffff0000, v220\n"
"v_lshlrev_b32 v218, 16, v221\n"
"v_and_b32 v219, 0xffff0000, v221\n"
"v_lshlrev_b32 v220, 16, v222\n"
"v_and_b32 v221, 0xffff0000, v222\n"
"v_lshlrev_b32 v222, 16, v223\n"
"v_and_b32 v223, 0xffff0000, v223\n"
"buffer_load_dwordx4 v237, s[16:19], s50 offen offset:1024 lds\n"
"v_readfirstlane_b32 s82, v247\n"
"v_add_f32 v224, v208, v209\n"
"v_add_f32 v226, v210, v211\n"
"v_add_f32 v224, v224, v212\n"
"v_add_f32 v226, v226, v213\n"
"v_add_f32 v224, v224, v214\n"
"v_add_f32 v226, v226, v215\n"
"v_add_f32 v224, v224, v216\n"
"v_add_f32 v226, v226, v217\n"
"v_add_f32 v224, v224, v218\n"
"v_add_f32 v226, v226, v219\n"
"v_add_f32 v224, v224, v220\n"
"v_add_f32 v226, v226, v221\n"
"v_add_f32 v224, v224, v222\n"
"v_add_f32 v226, v226, v223\n"
"v_add_f32 v224, v224, v226\n"
"s_nop 1\n"
"v_add_f32_dpp v224, v224, v224 row_ror:8 row_mask:0xf bank_mask:0xf\n"
"v_mov_b32 v226, v224\n"
"s_nop 1\n"
"v_permlane16_swap_b32 v224, v226\n"
"v_add_f32 v224, v224, v226\n"
"buffer_load_dwordx4 v250, s[16:19], s50 offen offset:2048 lds\n"
"v_mov_b32 v226, v224\n"
"s_nop 1\n"
"v_permlane32_swap_b32 v224, v226\n"
"v_add_f32 v224, v224, v226\n"
"v_mul_f32 v224, 0x3c000000, v224\n"
"v_sub_f32 v208, v208, v224\n"
"v_sub_f32 v209, v209, v224\n"
"v_sub_f32 v210, v210, v224\n"
"v_sub_f32 v211, v211, v224\n"
"v_sub_f32 v212, v212, v224\n"
"v_sub_f32 v213, v213, v224\n"
"v_sub_f32 v214, v214, v224\n"
"v_sub_f32 v215, v215, v224\n"
"v_sub_f32 v216, v216, v224\n"
"v_sub_f32 v217, v217, v224\n"
"v_sub_f32 v218, v218, v224\n"
"v_sub_f32 v219, v219, v224\n"
"v_sub_f32 v220, v220, v224\n"
"v_sub_f32 v221, v221, v224\n"
"v_sub_f32 v222, v222, v224\n"
"v_sub_f32 v223, v223, v224\n"
"buffer_load_dwordx4 v251, s[16:19], s50 offen offset:3072 lds\n"
"v_mul_f32 v225, v208, v208\n"
"v_mul_f32 v226, v209, v209\n"
"v_fmac_f32 v225, v210, v210\n"
"v_fmac_f32 v226, v211, v211\n"
"v_fmac_f32 v225, v212, v212\n"
"v_fmac_f32 v226, v213, v213\n"
"v_fmac_f32 v225, v214, v214\n"
"v_fmac_f32 v226, v215, v215\n"
"v_fmac_f32 v225, v216, v216\n"
"v_fmac_f32 v226, v217, v217\n"
"v_fmac_f32 v225, v218, v218\n"
"v_fmac_f32 v226, v219, v219\n"
"v_fmac_f32 v225, v220, v220\n"
"v_fmac_f32 v226, v221, v221\n"
"v_fmac_f32 v225, v222, v222\n"
"v_fmac_f32 v226, v223, v223\n"
"v_add_f32 v225, v225, v226\n"
"s_nop 1\n"
"v_add_f32_dpp v225, v225, v225 row_ror:8 row_mask:0xf bank_mask:0xf\n"
"v_mov_b32 v226, v225\n"
"s_nop 1\n"
"v_permlane16_swap_b32 v225, v226\n"
"s_add_u32 m0, s45, 36864\n"
"s_add_u32 s50, s50, 32768\n"
"buffer_load_dwordx4 v236, s[16:19], s50 offen offset:0 lds\n"
"v_add_f32 v225, v225, v226\n"
"v_mov_b32 v226, v225\n"
"s_nop 1\n"
"v_permlane32_swap_b32 v225, v226\n"
"v_add_f32 v225, v225, v226\n"
"s_add_u32 s82, s82, 1\n"
"s_waitcnt vmcnt(20)\n"
"v_lshlrev_b32 v228, 16, v228\n"
"v_lshlrev_b32 v229, 16, v229\n"
"v_lshlrev_b32 v230, 16, v230\n"
"v_lshlrev_b32 v231, 16, v231\n"
"v_mul_f32 v232, 0xbfb8aa3b, v228\n"
"v_mul_f32 v233, 0xbfb8aa3b, v229\n"
"v_mul_f32 v234, 0xbfb8aa3b, v230\n"
"v_mul_f32 v235, 0xbfb8aa3b, v231\n"
"v_exp_f32 v232, v232\n"
"v_exp_f32 v233, v233\n"
"v_exp_f32 v234, v234\n"
"v_exp_f32 v235, v235\n"
"v_add_f32 v232, 1.0, v232\n"
"v_add_f32 v233, 1.0, v233\n"
"buffer_load_dwordx4 v237, s[16:19], s50 offen offset:1024 lds\n"
"v_add_f32 v234, 1.0, v234\n"
"v_add_f32 v235, 1.0, v235\n"
"v_rcp_f32 v232, v232\n"
"v_rcp_f32 v233, v233\n"
"v_rcp_f32 v234, v234\n"
"v_rcp_f32 v235, v235\n"
"v_mul_f32 v228, v228, v232\n"
"v_mul_f32 v229, v229, v233\n"
"v_mul_f32 v230, v230, v234\n"
"v_mul_f32 v231, v231, v235\n"
"v_cvt_pk_bf16_f32 v232, v228, v229\n"
"v_cvt_pk_bf16_f32 v233, v230, v231\n"
"v_lshlrev_b32 v228, 16, v232\n"
"v_and_b32 v229, 0xffff0000, v232\n"
"v_lshlrev_b32 v230, 16, v233\n"
"v_and_b32 v231, 0xffff0000, v233\n"
"v_lshlrev_b32 v232, 16, v192\n"
"v_and_b32 v233, 0xffff0000, v192\n"
"v_lshlrev_b32 v234, 16, v196\n"
"v_and_b32 v235, 0xffff0000, v196\n"
"buffer_load_dwordx4 v250, s[16:19], s50 offen offset:2048 lds\n"
"v_mul_f32 v192, v228, v232\n"
"v_mul_f32 v196, v228, v233\n"
"v_fmac_f32 v192, v229, v234\n"
"v_fmac_f32 v196, v229, v235\n"
"v_lshlrev_b32 v232, 16, v200\n"
"v_and_b32 v233, 0xffff0000, v200\n"
"v_lshlrev_b32 v234, 16, v204\n"
"v_and_b32 v235, 0xffff0000, v204\n"
"v_fmac_f32 v192, v230, v232\n"
"v_fmac_f32 v196, v230, v233\n"
"v_fmac_f32 v192, v231, v234\n"
"v_fmac_f32 v196, v231, v235\n"
"v_lshlrev_b32 v232, 16, v193\n"
"v_and_b32 v233, 0xffff0000, v193\n"
"v_lshlrev_b32 v234, 16, v197\n"
"v_and_b32 v235, 0xffff0000, v197\n"
"v_mul_f32 v193, v228, v232\n"
"v_mul_f32 v197, v228, v233\n"
"v_fmac_f32 v193, v229, v234\n"
"v_fmac_f32 v197, v229, v235\n"
"buffer_load_dwordx4 v251, s[16:19], s50 offen offset:3072 lds\n"
"v_lshlrev_b32 v232, 16, v201\n"
"v_and_b32 v233, 0xffff0000, v201\n"
"v_lshlrev_b32 v234, 16, v205\n"
"v_and_b32 v235, 0xffff0000, v205\n"
"v_fmac_f32 v193, v230, v232\n"
"v_fmac_f32 v197, v230, v233\n"
"v_fmac_f32 v193, v231, v234\n"
"v_fmac_f32 v197, v231, v235\n"
"v_lshlrev_b32 v232, 16, v194\n"
"v_and_b32 v233, 0xffff0000, v194\n"
"v_lshlrev_b32 v234, 16, v198\n"
"v_and_b32 v235, 0xffff0000, v198\n"
"v_mul_f32 v194, v228, v232\n"
"v_mul_f32 v198, v228, v233\n"
"v_fmac_f32 v194, v229, v234\n"
"v_fmac_f32 v198, v229, v235\n"
"v_lshlrev_b32 v232, 16, v202\n"
"v_and_b32 v233, 0xffff0000, v202\n"
"v_lshlrev_b32 v234, 16, v206\n"
"v_and_b32 v235, 0xffff0000, v206\n"
"s_add_u32 m0, s46, 122752\n"
"s_add_u32 s92, s49, 524288\n"
"s_and_b32 s92, s92, 0x380000\n"
"buffer_load_dwordx4 v238, s[20:23], s92 offen offset:128 lds\n"
"v_fmac_f32 v194, v230, v232\n"
"v_fmac_f32 v198, v230, v233\n"
"v_fmac_f32 v194, v231, v234\n"
"v_fmac_f32 v198, v231, v235\n"
"v_lshlrev_b32 v232, 16, v195\n"
"v_and_b32 v233, 0xffff0000, v195\n"
"v_lshlrev_b32 v234, 16, v199\n"
"v_and_b32 v235, 0xffff0000, v199\n"
"v_mul_f32 v195, v228, v232\n"
"v_mul_f32 v199, v228, v233\n"
"v_fmac_f32 v195, v229, v234\n"
"v_fmac_f32 v199, v229, v235\n"
"v_lshlrev_b32 v232, 16, v203\n"
"v_and_b32 v233, 0xffff0000, v203\n"
"v_lshlrev_b32 v234, 16, v207\n"
"v_and_b32 v235, 0xffff0000, v207\n"
"v_fmac_f32 v195, v230, v232\n"
"v_fmac_f32 v199, v230, v233\n"
"v_fmac_f32 v195, v231, v234\n"
"v_fmac_f32 v199, v231, v235\n"
"buffer_load_dwordx4 v239, s[20:23], s92 offen offset:1152 lds\n"
"s_nop 1\n"
"v_permlane32_swap_b32 v192, v194\n"
"v_permlane32_swap_b32 v196, v198\n"
"v_permlane32_swap_b32 v193, v195\n"
"v_permlane32_swap_b32 v197, v199\n"
"v_add_f32 v192, v192, v194\n"
"v_add_f32 v196, v196, v198\n"
"v_add_f32 v193, v193, v195\n"
"v_add_f32 v197, v197, v199\n"
"s_nop 1\n"
"v_permlane16_swap_b32 v192, v193\n"
"v_permlane16_swap_b32 v196, v197\n"
"v_add_f32 v192, v192, v193\n"
"v_add_f32 v196, v196, v197\n"
"s_nop 1\n"
"v_add_f32_dpp v200, v192, v192 row_ror:8 row_mask:0xf bank_mask:0xf\n"
"v_add_f32_dpp v201, v196, v196 row_ror:8 row_mask:0xf bank_mask:0xf\n"
"v_and_b32 v205, 7, v255\n"
"v_lshlrev_b32 v202, 2, v205\n"
"v_lshrrev_b32 v203, 4, v255\n"
"v_add_u32 v202, v202, v203\n"
"v_lshlrev_b32 v202, 3, v202\n"
"s_lshl_b32 s94, s83, 8\n"
"s_add_u32 s94, s94, 0x24000\n"
"v_add_u32 v202, s94, v202\n"
"v_lshlrev_b32 v203, 5, v205\n"
"v_add_u32 v203, 0x24400, v203\n"
"s_lshl_b32 s94, s83, 3\n"
"v_add_u32 v204, s94, v203\n"
"ds_write_b64 v202, v[200:201]\n"
"ds_write_b64 v204, v[224:225]\n"
"v_and_b32 v205, 31, v255\n"
"v_lshlrev_b32 v206, 3, v205\n"
"v_add_u32 v206, 0x24000, v206\n"
"v_lshlrev_b32 v205, 4, v205\n"
"s_lshl_b32 s94, s85, 19\n"
"s_add_u32 s95, s94, 0x280000\n"
"s_add_u32 s28, s80, s95\n"
"s_addc_u32 s29, s81, 0\n"
"s_and_b32 s29, s29, 0xffff\n"
"s_mov_b32 s30, 0x80000\n"
"s_mov_b32 s31, 0x20000\n"
"s_add_u32 s95, s94, 0x1280000\n"
"s_add_u32 s36, s80, s95\n"
"s_addc_u32 s37, s81, 0\n"
"s_and_b32 s37, s37, 0xffff\n"
"s_mov_b32 s38, 0x80000\n"
"s_mov_b32 s39, 0x20000\n"
"s_add_u32 s40, s80, 0x80000\n"
"s_addc_u32 s41, s81, 0\n"
"s_and_b32 s41, s41, 0xffff\n"
"s_mov_b32 s42, 0x200000\n"
"s_mov_b32 s43, 0x20000\n"
"s_waitcnt vmcnt(13) lgkmcnt(0)\n"
"s_barrier\n"
"ds_read_b64_tr_b16 v[128:129], v242 offset:32768\n"
"ds_read_b64_tr_b16 v[130:131], v242 offset:33280\n"
"ds_read_b64_tr_b16 v[132:133], v243 offset:32768\n"
"ds_read_b64_tr_b16 v[134:135], v243 offset:33280\n"
"ds_read_b64_tr_b16 v[136:137], v244 offset:32768\n"
"ds_read_b64_tr_b16 v[138:139], v244 offset:33280\n"
"ds_read_b64_tr_b16 v[140:141], v245 offset:32768\n"
"ds_read_b64_tr_b16 v[142:143], v245 offset:33280\n"
"ds_read_b64_tr_b16 v[144:145], v242 offset:36864\n"
"ds_read_b64_tr_b16 v[146:147], v242 offset:37376\n"
"ds_read_b64_tr_b16 v[148:149], v243 offset:36864\n"
"ds_read_b64_tr_b16 v[150:151], v243 offset:37376\n"
"ds_read_b64_tr_b16 v[152:153], v244 offset:36864\n"
"ds_read_b64_tr_b16 v[154:155], v244 offset:37376\n"
"ds_read_b64_tr_b16 v[156:157], v245 offset:36864\n"
"ds_read_b64_tr_b16 v[158:159], v245 offset:37376\n"
"ds_read_b128 a[0:3], v240 offset:0\n"
"ds_read_b128 a[4:7], v240 offset:2048\n"
"ds_read_b128 a[8:11], v240 offset:4096\n"
"ds_read_b128 a[12:15], v240 offset:6144\n"
"ds_read_b128 a[16:19], v241 offset:0\n"
"ds_read_b128 a[20:23], v241 offset:2048\n"
"ds_read_b128 a[24:27], v241 offset:4096\n"
"ds_read_b128 a[28:31], v241 offset:6144\n"
"ds_read_b128 v[228:231], v203\n"
"ds_read_b128 v[232:235], v203 offset:16\n"
"ds_read_b64 v[192:193], v206 offset:0\n"
"ds_read_b64 v[194:195], v206 offset:256\n"
"ds_read_b64 v[196:197], v206 offset:512\n"
"ds_read_b64 v[198:199], v206 offset:768\n"
"s_waitcnt lgkmcnt(0)\n"
"v_mfma_f32_16x16x32_bf16 v[0:3], v[128:131], a[0:3], 0\n"
"ds_read_b64_tr_b16 v[160:161], v242 offset:40960\n"
"s_add_u32 m0, s46, 131072\n"
"v_mfma_f32_16x16x32_bf16 v[0:3], v[144:147], a[16:19], v[0:3]\n"
"ds_read_b64_tr_b16 v[162:163], v242 offset:41472\n"
"s_add_u32 s92, s49, 1048576\n"
"v_mfma_f32_16x16x32_bf16 v[16:19], v[128:131], a[4:7], 0\n"
"v_add_f32 v192, v192, v194\n"
"v_add_f32 v193, v193, v195\n"
"v_mfma_f32_16x16x32_bf16 v[16:19], v[144:147], a[20:23], v[16:19]\n"
"v_add_f32 v196, v196, v198\n"
"v_add_f32 v197, v197, v199\n"
"v_mfma_f32_16x16x32_bf16 v[32:35], v[128:131], a[8:11], 0\n"
"ds_read_b64_tr_b16 v[164:165], v243 offset:40960\n"
"s_and_b32 s92, s92, 0x380000\n"
"v_mfma_f32_16x16x32_bf16 v[32:35], v[144:147], a[24:27], v[32:35]\n"
"v_add_f32 v192, v192, v196\n"
"ds_read_b64_tr_b16 v[166:167], v243 offset:41472\n"
"v_mfma_f32_16x16x32_bf16 v[48:51], v[128:131], a[12:15], 0\n"
"v_add_f32 v194, v193, v197\n"
"v_mov_b32 v193, s82\n"
"v_mfma_f32_16x16x32_bf16 v[48:51], v[144:147], a[28:31], v[48:51]\n"
"buffer_load_dwordx4 v238, s[20:23], s92 offen offset:0 lds\n"
"v_mfma_f32_16x16x32_bf16 v[4:7], v[132:135], a[0:3], 0\n"
"v_mov_b32 v195, s82\n"
"s_lshl_b32 s94, s87, 9\n"
"v_mfma_f32_16x16x32_bf16 v[4:7], v[148:151], a[16:19], v[4:7]\n"
"ds_read_b64_tr_b16 v[168:169], v244 offset:40960\n"
"s_add_u32 s94, s94, 0x50000\n"
"v_mfma_f32_16x16x32_bf16 v[20:23], v[132:135], a[4:7], 0\n"
"ds_read_b64_tr_b16 v[170:171], v244 offset:41472\n"
"buffer_load_dwordx4 v239, s[20:23], s92 offen offset:1024 lds\n"
"v_mfma_f32_16x16x32_bf16 v[20:23], v[148:151], a[20:23], v[20:23]\n"
"s_add_u32 s94, s94, s51\n"
"v_mfma_f32_16x16x32_bf16 v[36:39], v[132:135], a[8:11], 0\n"
"buffer_store_dwordx4 v[192:195], v205, s[88:91], s94 offen sc1\n"
"v_mfma_f32_16x16x32_bf16 v[36:39], v[148:151], a[24:27], v[36:39]\n"
"v_add_f32 v225, v228, v230\n"
"ds_read_b64_tr_b16 v[172:173], v245 offset:40960\n"
"v_mfma_f32_16x16x32_bf16 v[52:55], v[132:135], a[12:15], 0\n"
"s_add_u32 s50, s44, 256\n"
"v_add_f32 v227, v232, v234\n"
"v_mfma_f32_16x16x32_bf16 v[52:55], v[148:151], a[28:31], v[52:55]\n"
"v_add_f32 v225, v225, v227\n"
"ds_read_b64_tr_b16 v[174:175], v245 offset:41472\n"
"v_mfma_f32_16x16x32_bf16 v[8:11], v[136:139], a[0:3], 0\n"
"v_mul_f32 v225, 0x3e800000, v225\n"
"v_sub_f32 v228, v228, v225\n"
"v_mfma_f32_16x16x32_bf16 v[8:11], v[152:155], a[16:19], v[8:11]\n"
"s_and_b32 s50, s50, 0x380\n"
"v_sub_f32 v230, v230, v225\n"
"v_mfma_f32_16x16x32_bf16 v[24:27], v[136:139], a[4:7], 0\n"
"ds_read_b64_tr_b16 v[176:177], v242 offset:45056\n"
"v_sub_f32 v232, v232, v225\n"
"v_mfma_f32_16x16x32_bf16 v[24:27], v[152:155], a[20:23], v[24:27]\n"
"v_sub_f32 v234, v234, v225\n"
"s_add_u32 m0, s45, 65536\n"
"v_mfma_f32_16x16x32_bf16 v[40:43], v[136:139], a[8:11], 0\n"
"v_add_f32 v229, v229, v231\n"
"ds_read_b64_tr_b16 v[178:179], v242 offset:45568\n"
"v_mfma_f32_16x16x32_bf16 v[40:43], v[152:155], a[24:27], v[40:43]\n"
"v_add_f32 v233, v233, v235\n"
"v_mul_f32 v231, v228, v228\n"
"v_mfma_f32_16x16x32_bf16 v[56:59], v[136:139], a[12:15], 0\n"
"ds_read_b64_tr_b16 v[180:181], v243 offset:45056\n"
"s_nop 0\n"
"buffer_load_dwordx4 v236, s[16:19], s50 offen offset:0 lds\n"
"v_mfma_f32_16x16x32_bf16 v[56:59], v[152:155], a[28:31], v[56:59]\n"
"v_mfma_f32_16x16x32_bf16 v[12:15], v[140:143], a[0:3], 0\n"
"v_fmac_f32 v231, v230, v230\n"
"v_fmac_f32 v231, v232, v232\n"
"v_mfma_f32_16x16x32_bf16 v[12:15], v[156:159], a[16:19], v[12:15]\n"
"buffer_load_dwordx4 v237, s[16:19], s50 offen offset:1024 lds\n"
"v_mfma_f32_16x16x32_bf16 v[28:31], v[140:143], a[4:7], 0\n"
"v_fmac_f32 v231, v234, v234\n"
"ds_read_b64_tr_b16 v[182:183], v243 offset:45568\n"
"v_mfma_f32_16x16x32_bf16 v[28:31], v[156:159], a[20:23], v[28:31]\n"
"v_add_f32 v229, v229, v233\n"
"v_fmamk_f32 v229, v231, 0x43000000, v229\n"
"v_mfma_f32_16x16x32_bf16 v[44:47], v[140:143], a[8:11], 0\n"
"ds_read_b64_tr_b16 v[184:185], v244 offset:45056\n"
"buffer_load_dwordx4 v250, s[16:19], s50 offen offset:2048 lds\n"
"v_mfma_f32_16x16x32_bf16 v[44:47], v[156:159], a[24:27], v[44:47]\n"
"v_mov_b32 v231, 0x3727c5ac\n"
"v_mfma_f32_16x16x32_bf16 v[60:63], v[140:143], a[12:15], 0\n"
"v_fmamk_f32 v229, v229, 0x3b000000, v231\n"
"v_rsq_f32 v226, v229\n"
"v_mfma_f32_16x16x32_bf16 v[60:63], v[156:159], a[28:31], v[60:63]\n"
"ds_read_b64_tr_b16 v[186:187], v244 offset:45568\n"
"buffer_load_dwordx4 v251, s[16:19], s50 offen offset:3072 lds\n"
"v_sub_f32 v227, v224, v225\n"
"s_nop 0\n"
"v_mul_f32 v226, 0x3e0293ee, v226\n"
"ds_read_b64_tr_b16 v[188:189], v245 offset:45056\n"
"v_mul_f32 v227, v227, v226\n"
"s_add_u32 m0, s45, 69632\n"
"v_fma_f32 v208, v208, v226, v227\n"
"v_fma_f32 v209, v209, v226, v227\n"
"ds_read_b64_tr_b16 v[190:191], v245 offset:45568\n"
"v_fma_f32 v210, v210, v226, v227\n"
"v_fma_f32 v211, v211, v226, v227\n"
"s_add_u32 s50, s50, 32768\n"
"v_fma_f32 v212, v212, v226, v227\n"
"v_fma_f32 v213, v213, v226, v227\n"
"v_fma_f32 v214, v214, v226, v227\n"
"buffer_load_dwordx4 v236, s[16:19], s50 offen offset:0 lds\n"
"v_fma_f32 v215, v215, v226, v227\n"
"v_fma_f32 v216, v216, v226, v227\n"
"v_fma_f32 v217, v217, v226, v227\n"
"buffer_load_dwordx4 v237, s[16:19], s50 offen offset:1024 lds\n"
"v_fma_f32 v218, v218, v226, v227\n"
"v_fma_f32 v219, v219, v226, v227\n"
"v_fma_f32 v220, v220, v226, v227\n"
"buffer_load_dwordx4 v250, s[16:19], s50 offen offset:2048 lds\n"
"v_fma_f32 v221, v221, v226, v227\n"
"v_fma_f32 v222, v222, v226, v227\n"
"v_fma_f32 v223, v223, v226, v227\n"
"s_waitcnt vmcnt(10) lgkmcnt(0)\n"
"s_barrier\n"
"v_mfma_f32_16x16x32_bf16 v[64:67], v[160:163], a[0:3], 0\n"
"v_add_u32 v240, 0x8000, v240\n"
"v_add_u32 v241, 0x8000, v241\n"
"v_mfma_f32_16x16x32_bf16 v[64:67], v[176:179], a[16:19], v[64:67]\n"
"buffer_load_dwordx4 v251, s[16:19], s50 offen offset:3072 lds\n"
"v_mfma_f32_16x16x32_bf16 v[80:83], v[160:163], a[4:7], 0\n"
"ds_read_b64_tr_b16 v[128:129], v242 offset:49152\n"
"ds_read_b128 a[32:35], v240 offset:0\n"
"v_mfma_f32_16x16x32_bf16 v[80:83], v[176:179], a[20:23], v[80:83]\n"
"s_add_u32 m0, s46, 139136\n"
"ds_read_b64_tr_b16 v[130:131], v242 offset:49664\n"
"v_mfma_f32_16x16x32_bf16 v[96:99], v[160:163], a[8:11], 0\n"
"ds_read_b128 a[36:39], v240 offset:2048\n"
"s_add_u32 s92, s49, 1048576\n"
"v_mfma_f32_16x16x32_bf16 v[96:99], v[176:179], a[24:27], v[96:99]\n"
"ds_read_b64_tr_b16 v[132:133], v243 offset:49152\n"
"v_mfma_f32_16x16x32_bf16 v[112:115], v[160:163], a[12:15], 0\n"
"ds_read_b128 a[40:43], v240 offset:4096\n"
"s_and_b32 s92, s92, 0x380000\n"
"v_mfma_f32_16x16x32_bf16 v[112:115], v[176:179], a[28:31], v[112:115]\n"
"ds_read_b64_tr_b16 v[134:135], v243 offset:49664\n"
"v_mfma_f32_16x16x32_bf16 v[68:71], v[164:167], a[0:3], 0\n"
"ds_read_b128 a[44:47], v240 offset:6144\n"
"buffer_load_dwordx4 v238, s[20:23], s92 offen offset:128 lds\n"
"v_mfma_f32_16x16x32_bf16 v[68:71], v[180:183], a[16:19], v[68:71]\n"
"ds_read_b64_tr_b16 v[136:137], v244 offset:49152\n"
"v_mfma_f32_16x16x32_bf16 v[84:87], v[164:167], a[4:7], 0\n"
"ds_read_b128 a[48:51], v241 offset:0\n"
"buffer_load_dwordx4 v239, s[20:23], s92 offen offset:1152 lds\n"
"v_mfma_f32_16x16x32_bf16 v[84:87], v[180:183], a[20:23], v[84:87]\n"
"ds_read_b64_tr_b16 v[138:139], v244 offset:49664\n"
"v_mfma_f32_16x16x32_bf16 v[100:103], v[164:167], a[8:11], 0\n"
"ds_read_b128 a[52:55], v241 offset:2048\n"
"s_add_u32 m0, s46, 98304\n"
"v_mfma_f32_16x16x32_bf16 v[100:103], v[180:183], a[24:27], v[100:103]\n"
"ds_read_b64_tr_b16 v[140:141], v245 offset:49152\n"
"v_mfma_f32_16x16x32_bf16 v[116:119], v[164:167], a[12:15], 0\n"
"ds_read_b128 a[56:59], v241 offset:4096\n"
"s_add_u32 s92, s49, 1572864\n"
"v_mfma_f32_16x16x32_bf16 v[116:119], v[180:183], a[28:31], v[116:119]\n"
"ds_read_b64_tr_b16 v[142:143], v245 offset:49664\n"
"v_mfma_f32_16x16x32_bf16 v[72:75], v[168:171], a[0:3], 0\n"
"ds_read_b128 a[60:63], v241 offset:6144\n"
"s_and_b32 s92, s92, 0x380000\n"
"v_mfma_f32_16x16x32_bf16 v[72:75], v[184:187], a[16:19], v[72:75]\n"
"ds_read_b64_tr_b16 v[144:145], v242 offset:53248\n"
"v_mfma_f32_16x16x32_bf16 v[88:91], v[168:171], a[4:7], 0\n"
"ds_read_b64_tr_b16 v[146:147], v242 offset:53760\n"
"buffer_load_dwordx4 v238, s[20:23], s92 offen offset:0 lds\n"
"v_mfma_f32_16x16x32_bf16 v[88:91], v[184:187], a[20:23], v[88:91]\n"
"ds_read_b64_tr_b16 v[148:149], v243 offset:53248\n"
"v_mfma_f32_16x16x32_bf16 v[104:107], v[168:171], a[8:11], 0\n"
"ds_read_b64_tr_b16 v[150:151], v243 offset:53760\n"
"buffer_load_dwordx4 v239, s[20:23], s92 offen offset:1024 lds\n"
"v_mfma_f32_16x16x32_bf16 v[104:107], v[184:187], a[24:27], v[104:107]\n"
"ds_read_b64_tr_b16 v[152:153], v244 offset:53248\n"
"v_mfma_f32_16x16x32_bf16 v[120:123], v[168:171], a[12:15], 0\n"
"ds_read_b64_tr_b16 v[154:155], v244 offset:53760\n"
"s_add_u32 s50, s44, 384\n"
"v_mfma_f32_16x16x32_bf16 v[120:123], v[184:187], a[28:31], v[120:123]\n"
"ds_read_b64_tr_b16 v[156:157], v245 offset:53248\n"
"v_mfma_f32_16x16x32_bf16 v[76:79], v[172:175], a[0:3], 0\n"
"ds_read_b64_tr_b16 v[158:159], v245 offset:53760\n"
"s_and_b32 s50, s50, 0x380\n"
"v_mfma_f32_16x16x32_bf16 v[76:79], v[188:191], a[16:19], v[76:79]\n"
"v_mfma_f32_16x16x32_bf16 v[92:95], v[172:175], a[4:7], 0\n"
"s_add_u32 m0, s45, 0\n"
"v_mfma_f32_16x16x32_bf16 v[92:95], v[188:191], a[20:23], v[92:95]\n"
"v_mfma_f32_16x16x32_bf16 v[108:111], v[172:175], a[8:11], 0\n"
"s_nop 0\n"
"buffer_load_dwordx4 v236, s[16:19], s50 offen offset:0 lds\n"
"v_mfma_f32_16x16x32_bf16 v[108:111], v[188:191], a[24:27], v[108:111]\n"
"v_mfma_f32_16x16x32_bf16 v[124:127], v[172:175], a[12:15], 0\n"
"buffer_load_dwordx4 v237, s[16:19], s50 offen offset:1024 lds\n"
"v_mfma_f32_16x16x32_bf16 v[124:127], v[188:191], a[28:31], v[124:127]\n"
"s_waitcnt lgkmcnt(0)\n"
"v_mfma_f32_16x16x32_bf16 v[0:3], v[128:131], a[32:35], v[0:3]\n"
"ds_read_b64_tr_b16 v[160:161], v242 offset:57344\n"
"buffer_load_dwordx4 v250, s[16:19], s50 offen offset:2048 lds\n"
"v_mfma_f32_16x16x32_bf16 v[0:3], v[144:147], a[48:51], v[0:3]\n"
"ds_read_b64_tr_b16 v[162:163], v242 offset:57856\n"
"v_mfma_f32_16x16x32_bf16 v[16:19], v[128:131], a[36:39], v[16:19]\n"
"s_and_b32 s94, s87, 3\n"
"buffer_load_dwordx4 v251, s[16:19], s50 offen offset:3072 lds\n"
"v_mfma_f32_16x16x32_bf16 v[16:19], v[144:147], a[52:55], v[16:19]\n"
"s_lshl_b32 s94, s94, 7\n"
"v_mfma_f32_16x16x32_bf16 v[32:35], v[128:131], a[40:43], v[32:35]\n"
"ds_read_b64_tr_b16 v[164:165], v243 offset:57344\n"
"s_andn2_b32 s55, s87, 3\n"
"v_mfma_f32_16x16x32_bf16 v[32:35], v[144:147], a[56:59], v[32:35]\n"
"ds_read_b64_tr_b16 v[166:167], v243 offset:57856\n"
"s_add_u32 m0, s45, 4096\n"
"v_mfma_f32_16x16x32_bf16 v[48:51], v[128:131], a[44:47], v[48:51]\n"
"s_lshl_b32 s55, s55, 9\n"
"ds_read_b64_tr_b16 v[168:169], v244 offset:57344\n"
"v_mfma_f32_16x16x32_bf16 v[48:51], v[144:147], a[60:63], v[48:51]\n"
"s_add_u32 s55, s55, s94\n"
"ds_read_b64_tr_b16 v[170:171], v244 offset:57856\n"
"v_mfma_f32_16x16x32_bf16 v[4:7], v[132:135], a[32:35], v[4:7]\n"
"s_add_u32 s50, s50, 32768\n"
"s_add_u32 s55, s55, 0x50000\n"
"v_mfma_f32_16x16x32_bf16 v[4:7], v[148:151], a[48:51], v[4:7]\n"
"ds_read_b64_tr_b16 v[172:173], v245 offset:57344\n"
"buffer_load_dwordx4 v236, s[16:19], s50 offen offset:0 lds\n"
"v_mfma_f32_16x16x32_bf16 v[20:23], v[132:135], a[36:39], v[20:23]\n"
"v_and_b32 v225, 7, v255\n"
"v_mfma_f32_16x16x32_bf16 v[20:23], v[148:151], a[52:55], v[20:23]\n"
"ds_read_b64_tr_b16 v[174:175], v245 offset:57856\n"
"v_lshlrev_b32 v224, 4, v225\n"
"v_mfma_f32_16x16x32_bf16 v[36:39], v[132:135], a[40:43], v[36:39]\n"
"ds_read_b64_tr_b16 v[176:177], v242 offset:61440\n"
"buffer_load_dwordx4 v237, s[16:19], s50 offen offset:1024 lds\n"
"v_mfma_f32_16x16x32_bf16 v[36:39], v[148:151], a[56:59], v[36:39]\n"
"s_lshl_b32 s94, s87, 3\n"
"v_mfma_f32_16x16x32_bf16 v[52:55], v[132:135], a[44:47], v[52:55]\n"
"ds_read_b64_tr_b16 v[178:179], v242 offset:61952\n"
"buffer_load_dwordx4 v250, s[16:19], s50 offen offset:2048 lds\n"
"v_mfma_f32_16x16x32_bf16 v[52:55], v[148:151], a[60:63], v[52:55]\n"
"v_add_u32 v226, s94, v225\n"
"v_mfma_f32_16x16x32_bf16 v[8:11], v[136:139], a[32:35], v[8:11]\n"
"ds_read_b64_tr_b16 v[180:181], v243 offset:61440\n"
"v_lshlrev_b32 v225, 3, v225\n"
"v_mfma_f32_16x16x32_bf16 v[8:11], v[152:155], a[48:51], v[8:11]\n"
"ds_read_b64_tr_b16 v[182:183], v243 offset:61952\n"
"buffer_load_dwordx4 v251, s[16:19], s50 offen offset:3072 lds\n"
"v_mfma_f32_16x16x32_bf16 v[24:27], v[136:139], a[36:39], v[24:27]\n"
"v_lshrrev_b32 v226, 8, v226\n"
"v_mfma_f32_16x16x32_bf16 v[24:27], v[152:155], a[52:55], v[24:27]\n"
"ds_read_b64_tr_b16 v[184:185], v244 offset:61440\n"
"v_cmp_eq_u32 s[94:95], 1, v226\n"
"v_mfma_f32_16x16x32_bf16 v[40:43], v[136:139], a[40:43], v[40:43]\n"
"s_add_u32 m0, s46, 106368\n"
"ds_read_b64_tr_b16 v[186:187], v244 offset:61952\n"
"v_mfma_f32_16x16x32_bf16 v[40:43], v[152:155], a[56:59], v[40:43]\n"
"v_cmp_eq_u32 vcc, 4, v226\n"
"ds_read_b64_tr_b16 v[188:189], v245 offset:61440\n"
"v_mfma_f32_16x16x32_bf16 v[56:59], v[136:139], a[44:47], v[56:59]\n"
"s_or_b64 vcc, vcc, s[94:95]\n"
"s_add_u32 s92, s49, 1572864\n"
"v_mfma_f32_16x16x32_bf16 v[56:59], v[152:155], a[60:63], v[56:59]\n"
"ds_read_b64_tr_b16 v[190:191], v245 offset:61952\n"
"v_mov_b32 v226, 1.0\n"
"v_mfma_f32_16x16x32_bf16 v[12:15], v[140:143], a[32:35], v[12:15]\n"
"s_and_b32 s92, s92, 0x380000\n"
"s_nop 0\n"
"v_cndmask_b32 v226, 0, v226, vcc\n"
"v_mfma_f32_16x16x32_bf16 v[12:15], v[156:159], a[48:51], v[12:15]\n"
"v_mfma_f32_16x16x32_bf16 v[28:31], v[140:143], a[36:39], v[28:31]\n"
"buffer_load_dwordx4 v[192:195], v224, s[88:91], s55 offen offset:0 sc1\n"
"v_mfma_f32_16x16x32_bf16 v[28:31], v[156:159], a[52:55], v[28:31]\n"
"buffer_load_dwordx4 v238, s[20:23], s92 offen offset:128 lds\n"
"v_mfma_f32_16x16x32_bf16 v[44:47], v[140:143], a[40:43], v[44:47]\n"
"buffer_load_dwordx4 v[196:199], v224, s[88:91], s55 offen offset:512 sc1\n"
"v_mfma_f32_16x16x32_bf16 v[44:47], v[156:159], a[56:59], v[44:47]\n"
"buffer_load_dwordx4 v239, s[20:23], s92 offen offset:1152 lds\n"
"v_mfma_f32_16x16x32_bf16 v[60:63], v[140:143], a[44:47], v[60:63]\n"
"buffer_load_dwordx4 v[200:203], v224, s[88:91], s55 offen offset:1024 sc1\n"
"v_mfma_f32_16x16x32_bf16 v[60:63], v[156:159], a[60:63], v[60:63]\n"
"buffer_load_dwordx4 v[204:207], v224, s[88:91], s55 offen offset:1536 sc1\n"
"s_waitcnt vmcnt(16) lgkmcnt(0)\n"
"s_barrier\n"
"v_mfma_f32_16x16x32_bf16 v[64:67], v[160:163], a[32:35], v[64:67]\n"
"v_add_u32 v242, 0x10000, v242\n"
"v_add_u32 v243, 0x10000, v243\n"
"v_mfma_f32_16x16x32_bf16 v[64:67], v[176:179], a[48:51], v[64:67]\n"
"s_add_u32 m0, s46, 114688\n"
"v_add_u32 v244, 0x10000, v244\n"
"v_mfma_f32_16x16x32_bf16 v[80:83], v[160:163], a[36:39], v[80:83]\n"
"v_add_u32 v245, 0x10000, v245\n"
"s_add_u32 s92, s49, 2097152\n"
"v_mfma_f32_16x16x32_bf16 v[80:83], v[176:179], a[52:55], v[80:83]\n"
"v_add_u32 v240, 0x8000, v240\n"
"v_mfma_f32_16x16x32_bf16 v[96:99], v[160:163], a[40:43], v[96:99]\n"
"v_add_u32 v241, 0x8000, v241\n"
"v_mfma_f32_16x16x32_bf16 v[96:99], v[176:179], a[56:59], v[96:99]\n"
"ds_read_b64_tr_b16 v[128:129], v242 offset:0\n"
"ds_read_b128 a[64:67], v240 offset:0\n"
"v_mfma_f32_16x16x32_bf16 v[112:115], v[160:163], a[44:47], v[112:115]\n"
"s_and_b32 s92, s92, 0x380000\n"
"ds_read_b64_tr_b16 v[130:131], v242 offset:512\n"
"v_mfma_f32_16x16x32_bf16 v[112:115], v[176:179], a[60:63], v[112:115]\n"
"ds_read_b128 a[68:71], v240 offset:2048\n"
"v_mfma_f32_16x16x32_bf16 v[68:71], v[164:167], a[32:35], v[68:71]\n"
"ds_read_b64_tr_b16 v[132:133], v243 offset:0\n"
"buffer_load_dwordx4 v238, s[20:23], s92 offen offset:0 lds\n"
"v_mfma_f32_16x16x32_bf16 v[68:71], v[180:183], a[48:51], v[68:71]\n"
"ds_read_b128 a[72:75], v240 offset:4096\n"
"v_mfma_f32_16x16x32_bf16 v[84:87], v[164:167], a[36:39], v[84:87]\n"
"ds_read_b64_tr_b16 v[134:135], v243 offset:512\n"
"ds_read_b128 a[76:79], v240 offset:6144\n"
"v_mfma_f32_16x16x32_bf16 v[84:87], v[180:183], a[52:55], v[84:87]\n"
"buffer_load_dwordx4 v239, s[20:23], s92 offen offset:1024 lds\n"
"v_mfma_f32_16x16x32_bf16 v[100:103], v[164:167], a[40:43], v[100:103]\n"
"ds_read_b64_tr_b16 v[136:137], v244 offset:0\n"
"ds_read_b128 a[80:83], v241 offset:0\n"
"v_mfma_f32_16x16x32_bf16 v[100:103], v[180:183], a[56:59], v[100:103]\n"
"ds_read_b64_tr_b16 v[138:139], v244 offset:512\n"
"s_add_u32 s50, s44, 512\n"
"v_mfma_f32_16x16x32_bf16 v[116:119], v[164:167], a[44:47], v[116:119]\n"
"ds_read_b128 a[84:87], v241 offset:2048\n"
"v_mfma_f32_16x16x32_bf16 v[116:119], v[180:183], a[60:63], v[116:119]\n"
"ds_read_b64_tr_b16 v[140:141], v245 offset:0\n"
"ds_read_b128 a[88:91], v241 offset:4096\n"
"v_mfma_f32_16x16x32_bf16 v[72:75], v[168:171], a[32:35], v[72:75]\n"
"ds_read_b64_tr_b16 v[142:143], v245 offset:512\n"
"s_and_b32 s50, s50, 0x380\n"
"v_mfma_f32_16x16x32_bf16 v[72:75], v[184:187], a[48:51], v[72:75]\n"
"ds_read_b128 a[92:95], v241 offset:6144\n"
"v_mfma_f32_16x16x32_bf16 v[88:91], v[168:171], a[36:39], v[88:91]\n"
"ds_read_b64_tr_b16 v[144:145], v242 offset:4096\n"
"s_add_u32 m0, s45, 32768\n"
"v_mfma_f32_16x16x32_bf16 v[88:91], v[184:187], a[52:55], v[88:91]\n"
"ds_read_b64_tr_b16 v[146:147], v242 offset:4608\n"
"v_mfma_f32_16x16x32_bf16 v[104:107], v[168:171], a[40:43], v[104:107]\n"
"ds_read_b64_tr_b16 v[148:149], v243 offset:4096\n"
"ds_read_b64_tr_b16 v[150:151], v243 offset:4608\n"
"v_mfma_f32_16x16x32_bf16 v[104:107], v[184:187], a[56:59], v[104:107]\n"
"ds_read_b64_tr_b16 v[152:153], v244 offset:4096\n"
"s_nop 0\n"
"buffer_load_dwordx4 v236, s[16:19], s50 offen offset:0 lds\n"
"v_mfma_f32_16x16x32_bf16 v[120:123], v[168:171], a[44:47], v[120:123]\n"
"v_mfma_f32_16x16x32_bf16 v[120:123], v[184:187], a[60:63], v[120:123]\n"
"ds_read_b64_tr_b16 v[154:155], v244 offset:4608\n"
"ds_read_b64_tr_b16 v[156:157], v245 offset:4096\n"
"v_mfma_f32_16x16x32_bf16 v[76:79], v[172:175], a[32:35], v[76:79]\n"
"ds_read_b64_tr_b16 v[158:159], v245 offset:4608\n"
"buffer_load_dwordx4 v237, s[16:19], s50 offen offset:1024 lds\n"
"v_mfma_f32_16x16x32_bf16 v[76:79], v[188:191], a[48:51], v[76:79]\n"
"v_mfma_f32_16x16x32_bf16 v[92:95], v[172:175], a[36:39], v[92:95]\n"
"buffer_load_dwordx4 v250, s[16:19], s50 offen offset:2048 lds\n"
"v_mfma_f32_16x16x32_bf16 v[92:95], v[188:191], a[52:55], v[92:95]\n"
"v_mfma_f32_16x16x32_bf16 v[108:111], v[172:175], a[40:43], v[108:111]\n"
"v_mfma_f32_16x16x32_bf16 v[108:111], v[188:191], a[56:59], v[108:111]\n"
"buffer_load_dwordx4 v251, s[16:19], s50 offen offset:3072 lds\n"
"v_mfma_f32_16x16x32_bf16 v[124:127], v[172:175], a[44:47], v[124:127]\n"
"v_mfma_f32_16x16x32_bf16 v[124:127], v[188:191], a[60:63], v[124:127]\n"
"s_waitcnt lgkmcnt(0)\n"
"v_mfma_f32_16x16x32_bf16 v[0:3], v[128:131], a[64:67], v[0:3]\n"
"ds_read_b64_tr_b16 v[160:161], v242 offset:8192\n"
"s_add_u32 m0, s45, 36864\n"
"v_mfma_f32_16x16x32_bf16 v[0:3], v[144:147], a[80:83], v[0:3]\n"
"ds_read_b64_tr_b16 v[162:163], v242 offset:8704\n"
"s_waitcnt vmcnt(6)\n"
"v_mfma_f32_16x16x32_bf16 v[16:19], v[128:131], a[68:71], v[16:19]\n"
"s_add_u32 s50, s50, 32768\n"
"v_xor_b32 v227, s82, v193\n"
"v_mfma_f32_16x16x32_bf16 v[16:19], v[144:147], a[84:87], v[16:19]\n"
"ds_read_b64_tr_b16 v[164:165], v243 offset:8192\n"
"v_bitop3_b32 v227, v195, s82, v227 bitop3:0xbe\n"
"v_mfma_f32_16x16x32_bf16 v[32:35], v[128:131], a[72:75], v[32:35]\n"
"ds_read_b64_tr_b16 v[166:167], v243 offset:8704\n"
"v_bitop3_b32 v227, v197, s82, v227 bitop3:0xbe\n"
"v_mfma_f32_16x16x32_bf16 v[32:35], v[144:147], a[88:91], v[32:35]\n"
"buffer_load_dwordx4 v236, s[16:19], s50 offen offset:0 lds\n"
"v_mfma_f32_16x16x32_bf16 v[48:51], v[128:131], a[76:79], v[48:51]\n"
"v_bitop3_b32 v227, v199, s82, v227 bitop3:0xbe\n"
"ds_read_b64_tr_b16 v[168:169], v244 offset:8192\n"
"v_mfma_f32_16x16x32_bf16 v[48:51], v[144:147], a[92:95], v[48:51]\n"
"v_bitop3_b32 v227, v201, s82, v227 bitop3:0xbe\n"
"ds_read_b64_tr_b16 v[170:171], v244 offset:8704\n"
"v_mfma_f32_16x16x32_bf16 v[4:7], v[132:135], a[64:67], v[4:7]\n"
"v_bitop3_b32 v227, v203, s82, v227 bitop3:0xbe\n"
"buffer_load_dwordx4 v237, s[16:19], s50 offen offset:1024 lds\n"
"v_mfma_f32_16x16x32_bf16 v[4:7], v[148:151], a[80:83], v[4:7]\n"
"v_bitop3_b32 v227, v205, s82, v227 bitop3:0xbe\n"
"v_mfma_f32_16x16x32_bf16 v[20:23], v[132:135], a[68:71], v[20:23]\n"
"ds_read_b64_tr_b16 v[172:173], v245 offset:8192\n"
"v_bitop3_b32 v227, v207, s82, v227 bitop3:0xbe\n"
"v_mfma_f32_16x16x32_bf16 v[20:23], v[148:151], a[84:87], v[20:23]\n"
"ds_read_b64_tr_b16 v[174:175], v245 offset:8704\n"
"buffer_load_dwordx4 v250, s[16:19], s50 offen offset:2048 lds\n"
"v_mfma_f32_16x16x32_bf16 v[36:39], v[132:135], a[72:75], v[36:39]\n"
"v_cmp_ne_u32 vcc, 0, v227\n"
"v_mfma_f32_16x16x32_bf16 v[36:39], v[148:151], a[88:91], v[36:39]\n"
"ds_read_b64_tr_b16 v[176:177], v242 offset:12288\n"
"s_cbranch_vccnz Lgs2_%=\n"
"Lgb1_%=:\n"
"v_mfma_f32_16x16x32_bf16 v[52:55], v[132:135], a[76:79], v[52:55]\n"
"buffer_load_dwordx4 v251, s[16:19], s50 offen offset:3072 lds\n"
"v_mfma_f32_16x16x32_bf16 v[52:55], v[148:151], a[92:95], v[52:55]\n"
"v_add_f32 v192, v192, v196\n"
"ds_read_b64_tr_b16 v[178:179], v242 offset:12800\n"
"v_mfma_f32_16x16x32_bf16 v[8:11], v[136:139], a[64:67], v[8:11]\n"
"v_add_f32 v194, v194, v198\n"
"ds_read_b64_tr_b16 v[180:181], v243 offset:12288\n"
"v_mfma_f32_16x16x32_bf16 v[8:11], v[152:155], a[80:83], v[8:11]\n"
"v_add_f32 v200, v200, v204\n"
"s_add_u32 m0, s46, 122752\n"
"v_mfma_f32_16x16x32_bf16 v[24:27], v[136:139], a[68:71], v[24:27]\n"
"v_add_f32 v202, v202, v206\n"
"ds_read_b64_tr_b16 v[182:183], v243 offset:12800\n"
"v_mfma_f32_16x16x32_bf16 v[24:27], v[152:155], a[84:87], v[24:27]\n"
"v_add_f32 v192, v192, v200\n"
"ds_read_b64_tr_b16 v[184:185], v244 offset:12288\n"
"v_mfma_f32_16x16x32_bf16 v[40:43], v[136:139], a[72:75], v[40:43]\n"
"s_add_u32 s92, s49, 2097152\n"
"v_add_f32 v193, v194, v202\n"
"v_mfma_f32_16x16x32_bf16 v[40:43], v[152:155], a[88:91], v[40:43]\n"
"v_cvt_pk_bf16_f32 v194, v192, v193\n"
"ds_read_b64_tr_b16 v[186:187], v244 offset:12800\n"
"v_mfma_f32_16x16x32_bf16 v[56:59], v[136:139], a[76:79], v[56:59]\n"
"v_lshlrev_b32 v192, 16, v194\n"
"ds_read_b64_tr_b16 v[188:189], v245 offset:12288\n"
"v_mfma_f32_16x16x32_bf16 v[56:59], v[152:155], a[92:95], v[56:59]\n"
"s_and_b32 s92, s92, 0x380000\n"
"v_and_b32 v193, 0xffff0000, v194\n"
"v_mfma_f32_16x16x32_bf16 v[12:15], v[140:143], a[64:67], v[12:15]\n"
"ds_read_b64_tr_b16 v[190:191], v245 offset:12800\n"
"v_add_f32 v192, v192, v226\n"
"v_mfma_f32_16x16x32_bf16 v[12:15], v[156:159], a[80:83], v[12:15]\n"
"buffer_load_dwordx4 v238, s[20:23], s92 offen offset:128 lds\n"
"v_mfma_f32_16x16x32_bf16 v[28:31], v[140:143], a[68:71], v[28:31]\n"
"v_add_f32 v193, v193, v226\n"
"v_cvt_pk_bf16_f32 v194, v192, v193\n"
"v_mfma_f32_16x16x32_bf16 v[28:31], v[156:159], a[84:87], v[28:31]\n"
"buffer_load_dwordx4 v239, s[20:23], s92 offen offset:1152 lds\n"
"v_mfma_f32_16x16x32_bf16 v[44:47], v[140:143], a[72:75], v[44:47]\n"
"v_mov_b32 v195, s82\n"
"s_lshl_b32 s94, s87, 6\n"
"v_mfma_f32_16x16x32_bf16 v[44:47], v[156:159], a[88:91], v[44:47]\n"
"s_add_u32 s94, s94, 0x1000\n"
"global_load_dword v249, v252, s[64:65]\n"
"v_mfma_f32_16x16x32_bf16 v[60:63], v[140:143], a[76:79], v[60:63]\n"
"s_add_u32 s94, s94, s51\n"
"v_mfma_f32_16x16x32_bf16 v[60:63], v[156:159], a[92:95], v[60:63]\n"
"s_mov_b64 exec, 0xff\n"
"buffer_store_dwordx2 v[194:195], v225, s[88:91], s94 offen sc1\n"
"s_mov_b64 exec, -1\n"
"s_waitcnt vmcnt(14) lgkmcnt(0)\n"
"s_barrier\n"
"v_mfma_f32_16x16x32_bf16 v[64:67], v[160:163], a[64:67], v[64:67]\n"
"v_add_u32 v242, 0xffff0000, v242\n"
"v_add_u32 v243, 0xffff0000, v243\n"
"v_mfma_f32_16x16x32_bf16 v[64:67], v[176:179], a[80:83], v[64:67]\n"
"s_add_u32 m0, s46, 131072\n"
"v_add_u32 v244, 0xffff0000, v244\n"
"v_mfma_f32_16x16x32_bf16 v[80:83], v[160:163], a[68:71], v[80:83]\n"
"v_add_u32 v245, 0xffff0000, v245\n"
"s_add_u32 s92, s49, 2621440\n"
"v_mfma_f32_16x16x32_bf16 v[80:83], v[176:179], a[84:87], v[80:83]\n"
"v_add_u32 v240, 0xffff0000, v240\n"
"v_mfma_f32_16x16x32_bf16 v[96:99], v[160:163], a[72:75], v[96:99]\n"
"v_add_u32 v241, 0xffff0000, v241\n"
"v_mfma_f32_16x16x32_bf16 v[96:99], v[176:179], a[88:91], v[96:99]\n"
"ds_read_b64_tr_b16 v[128:129], v242 offset:32768\n"
"ds_read_b128 a[96:99], v240 offset:0\n"
"v_mfma_f32_16x16x32_bf16 v[112:115], v[160:163], a[76:79], v[112:115]\n"
"s_and_b32 s92, s92, 0x380000\n"
"ds_read_b64_tr_b16 v[130:131], v242 offset:33280\n"
"v_mfma_f32_16x16x32_bf16 v[112:115], v[176:179], a[92:95], v[112:115]\n"
"ds_read_b128 a[100:103], v240 offset:2048\n"
"v_mfma_f32_16x16x32_bf16 v[68:71], v[164:167], a[64:67], v[68:71]\n"
"ds_read_b64_tr_b16 v[132:133], v243 offset:32768\n"
"buffer_load_dwordx4 v238, s[20:23], s92 offen offset:0 lds\n"
"v_mfma_f32_16x16x32_bf16 v[68:71], v[180:183], a[80:83], v[68:71]\n"
"ds_read_b128 a[104:107], v240 offset:4096\n"
"v_mfma_f32_16x16x32_bf16 v[84:87], v[164:167], a[68:71], v[84:87]\n"
"ds_read_b64_tr_b16 v[134:135], v243 offset:33280\n"
"ds_read_b128 a[108:111], v240 offset:6144\n"
"v_mfma_f32_16x16x32_bf16 v[84:87], v[180:183], a[84:87], v[84:87]\n"
"buffer_load_dwordx4 v239, s[20:23], s92 offen offset:1024 lds\n"
"v_mfma_f32_16x16x32_bf16 v[100:103], v[164:167], a[72:75], v[100:103]\n"
"ds_read_b64_tr_b16 v[136:137], v244 offset:32768\n"
"ds_read_b128 a[112:115], v241 offset:0\n"
"v_mfma_f32_16x16x32_bf16 v[100:103], v[180:183], a[88:91], v[100:103]\n"
"ds_read_b64_tr_b16 v[138:139], v244 offset:33280\n"
"s_add_u32 s50, s44, 640\n"
"v_mfma_f32_16x16x32_bf16 v[116:119], v[164:167], a[76:79], v[116:119]\n"
"ds_read_b128 a[116:119], v241 offset:2048\n"
"v_mfma_f32_16x16x32_bf16 v[116:119], v[180:183], a[92:95], v[116:119]\n"
"ds_read_b64_tr_b16 v[140:141], v245 offset:32768\n"
"ds_read_b128 a[120:123], v241 offset:4096\n"
"v_mfma_f32_16x16x32_bf16 v[72:75], v[168:171], a[64:67], v[72:75]\n"
"ds_read_b64_tr_b16 v[142:143], v245 offset:33280\n"
"s_and_b32 s50, s50, 0x380\n"
"v_mfma_f32_16x16x32_bf16 v[72:75], v[184:187], a[80:83], v[72:75]\n"
"ds_read_b128 a[124:127], v241 offset:6144\n"
"v_mfma_f32_16x16x32_bf16 v[88:91], v[168:171], a[68:71], v[88:91]\n"
"ds_read_b64_tr_b16 v[144:145], v242 offset:36864\n"
"s_add_u32 m0, s45, 65536\n"
"v_mfma_f32_16x16x32_bf16 v[88:91], v[184:187], a[84:87], v[88:91]\n"
"ds_read_b64_tr_b16 v[146:147], v242 offset:37376\n"
"v_mfma_f32_16x16x32_bf16 v[104:107], v[168:171], a[72:75], v[104:107]\n"
"ds_read_b64_tr_b16 v[148:149], v243 offset:36864\n"
"ds_read_b64_tr_b16 v[150:151], v243 offset:37376\n"
"v_mfma_f32_16x16x32_bf16 v[104:107], v[184:187], a[88:91], v[104:107]\n"
"ds_read_b64_tr_b16 v[152:153], v244 offset:36864\n"
"s_nop 0\n"
"buffer_load_dwordx4 v236, s[16:19], s50 offen offset:0 lds\n"
"v_mfma_f32_16x16x32_bf16 v[120:123], v[168:171], a[76:79], v[120:123]\n"
"v_mfma_f32_16x16x32_bf16 v[120:123], v[184:187], a[92:95], v[120:123]\n"
"ds_read_b64_tr_b16 v[154:155], v244 offset:37376\n"
"ds_read_b64_tr_b16 v[156:157], v245 offset:36864\n"
"v_mfma_f32_16x16x32_bf16 v[76:79], v[172:175], a[64:67], v[76:79]\n"
"ds_read_b64_tr_b16 v[158:159], v245 offset:37376\n"
"buffer_load_dwordx4 v237, s[16:19], s50 offen offset:1024 lds\n"
"v_mfma_f32_16x16x32_bf16 v[76:79], v[188:191], a[80:83], v[76:79]\n"
"v_mfma_f32_16x16x32_bf16 v[92:95], v[172:175], a[68:71], v[92:95]\n"
"buffer_load_dwordx4 v250, s[16:19], s50 offen offset:2048 lds\n"
"v_mfma_f32_16x16x32_bf16 v[92:95], v[188:191], a[84:87], v[92:95]\n"
"v_mfma_f32_16x16x32_bf16 v[108:111], v[172:175], a[72:75], v[108:111]\n"
"v_mfma_f32_16x16x32_bf16 v[108:111], v[188:191], a[88:91], v[108:111]\n"
"buffer_load_dwordx4 v251, s[16:19], s50 offen offset:3072 lds\n"
"v_mfma_f32_16x16x32_bf16 v[124:127], v[172:175], a[76:79], v[124:127]\n"
"v_mfma_f32_16x16x32_bf16 v[124:127], v[188:191], a[92:95], v[124:127]\n"
"s_waitcnt lgkmcnt(0)\n"
"v_mfma_f32_16x16x32_bf16 v[0:3], v[128:131], a[96:99], v[0:3]\n"
"ds_read_b64_tr_b16 v[160:161], v242 offset:40960\n"
"s_add_u32 m0, s45, 69632\n"
"v_mfma_f32_16x16x32_bf16 v[0:3], v[144:147], a[112:115], v[0:3]\n"
"ds_read_b64_tr_b16 v[162:163], v242 offset:41472\n"
"v_mfma_f32_16x16x32_bf16 v[16:19], v[128:131], a[100:103], v[16:19]\n"
"s_add_u32 s50, s50, 32768\n"
"v_mfma_f32_16x16x32_bf16 v[16:19], v[144:147], a[116:119], v[16:19]\n"
"ds_read_b64_tr_b16 v[164:165], v243 offset:40960\n"
"v_mfma_f32_16x16x32_bf16 v[32:35], v[128:131], a[104:107], v[32:35]\n"
"ds_read_b64_tr_b16 v[166:167], v243 offset:41472\n"
"v_mfma_f32_16x16x32_bf16 v[32:35], v[144:147], a[120:123], v[32:35]\n"
"buffer_load_dwordx4 v236, s[16:19], s50 offen offset:0 lds\n"
"v_mfma_f32_16x16x32_bf16 v[48:51], v[128:131], a[108:111], v[48:51]\n"
"ds_read_b64_tr_b16 v[168:169], v244 offset:40960\n"
"v_mfma_f32_16x16x32_bf16 v[48:51], v[144:147], a[124:127], v[48:51]\n"
"ds_read_b64_tr_b16 v[170:171], v244 offset:41472\n"
"v_mfma_f32_16x16x32_bf16 v[4:7], v[132:135], a[96:99], v[4:7]\n"
"buffer_load_dwordx4 v237, s[16:19], s50 offen offset:1024 lds\n"
"v_mfma_f32_16x16x32_bf16 v[4:7], v[148:151], a[112:115], v[4:7]\n"
"ds_read_b64_tr_b16 v[172:173], v245 offset:40960\n"
"v_mfma_f32_16x16x32_bf16 v[20:23], v[132:135], a[100:103], v[20:23]\n"
"ds_read_b64_tr_b16 v[174:175], v245 offset:41472\n"
"v_mfma_f32_16x16x32_bf16 v[20:23], v[148:151], a[116:119], v[20:23]\n"
"buffer_load_dwordx4 v250, s[16:19], s50 offen offset:2048 lds\n"
"v_mfma_f32_16x16x32_bf16 v[36:39], v[132:135], a[104:107], v[36:39]\n"
"ds_read_b64_tr_b16 v[176:177], v242 offset:45056\n"
"v_mfma_f32_16x16x32_bf16 v[36:39], v[148:151], a[120:123], v[36:39]\n"
"v_mfma_f32_16x16x32_bf16 v[52:55], v[132:135], a[108:111], v[52:55]\n"
"ds_read_b64_tr_b16 v[178:179], v242 offset:45568\n"
"buffer_load_dwordx4 v251, s[16:19], s50 offen offset:3072 lds\n"
"v_mfma_f32_16x16x32_bf16 v[52:55], v[148:151], a[124:127], v[52:55]\n"
"ds_read_b64_tr_b16 v[180:181], v243 offset:45056\n"
"v_mfma_f32_16x16x32_bf16 v[8:11], v[136:139], a[96:99], v[8:11]\n"
"v_mfma_f32_16x16x32_bf16 v[8:11], v[152:155], a[112:115], v[8:11]\n"
"ds_read_b64_tr_b16 v[182:183], v243 offset:45568\n"
"s_add_u32 m0, s46, 139136\n"
"v_mfma_f32_16x16x32_bf16 v[24:27], v[136:139], a[100:103], v[24:27]\n"
"ds_read_b64_tr_b16 v[184:185], v244 offset:45056\n"
"v_mfma_f32_16x16x32_bf16 v[24:27], v[152:155], a[116:119], v[24:27]\n"
"v_mfma_f32_16x16x32_bf16 v[40:43], v[136:139], a[104:107], v[40:43]\n"
"ds_read_b64_tr_b16 v[186:187], v244 offset:45568\n"
"s_add_u32 s92, s49, 2621440\n"
"v_mfma_f32_16x16x32_bf16 v[40:43], v[152:155], a[120:123], v[40:43]\n"
"ds_read_b64_tr_b16 v[188:189], v245 offset:45056\n"
"v_mfma_f32_16x16x32_bf16 v[56:59], v[136:139], a[108:111], v[56:59]\n"
"v_mfma_f32_16x16x32_bf16 v[56:59], v[152:155], a[124:127], v[56:59]\n"
"ds_read_b64_tr_b16 v[190:191], v245 offset:45568\n"
"s_and_b32 s92, s92, 0x380000\n"
"v_mfma_f32_16x16x32_bf16 v[12:15], v[140:143], a[96:99], v[12:15]\n"
"v_mfma_f32_16x16x32_bf16 v[12:15], v[156:159], a[112:115], v[12:15]\n"
"v_mfma_f32_16x16x32_bf16 v[28:31], v[140:143], a[100:103], v[28:31]\n"
"buffer_load_dwordx4 v238, s[20:23], s92 offen offset:128 lds\n"
"v_mfma_f32_16x16x32_bf16 v[28:31], v[156:159], a[116:119], v[28:31]\n"
"v_mfma_f32_16x16x32_bf16 v[44:47], v[140:143], a[104:107], v[44:47]\n"
"v_mfma_f32_16x16x32_bf16 v[44:47], v[156:159], a[120:123], v[44:47]\n"
"buffer_load_dwordx4 v239, s[20:23], s92 offen offset:1152 lds\n"
"v_mfma_f32_16x16x32_bf16 v[60:63], v[140:143], a[108:111], v[60:63]\n"
"v_mfma_f32_16x16x32_bf16 v[60:63], v[156:159], a[124:127], v[60:63]\n"
"s_waitcnt vmcnt(14) lgkmcnt(0)\n"
"s_barrier\n"
"v_mfma_f32_16x16x32_bf16 v[64:67], v[160:163], a[96:99], v[64:67]\n"
"v_add_u32 v240, 0x8000, v240\n"
"v_add_u32 v241, 0x8000, v241\n"
"v_mfma_f32_16x16x32_bf16 v[64:67], v[176:179], a[112:115], v[64:67]\n"
"s_add_u32 m0, s46, 98304\n"
"ds_read_b64_tr_b16 v[128:129], v242 offset:49152\n"
"v_mfma_f32_16x16x32_bf16 v[80:83], v[160:163], a[100:103], v[80:83]\n"
"ds_read_b128 a[128:131], v240 offset:0\n"
"s_add_u32 s92, s49, 3145728\n"
"v_mfma_f32_16x16x32_bf16 v[80:83], v[176:179], a[116:119], v[80:83]\n"
"ds_read_b64_tr_b16 v[130:131], v242 offset:49664\n"
"v_mfma_f32_16x16x32_bf16 v[96:99], v[160:163], a[104:107], v[96:99]\n"
"ds_read_b128 a[132:135], v240 offset:2048\n"
"v_mfma_f32_16x16x32_bf16 v[96:99], v[176:179], a[120:123], v[96:99]\n"
"ds_read_b64_tr_b16 v[132:133], v243 offset:49152\n"
"s_and_b32 s92, s92, 0x380000\n"
"v_mfma_f32_16x16x32_bf16 v[112:115], v[160:163], a[108:111], v[112:115]\n"
"ds_read_b128 a[136:139], v240 offset:4096\n"
"v_mfma_f32_16x16x32_bf16 v[112:115], v[176:179], a[124:127], v[112:115]\n"
"ds_read_b64_tr_b16 v[134:135], v243 offset:49664\n"
"v_mfma_f32_16x16x32_bf16 v[68:71], v[164:167], a[96:99], v[68:71]\n"
"ds_read_b128 a[140:143], v240 offset:6144\n"
"buffer_load_dwordx4 v238, s[20:23], s92 offen offset:0 lds\n"
"v_mfma_f32_16x16x32_bf16 v[68:71], v[180:183], a[112:115], v[68:71]\n"
"ds_read_b64_tr_b16 v[136:137], v244 offset:49152\n"
"v_mfma_f32_16x16x32_bf16 v[84:87], v[164:167], a[100:103], v[84:87]\n"
"ds_read_b128 a[144:147], v241 offset:0\n"
"buffer_load_dwordx4 v239, s[20:23], s92 offen offset:1024 lds\n"
"v_mfma_f32_16x16x32_bf16 v[84:87], v[180:183], a[116:119], v[84:87]\n"
"ds_read_b64_tr_b16 v[138:139], v244 offset:49664\n"
"v_mfma_f32_16x16x32_bf16 v[100:103], v[164:167], a[104:107], v[100:103]\n"
"ds_read_b128 a[148:151], v241 offset:2048\n"
"v_mfma_f32_16x16x32_bf16 v[100:103], v[180:183], a[120:123], v[100:103]\n"
"ds_read_b64_tr_b16 v[140:141], v245 offset:49152\n"
"s_add_u32 s50, s44, 768\n"
"v_mfma_f32_16x16x32_bf16 v[116:119], v[164:167], a[108:111], v[116:119]\n"
"ds_read_b128 a[152:155], v241 offset:4096\n"
"v_mfma_f32_16x16x32_bf16 v[116:119], v[180:183], a[124:127], v[116:119]\n"
"ds_read_b64_tr_b16 v[142:143], v245 offset:49664\n"
"v_mfma_f32_16x16x32_bf16 v[72:75], v[168:171], a[96:99], v[72:75]\n"
"ds_read_b128 a[156:159], v241 offset:6144\n"
"s_and_b32 s50, s50, 0x380\n"
"v_mfma_f32_16x16x32_bf16 v[72:75], v[184:187], a[112:115], v[72:75]\n"
"ds_read_b64_tr_b16 v[144:145], v242 offset:53248\n"
"v_mfma_f32_16x16x32_bf16 v[88:91], v[168:171], a[100:103], v[88:91]\n"
"ds_read_b64_tr_b16 v[146:147], v242 offset:53760\n"
"s_add_u32 m0, s45, 0\n"
"v_mfma_f32_16x16x32_bf16 v[88:91], v[184:187], a[116:119], v[88:91]\n"
"ds_read_b64_tr_b16 v[148:149], v243 offset:53248\n"
"v_mfma_f32_16x16x32_bf16 v[104:107], v[168:171], a[104:107], v[104:107]\n"
"ds_read_b64_tr_b16 v[150:151], v243 offset:53760\n"
"v_mfma_f32_16x16x32_bf16 v[104:107], v[184:187], a[120:123], v[104:107]\n"
"ds_read_b64_tr_b16 v[152:153], v244 offset:53248\n"
"s_nop 0\n"
"buffer_load_dwordx4 v236, s[16:19], s50 offen offset:0 lds\n"
"v_mfma_f32_16x16x32_bf16 v[120:123], v[168:171], a[108:111], v[120:123]\n"
"v_mfma_f32_16x16x32_bf16 v[120:123], v[184:187], a[124:127], v[120:123]\n"
"ds_read_b64_tr_b16 v[154:155], v244 offset:53760\n"
"ds_read_b64_tr_b16 v[156:157], v245 offset:53248\n"
"v_mfma_f32_16x16x32_bf16 v[76:79], v[172:175], a[96:99], v[76:79]\n"
"ds_read_b64_tr_b16 v[158:159], v245 offset:53760\n"
"buffer_load_dwordx4 v237, s[16:19], s50 offen offset:1024 lds\n"
"v_mfma_f32_16x16x32_bf16 v[76:79], v[188:191], a[112:115], v[76:79]\n"
"v_mfma_f32_16x16x32_bf16 v[92:95], v[172:175], a[100:103], v[92:95]\n"
"buffer_load_dwordx4 v250, s[16:19], s50 offen offset:2048 lds\n"
"v_mfma_f32_16x16x32_bf16 v[92:95], v[188:191], a[116:119], v[92:95]\n"
"v_mfma_f32_16x16x32_bf16 v[108:111], v[172:175], a[104:107], v[108:111]\n"
"v_mfma_f32_16x16x32_bf16 v[108:111], v[188:191], a[120:123], v[108:111]\n"
"buffer_load_dwordx4 v251, s[16:19], s50 offen offset:3072 lds\n"
"v_mfma_f32_16x16x32_bf16 v[124:127], v[172:175], a[108:111], v[124:127]\n"
"v_mfma_f32_16x16x32_bf16 v[124:127], v[188:191], a[124:127], v[124:127]\n"
"s_waitcnt lgkmcnt(0)\n"
"v_mfma_f32_16x16x32_bf16 v[0:3], v[128:131], a[128:131], v[0:3]\n"
"ds_read_b64_tr_b16 v[160:161], v242 offset:57344\n"
"s_add_u32 m0, s45, 4096\n"
"v_mfma_f32_16x16x32_bf16 v[0:3], v[144:147], a[144:147], v[0:3]\n"
"ds_read_b64_tr_b16 v[162:163], v242 offset:57856\n"
"v_mfma_f32_16x16x32_bf16 v[16:19], v[128:131], a[132:135], v[16:19]\n"
"s_add_u32 s50, s50, 32768\n"
"v_mfma_f32_16x16x32_bf16 v[16:19], v[144:147], a[148:151], v[16:19]\n"
"ds_read_b64_tr_b16 v[164:165], v243 offset:57344\n"
"v_mfma_f32_16x16x32_bf16 v[32:35], v[128:131], a[136:139], v[32:35]\n"
"ds_read_b64_tr_b16 v[166:167], v243 offset:57856\n"
"v_mfma_f32_16x16x32_bf16 v[32:35], v[144:147], a[152:155], v[32:35]\n"
"buffer_load_dwordx4 v236, s[16:19], s50 offen offset:0 lds\n"
"v_mfma_f32_16x16x32_bf16 v[48:51], v[128:131], a[140:143], v[48:51]\n"
"ds_read_b64_tr_b16 v[168:169], v244 offset:57344\n"
"v_mfma_f32_16x16x32_bf16 v[48:51], v[144:147], a[156:159], v[48:51]\n"
"ds_read_b64_tr_b16 v[170:171], v244 offset:57856\n"
"v_mfma_f32_16x16x32_bf16 v[4:7], v[132:135], a[128:131], v[4:7]\n"
"buffer_load_dwordx4 v237, s[16:19], s50 offen offset:1024 lds\n"
"v_mfma_f32_16x16x32_bf16 v[4:7], v[148:151], a[144:147], v[4:7]\n"
"ds_read_b64_tr_b16 v[172:173], v245 offset:57344\n"
"v_mfma_f32_16x16x32_bf16 v[20:23], v[132:135], a[132:135], v[20:23]\n"
"ds_read_b64_tr_b16 v[174:175], v245 offset:57856\n"
"buffer_load_dwordx4 v250, s[16:19], s50 offen offset:2048 lds\n"
"v_mfma_f32_16x16x32_bf16 v[20:23], v[148:151], a[148:151], v[20:23]\n"
"v_mfma_f32_16x16x32_bf16 v[36:39], v[132:135], a[136:139], v[36:39]\n"
"ds_read_b64_tr_b16 v[176:177], v242 offset:61440\n"
"v_mfma_f32_16x16x32_bf16 v[36:39], v[148:151], a[152:155], v[36:39]\n"
"buffer_load_dwordx4 v251, s[16:19], s50 offen offset:3072 lds\n"
"v_mfma_f32_16x16x32_bf16 v[52:55], v[132:135], a[140:143], v[52:55]\n"
"ds_read_b64_tr_b16 v[178:179], v242 offset:61952\n"
"v_mfma_f32_16x16x32_bf16 v[52:55], v[148:151], a[156:159], v[52:55]\n"
"ds_read_b64_tr_b16 v[180:181], v243 offset:61440\n"
"v_mfma_f32_16x16x32_bf16 v[8:11], v[136:139], a[128:131], v[8:11]\n"
"s_add_u32 m0, s46, 106368\n"
"v_mfma_f32_16x16x32_bf16 v[8:11], v[152:155], a[144:147], v[8:11]\n"
"ds_read_b64_tr_b16 v[182:183], v243 offset:61952\n"
"v_mfma_f32_16x16x32_bf16 v[24:27], v[136:139], a[132:135], v[24:27]\n"
"ds_read_b64_tr_b16 v[184:185], v244 offset:61440\n"
"s_add_u32 s92, s49, 3145728\n"
"v_mfma_f32_16x16x32_bf16 v[24:27], v[152:155], a[148:151], v[24:27]\n"
"v_mfma_f32_16x16x32_bf16 v[40:43], v[136:139], a[136:139], v[40:43]\n"
"ds_read_b64_tr_b16 v[186:187], v244 offset:61952\n"
"s_lshl_b32 s55, s83, 9\n"
"v_mfma_f32_16x16x32_bf16 v[40:43], v[152:155], a[152:155], v[40:43]\n"
"ds_read_b64_tr_b16 v[188:189], v245 offset:61440\n"
"s_and_b32 s92, s92, 0x380000\n"
"v_mfma_f32_16x16x32_bf16 v[56:59], v[136:139], a[140:143], v[56:59]\n"
"s_add_u32 s55, s55, 0x1000\n"
"v_mfma_f32_16x16x32_bf16 v[56:59], v[152:155], a[156:159], v[56:59]\n"
"ds_read_b64_tr_b16 v[190:191], v245 offset:61952\n"
"v_mfma_f32_16x16x32_bf16 v[12:15], v[140:143], a[128:131], v[12:15]\n"
"buffer_load_dwordx4 v238, s[20:23], s92 offen offset:128 lds\n"
"v_mfma_f32_16x16x32_bf16 v[12:15], v[156:159], a[144:147], v[12:15]\n"
"v_lshrrev_b32 v247, 3, v255\n"
"v_mfma_f32_16x16x32_bf16 v[28:31], v[140:143], a[132:135], v[28:31]\n"
"buffer_load_dwordx4 v239, s[20:23], s92 offen offset:1152 lds\n"
"v_mfma_f32_16x16x32_bf16 v[28:31], v[156:159], a[148:151], v[28:31]\n"
"v_lshlrev_b32 v247, 5, v247\n"
"v_mfma_f32_16x16x32_bf16 v[44:47], v[140:143], a[136:139], v[44:47]\n"
"v_mfma_f32_16x16x32_bf16 v[44:47], v[156:159], a[152:155], v[44:47]\n"
"global_load_dword v249, v252, s[60:61]\n"
"v_mfma_f32_16x16x32_bf16 v[60:63], v[140:143], a[140:143], v[60:63]\n"
"v_mfma_f32_16x16x32_bf16 v[60:63], v[156:159], a[156:159], v[60:63]\n"
"s_waitcnt vmcnt(13) lgkmcnt(0)\n"
"s_barrier\n"
"v_mfma_f32_16x16x32_bf16 v[64:67], v[160:163], a[128:131], v[64:67]\n"
"v_add_u32 v242, 0x10000, v242\n"
"v_add_u32 v243, 0x10000, v243\n"
"v_mfma_f32_16x16x32_bf16 v[64:67], v[176:179], a[144:147], v[64:67]\n"
"s_add_u32 m0, s46, 114688\n"
"v_add_u32 v244, 0x10000, v244\n"
"v_mfma_f32_16x16x32_bf16 v[80:83], v[160:163], a[132:135], v[80:83]\n"
"v_add_u32 v245, 0x10000, v245\n"
"s_add_u32 s92, s49, 3670016\n"
"v_mfma_f32_16x16x32_bf16 v[80:83], v[176:179], a[148:151], v[80:83]\n"
"v_add_u32 v240, 0x8000, v240\n"
"v_mfma_f32_16x16x32_bf16 v[96:99], v[160:163], a[136:139], v[96:99]\n"
"v_add_u32 v241, 0x8000, v241\n"
"buffer_load_dwordx4 v[200:203], v247, s[88:91], s55 offen offset:0 sc1\n"
"v_mfma_f32_16x16x32_bf16 v[96:99], v[176:179], a[152:155], v[96:99]\n"
"ds_read_b64_tr_b16 v[128:129], v242 offset:0\n"
"v_mfma_f32_16x16x32_bf16 v[112:115], v[160:163], a[140:143], v[112:115]\n"
"ds_read_b128 a[160:163], v240 offset:0\n"
"s_and_b32 s92, s92, 0x380000\n"
"v_mfma_f32_16x16x32_bf16 v[112:115], v[176:179], a[156:159], v[112:115]\n"
"ds_read_b64_tr_b16 v[130:131], v242 offset:512\n"
"buffer_load_dwordx4 v[204:207], v247, s[88:91], s55 offen offset:16 sc1\n"
"v_mfma_f32_16x16x32_bf16 v[68:71], v[164:167], a[128:131], v[68:71]\n"
"ds_read_b128 a[164:167], v240 offset:2048\n"
"v_mfma_f32_16x16x32_bf16 v[68:71], v[180:183], a[144:147], v[68:71]\n"
"ds_read_b64_tr_b16 v[132:133], v243 offset:0\n"
"buffer_load_dwordx4 v238, s[20:23], s92 offen offset:0 lds\n"
"v_mfma_f32_16x16x32_bf16 v[84:87], v[164:167], a[132:135], v[84:87]\n"
"buffer_load_dwordx4 v[224:227], v247, s[88:91], s55 offen offset:2048 sc1\n"
"v_mfma_f32_16x16x32_bf16 v[84:87], v[180:183], a[148:151], v[84:87]\n"
"ds_read_b128 a[168:171], v240 offset:4096\n"
"v_mfma_f32_16x16x32_bf16 v[100:103], v[164:167], a[136:139], v[100:103]\n"
"ds_read_b64_tr_b16 v[134:135], v243 offset:512\n"
"ds_read_b128 a[172:175], v240 offset:6144\n"
"v_mfma_f32_16x16x32_bf16 v[100:103], v[180:183], a[152:155], v[100:103]\n"
"buffer_load_dwordx4 v239, s[20:23], s92 offen offset:1024 lds\n"
"v_mfma_f32_16x16x32_bf16 v[116:119], v[164:167], a[140:143], v[116:119]\n"
"buffer_load_dwordx4 v[228:231], v247, s[88:91], s55 offen offset:2064 sc1\n"
"v_mfma_f32_16x16x32_bf16 v[116:119], v[180:183], a[156:159], v[116:119]\n"
"ds_read_b64_tr_b16 v[136:137], v244 offset:0\n"
"ds_read_b128 a[176:179], v241 offset:0\n"
"v_mfma_f32_16x16x32_bf16 v[72:75], v[168:171], a[128:131], v[72:75]\n"
"ds_read_b64_tr_b16 v[138:139], v244 offset:512\n"
"s_add_u32 s50, s44, 896\n"
"v_mfma_f32_16x16x32_bf16 v[72:75], v[184:187], a[144:147], v[72:75]\n"
"ds_read_b128 a[180:183], v241 offset:2048\n"
"ds_read_b64_tr_b16 v[140:141], v245 offset:0\n"
"v_mfma_f32_16x16x32_bf16 v[88:91], v[168:171], a[132:135], v[88:91]\n"
"ds_read_b128 a[184:187], v241 offset:4096\n"
"ds_read_b64_tr_b16 v[142:143], v245 offset:512\n"
"v_mfma_f32_16x16x32_bf16 v[88:91], v[184:187], a[148:151], v[88:91]\n"
"s_and_b32 s50, s50, 0x380\n"
"ds_read_b128 a[188:191], v241 offset:6144\n"
"v_mfma_f32_16x16x32_bf16 v[104:107], v[168:171], a[136:139], v[104:107]\n"
"ds_read_b64_tr_b16 v[144:145], v242 offset:4096\n"
"s_add_u32 m0, s45, 32768\n"
"v_mfma_f32_16x16x32_bf16 v[104:107], v[184:187], a[152:155], v[104:107]\n"
"ds_read_b64_tr_b16 v[146:147], v242 offset:4608\n"
"ds_read_b64_tr_b16 v[148:149], v243 offset:4096\n"
"v_mfma_f32_16x16x32_bf16 v[120:123], v[168:171], a[140:143], v[120:123]\n"
"ds_read_b64_tr_b16 v[150:151], v243 offset:4608\n"
"ds_read_b64_tr_b16 v[152:153], v244 offset:4096\n"
"v_mfma_f32_16x16x32_bf16 v[120:123], v[184:187], a[156:159], v[120:123]\n"
"s_nop 0\n"
"buffer_load_dwordx4 v236, s[16:19], s50 offen offset:0 lds\n"
"v_mfma_f32_16x16x32_bf16 v[76:79], v[172:175], a[128:131], v[76:79]\n"
"ds_read_b64_tr_b16 v[154:155], v244 offset:4608\n"
"v_mfma_f32_16x16x32_bf16 v[76:79], v[188:191], a[144:147], v[76:79]\n"
"ds_read_b64_tr_b16 v[156:157], v245 offset:4096\n"
"ds_read_b64_tr_b16 v[158:159], v245 offset:4608\n"
"v_mfma_f32_16x16x32_bf16 v[92:95], v[172:175], a[132:135], v[92:95]\n"
"buffer_load_dwordx4 v237, s[16:19], s50 offen offset:1024 lds\n"
"v_mfma_f32_16x16x32_bf16 v[92:95], v[188:191], a[148:151], v[92:95]\n"
"buffer_load_dwordx4 v250, s[16:19], s50 offen offset:2048 lds\n"
"v_mfma_f32_16x16x32_bf16 v[108:111], v[172:175], a[136:139], v[108:111]\n"
"v_mfma_f32_16x16x32_bf16 v[108:111], v[188:191], a[152:155], v[108:111]\n"
"buffer_load_dwordx4 v251, s[16:19], s50 offen offset:3072 lds\n"
"v_mfma_f32_16x16x32_bf16 v[124:127], v[172:175], a[140:143], v[124:127]\n"
"v_mfma_f32_16x16x32_bf16 v[124:127], v[188:191], a[156:159], v[124:127]\n"
"s_waitcnt lgkmcnt(0)\n"
"v_mfma_f32_16x16x32_bf16 v[0:3], v[128:131], a[160:163], v[0:3]\n"
"ds_read_b64_tr_b16 v[160:161], v242 offset:8192\n"
"s_add_u32 m0, s45, 36864\n"
"v_mfma_f32_16x16x32_bf16 v[0:3], v[144:147], a[176:179], v[0:3]\n"
"ds_read_b64_tr_b16 v[162:163], v242 offset:8704\n"
"v_mfma_f32_16x16x32_bf16 v[16:19], v[128:131], a[164:167], v[16:19]\n"
"s_add_u32 s50, s50, 32768\n"
"v_mfma_f32_16x16x32_bf16 v[16:19], v[144:147], a[180:183], v[16:19]\n"
"ds_read_b64_tr_b16 v[164:165], v243 offset:8192\n"
"v_mfma_f32_16x16x32_bf16 v[32:35], v[128:131], a[168:171], v[32:35]\n"
"ds_read_b64_tr_b16 v[166:167], v243 offset:8704\n"
"v_mfma_f32_16x16x32_bf16 v[32:35], v[144:147], a[184:187], v[32:35]\n"
"buffer_load_dwordx4 v236, s[16:19], s50 offen offset:0 lds\n"
"v_mfma_f32_16x16x32_bf16 v[48:51], v[128:131], a[172:175], v[48:51]\n"
"ds_read_b64_tr_b16 v[168:169], v244 offset:8192\n"
"v_mfma_f32_16x16x32_bf16 v[48:51], v[144:147], a[188:191], v[48:51]\n"
"ds_read_b64_tr_b16 v[170:171], v244 offset:8704\n"
"v_mfma_f32_16x16x32_bf16 v[4:7], v[132:135], a[160:163], v[4:7]\n"
"buffer_load_dwordx4 v237, s[16:19], s50 offen offset:1024 lds\n"
"v_mfma_f32_16x16x32_bf16 v[4:7], v[148:151], a[176:179], v[4:7]\n"
"ds_read_b64_tr_b16 v[172:173], v245 offset:8192\n"
"v_mfma_f32_16x16x32_bf16 v[20:23], v[132:135], a[164:167], v[20:23]\n"
"ds_read_b64_tr_b16 v[174:175], v245 offset:8704\n"
"v_mfma_f32_16x16x32_bf16 v[20:23], v[148:151], a[180:183], v[20:23]\n"
"buffer_load_dwordx4 v250, s[16:19], s50 offen offset:2048 lds\n"
"v_mfma_f32_16x16x32_bf16 v[36:39], v[132:135], a[168:171], v[36:39]\n"
"ds_read_b64_tr_b16 v[176:177], v242 offset:12288\n"
"v_mfma_f32_16x16x32_bf16 v[36:39], v[148:151], a[184:187], v[36:39]\n"
"v_mfma_f32_16x16x32_bf16 v[52:55], v[132:135], a[172:175], v[52:55]\n"
"ds_read_b64_tr_b16 v[178:179], v242 offset:12800\n"
"buffer_load_dwordx4 v251, s[16:19], s50 offen offset:3072 lds\n"
"v_mfma_f32_16x16x32_bf16 v[52:55], v[148:151], a[188:191], v[52:55]\n"
"ds_read_b64_tr_b16 v[180:181], v243 offset:12288\n"
"v_mfma_f32_16x16x32_bf16 v[8:11], v[136:139], a[160:163], v[8:11]\n"
"v_mfma_f32_16x16x32_bf16 v[8:11], v[152:155], a[176:179], v[8:11]\n"
"ds_read_b64_tr_b16 v[182:183], v243 offset:12800\n"
"s_add_u32 m0, s46, 122752\n"
"v_mfma_f32_16x16x32_bf16 v[24:27], v[136:139], a[164:167], v[24:27]\n"
"ds_read_b64_tr_b16 v[184:185], v244 offset:12288\n"
"v_mfma_f32_16x16x32_bf16 v[24:27], v[152:155], a[180:183], v[24:27]\n"
"v_mfma_f32_16x16x32_bf16 v[40:43], v[136:139], a[168:171], v[40:43]\n"
"ds_read_b64_tr_b16 v[186:187], v244 offset:12800\n"
"s_add_u32 s92, s49, 3670016\n"
"v_mfma_f32_16x16x32_bf16 v[40:43], v[152:155], a[184:187], v[40:43]\n"
"ds_read_b64_tr_b16 v[188:189], v245 offset:12288\n"
"v_mfma_f32_16x16x32_bf16 v[56:59], v[136:139], a[172:175], v[56:59]\n"
"v_mfma_f32_16x16x32_bf16 v[56:59], v[152:155], a[188:191], v[56:59]\n"
"ds_read_b64_tr_b16 v[190:191], v245 offset:12800\n"
"s_and_b32 s92, s92, 0x380000\n"
"v_mfma_f32_16x16x32_bf16 v[12:15], v[140:143], a[160:163], v[12:15]\n"
"v_mfma_f32_16x16x32_bf16 v[12:15], v[156:159], a[176:179], v[12:15]\n"
"v_mfma_f32_16x16x32_bf16 v[28:31], v[140:143], a[164:167], v[28:31]\n"
"buffer_load_dwordx4 v238, s[20:23], s92 offen offset:128 lds\n"
"v_mfma_f32_16x16x32_bf16 v[28:31], v[156:159], a[180:183], v[28:31]\n"
"v_mfma_f32_16x16x32_bf16 v[44:47], v[140:143], a[168:171], v[44:47]\n"
"v_mfma_f32_16x16x32_bf16 v[44:47], v[156:159], a[184:187], v[44:47]\n"
"buffer_load_dwordx4 v239, s[20:23], s92 offen offset:1152 lds\n"
"v_mfma_f32_16x16x32_bf16 v[60:63], v[140:143], a[172:175], v[60:63]\n"
"v_mfma_f32_16x16x32_bf16 v[60:63], v[156:159], a[188:191], v[60:63]\n"
"s_waitcnt vmcnt(17) lgkmcnt(0)\n"
"s_barrier\n"
"v_mfma_f32_16x16x32_bf16 v[64:67], v[160:163], a[160:163], v[64:67]\n"
"v_add_u32 v242, 0xffff0000, v242\n"
"v_add_u32 v243, 0xffff0000, v243\n"
"v_mfma_f32_16x16x32_bf16 v[64:67], v[176:179], a[176:179], v[64:67]\n"
"s_add_u32 m0, s46, 65536\n"
"v_add_u32 v244, 0xffff0000, v244\n"
"v_mfma_f32_16x16x32_bf16 v[80:83], v[160:163], a[164:167], v[80:83]\n"
"s_waitcnt vmcnt(10)\n"
"v_xor_b32 v232, s82, v201\n"
"v_mfma_f32_16x16x32_bf16 v[80:83], v[176:179], a[180:183], v[80:83]\n"
"v_add_u32 v245, 0xffff0000, v245\n"
"s_add_u32 s92, s49, 0\n"
"v_mfma_f32_16x16x32_bf16 v[96:99], v[160:163], a[168:171], v[96:99]\n"
"v_bitop3_b32 v232, v203, s82, v232 bitop3:0xbe\n"
"v_bitop3_b32 v232, v205, s82, v232 bitop3:0xbe\n"
"v_mfma_f32_16x16x32_bf16 v[96:99], v[176:179], a[184:187], v[96:99]\n"
"v_add_u32 v240, 0xffff0000, v240\n"
"v_bitop3_b32 v232, v207, s82, v232 bitop3:0xbe\n"
"v_mfma_f32_16x16x32_bf16 v[112:115], v[160:163], a[172:175], v[112:115]\n"
"v_add_u32 v241, 0xffff0000, v241\n"
"s_and_b32 s92, s92, 0x380000\n"
"v_mfma_f32_16x16x32_bf16 v[112:115], v[176:179], a[188:191], v[112:115]\n"
"v_bitop3_b32 v232, v225, s82, v232 bitop3:0xbe\n"
"v_bitop3_b32 v232, v227, s82, v232 bitop3:0xbe\n"
"v_mfma_f32_16x16x32_bf16 v[68:71], v[164:167], a[160:163], v[68:71]\n"
"ds_read_b64_tr_b16 v[128:129], v242 offset:32768\n"
"ds_read_b128 a[192:195], v240 offset:0\n"
"v_mfma_f32_16x16x32_bf16 v[68:71], v[180:183], a[176:179], v[68:71]\n"
"v_bitop3_b32 v232, v229, s82, v232 bitop3:0xbe\n"
"v_bitop3_b32 v232, v231, s82, v232 bitop3:0xbe\n"
"v_mfma_f32_16x16x32_bf16 v[84:87], v[164:167], a[164:167], v[84:87]\n"
"ds_read_b64_tr_b16 v[130:131], v242 offset:33280\n"
"buffer_load_dwordx4 v238, s[24:27], s92 offen offset:0 lds\n"
"v_mfma_f32_16x16x32_bf16 v[84:87], v[180:183], a[180:183], v[84:87]\n"
"v_cmp_ne_u32 vcc, 0, v232\n"
"v_mfma_f32_16x16x32_bf16 v[100:103], v[164:167], a[168:171], v[100:103]\n"
"ds_read_b128 a[196:199], v240 offset:2048\n"
"s_cbranch_vccnz Lgs5_%=\n"
"Lgb4_%=:\n"
"v_mfma_f32_16x16x32_bf16 v[100:103], v[180:183], a[184:187], v[100:103]\n"
"v_lshlrev_b32 v232, 16, v224\n"
"ds_read_b64_tr_b16 v[132:133], v243 offset:32768\n"
"v_mfma_f32_16x16x32_bf16 v[116:119], v[164:167], a[172:175], v[116:119]\n"
"buffer_load_dwordx4 v239, s[24:27], s92 offen offset:1024 lds\n"
"v_mfma_f32_16x16x32_bf16 v[116:119], v[180:183], a[188:191], v[116:119]\n"
"v_and_b32 v233, 0xffff0000, v224\n"
"v_lshlrev_b32 v234, 16, v200\n"
"v_mfma_f32_16x16x32_bf16 v[72:75], v[168:171], a[160:163], v[72:75]\n"
"ds_read_b128 a[200:203], v240 offset:4096\n"
"v_and_b32 v235, 0xffff0000, v200\n"
"v_mfma_f32_16x16x32_bf16 v[72:75], v[184:187], a[176:179], v[72:75]\n"
"ds_read_b64_tr_b16 v[134:135], v243 offset:33280\n"
"ds_read_b128 a[204:207], v240 offset:6144\n"
"v_mfma_f32_16x16x32_bf16 v[88:91], v[168:171], a[164:167], v[88:91]\n"
"s_add_u32 m0, s46, 73728\n"
"v_mul_f32 v232, v208, v232\n"
"v_mfma_f32_16x16x32_bf16 v[88:91], v[184:187], a[180:183], v[88:91]\n"
"v_mul_f32 v233, v209, v233\n"
"ds_read_b64_tr_b16 v[136:137], v244 offset:32768\n"
"v_mfma_f32_16x16x32_bf16 v[104:107], v[168:171], a[168:171], v[104:107]\n"
"v_fmamk_f32 v232, v234, 0x3e0293ee, v232\n"
"v_fmamk_f32 v233, v235, 0x3e0293ee, v233\n"
"v_mfma_f32_16x16x32_bf16 v[104:107], v[184:187], a[184:187], v[104:107]\n"
"ds_read_b128 a[208:211], v241 offset:0\n"
"s_add_u32 s92, s49, 524288\n"
"v_mfma_f32_16x16x32_bf16 v[120:123], v[168:171], a[172:175], v[120:123]\n"
"v_cvt_pk_bf16_f32 v208, v232, v233\n"
"ds_read_b64_tr_b16 v[138:139], v244 offset:33280\n"
"v_mfma_f32_16x16x32_bf16 v[120:123], v[184:187], a[188:191], v[120:123]\n"
"v_lshlrev_b32 v232, 16, v226\n"
"v_and_b32 v233, 0xffff0000, v226\n"
"v_mfma_f32_16x16x32_bf16 v[76:79], v[172:175], a[160:163], v[76:79]\n"
"ds_read_b128 a[212:215], v241 offset:2048\n"
"s_and_b32 s92, s92, 0x380000\n"
"v_mfma_f32_16x16x32_bf16 v[76:79], v[188:191], a[176:179], v[76:79]\n"
"v_lshlrev_b32 v234, 16, v202\n"
"v_and_b32 v235, 0xffff0000, v202\n"
"v_mfma_f32_16x16x32_bf16 v[92:95], v[172:175], a[164:167], v[92:95]\n"
"ds_read_b64_tr_b16 v[140:141], v245 offset:32768\n"
"ds_read_b128 a[216:219], v241 offset:4096\n"
"v_mfma_f32_16x16x32_bf16 v[92:95], v[188:191], a[180:183], v[92:95]\n"
"v_mul_f32 v232, v210, v232\n"
"ds_read_b64_tr_b16 v[142:143], v245 offset:33280\n"
"v_mfma_f32_16x16x32_bf16 v[108:111], v[172:175], a[168:171], v[108:111]\n"
"v_mul_f32 v233, v211, v233\n"
"v_fmamk_f32 v232, v234, 0x3e0293ee, v232\n"
"v_mfma_f32_16x16x32_bf16 v[108:111], v[188:191], a[184:187], v[108:111]\n"
"ds_read_b128 a[220:223], v241 offset:6144\n"
"buffer_load_dwordx4 v238, s[24:27], s92 offen offset:0 lds\n"
"v_mfma_f32_16x16x32_bf16 v[124:127], v[172:175], a[172:175], v[124:127]\n"
"v_fmamk_f32 v233, v235, 0x3e0293ee, v233\n"
"v_mfma_f32_16x16x32_bf16 v[124:127], v[188:191], a[188:191], v[124:127]\n"
"ds_read_b64_tr_b16 v[144:145], v242 offset:36864\n"
"v_cvt_pk_bf16_f32 v209, v232, v233\n"
"v_lshlrev_b32 v232, 16, v228\n"
"ds_read_b64_tr_b16 v[146:147], v242 offset:37376\n"
"buffer_load_dwordx4 v239, s[24:27], s92 offen offset:1024 lds\n"
"v_and_b32 v233, 0xffff0000, v228\n"
"v_lshlrev_b32 v234, 16, v204\n"
"ds_read_b64_tr_b16 v[148:149], v243 offset:36864\n"
"ds_read_b64_tr_b16 v[150:151], v243 offset:37376\n"
"v_and_b32 v235, 0xffff0000, v204\n"
"ds_read_b64_tr_b16 v[152:153], v244 offset:36864\n"
"s_add_u32 m0, s46, 81920\n"
"v_mul_f32 v232, v212, v232\n"
"v_mul_f32 v233, v213, v233\n"
"ds_read_b64_tr_b16 v[154:155], v244 offset:37376\n"
"v_fmamk_f32 v232, v234, 0x3e0293ee, v232\n"
"v_fmamk_f32 v233, v235, 0x3e0293ee, v233\n"
"ds_read_b64_tr_b16 v[156:157], v245 offset:36864\n"
"s_add_u32 s92, s49, 1048576\n"
"v_cvt_pk_bf16_f32 v210, v232, v233\n"
"ds_read_b64_tr_b16 v[158:159], v245 offset:37376\n"
"v_lshlrev_b32 v232, 16, v230\n"
"v_and_b32 v233, 0xffff0000, v230\n"
"s_and_b32 s92, s92, 0x380000\n"
"v_lshlrev_b32 v234, 16, v206\n"
"v_and_b32 v235, 0xffff0000, v206\n"
"v_mul_f32 v232, v214, v232\n"
"buffer_load_dwordx4 v238, s[24:27], s92 offen offset:0 lds\n"
"v_mul_f32 v233, v215, v233\n"
"v_fmamk_f32 v232, v234, 0x3e0293ee, v232\n"
"v_fmamk_f32 v233, v235, 0x3e0293ee, v233\n"
"v_cvt_pk_bf16_f32 v211, v232, v233\n"
"buffer_load_dwordx4 v239, s[24:27], s92 offen offset:1024 lds\n"
"buffer_load_dwordx4 v[200:203], v247, s[88:91], s55 offen offset:256 sc1\n"
"buffer_load_dwordx4 v[204:207], v247, s[88:91], s55 offen offset:272 sc1\n"
"buffer_load_dwordx4 v[224:227], v247, s[88:91], s55 offen offset:2304 sc1\n"
"buffer_load_dwordx4 v[228:231], v247, s[88:91], s55 offen offset:2320 sc1\n"
"s_waitcnt lgkmcnt(0)\n"
"v_mfma_f32_16x16x32_bf16 v[0:3], v[128:131], a[192:195], v[0:3]\n"
"ds_read_b64_tr_b16 v[160:161], v242 offset:40960\n"
"s_add_u32 m0, s46, 90112\n"
"v_mfma_f32_16x16x32_bf16 v[0:3], v[144:147], a[208:211], v[0:3]\n"
"ds_read_b64_tr_b16 v[162:163], v242 offset:41472\n"
"v_mfma_f32_16x16x32_bf16 v[16:19], v[128:131], a[196:199], v[16:19]\n"
"s_add_u32 s92, s49, 1572864\n"
"v_mfma_f32_16x16x32_bf16 v[16:19], v[144:147], a[212:215], v[16:19]\n"
"ds_read_b64_tr_b16 v[164:165], v243 offset:40960\n"
"v_mfma_f32_16x16x32_bf16 v[32:35], v[128:131], a[200:203], v[32:35]\n"
"ds_read_b64_tr_b16 v[166:167], v243 offset:41472\n"
"s_and_b32 s92, s92, 0x380000\n"
"v_mfma_f32_16x16x32_bf16 v[32:35], v[144:147], a[216:219], v[32:35]\n"
"v_mfma_f32_16x16x32_bf16 v[48:51], v[128:131], a[204:207], v[48:51]\n"
"ds_read_b64_tr_b16 v[168:169], v244 offset:40960\n"
"buffer_load_dwordx4 v238, s[24:27], s92 offen offset:0 lds\n"
"v_mfma_f32_16x16x32_bf16 v[48:51], v[144:147], a[220:223], v[48:51]\n"
"ds_read_b64_tr_b16 v[170:171], v244 offset:41472\n"
"v_mfma_f32_16x16x32_bf16 v[4:7], v[132:135], a[192:195], v[4:7]\n"
"buffer_load_dwordx4 v239, s[24:27], s92 offen offset:1024 lds\n"
"v_mfma_f32_16x16x32_bf16 v[4:7], v[148:151], a[208:211], v[4:7]\n"
"ds_read_b64_tr_b16 v[172:173], v245 offset:40960\n"
"v_mfma_f32_16x16x32_bf16 v[20:23], v[132:135], a[196:199], v[20:23]\n"
"ds_read_b64_tr_b16 v[174:175], v245 offset:41472\n"
"s_add_u32 m0, s46, 130944\n"
"v_mfma_f32_16x16x32_bf16 v[20:23], v[148:151], a[212:215], v[20:23]\n"
"v_mfma_f32_16x16x32_bf16 v[36:39], v[132:135], a[200:203], v[36:39]\n"
"ds_read_b64_tr_b16 v[176:177], v242 offset:45056\n"
"s_add_u32 s92, s49, 0\n"
"v_mfma_f32_16x16x32_bf16 v[36:39], v[148:151], a[216:219], v[36:39]\n"
"v_mfma_f32_16x16x32_bf16 v[52:55], v[132:135], a[204:207], v[52:55]\n"
"ds_read_b64_tr_b16 v[178:179], v242 offset:45568\n"
"s_and_b32 s92, s92, 0x380000\n"
"v_mfma_f32_16x16x32_bf16 v[52:55], v[148:151], a[220:223], v[52:55]\n"
"ds_read_b64_tr_b16 v[180:181], v243 offset:45056\n"
"v_mfma_f32_16x16x32_bf16 v[8:11], v[136:139], a[192:195], v[8:11]\n"
"v_mfma_f32_16x16x32_bf16 v[8:11], v[152:155], a[208:211], v[8:11]\n"
"ds_read_b64_tr_b16 v[182:183], v243 offset:45568\n"
"buffer_load_dwordx4 v238, s[24:27], s92 offen offset:128 lds\n"
"v_mfma_f32_16x16x32_bf16 v[24:27], v[136:139], a[196:199], v[24:27]\n"
"ds_read_b64_tr_b16 v[184:185], v244 offset:45056\n"
"v_mfma_f32_16x16x32_bf16 v[24:27], v[152:155], a[212:215], v[24:27]\n"
"buffer_load_dwordx4 v239, s[24:27], s92 offen offset:1152 lds\n"
"v_mfma_f32_16x16x32_bf16 v[40:43], v[136:139], a[200:203], v[40:43]\n"
"ds_read_b64_tr_b16 v[186:187], v244 offset:45568\n"
"v_mfma_f32_16x16x32_bf16 v[40:43], v[152:155], a[216:219], v[40:43]\n"
"ds_read_b64_tr_b16 v[188:189], v245 offset:45056\n"
"s_add_u32 m0, s46, 139136\n"
"v_mfma_f32_16x16x32_bf16 v[56:59], v[136:139], a[204:207], v[56:59]\n"
"v_mfma_f32_16x16x32_bf16 v[56:59], v[152:155], a[220:223], v[56:59]\n"
"ds_read_b64_tr_b16 v[190:191], v245 offset:45568\n"
"s_add_u32 s92, s49, 524288\n"
"v_mfma_f32_16x16x32_bf16 v[12:15], v[140:143], a[192:195], v[12:15]\n"
"v_mfma_f32_16x16x32_bf16 v[12:15], v[156:159], a[208:211], v[12:15]\n"
"s_and_b32 s92, s92, 0x380000\n"
"v_mfma_f32_16x16x32_bf16 v[28:31], v[140:143], a[196:199], v[28:31]\n"
"v_mfma_f32_16x16x32_bf16 v[28:31], v[156:159], a[212:215], v[28:31]\n"
"buffer_load_dwordx4 v238, s[24:27], s92 offen offset:128 lds\n"
"v_mfma_f32_16x16x32_bf16 v[44:47], v[140:143], a[200:203], v[44:47]\n"
"v_mfma_f32_16x16x32_bf16 v[44:47], v[156:159], a[216:219], v[44:47]\n"
"buffer_load_dwordx4 v239, s[24:27], s92 offen offset:1152 lds\n"
"v_mfma_f32_16x16x32_bf16 v[60:63], v[140:143], a[204:207], v[60:63]\n"
"v_mfma_f32_16x16x32_bf16 v[60:63], v[156:159], a[220:223], v[60:63]\n"
"s_waitcnt vmcnt(16) lgkmcnt(0)\n"
"s_barrier\n"
"v_mfma_f32_16x16x32_bf16 v[64:67], v[160:163], a[192:195], v[64:67]\n"
"v_add_u32 v240, 0x8000, v240\n"
"v_add_u32 v241, 0x8000, v241\n"
"v_mfma_f32_16x16x32_bf16 v[64:67], v[176:179], a[208:211], v[64:67]\n"
"s_add_u32 m0, s46, 0\n"
"ds_read_b64_tr_b16 v[128:129], v242 offset:49152\n"
"v_mfma_f32_16x16x32_bf16 v[80:83], v[160:163], a[196:199], v[80:83]\n"
"s_waitcnt vmcnt(6)\n"
"v_xor_b32 v232, s82, v201\n"
"v_mfma_f32_16x16x32_bf16 v[80:83], v[176:179], a[212:215], v[80:83]\n"
"ds_read_b128 a[224:227], v240 offset:0\n"
"s_add_u32 s92, s49, 2097152\n"
"v_mfma_f32_16x16x32_bf16 v[96:99], v[160:163], a[200:203], v[96:99]\n"
"v_bitop3_b32 v232, v203, s82, v232 bitop3:0xbe\n"
"v_bitop3_b32 v232, v205, s82, v232 bitop3:0xbe\n"
"v_mfma_f32_16x16x32_bf16 v[96:99], v[176:179], a[216:219], v[96:99]\n"
"ds_read_b64_tr_b16 v[130:131], v242 offset:49664\n"
"v_bitop3_b32 v232, v207, s82, v232 bitop3:0xbe\n"
"v_mfma_f32_16x16x32_bf16 v[112:115], v[160:163], a[204:207], v[112:115]\n"
"ds_read_b128 a[228:231], v240 offset:2048\n"
"s_and_b32 s92, s92, 0x380000\n"
"v_mfma_f32_16x16x32_bf16 v[112:115], v[176:179], a[220:223], v[112:115]\n"
"v_bitop3_b32 v232, v225, s82, v232 bitop3:0xbe\n"
"v_bitop3_b32 v232, v227, s82, v232 bitop3:0xbe\n"
"v_mfma_f32_16x16x32_bf16 v[68:71], v[164:167], a[192:195], v[68:71]\n"
"ds_read_b64_tr_b16 v[132:133], v243 offset:49152\n"
"v_bitop3_b32 v232, v229, s82, v232 bitop3:0xbe\n"
"v_mfma_f32_16x16x32_bf16 v[68:71], v[180:183], a[208:211], v[68:71]\n"
"ds_read_b128 a[232:235], v240 offset:4096\n"
"buffer_load_dwordx4 v238, s[24:27], s92 offen offset:0 lds\n"
"v_mfma_f32_16x16x32_bf16 v[84:87], v[164:167], a[196:199], v[84:87]\n"
"v_bitop3_b32 v232, v231, s82, v232 bitop3:0xbe\n"
"v_mfma_f32_16x16x32_bf16 v[84:87], v[180:183], a[212:215], v[84:87]\n"
"v_cmp_ne_u32 vcc, 0, v232\n"
"ds_read_b64_tr_b16 v[134:135], v243 offset:49664\n"
"v_mfma_f32_16x16x32_bf16 v[100:103], v[164:167], a[200:203], v[100:103]\n"
"s_cbranch_vccnz Lgs8_%=\n"
"Lgb7_%=:\n"
"ds_read_b128 a[236:239], v240 offset:6144\n"
"v_mfma_f32_16x16x32_bf16 v[100:103], v[180:183], a[216:219], v[100:103]\n"
"buffer_load_dwordx4 v239, s[24:27], s92 offen offset:1024 lds\n"
"v_mfma_f32_16x16x32_bf16 v[116:119], v[164:167], a[204:207], v[116:119]\n"
"v_lshlrev_b32 v232, 16, v224\n"
"v_and_b32 v233, 0xffff0000, v224\n"
"v_mfma_f32_16x16x32_bf16 v[116:119], v[180:183], a[220:223], v[116:119]\n"
"ds_read_b64_tr_b16 v[136:137], v244 offset:49152\n"
"v_lshlrev_b32 v234, 16, v200\n"
"v_mfma_f32_16x16x32_bf16 v[72:75], v[168:171], a[192:195], v[72:75]\n"
"ds_read_b128 a[240:243], v241 offset:0\n"
"s_add_u32 m0, s46, 8192\n"
"v_mfma_f32_16x16x32_bf16 v[72:75], v[184:187], a[208:211], v[72:75]\n"
"v_and_b32 v235, 0xffff0000, v200\n"
"v_mul_f32 v232, v216, v232\n"
"v_mfma_f32_16x16x32_bf16 v[88:91], v[168:171], a[196:199], v[88:91]\n"
"ds_read_b64_tr_b16 v[138:139], v244 offset:49664\n"
"v_mul_f32 v233, v217, v233\n"
"v_mfma_f32_16x16x32_bf16 v[88:91], v[184:187], a[212:215], v[88:91]\n"
"ds_read_b128 a[244:247], v241 offset:2048\n"
"s_add_u32 s92, s49, 2621440\n"
"v_mfma_f32_16x16x32_bf16 v[104:107], v[168:171], a[200:203], v[104:107]\n"
"v_fmamk_f32 v232, v234, 0x3e0293ee, v232\n"
"v_fmamk_f32 v233, v235, 0x3e0293ee, v233\n"
"v_mfma_f32_16x16x32_bf16 v[104:107], v[184:187], a[216:219], v[104:107]\n"
"ds_read_b64_tr_b16 v[140:141], v245 offset:49152\n"
"v_cvt_pk_bf16_f32 v216, v232, v233\n"
"v_mfma_f32_16x16x32_bf16 v[120:123], v[168:171], a[204:207], v[120:123]\n"
"ds_read_b128 a[248:251], v241 offset:4096\n"
"s_and_b32 s92, s92, 0x380000\n"
"v_mfma_f32_16x16x32_bf16 v[120:123], v[184:187], a[220:223], v[120:123]\n"
"v_lshlrev_b32 v232, 16, v226\n"
"v_and_b32 v233, 0xffff0000, v226\n"
"v_mfma_f32_16x16x32_bf16 v[76:79], v[172:175], a[192:195], v[76:79]\n"
"ds_read_b64_tr_b16 v[142:143], v245 offset:49664\n"
"v_lshlrev_b32 v234, 16, v202\n"
"v_mfma_f32_16x16x32_bf16 v[76:79], v[188:191], a[208:211], v[76:79]\n"
"ds_read_b128 a[252:255], v241 offset:6144\n"
"v_and_b32 v235, 0xffff0000, v202\n"
"v_mfma_f32_16x16x32_bf16 v[92:95], v[172:175], a[196:199], v[92:95]\n"
"v_mul_f32 v232, v218, v232\n"
"ds_read_b64_tr_b16 v[144:145], v242 offset:53248\n"
"v_mfma_f32_16x16x32_bf16 v[92:95], v[188:191], a[212:215], v[92:95]\n"
"buffer_load_dwordx4 v238, s[24:27], s92 offen offset:0 lds\n"
"v_mfma_f32_16x16x32_bf16 v[108:111], v[172:175], a[200:203], v[108:111]\n"
"v_mul_f32 v233, v219, v233\n"
"ds_read_b64_tr_b16 v[146:147], v242 offset:53760\n"
"v_mfma_f32_16x16x32_bf16 v[108:111], v[188:191], a[216:219], v[108:111]\n"
"v_fmamk_f32 v232, v234, 0x3e0293ee, v232\n"
"v_fmamk_f32 v233, v235, 0x3e0293ee, v233\n"
"v_mfma_f32_16x16x32_bf16 v[124:127], v[172:175], a[204:207], v[124:127]\n"
"ds_read_b64_tr_b16 v[148:149], v243 offset:53248\n"
"buffer_load_dwordx4 v239, s[24:27], s92 offen offset:1024 lds\n"
"v_mfma_f32_16x16x32_bf16 v[124:127], v[188:191], a[220:223], v[124:127]\n"
"v_cvt_pk_bf16_f32 v217, v232, v233\n"
"ds_read_b64_tr_b16 v[150:151], v243 offset:53760\n"
"v_lshlrev_b32 v232, 16, v228\n"
"v_and_b32 v233, 0xffff0000, v228\n"
"ds_read_b64_tr_b16 v[152:153], v244 offset:53248\n"
"s_add_u32 m0, s46, 147328\n"
"v_lshlrev_b32 v234, 16, v204\n"
"ds_read_b64_tr_b16 v[154:155], v244 offset:53760\n"
"v_and_b32 v235, 0xffff0000, v204\n"
"v_mul_f32 v232, v220, v232\n"
"ds_read_b64_tr_b16 v[156:157], v245 offset:53248\n"
"s_add_u32 s92, s49, 1048576\n"
"v_mul_f32 v233, v221, v233\n"
"ds_read_b64_tr_b16 v[158:159], v245 offset:53760\n"
"v_fmamk_f32 v232, v234, 0x3e0293ee, v232\n"
"v_fmamk_f32 v233, v235, 0x3e0293ee, v233\n"
"s_and_b32 s92, s92, 0x380000\n"
"v_cvt_pk_bf16_f32 v218, v232, v233\n"
"v_lshlrev_b32 v232, 16, v230\n"
"v_and_b32 v233, 0xffff0000, v230\n"
"buffer_load_dwordx4 v238, s[24:27], s92 offen offset:128 lds\n"
"v_lshlrev_b32 v234, 16, v206\n"
"v_and_b32 v235, 0xffff0000, v206\n"
"v_mul_f32 v232, v222, v232\n"
"buffer_load_dwordx4 v239, s[24:27], s92 offen offset:1152 lds\n"
"v_mul_f32 v233, v223, v233\n"
"v_fmamk_f32 v232, v234, 0x3e0293ee, v232\n"
"v_fmamk_f32 v233, v235, 0x3e0293ee, v233\n"
"v_cvt_pk_bf16_f32 v219, v232, v233\n"
"s_waitcnt lgkmcnt(0)\n"
"v_mfma_f32_16x16x32_bf16 v[0:3], v[128:131], a[224:227], v[0:3]\n"
"ds_read_b64_tr_b16 v[160:161], v242 offset:57344\n"
"s_add_u32 m0, s46, 16384\n"
"v_mfma_f32_16x16x32_bf16 v[0:3], v[144:147], a[240:243], v[0:3]\n"
"ds_read_b64_tr_b16 v[162:163], v242 offset:57856\n"
"v_mfma_f32_16x16x32_bf16 v[16:19], v[128:131], a[228:231], v[16:19]\n"
"s_add_u32 s92, s49, 3145728\n"
"v_mfma_f32_16x16x32_bf16 v[16:19], v[144:147], a[244:247], v[16:19]\n"
"ds_read_b64_tr_b16 v[164:165], v243 offset:57344\n"
"v_lshrrev_b32 v232, 3, v255\n"
"v_mfma_f32_16x16x32_bf16 v[32:35], v[128:131], a[232:235], v[32:35]\n"
"v_lshlrev_b32 v232, 8, v232\n"
"ds_read_b64_tr_b16 v[166:167], v243 offset:57856\n"
"v_mfma_f32_16x16x32_bf16 v[32:35], v[144:147], a[248:251], v[32:35]\n"
"s_and_b32 s92, s92, 0x380000\n"
"v_and_b32 v233, 7, v255\n"
"v_mfma_f32_16x16x32_bf16 v[48:51], v[128:131], a[236:239], v[48:51]\n"
"v_lshl_add_u32 v232, v233, 4, v232\n"
"ds_read_b64_tr_b16 v[168:169], v244 offset:57344\n"
"v_mfma_f32_16x16x32_bf16 v[48:51], v[144:147], a[252:255], v[48:51]\n"
"buffer_load_dwordx4 v238, s[24:27], s92 offen offset:0 lds\n"
"v_mfma_f32_16x16x32_bf16 v[4:7], v[132:135], a[224:227], v[4:7]\n"
"s_lshr_b32 s55, s84, 1\n"
"s_lshl_b32 s55, s55, 14\n"
"v_mfma_f32_16x16x32_bf16 v[4:7], v[148:151], a[240:243], v[4:7]\n"
"ds_read_b64_tr_b16 v[170:171], v244 offset:57856\n"
"s_add_u32 s55, s55, s54\n"
"v_mfma_f32_16x16x32_bf16 v[20:23], v[132:135], a[228:231], v[20:23]\n"
"buffer_load_dwordx4 v239, s[24:27], s92 offen offset:1024 lds\n"
"v_mfma_f32_16x16x32_bf16 v[20:23], v[148:151], a[244:247], v[20:23]\n"
"s_and_b32 s95, s84, 1\n"
"ds_read_b64_tr_b16 v[172:173], v245 offset:57344\n"
"v_mfma_f32_16x16x32_bf16 v[36:39], v[132:135], a[232:235], v[36:39]\n"
"s_lshl_b32 s95, s95, 7\n"
"s_add_u32 s55, s55, s95\n"
"v_mfma_f32_16x16x32_bf16 v[36:39], v[148:151], a[248:251], v[36:39]\n"
"ds_read_b64_tr_b16 v[174:175], v245 offset:57856\n"
"s_add_u32 m0, s46, 24576\n"
"v_mfma_f32_16x16x32_bf16 v[52:55], v[132:135], a[236:239], v[52:55]\n"
"buffer_store_dwordx4 v[208:211], v232, s[40:43], s55 offen sc1\n"
"v_mfma_f32_16x16x32_bf16 v[52:55], v[148:151], a[252:255], v[52:55]\n"
"buffer_store_dwordx4 v[216:219], v232, s[40:43], s55 offen offset:2048 sc1\n"
"v_mfma_f32_16x16x32_bf16 v[8:11], v[136:139], a[224:227], v[8:11]\n"
"ds_read_b64_tr_b16 v[176:177], v242 offset:61440\n"
"s_add_u32 s92, s49, 3670016\n"
"v_mfma_f32_16x16x32_bf16 v[8:11], v[152:155], a[240:243], v[8:11]\n"
"ds_read_b64_tr_b16 v[178:179], v242 offset:61952\n"
"s_and_b32 s92, s92, 0x380000\n"
"v_mfma_f32_16x16x32_bf16 v[24:27], v[136:139], a[228:231], v[24:27]\n"
"ds_read_b64_tr_b16 v[180:181], v243 offset:61440\n"
"ds_read_b64_tr_b16 v[182:183], v243 offset:61952\n"
"v_mfma_f32_16x16x32_bf16 v[24:27], v[152:155], a[244:247], v[24:27]\n"
"buffer_load_dwordx4 v238, s[24:27], s92 offen offset:0 lds\n"
"v_mfma_f32_16x16x32_bf16 v[40:43], v[136:139], a[232:235], v[40:43]\n"
"ds_read_b64_tr_b16 v[184:185], v244 offset:61440\n"
"buffer_load_dwordx4 v239, s[24:27], s92 offen offset:1024 lds\n"
"v_mfma_f32_16x16x32_bf16 v[40:43], v[152:155], a[248:251], v[40:43]\n"
"ds_read_b64_tr_b16 v[186:187], v244 offset:61952\n"
"v_mfma_f32_16x16x32_bf16 v[56:59], v[136:139], a[236:239], v[56:59]\n"
"ds_read_b64_tr_b16 v[188:189], v245 offset:61440\n"
"s_add_u32 m0, s46, 155520\n"
"v_mfma_f32_16x16x32_bf16 v[56:59], v[152:155], a[252:255], v[56:59]\n"
"ds_read_b64_tr_b16 v[190:191], v245 offset:61952\n"
"s_add_u32 s92, s49, 1572864\n"
"v_mfma_f32_16x16x32_bf16 v[12:15], v[140:143], a[224:227], v[12:15]\n"
"v_mfma_f32_16x16x32_bf16 v[12:15], v[156:159], a[240:243], v[12:15]\n"
"s_and_b32 s92, s92, 0x380000\n"
"v_mfma_f32_16x16x32_bf16 v[28:31], v[140:143], a[228:231], v[28:31]\n"
"v_mfma_f32_16x16x32_bf16 v[28:31], v[156:159], a[244:247], v[28:31]\n"
"buffer_load_dwordx4 v238, s[24:27], s92 offen offset:128 lds\n"
"v_mfma_f32_16x16x32_bf16 v[44:47], v[140:143], a[232:235], v[44:47]\n"
"v_mfma_f32_16x16x32_bf16 v[44:47], v[156:159], a[248:251], v[44:47]\n"
"buffer_load_dwordx4 v239, s[24:27], s92 offen offset:1152 lds\n"
"v_mfma_f32_16x16x32_bf16 v[60:63], v[140:143], a[236:239], v[60:63]\n"
"v_mfma_f32_16x16x32_bf16 v[60:63], v[156:159], a[252:255], v[60:63]\n"
"s_waitcnt vmcnt(18) lgkmcnt(0)\n"
"s_barrier\n"
"v_mfma_f32_16x16x32_bf16 v[64:67], v[160:163], a[224:227], v[64:67]\n"
"ds_read_b64_tr_b16 v[128:129], v242 offset:0\n"
"v_mfma_f32_16x16x32_bf16 v[64:67], v[176:179], a[240:243], v[64:67]\n"
"ds_read_b64_tr_b16 v[130:131], v242 offset:512\n"
"v_mfma_f32_16x16x32_bf16 v[80:83], v[160:163], a[228:231], v[80:83]\n"
"v_cvt_pk_bf16_f32 v192, v0, v1\n"
"v_cvt_pk_bf16_f32 v194, v16, v17\n"
"v_mfma_f32_16x16x32_bf16 v[80:83], v[176:179], a[244:247], v[80:83]\n"
"v_cvt_pk_bf16_f32 v193, v2, v3\n"
"ds_read_b64_tr_b16 v[132:133], v243 offset:0\n"
"v_mfma_f32_16x16x32_bf16 v[96:99], v[160:163], a[232:235], v[96:99]\n"
"v_cvt_pk_bf16_f32 v195, v18, v19\n"
"s_add_u32 s87, s48, 0\n"
"v_mfma_f32_16x16x32_bf16 v[96:99], v[176:179], a[248:251], v[96:99]\n"
"ds_read_b64_tr_b16 v[134:135], v243 offset:512\n"
"v_permlane16_swap_b32 v192, v194\n"
"v_mfma_f32_16x16x32_bf16 v[112:115], v[160:163], a[236:239], v[112:115]\n"
"v_permlane16_swap_b32 v193, v195\n"
"buffer_store_dwordx4 v[192:195], v246, s[28:31], s87 offen offset:0 sc1\n"
"v_mfma_f32_16x16x32_bf16 v[112:115], v[176:179], a[252:255], v[112:115]\n"
"v_cvt_pk_bf16_f32 v196, v4, v5\n"
"v_mfma_f32_16x16x32_bf16 v[68:71], v[164:167], a[224:227], v[68:71]\n"
"ds_read_b64_tr_b16 v[136:137], v244 offset:0\n"
"v_cvt_pk_bf16_f32 v198, v20, v21\n"
"v_mfma_f32_16x16x32_bf16 v[68:71], v[180:183], a[240:243], v[68:71]\n"
"v_cvt_pk_bf16_f32 v197, v6, v7\n"
"ds_read_b64_tr_b16 v[138:139], v244 offset:512\n"
"v_mfma_f32_16x16x32_bf16 v[84:87], v[164:167], a[228:231], v[84:87]\n"
"v_cvt_pk_bf16_f32 v199, v22, v23\n"
"s_add_u32 s87, s48, 0\n"
"v_mfma_f32_16x16x32_bf16 v[84:87], v[180:183], a[244:247], v[84:87]\n"
"v_permlane16_swap_b32 v196, v198\n"
"v_permlane16_swap_b32 v197, v199\n"
"v_mfma_f32_16x16x32_bf16 v[100:103], v[164:167], a[232:235], v[100:103]\n"
"ds_read_b64_tr_b16 v[140:141], v245 offset:0\n"
"buffer_store_dwordx4 v[196:199], v246, s[28:31], s87 offen offset:1024 sc1\n"
"v_mfma_f32_16x16x32_bf16 v[100:103], v[180:183], a[248:251], v[100:103]\n"
"v_cvt_pk_bf16_f32 v192, v8, v9\n"
"v_mfma_f32_16x16x32_bf16 v[116:119], v[164:167], a[236:239], v[116:119]\n"
"v_cvt_pk_bf16_f32 v194, v24, v25\n"
"ds_read_b64_tr_b16 v[142:143], v245 offset:512\n"
"v_mfma_f32_16x16x32_bf16 v[116:119], v[180:183], a[252:255], v[116:119]\n"
"v_cvt_pk_bf16_f32 v193, v10, v11\n"
"v_cvt_pk_bf16_f32 v195, v26, v27\n"
"v_mfma_f32_16x16x32_bf16 v[72:75], v[168:171], a[224:227], v[72:75]\n"
"s_add_u32 s87, s48, 0\n"
"v_permlane16_swap_b32 v192, v194\n"
"v_mfma_f32_16x16x32_bf16 v[72:75], v[184:187], a[240:243], v[72:75]\n"
"ds_read_b64_tr_b16 v[144:145], v242 offset:4096\n"
"v_permlane16_swap_b32 v193, v195\n"
"v_mfma_f32_16x16x32_bf16 v[88:91], v[168:171], a[228:231], v[88:91]\n"
"buffer_store_dwordx4 v[192:195], v246, s[28:31], s87 offen offset:2048 sc1\n"
"v_mfma_f32_16x16x32_bf16 v[88:91], v[184:187], a[244:247], v[88:91]\n"
"v_cvt_pk_bf16_f32 v196, v12, v13\n"
"v_cvt_pk_bf16_f32 v198, v28, v29\n"
"v_mfma_f32_16x16x32_bf16 v[104:107], v[168:171], a[232:235], v[104:107]\n"
"ds_read_b64_tr_b16 v[146:147], v242 offset:4608\n"
"v_cvt_pk_bf16_f32 v197, v14, v15\n"
"v_mfma_f32_16x16x32_bf16 v[104:107], v[184:187], a[248:251], v[104:107]\n"
"v_cvt_pk_bf16_f32 v199, v30, v31\n"
"ds_read_b64_tr_b16 v[148:149], v243 offset:4096\n"
"v_mfma_f32_16x16x32_bf16 v[120:123], v[168:171], a[236:239], v[120:123]\n"
"s_add_u32 s87, s48, 0\n"
"v_permlane16_swap_b32 v196, v198\n"
"v_mfma_f32_16x16x32_bf16 v[120:123], v[184:187], a[252:255], v[120:123]\n"
"v_permlane16_swap_b32 v197, v199\n"
"buffer_store_dwordx4 v[196:199], v246, s[28:31], s87 offen offset:3072 sc1\n"
"v_mfma_f32_16x16x32_bf16 v[76:79], v[172:175], a[224:227], v[76:79]\n"
"ds_read_b64_tr_b16 v[150:151], v243 offset:4608\n"
"v_mfma_f32_16x16x32_bf16 v[76:79], v[188:191], a[240:243], v[76:79]\n"
"v_cvt_pk_bf16_f32 v192, v32, v33\n"
"v_cvt_pk_bf16_f32 v194, v48, v49\n"
"v_mfma_f32_16x16x32_bf16 v[92:95], v[172:175], a[228:231], v[92:95]\n"
"v_cvt_pk_bf16_f32 v193, v34, v35\n"
"ds_read_b64_tr_b16 v[152:153], v244 offset:4096\n"
"v_mfma_f32_16x16x32_bf16 v[92:95], v[188:191], a[244:247], v[92:95]\n"
"v_cvt_pk_bf16_f32 v195, v50, v51\n"
"s_add_u32 s87, s48, 8192\n"
"v_mfma_f32_16x16x32_bf16 v[108:111], v[172:175], a[232:235], v[108:111]\n"
"v_permlane16_swap_b32 v192, v194\n"
"v_permlane16_swap_b32 v193, v195\n"
"v_mfma_f32_16x16x32_bf16 v[108:111], v[188:191], a[248:251], v[108:111]\n"
"ds_read_b64_tr_b16 v[154:155], v244 offset:4608\n"
"buffer_store_dwordx4 v[192:195], v246, s[28:31], s87 offen offset:0 sc1\n"
"v_mfma_f32_16x16x32_bf16 v[124:127], v[172:175], a[236:239], v[124:127]\n"
"v_cvt_pk_bf16_f32 v196, v36, v37\n"
"v_mfma_f32_16x16x32_bf16 v[124:127], v[188:191], a[252:255], v[124:127]\n"
"ds_read_b64_tr_b16 v[156:157], v245 offset:4096\n"
"v_cvt_pk_bf16_f32 v198, v52, v53\n"
"v_cvt_pk_bf16_f32 v197, v38, v39\n"
"v_cvt_pk_bf16_f32 v199, v54, v55\n"
"s_add_u32 s87, s48, 8192\n"
"ds_read_b64_tr_b16 v[158:159], v245 offset:4608\n"
"v_permlane16_swap_b32 v196, v198\n"
"v_permlane16_swap_b32 v197, v199\n"
"buffer_store_dwordx4 v[196:199], v246, s[28:31], s87 offen offset:1024 sc1\n"
"v_cvt_pk_bf16_f32 v192, v40, v41\n"
"v_cvt_pk_bf16_f32 v194, v56, v57\n"
"v_cvt_pk_bf16_f32 v193, v42, v43\n"
"v_cvt_pk_bf16_f32 v195, v58, v59\n"
"s_add_u32 s87, s48, 8192\n"
"v_permlane16_swap_b32 v192, v194\n"
"v_permlane16_swap_b32 v193, v195\n"
"buffer_store_dwordx4 v[192:195], v246, s[28:31], s87 offen offset:2048 sc1\n"
"v_cvt_pk_bf16_f32 v196, v44, v45\n"
"v_cvt_pk_bf16_f32 v198, v60, v61\n"
"v_cvt_pk_bf16_f32 v197, v46, v47\n"
"v_cvt_pk_bf16_f32 v199, v62, v63\n"
"s_add_u32 s87, s48, 8192\n"
"v_permlane16_swap_b32 v196, v198\n"
"v_permlane16_swap_b32 v197, v199\n"
"buffer_store_dwordx4 v[196:199], v246, s[28:31], s87 offen offset:3072 sc1\n"
"s_waitcnt lgkmcnt(0)\n"
"v_mfma_f32_16x16x32_bf16 v[0:3], a[0:3], v[128:131], 0\n"
"ds_read_b64_tr_b16 v[160:161], v242 offset:8192\n"
"s_add_u32 m0, s46, 98176\n"
"v_mfma_f32_16x16x32_bf16 v[0:3], a[16:19], v[144:147], v[0:3]\n"
"ds_read_b64_tr_b16 v[162:163], v242 offset:8704\n"
"v_mfma_f32_16x16x32_bf16 v[16:19], a[4:7], v[128:131], 0\n"
"v_mfma_f32_16x16x32_bf16 v[16:19], a[20:23], v[144:147], v[16:19]\n"
"ds_read_b64_tr_b16 v[164:165], v243 offset:8192\n"
"s_add_u32 s92, s49, 2097152\n"
"v_mfma_f32_16x16x32_bf16 v[32:35], a[8:11], v[128:131], 0\n"
"ds_read_b64_tr_b16 v[166:167], v243 offset:8704\n"
"v_mfma_f32_16x16x32_bf16 v[32:35], a[24:27], v[144:147], v[32:35]\n"
"v_mfma_f32_16x16x32_bf16 v[48:51], a[12:15], v[128:131], 0\n"
"ds_read_b64_tr_b16 v[168:169], v244 offset:8192\n"
"s_and_b32 s92, s92, 0x380000\n"
"v_mfma_f32_16x16x32_bf16 v[48:51], a[28:31], v[144:147], v[48:51]\n"
"v_cvt_pk_bf16_f32 v192, v64, v65\n"
"ds_read_b64_tr_b16 v[170:171], v244 offset:8704\n"
"v_mfma_f32_16x16x32_bf16 v[4:7], a[0:3], v[132:135], 0\n"
"v_cvt_pk_bf16_f32 v194, v80, v81\n"
"v_mfma_f32_16x16x32_bf16 v[4:7], a[16:19], v[148:151], v[4:7]\n"
"ds_read_b64_tr_b16 v[172:173], v245 offset:8192\n"
"buffer_load_dwordx4 v238, s[24:27], s92 offen offset:128 lds\n"
"v_mfma_f32_16x16x32_bf16 v[20:23], a[4:7], v[132:135], 0\n"
"ds_read_b64_tr_b16 v[174:175], v245 offset:8704\n"
"v_mfma_f32_16x16x32_bf16 v[20:23], a[20:23], v[148:151], v[20:23]\n"
"v_cvt_pk_bf16_f32 v193, v66, v67\n"
"v_mfma_f32_16x16x32_bf16 v[36:39], a[8:11], v[132:135], 0\n"
"ds_read_b64_tr_b16 v[176:177], v242 offset:12288\n"
"buffer_load_dwordx4 v239, s[24:27], s92 offen offset:1152 lds\n"
"v_mfma_f32_16x16x32_bf16 v[36:39], a[24:27], v[148:151], v[36:39]\n"
"v_mfma_f32_16x16x32_bf16 v[52:55], a[12:15], v[132:135], 0\n"
"ds_read_b64_tr_b16 v[178:179], v242 offset:12800\n"
"v_cvt_pk_bf16_f32 v195, v82, v83\n"
"v_mfma_f32_16x16x32_bf16 v[52:55], a[28:31], v[148:151], v[52:55]\n"
"ds_read_b64_tr_b16 v[180:181], v243 offset:12288\n"
"v_mfma_f32_16x16x32_bf16 v[8:11], a[0:3], v[136:139], 0\n"
"s_add_u32 m0, s46, 106368\n"
"v_mfma_f32_16x16x32_bf16 v[8:11], a[16:19], v[152:155], v[8:11]\n"
"ds_read_b64_tr_b16 v[182:183], v243 offset:12800\n"
"s_add_u32 s87, s48, 4096\n"
"v_mfma_f32_16x16x32_bf16 v[24:27], a[4:7], v[136:139], 0\n"
"ds_read_b64_tr_b16 v[184:185], v244 offset:12288\n"
"v_mfma_f32_16x16x32_bf16 v[24:27], a[20:23], v[152:155], v[24:27]\n"
"s_add_u32 s92, s49, 2621440\n"
"v_mfma_f32_16x16x32_bf16 v[40:43], a[8:11], v[136:139], 0\n"
"ds_read_b64_tr_b16 v[186:187], v244 offset:12800\n"
"v_permlane16_swap_b32 v192, v194\n"
"v_mfma_f32_16x16x32_bf16 v[40:43], a[24:27], v[152:155], v[40:43]\n"
"ds_read_b64_tr_b16 v[188:189], v245 offset:12288\n"
"v_mfma_f32_16x16x32_bf16 v[56:59], a[12:15], v[136:139], 0\n"
"s_and_b32 s92, s92, 0x380000\n"
"v_mfma_f32_16x16x32_bf16 v[56:59], a[28:31], v[152:155], v[56:59]\n"
"ds_read_b64_tr_b16 v[190:191], v245 offset:12800\n"
"v_permlane16_swap_b32 v193, v195\n"
"v_mfma_f32_16x16x32_bf16 v[12:15], a[0:3], v[140:143], 0\n"
"v_mfma_f32_16x16x32_bf16 v[12:15], a[16:19], v[156:159], v[12:15]\n"
"buffer_load_dwordx4 v238, s[24:27], s92 offen offset:128 lds\n"
"v_mfma_f32_16x16x32_bf16 v[28:31], a[4:7], v[140:143], 0\n"
"buffer_store_dwordx4 v[192:195], v246, s[28:31], s87 offen offset:0 sc1\n"
"v_mfma_f32_16x16x32_bf16 v[28:31], a[20:23], v[156:159], v[28:31]\n"
"v_mfma_f32_16x16x32_bf16 v[44:47], a[8:11], v[140:143], 0\n"
"buffer_load_dwordx4 v239, s[24:27], s92 offen offset:1152 lds\n"
"v_mfma_f32_16x16x32_bf16 v[44:47], a[24:27], v[156:159], v[44:47]\n"
"v_mfma_f32_16x16x32_bf16 v[60:63], a[12:15], v[140:143], 0\n"
"v_mfma_f32_16x16x32_bf16 v[60:63], a[28:31], v[156:159], v[60:63]\n"
"s_waitcnt lgkmcnt(0)\n"
"v_mfma_f32_16x16x32_bf16 v[0:3], a[32:35], v[160:163], v[0:3]\n"
"ds_read_b64_tr_b16 v[128:129], v242 offset:16384\n"
"s_add_u32 m0, s46, 114560\n"
"v_mfma_f32_16x16x32_bf16 v[0:3], a[48:51], v[176:179], v[0:3]\n"
"ds_read_b64_tr_b16 v[130:131], v242 offset:16896\n"
"v_mfma_f32_16x16x32_bf16 v[16:19], a[36:39], v[160:163], v[16:19]\n"
"v_mfma_f32_16x16x32_bf16 v[16:19], a[52:55], v[176:179], v[16:19]\n"
"ds_read_b64_tr_b16 v[132:133], v243 offset:16384\n"
"s_add_u32 s92, s49, 3145728\n"
"v_mfma_f32_16x16x32_bf16 v[32:35], a[40:43], v[160:163], v[32:35]\n"
"s_lshl_b32 s95, s85, 8\n"
"ds_read_b64_tr_b16 v[134:135], v243 offset:16896\n"
"v_mfma_f32_16x16x32_bf16 v[32:35], a[56:59], v[176:179], v[32:35]\n"
"s_add_u32 s20, s60, s95\n"
"s_addc_u32 s21, s61, 0\n"
"v_mfma_f32_16x16x32_bf16 v[48:51], a[44:47], v[160:163], v[48:51]\n"
"ds_read_b64_tr_b16 v[136:137], v244 offset:16384\n"
"s_and_b32 s92, s92, 0x380000\n"
"v_mfma_f32_16x16x32_bf16 v[48:51], a[60:63], v[176:179], v[48:51]\n"
"v_cvt_pk_bf16_f32 v196, v68, v69\n"
"ds_read_b64_tr_b16 v[138:139], v244 offset:16896\n"
"v_mfma_f32_16x16x32_bf16 v[4:7], a[32:35], v[164:167], v[4:7]\n"
"s_and_b32 s21, s21, 0xffff\n"
"v_cvt_pk_bf16_f32 v198, v84, v85\n"
"v_mfma_f32_16x16x32_bf16 v[4:7], a[48:51], v[180:183], v[4:7]\n"
"ds_read_b64_tr_b16 v[140:141], v245 offset:16384\n"
"buffer_load_dwordx4 v238, s[24:27], s92 offen offset:128 lds\n"
"v_mfma_f32_16x16x32_bf16 v[20:23], a[36:39], v[164:167], v[20:23]\n"
"s_mov_b32 s22, 0x400000\n"
"v_mfma_f32_16x16x32_bf16 v[20:23], a[52:55], v[180:183], v[20:23]\n"
"ds_read_b64_tr_b16 v[142:143], v245 offset:16896\n"
"s_mov_b32 s23, 0x20000\n"
"v_mfma_f32_16x16x32_bf16 v[36:39], a[40:43], v[164:167], v[36:39]\n"
"v_cvt_pk_bf16_f32 v197, v70, v71\n"
"ds_read_b64_tr_b16 v[144:145], v242 offset:20480\n"
"v_mfma_f32_16x16x32_bf16 v[36:39], a[56:59], v[180:183], v[36:39]\n"
"buffer_load_dwordx4 v239, s[24:27], s92 offen offset:1152 lds\n"
"v_mfma_f32_16x16x32_bf16 v[52:55], a[44:47], v[164:167], v[52:55]\n"
"ds_read_b64_tr_b16 v[146:147], v242 offset:20992\n"
"s_sub_u32 s22, s22, s95\n"
"v_mfma_f32_16x16x32_bf16 v[52:55], a[60:63], v[180:183], v[52:55]\n"
"v_cvt_pk_bf16_f32 v199, v86, v87\n"
"ds_read_b64_tr_b16 v[148:149], v243 offset:20480\n"
"v_mfma_f32_16x16x32_bf16 v[8:11], a[32:35], v[168:171], v[8:11]\n"
"s_add_u32 m0, s46, 122752\n"
"s_lshl_b32 s95, s84, 7\n"
"v_mfma_f32_16x16x32_bf16 v[8:11], a[48:51], v[184:187], v[8:11]\n"
"ds_read_b64_tr_b16 v[150:151], v243 offset:20992\n"
"s_add_u32 s87, s48, 4096\n"
"v_mfma_f32_16x16x32_bf16 v[24:27], a[36:39], v[168:171], v[24:27]\n"
"ds_read_b64_tr_b16 v[152:153], v244 offset:20480\n"
"s_lshl_b32 s94, s83, 2\n"
"v_mfma_f32_16x16x32_bf16 v[24:27], a[52:55], v[184:187], v[24:27]\n"
"s_add_u32 s92, s49, 3670016\n"
"v_mfma_f32_16x16x32_bf16 v[40:43], a[40:43], v[168:171], v[40:43]\n"
"ds_read_b64_tr_b16 v[154:155], v244 offset:20992\n"
"s_add_u32 s95, s95, s94\n"
"v_mfma_f32_16x16x32_bf16 v[40:43], a[56:59], v[184:187], v[40:43]\n"
"v_permlane16_swap_b32 v196, v198\n"
"ds_read_b64_tr_b16 v[156:157], v245 offset:20480\n"
"v_mfma_f32_16x16x32_bf16 v[56:59], a[44:47], v[168:171], v[56:59]\n"
"s_and_b32 s92, s92, 0x380000\n"
"v_mfma_f32_16x16x32_bf16 v[56:59], a[60:63], v[184:187], v[56:59]\n"
"ds_read_b64_tr_b16 v[158:159], v245 offset:20992\n"
"s_add_u32 s95, s95, 0x8000\n"
"v_mfma_f32_16x16x32_bf16 v[12:15], a[32:35], v[172:175], v[12:15]\n"
"v_permlane16_swap_b32 v197, v199\n"
"v_mfma_f32_16x16x32_bf16 v[12:15], a[48:51], v[188:191], v[12:15]\n"
"buffer_load_dwordx4 v238, s[24:27], s92 offen offset:128 lds\n"
"v_mfma_f32_16x16x32_bf16 v[28:31], a[36:39], v[172:175], v[28:31]\n"
"s_waitcnt vmcnt(20)\n"
"buffer_store_dwordx4 v[196:199], v246, s[28:31], s87 offen offset:1024 sc1\n"
"v_mfma_f32_16x16x32_bf16 v[28:31], a[52:55], v[188:191], v[28:31]\n"
"v_mov_b32 v235, s82\n"
"v_mfma_f32_16x16x32_bf16 v[44:47], a[40:43], v[172:175], v[44:47]\n"
"buffer_load_dwordx4 v239, s[24:27], s92 offen offset:1152 lds\n"
"v_mfma_f32_16x16x32_bf16 v[44:47], a[56:59], v[188:191], v[44:47]\n"
"s_mov_b64 exec, 1\n"
"buffer_store_dword v235, off, s[88:91], s95 sc1\n"
"s_mov_b64 exec, -1\n"
"v_mfma_f32_16x16x32_bf16 v[60:63], a[44:47], v[172:175], v[60:63]\n"
"v_mfma_f32_16x16x32_bf16 v[60:63], a[60:63], v[188:191], v[60:63]\n"
"s_waitcnt lgkmcnt(0)\n"
"v_mfma_f32_16x16x32_bf16 v[0:3], a[64:67], v[128:131], v[0:3]\n"
"ds_read_b64_tr_b16 v[160:161], v242 offset:24576\n"
"s_add_u32 m0, s46, 32768\n"
"v_mfma_f32_16x16x32_bf16 v[0:3], a[80:83], v[144:147], v[0:3]\n"
"ds_read_b64_tr_b16 v[162:163], v242 offset:25088\n"
"v_mfma_f32_16x16x32_bf16 v[16:19], a[68:71], v[128:131], v[16:19]\n"
"v_mfma_f32_16x16x32_bf16 v[16:19], a[84:87], v[144:147], v[16:19]\n"
"ds_read_b64_tr_b16 v[164:165], v243 offset:24576\n"
"v_mfma_f32_16x16x32_bf16 v[32:35], a[72:75], v[128:131], v[32:35]\n"
"ds_read_b64_tr_b16 v[166:167], v243 offset:25088\n"
"s_mov_b32 s92, 0\n"
"v_mfma_f32_16x16x32_bf16 v[32:35], a[88:91], v[144:147], v[32:35]\n"
"v_mfma_f32_16x16x32_bf16 v[48:51], a[76:79], v[128:131], v[48:51]\n"
"ds_read_b64_tr_b16 v[168:169], v244 offset:24576\n"
"v_cvt_pk_bf16_f32 v192, v72, v73\n"
"v_mfma_f32_16x16x32_bf16 v[48:51], a[92:95], v[144:147], v[48:51]\n"
"ds_read_b64_tr_b16 v[170:171], v244 offset:25088\n"
"v_mfma_f32_16x16x32_bf16 v[4:7], a[64:67], v[132:135], v[4:7]\n"
"buffer_load_dwordx4 v238, s[20:23], s92 offen offset:0 lds\n"
"v_mfma_f32_16x16x32_bf16 v[4:7], a[80:83], v[148:151], v[4:7]\n"
"v_cvt_pk_bf16_f32 v194, v88, v89\n"
"ds_read_b64_tr_b16 v[172:173], v245 offset:24576\n"
"v_mfma_f32_16x16x32_bf16 v[20:23], a[68:71], v[132:135], v[20:23]\n"
"ds_read_b64_tr_b16 v[174:175], v245 offset:25088\n"
"v_mfma_f32_16x16x32_bf16 v[20:23], a[84:87], v[148:151], v[20:23]\n"
"v_cvt_pk_bf16_f32 v193, v74, v75\n"
"v_mfma_f32_16x16x32_bf16 v[36:39], a[72:75], v[132:135], v[36:39]\n"
"ds_read_b64_tr_b16 v[176:177], v242 offset:28672\n"
"buffer_load_dwordx4 v239, s[20:23], s92 offen offset:1024 lds\n"
"v_mfma_f32_16x16x32_bf16 v[36:39], a[88:91], v[148:151], v[36:39]\n"
"v_mfma_f32_16x16x32_bf16 v[52:55], a[76:79], v[132:135], v[52:55]\n"
"ds_read_b64_tr_b16 v[178:179], v242 offset:29184\n"
"v_cvt_pk_bf16_f32 v195, v90, v91\n"
"v_mfma_f32_16x16x32_bf16 v[52:55], a[92:95], v[148:151], v[52:55]\n"
"ds_read_b64_tr_b16 v[180:181], v243 offset:28672\n"
"v_mfma_f32_16x16x32_bf16 v[8:11], a[64:67], v[136:139], v[8:11]\n"
"s_add_u32 m0, s46, 40960\n"
"v_mfma_f32_16x16x32_bf16 v[8:11], a[80:83], v[152:155], v[8:11]\n"
"ds_read_b64_tr_b16 v[182:183], v243 offset:29184\n"
"s_add_u32 s87, s48, 4096\n"
"v_mfma_f32_16x16x32_bf16 v[24:27], a[68:71], v[136:139], v[24:27]\n"
"ds_read_b64_tr_b16 v[184:185], v244 offset:28672\n"
"v_mfma_f32_16x16x32_bf16 v[24:27], a[84:87], v[152:155], v[24:27]\n"
"v_mfma_f32_16x16x32_bf16 v[40:43], a[72:75], v[136:139], v[40:43]\n"
"ds_read_b64_tr_b16 v[186:187], v244 offset:29184\n"
"s_mov_b32 s92, 524288\n"
"v_mfma_f32_16x16x32_bf16 v[40:43], a[88:91], v[152:155], v[40:43]\n"
"v_permlane16_swap_b32 v192, v194\n"
"ds_read_b64_tr_b16 v[188:189], v245 offset:28672\n"
"v_mfma_f32_16x16x32_bf16 v[56:59], a[76:79], v[136:139], v[56:59]\n"
"v_mfma_f32_16x16x32_bf16 v[56:59], a[92:95], v[152:155], v[56:59]\n"
"ds_read_b64_tr_b16 v[190:191], v245 offset:29184\n"
"v_permlane16_swap_b32 v193, v195\n"
"v_mfma_f32_16x16x32_bf16 v[12:15], a[64:67], v[140:143], v[12:15]\n"
"buffer_load_dwordx4 v238, s[20:23], s92 offen offset:0 lds\n"
"v_mfma_f32_16x16x32_bf16 v[12:15], a[80:83], v[156:159], v[12:15]\n"
"v_mfma_f32_16x16x32_bf16 v[28:31], a[68:71], v[140:143], v[28:31]\n"
"buffer_store_dwordx4 v[192:195], v246, s[28:31], s87 offen offset:2048 sc1\n"
"v_mfma_f32_16x16x32_bf16 v[28:31], a[84:87], v[156:159], v[28:31]\n"
"v_mfma_f32_16x16x32_bf16 v[44:47], a[72:75], v[140:143], v[44:47]\n"
"buffer_load_dwordx4 v239, s[20:23], s92 offen offset:1024 lds\n"
"v_mfma_f32_16x16x32_bf16 v[44:47], a[88:91], v[156:159], v[44:47]\n"
"v_mfma_f32_16x16x32_bf16 v[60:63], a[76:79], v[140:143], v[60:63]\n"
"v_mfma_f32_16x16x32_bf16 v[60:63], a[92:95], v[156:159], v[60:63]\n"
"s_waitcnt vmcnt(26) lgkmcnt(0)\n"
"s_barrier\n"
"v_mfma_f32_16x16x32_bf16 v[0:3], a[96:99], v[160:163], v[0:3]\n"
"v_add_u32 v242, 0xffff0000, v242\n"
"s_add_u32 m0, s46, 49152\n"
"v_mfma_f32_16x16x32_bf16 v[0:3], a[112:115], v[176:179], v[0:3]\n"
"v_add_u32 v243, 0xffff0000, v243\n"
"v_mfma_f32_16x16x32_bf16 v[16:19], a[100:103], v[160:163], v[16:19]\n"
"v_add_u32 v244, 0xffff0000, v244\n"
"v_mfma_f32_16x16x32_bf16 v[16:19], a[116:119], v[176:179], v[16:19]\n"
"v_add_u32 v245, 0xffff0000, v245\n"
"v_mfma_f32_16x16x32_bf16 v[32:35], a[104:107], v[160:163], v[32:35]\n"
"s_mov_b32 s92, 1048576\n"
"v_mfma_f32_16x16x32_bf16 v[32:35], a[120:123], v[176:179], v[32:35]\n"
"ds_read_b64_tr_b16 v[128:129], v242 offset:0\n"
"v_mfma_f32_16x16x32_bf16 v[48:51], a[108:111], v[160:163], v[48:51]\n"
"ds_read_b64_tr_b16 v[130:131], v242 offset:512\n"
"v_cvt_pk_bf16_f32 v196, v76, v77\n"
"v_mfma_f32_16x16x32_bf16 v[48:51], a[124:127], v[176:179], v[48:51]\n"
"ds_read_b64_tr_b16 v[132:133], v243 offset:0\n"
"v_mfma_f32_16x16x32_bf16 v[4:7], a[96:99], v[164:167], v[4:7]\n"
"ds_read_b64_tr_b16 v[134:135], v243 offset:512\n"
"buffer_load_dwordx4 v238, s[20:23], s92 offen offset:0 lds\n"
"v_mfma_f32_16x16x32_bf16 v[4:7], a[112:115], v[180:183], v[4:7]\n"
"v_cvt_pk_bf16_f32 v198, v92, v93\n"
"v_mfma_f32_16x16x32_bf16 v[20:23], a[100:103], v[164:167], v[20:23]\n"
"ds_read_b64_tr_b16 v[136:137], v244 offset:0\n"
"v_mfma_f32_16x16x32_bf16 v[20:23], a[116:119], v[180:183], v[20:23]\n"
"ds_read_b64_tr_b16 v[138:139], v244 offset:512\n"
"v_cvt_pk_bf16_f32 v197, v78, v79\n"
"v_mfma_f32_16x16x32_bf16 v[36:39], a[104:107], v[164:167], v[36:39]\n"
"ds_read_b64_tr_b16 v[140:141], v245 offset:0\n"
"buffer_load_dwordx4 v239, s[20:23], s92 offen offset:1024 lds\n"
"v_mfma_f32_16x16x32_bf16 v[36:39], a[120:123], v[180:183], v[36:39]\n"
"ds_read_b64_tr_b16 v[142:143], v245 offset:512\n"
"v_mfma_f32_16x16x32_bf16 v[52:55], a[108:111], v[164:167], v[52:55]\n"
"v_cvt_pk_bf16_f32 v199, v94, v95\n"
"v_mfma_f32_16x16x32_bf16 v[52:55], a[124:127], v[180:183], v[52:55]\n"
"ds_read_b64_tr_b16 v[144:145], v242 offset:4096\n"
"v_mfma_f32_16x16x32_bf16 v[8:11], a[96:99], v[168:171], v[8:11]\n"
"ds_read_b64_tr_b16 v[146:147], v242 offset:4608\n"
"s_add_u32 m0, s46, 57344\n"
"v_mfma_f32_16x16x32_bf16 v[8:11], a[112:115], v[184:187], v[8:11]\n"
"ds_read_b64_tr_b16 v[148:149], v243 offset:4096\n"
"s_add_u32 s87, s48, 4096\n"
"v_mfma_f32_16x16x32_bf16 v[24:27], a[100:103], v[168:171], v[24:27]\n"
"ds_read_b64_tr_b16 v[150:151], v243 offset:4608\n"
"v_mfma_f32_16x16x32_bf16 v[24:27], a[116:119], v[184:187], v[24:27]\n"
"v_mfma_f32_16x16x32_bf16 v[40:43], a[104:107], v[168:171], v[40:43]\n"
"ds_read_b64_tr_b16 v[152:153], v244 offset:4096\n"
"s_mov_b32 s92, 1572864\n"
"v_mfma_f32_16x16x32_bf16 v[40:43], a[120:123], v[184:187], v[40:43]\n"
"v_permlane16_swap_b32 v196, v198\n"
"ds_read_b64_tr_b16 v[154:155], v244 offset:4608\n"
"v_mfma_f32_16x16x32_bf16 v[56:59], a[108:111], v[168:171], v[56:59]\n"
"ds_read_b64_tr_b16 v[156:157], v245 offset:4096\n"
"v_mfma_f32_16x16x32_bf16 v[56:59], a[124:127], v[184:187], v[56:59]\n"
"ds_read_b64_tr_b16 v[158:159], v245 offset:4608\n"
"v_permlane16_swap_b32 v197, v199\n"
"v_mfma_f32_16x16x32_bf16 v[12:15], a[96:99], v[172:175], v[12:15]\n"
"buffer_load_dwordx4 v238, s[20:23], s92 offen offset:0 lds\n"
"v_mfma_f32_16x16x32_bf16 v[12:15], a[112:115], v[188:191], v[12:15]\n"
"v_mfma_f32_16x16x32_bf16 v[28:31], a[100:103], v[172:175], v[28:31]\n"
"buffer_store_dwordx4 v[196:199], v246, s[28:31], s87 offen offset:3072 sc1\n"
"v_mfma_f32_16x16x32_bf16 v[28:31], a[116:119], v[188:191], v[28:31]\n"
"v_mfma_f32_16x16x32_bf16 v[44:47], a[104:107], v[172:175], v[44:47]\n"
"buffer_load_dwordx4 v239, s[20:23], s92 offen offset:1024 lds\n"
"v_mfma_f32_16x16x32_bf16 v[44:47], a[120:123], v[188:191], v[44:47]\n"
"v_mfma_f32_16x16x32_bf16 v[60:63], a[108:111], v[172:175], v[60:63]\n"
"v_mfma_f32_16x16x32_bf16 v[60:63], a[124:127], v[188:191], v[60:63]\n"
"s_waitcnt lgkmcnt(0)\n"
"v_mfma_f32_16x16x32_bf16 v[0:3], a[128:131], v[128:131], v[0:3]\n"
"ds_read_b64_tr_b16 v[160:161], v242 offset:8192\n"
"s_add_u32 m0, s46, 65536\n"
"v_mfma_f32_16x16x32_bf16 v[0:3], a[144:147], v[144:147], v[0:3]\n"
"ds_read_b64_tr_b16 v[162:163], v242 offset:8704\n"
"v_mfma_f32_16x16x32_bf16 v[16:19], a[132:135], v[128:131], v[16:19]\n"
"v_mfma_f32_16x16x32_bf16 v[16:19], a[148:151], v[144:147], v[16:19]\n"
"ds_read_b64_tr_b16 v[164:165], v243 offset:8192\n"
"s_sub_u32 s95, 15, s86\n"
"v_mfma_f32_16x16x32_bf16 v[32:35], a[136:139], v[128:131], v[32:35]\n"
"s_lshl_b32 s95, s95, 11\n"
"ds_read_b64_tr_b16 v[166:167], v243 offset:8704\n"
"v_mfma_f32_16x16x32_bf16 v[32:35], a[152:155], v[144:147], v[32:35]\n"
"s_mov_b32 s92, 2097152\n"
"s_lshl_b32 s94, s86, 11\n"
"v_mfma_f32_16x16x32_bf16 v[48:51], a[140:143], v[128:131], v[48:51]\n"
"v_mov_b32 v233, s95\n"
"v_mov_b32 v235, s94\n"
"v_mfma_f32_16x16x32_bf16 v[48:51], a[156:159], v[144:147], v[48:51]\n"
"ds_read_b64_tr_b16 v[168:169], v244 offset:8192\n"
"v_and_b32 v232, 16, v255\n"
"v_mfma_f32_16x16x32_bf16 v[4:7], a[128:131], v[132:135], v[4:7]\n"
"v_cmp_eq_u32 vcc, 0, v232\n"
"v_cvt_pk_bf16_f32 v192, v96, v97\n"
"v_mfma_f32_16x16x32_bf16 v[4:7], a[144:147], v[148:151], v[4:7]\n"
"ds_read_b64_tr_b16 v[170:171], v244 offset:8704\n"
"v_bfe_u32 v232, v255, 2, 2\n"
"v_mfma_f32_16x16x32_bf16 v[20:23], a[132:135], v[132:135], v[20:23]\n"
"buffer_load_dwordx4 v238, s[20:23], s92 offen offset:0 lds\n"
"v_mfma_f32_16x16x32_bf16 v[20:23], a[148:151], v[148:151], v[20:23]\n"
"s_lshl_b32 s94, s83, 9\n"
"s_add_u32 s97, s94, 0x8000\n"
"v_mfma_f32_16x16x32_bf16 v[36:39], a[136:139], v[132:135], v[36:39]\n"
"v_cvt_pk_bf16_f32 v194, v112, v113\n"
"ds_read_b64_tr_b16 v[172:173], v245 offset:8192\n"
"v_mfma_f32_16x16x32_bf16 v[36:39], a[152:155], v[148:151], v[36:39]\n"
"v_cndmask_b32 v233, v235, v233, vcc\n"
"ds_read_b64_tr_b16 v[174:175], v245 offset:8704\n"
"v_mfma_f32_16x16x32_bf16 v[52:55], a[140:143], v[132:135], v[52:55]\n"
"v_lshl_add_u32 v232, v232, 7, v233\n"
"v_and_b32 v233, 3, v255\n"
"v_mfma_f32_16x16x32_bf16 v[52:55], a[156:159], v[148:151], v[52:55]\n"
"v_lshl_add_u32 v232, v233, 2, v232\n"
"v_cvt_pk_bf16_f32 v193, v98, v99\n"
"v_mfma_f32_16x16x32_bf16 v[8:11], a[128:131], v[136:139], v[8:11]\n"
"ds_read_b64_tr_b16 v[176:177], v242 offset:12288\n"
"buffer_load_dwordx4 v239, s[20:23], s92 offen offset:1024 lds\n"
"v_mfma_f32_16x16x32_bf16 v[8:11], a[144:147], v[152:155], v[8:11]\n"
"ds_read_b64_tr_b16 v[178:179], v242 offset:12800\n"
"v_mfma_f32_16x16x32_bf16 v[24:27], a[132:135], v[136:139], v[24:27]\n"
"v_cvt_pk_bf16_f32 v195, v114, v115\n"
"ds_read_b64_tr_b16 v[180:181], v243 offset:12288\n"
"v_mfma_f32_16x16x32_bf16 v[24:27], a[148:151], v[152:155], v[24:27]\n"
"s_add_u32 m0, s46, 73728\n"
"ds_read_b64_tr_b16 v[182:183], v243 offset:12800\n"
"v_mfma_f32_16x16x32_bf16 v[40:43], a[136:139], v[136:139], v[40:43]\n"
"s_add_u32 s87, s48, 12288\n"
"ds_read_b64_tr_b16 v[184:185], v244 offset:12288\n"
"v_mfma_f32_16x16x32_bf16 v[40:43], a[152:155], v[152:155], v[40:43]\n"
"ds_read_b64_tr_b16 v[186:187], v244 offset:12800\n"
"s_mov_b32 s92, 2621440\n"
"v_mfma_f32_16x16x32_bf16 v[56:59], a[140:143], v[136:139], v[56:59]\n"
"v_permlane16_swap_b32 v192, v194\n"
"ds_read_b64_tr_b16 v[188:189], v245 offset:12288\n"
"v_mfma_f32_16x16x32_bf16 v[56:59], a[156:159], v[152:155], v[56:59]\n"
"ds_read_b64_tr_b16 v[190:191], v245 offset:12800\n"
"v_permlane16_swap_b32 v193, v195\n"
"v_mfma_f32_16x16x32_bf16 v[12:15], a[128:131], v[140:143], v[12:15]\n"
"buffer_load_dwordx4 v238, s[20:23], s92 offen offset:0 lds\n"
"v_mfma_f32_16x16x32_bf16 v[12:15], a[144:147], v[156:159], v[12:15]\n"
"v_mfma_f32_16x16x32_bf16 v[28:31], a[132:135], v[140:143], v[28:31]\n"
"buffer_store_dwordx4 v[192:195], v246, s[28:31], s87 offen offset:0 sc1\n"
"v_mfma_f32_16x16x32_bf16 v[28:31], a[148:151], v[156:159], v[28:31]\n"
"v_mfma_f32_16x16x32_bf16 v[44:47], a[136:139], v[140:143], v[44:47]\n"
"buffer_load_dwordx4 v239, s[20:23], s92 offen offset:1024 lds\n"
"v_mfma_f32_16x16x32_bf16 v[44:47], a[152:155], v[156:159], v[44:47]\n"
"v_mfma_f32_16x16x32_bf16 v[60:63], a[140:143], v[140:143], v[60:63]\n"
"v_mfma_f32_16x16x32_bf16 v[60:63], a[156:159], v[156:159], v[60:63]\n"
"s_waitcnt lgkmcnt(0)\n"
"v_mfma_f32_16x16x32_bf16 v[0:3], a[160:163], v[160:163], v[0:3]\n"
"ds_read_b64_tr_b16 v[128:129], v242 offset:16384\n"
"s_add_u32 m0, s46, 81920\n"
"v_mfma_f32_16x16x32_bf16 v[0:3], a[176:179], v[176:179], v[0:3]\n"
"ds_read_b64_tr_b16 v[130:131], v242 offset:16896\n"
"v_mfma_f32_16x16x32_bf16 v[16:19], a[164:167], v[160:163], v[16:19]\n"
"v_mfma_f32_16x16x32_bf16 v[16:19], a[180:183], v[176:179], v[16:19]\n"
"ds_read_b64_tr_b16 v[132:133], v243 offset:16384\n"
"v_mfma_f32_16x16x32_bf16 v[32:35], a[168:171], v[160:163], v[32:35]\n"
"ds_read_b64_tr_b16 v[134:135], v243 offset:16896\n"
"s_mov_b32 s92, 3145728\n"
"v_mfma_f32_16x16x32_bf16 v[32:35], a[184:187], v[176:179], v[32:35]\n"
"buffer_load_dword v233, v232, s[88:91], s97 offen sc1\n"
"v_mfma_f32_16x16x32_bf16 v[48:51], a[172:175], v[160:163], v[48:51]\n"
"ds_read_b64_tr_b16 v[136:137], v244 offset:16384\n"
"v_cvt_pk_bf16_f32 v196, v100, v101\n"
"v_mfma_f32_16x16x32_bf16 v[48:51], a[188:191], v[176:179], v[48:51]\n"
"ds_read_b64_tr_b16 v[138:139], v244 offset:16896\n"
"v_mfma_f32_16x16x32_bf16 v[4:7], a[160:163], v[164:167], v[4:7]\n"
"buffer_load_dwordx4 v238, s[20:23], s92 offen offset:0 lds\n"
"v_mfma_f32_16x16x32_bf16 v[4:7], a[176:179], v[180:183], v[4:7]\n"
"v_cvt_pk_bf16_f32 v198, v116, v117\n"
"ds_read_b64_tr_b16 v[140:141], v245 offset:16384\n"
"v_mfma_f32_16x16x32_bf16 v[20:23], a[164:167], v[164:167], v[20:23]\n"
"ds_read_b64_tr_b16 v[142:143], v245 offset:16896\n"
"v_mfma_f32_16x16x32_bf16 v[20:23], a[180:183], v[180:183], v[20:23]\n"
"v_cvt_pk_bf16_f32 v197, v102, v103\n"
"v_mfma_f32_16x16x32_bf16 v[36:39], a[168:171], v[164:167], v[36:39]\n"
"ds_read_b64_tr_b16 v[144:145], v242 offset:20480\n"
"buffer_load_dwordx4 v239, s[20:23], s92 offen offset:1024 lds\n"
"v_mfma_f32_16x16x32_bf16 v[36:39], a[184:187], v[180:183], v[36:39]\n"
"v_mfma_f32_16x16x32_bf16 v[52:55], a[172:175], v[164:167], v[52:55]\n"
"ds_read_b64_tr_b16 v[146:147], v242 offset:20992\n"
"v_cvt_pk_bf16_f32 v199, v118, v119\n"
"v_mfma_f32_16x16x32_bf16 v[52:55], a[188:191], v[180:183], v[52:55]\n"
"ds_read_b64_tr_b16 v[148:149], v243 offset:20480\n"
"v_mfma_f32_16x16x32_bf16 v[8:11], a[160:163], v[168:171], v[8:11]\n"
"s_add_u32 m0, s46, 90112\n"
"v_mfma_f32_16x16x32_bf16 v[8:11], a[176:179], v[184:187], v[8:11]\n"
"ds_read_b64_tr_b16 v[150:151], v243 offset:20992\n"
"s_add_u32 s87, s48, 12288\n"
"v_mfma_f32_16x16x32_bf16 v[24:27], a[164:167], v[168:171], v[24:27]\n"
"ds_read_b64_tr_b16 v[152:153], v244 offset:20480\n"
"v_mfma_f32_16x16x32_bf16 v[24:27], a[180:183], v[184:187], v[24:27]\n"
"v_mfma_f32_16x16x32_bf16 v[40:43], a[168:171], v[168:171], v[40:43]\n"
"ds_read_b64_tr_b16 v[154:155], v244 offset:20992\n"
"s_mov_b32 s92, 3670016\n"
"v_mfma_f32_16x16x32_bf16 v[40:43], a[184:187], v[184:187], v[40:43]\n"
"v_permlane16_swap_b32 v196, v198\n"
"ds_read_b64_tr_b16 v[156:157], v245 offset:20480\n"
"v_mfma_f32_16x16x32_bf16 v[56:59], a[172:175], v[168:171], v[56:59]\n"
"v_mfma_f32_16x16x32_bf16 v[56:59], a[188:191], v[184:187], v[56:59]\n"
"ds_read_b64_tr_b16 v[158:159], v245 offset:20992\n"
"v_permlane16_swap_b32 v197, v199\n"
"v_mfma_f32_16x16x32_bf16 v[12:15], a[160:163], v[172:175], v[12:15]\n"
"buffer_load_dwordx4 v238, s[20:23], s92 offen offset:0 lds\n"
"v_mfma_f32_16x16x32_bf16 v[12:15], a[176:179], v[188:191], v[12:15]\n"
"v_mfma_f32_16x16x32_bf16 v[28:31], a[164:167], v[172:175], v[28:31]\n"
"buffer_store_dwordx4 v[196:199], v246, s[28:31], s87 offen offset:1024 sc1\n"
"v_mfma_f32_16x16x32_bf16 v[28:31], a[180:183], v[188:191], v[28:31]\n"
"v_mfma_f32_16x16x32_bf16 v[44:47], a[168:171], v[172:175], v[44:47]\n"
"buffer_load_dwordx4 v239, s[20:23], s92 offen offset:1024 lds\n"
"v_mfma_f32_16x16x32_bf16 v[44:47], a[184:187], v[188:191], v[44:47]\n"
"v_mfma_f32_16x16x32_bf16 v[60:63], a[172:175], v[172:175], v[60:63]\n"
"v_mfma_f32_16x16x32_bf16 v[60:63], a[188:191], v[188:191], v[60:63]\n"
"s_waitcnt lgkmcnt(0)\n"
"v_mfma_f32_16x16x32_bf16 v[0:3], a[192:195], v[128:131], v[0:3]\n"
"ds_read_b64_tr_b16 v[160:161], v242 offset:24576\n"
"v_mfma_f32_16x16x32_bf16 v[0:3], a[208:211], v[144:147], v[0:3]\n"
"ds_read_b64_tr_b16 v[162:163], v242 offset:25088\n"
"v_mfma_f32_16x16x32_bf16 v[16:19], a[196:199], v[128:131], v[16:19]\n"
"v_mfma_f32_16x16x32_bf16 v[16:19], a[212:215], v[144:147], v[16:19]\n"
"ds_read_b64_tr_b16 v[164:165], v243 offset:24576\n"
"v_mfma_f32_16x16x32_bf16 v[32:35], a[200:203], v[128:131], v[32:35]\n"
"ds_read_b64_tr_b16 v[166:167], v243 offset:25088\n"
"v_mfma_f32_16x16x32_bf16 v[32:35], a[216:219], v[144:147], v[32:35]\n"
"v_mfma_f32_16x16x32_bf16 v[48:51], a[204:207], v[128:131], v[48:51]\n"
"ds_read_b64_tr_b16 v[168:169], v244 offset:24576\n"
"v_cvt_pk_bf16_f32 v192, v104, v105\n"
"v_mfma_f32_16x16x32_bf16 v[48:51], a[220:223], v[144:147], v[48:51]\n"
"ds_read_b64_tr_b16 v[170:171], v244 offset:25088\n"
"v_mfma_f32_16x16x32_bf16 v[4:7], a[192:195], v[132:135], v[4:7]\n"
"v_cvt_pk_bf16_f32 v194, v120, v121\n"
"v_mfma_f32_16x16x32_bf16 v[4:7], a[208:211], v[148:151], v[4:7]\n"
"ds_read_b64_tr_b16 v[172:173], v245 offset:24576\n"
"v_mfma_f32_16x16x32_bf16 v[20:23], a[196:199], v[132:135], v[20:23]\n"
"ds_read_b64_tr_b16 v[174:175], v245 offset:25088\n"
"v_mfma_f32_16x16x32_bf16 v[20:23], a[212:215], v[148:151], v[20:23]\n"
"v_cvt_pk_bf16_f32 v193, v106, v107\n"
"v_mfma_f32_16x16x32_bf16 v[36:39], a[200:203], v[132:135], v[36:39]\n"
"ds_read_b64_tr_b16 v[176:177], v242 offset:28672\n"
"v_mfma_f32_16x16x32_bf16 v[36:39], a[216:219], v[148:151], v[36:39]\n"
"v_mfma_f32_16x16x32_bf16 v[52:55], a[204:207], v[132:135], v[52:55]\n"
"ds_read_b64_tr_b16 v[178:179], v242 offset:29184\n"
"v_cvt_pk_bf16_f32 v195, v122, v123\n"
"v_mfma_f32_16x16x32_bf16 v[52:55], a[220:223], v[148:151], v[52:55]\n"
"ds_read_b64_tr_b16 v[180:181], v243 offset:28672\n"
"v_mfma_f32_16x16x32_bf16 v[8:11], a[192:195], v[136:139], v[8:11]\n"
"v_mfma_f32_16x16x32_bf16 v[8:11], a[208:211], v[152:155], v[8:11]\n"
"ds_read_b64_tr_b16 v[182:183], v243 offset:29184\n"
"s_add_u32 s87, s48, 12288\n"
"v_mfma_f32_16x16x32_bf16 v[24:27], a[196:199], v[136:139], v[24:27]\n"
"ds_read_b64_tr_b16 v[184:185], v244 offset:28672\n"
"v_mfma_f32_16x16x32_bf16 v[24:27], a[212:215], v[152:155], v[24:27]\n"
"v_mfma_f32_16x16x32_bf16 v[40:43], a[200:203], v[136:139], v[40:43]\n"
"ds_read_b64_tr_b16 v[186:187], v244 offset:29184\n"
"v_permlane16_swap_b32 v192, v194\n"
"v_mfma_f32_16x16x32_bf16 v[40:43], a[216:219], v[152:155], v[40:43]\n"
"ds_read_b64_tr_b16 v[188:189], v245 offset:28672\n"
"v_mfma_f32_16x16x32_bf16 v[56:59], a[204:207], v[136:139], v[56:59]\n"
"v_mfma_f32_16x16x32_bf16 v[56:59], a[220:223], v[152:155], v[56:59]\n"
"ds_read_b64_tr_b16 v[190:191], v245 offset:29184\n"
"v_permlane16_swap_b32 v193, v195\n"
"v_mfma_f32_16x16x32_bf16 v[12:15], a[192:195], v[140:143], v[12:15]\n"
"v_mfma_f32_16x16x32_bf16 v[12:15], a[208:211], v[156:159], v[12:15]\n"
"v_mfma_f32_16x16x32_bf16 v[28:31], a[196:199], v[140:143], v[28:31]\n"
"buffer_store_dwordx4 v[192:195], v246, s[28:31], s87 offen offset:2048 sc1\n"
"v_mfma_f32_16x16x32_bf16 v[28:31], a[212:215], v[156:159], v[28:31]\n"
"v_mfma_f32_16x16x32_bf16 v[44:47], a[200:203], v[140:143], v[44:47]\n"
"v_mfma_f32_16x16x32_bf16 v[44:47], a[216:219], v[156:159], v[44:47]\n"
"v_mfma_f32_16x16x32_bf16 v[60:63], a[204:207], v[140:143], v[60:63]\n"
"v_mfma_f32_16x16x32_bf16 v[60:63], a[220:223], v[156:159], v[60:63]\n"
"s_waitcnt vmcnt(41) lgkmcnt(0)\n"
"s_barrier\n"
"v_mfma_f32_16x16x32_bf16 v[0:3], a[224:227], v[160:163], v[0:3]\n"
"v_add_u32 v242, 0x20000, v242\n"
"s_add_u32 m0, s46, 0\n"
"v_mfma_f32_16x16x32_bf16 v[0:3], a[240:243], v[176:179], v[0:3]\n"
"v_add_u32 v243, 0x20000, v243\n"
"v_mfma_f32_16x16x32_bf16 v[16:19], a[228:231], v[160:163], v[16:19]\n"
"v_add_u32 v244, 0x20000, v244\n"
"v_mfma_f32_16x16x32_bf16 v[16:19], a[244:247], v[176:179], v[16:19]\n"
"v_add_u32 v245, 0x20000, v245\n"
"s_sub_u32 s95, 15, s86\n"
"v_mfma_f32_16x16x32_bf16 v[32:35], a[232:235], v[160:163], v[32:35]\n"
"s_mov_b32 s92, 128\n"
"s_lshl_b32 s95, s95, 17\n"
"v_mfma_f32_16x16x32_bf16 v[32:35], a[248:251], v[176:179], v[32:35]\n"
"ds_read_b64_tr_b16 v[128:129], v242 offset:0\n"
"s_lshl_b32 s94, s83, 15\n"
"v_mfma_f32_16x16x32_bf16 v[48:51], a[236:239], v[160:163], v[48:51]\n"
"ds_read_b64_tr_b16 v[130:131], v242 offset:512\n"
"s_add_u32 s50, s95, s94\n"
"v_mfma_f32_16x16x32_bf16 v[48:51], a[252:255], v[176:179], v[48:51]\n"
"v_cvt_pk_bf16_f32 v196, v108, v109\n"
"ds_read_b64_tr_b16 v[132:133], v243 offset:0\n"
"v_mfma_f32_16x16x32_bf16 v[4:7], a[224:227], v[164:167], v[4:7]\n"
"s_add_u32 s51, s50, 0x4000\n"
"ds_read_b64_tr_b16 v[134:135], v243 offset:512\n"
"v_mfma_f32_16x16x32_bf16 v[4:7], a[240:243], v[180:183], v[4:7]\n"
"buffer_load_dwordx4 v238, s[20:23], s92 offen offset:0 lds\n"
"v_mfma_f32_16x16x32_bf16 v[20:23], a[228:231], v[164:167], v[20:23]\n"
"s_lshl_b32 s95, s86, 17\n"
"v_cvt_pk_bf16_f32 v198, v124, v125\n"
"v_mfma_f32_16x16x32_bf16 v[20:23], a[244:247], v[180:183], v[20:23]\n"
"s_add_u32 s52, s95, s94\n"
"ds_read_b64_tr_b16 v[136:137], v244 offset:0\n"
"v_mfma_f32_16x16x32_bf16 v[36:39], a[232:235], v[164:167], v[36:39]\n"
"s_add_u32 s53, s52, 0x4000\n"
"ds_read_b64_tr_b16 v[138:139], v244 offset:512\n"
"v_mfma_f32_16x16x32_bf16 v[36:39], a[248:251], v[180:183], v[36:39]\n"
"v_cvt_pk_bf16_f32 v197, v110, v111\n"
"ds_read_b64_tr_b16 v[140:141], v245 offset:0\n"
"v_mfma_f32_16x16x32_bf16 v[52:55], a[236:239], v[164:167], v[52:55]\n"
"buffer_load_dwordx4 v239, s[20:23], s92 offen offset:1024 lds\n"
"v_mfma_f32_16x16x32_bf16 v[52:55], a[252:255], v[180:183], v[52:55]\n"
"ds_read_b64_tr_b16 v[142:143], v245 offset:512\n"
"v_cvt_pk_bf16_f32 v199, v126, v127\n"
"v_mfma_f32_16x16x32_bf16 v[8:11], a[224:227], v[168:171], v[8:11]\n"
"ds_read_b64_tr_b16 v[144:145], v242 offset:4096\n"
"ds_read_b64_tr_b16 v[146:147], v242 offset:4608\n"
"v_mfma_f32_16x16x32_bf16 v[8:11], a[240:243], v[184:187], v[8:11]\n"
"s_add_u32 m0, s46, 8192\n"
"ds_read_b64_tr_b16 v[148:149], v243 offset:4096\n"
"v_mfma_f32_16x16x32_bf16 v[24:27], a[228:231], v[168:171], v[24:27]\n"
"s_add_u32 s87, s48, 12288\n"
"ds_read_b64_tr_b16 v[150:151], v243 offset:4608\n"
"v_mfma_f32_16x16x32_bf16 v[24:27], a[244:247], v[184:187], v[24:27]\n"
"v_mfma_f32_16x16x32_bf16 v[40:43], a[232:235], v[168:171], v[40:43]\n"
"ds_read_b64_tr_b16 v[152:153], v244 offset:4096\n"
"s_mov_b32 s92, 524416\n"
"v_mfma_f32_16x16x32_bf16 v[40:43], a[248:251], v[184:187], v[40:43]\n"
"v_permlane16_swap_b32 v196, v198\n"
"ds_read_b64_tr_b16 v[154:155], v244 offset:4608\n"
"v_mfma_f32_16x16x32_bf16 v[56:59], a[236:239], v[168:171], v[56:59]\n"
"ds_read_b64_tr_b16 v[156:157], v245 offset:4096\n"
"v_mfma_f32_16x16x32_bf16 v[56:59], a[252:255], v[184:187], v[56:59]\n"
"ds_read_b64_tr_b16 v[158:159], v245 offset:4608\n"
"v_permlane16_swap_b32 v197, v199\n"
"v_mfma_f32_16x16x32_bf16 v[12:15], a[224:227], v[172:175], v[12:15]\n"
"buffer_load_dwordx4 v238, s[20:23], s92 offen offset:0 lds\n"
"v_mfma_f32_16x16x32_bf16 v[12:15], a[240:243], v[188:191], v[12:15]\n"
"v_mfma_f32_16x16x32_bf16 v[28:31], a[228:231], v[172:175], v[28:31]\n"
"buffer_store_dwordx4 v[196:199], v246, s[28:31], s87 offen offset:3072 sc1\n"
"v_mfma_f32_16x16x32_bf16 v[28:31], a[244:247], v[188:191], v[28:31]\n"
"v_mfma_f32_16x16x32_bf16 v[44:47], a[232:235], v[172:175], v[44:47]\n"
"buffer_load_dwordx4 v239, s[20:23], s92 offen offset:1024 lds\n"
"v_mfma_f32_16x16x32_bf16 v[44:47], a[248:251], v[188:191], v[44:47]\n"
"v_mfma_f32_16x16x32_bf16 v[60:63], a[236:239], v[172:175], v[60:63]\n"
"v_mfma_f32_16x16x32_bf16 v[60:63], a[252:255], v[188:191], v[60:63]\n"
"s_waitcnt lgkmcnt(0)\n"
"v_mfma_f32_16x16x32_bf16 v[64:67], a[0:3], v[128:131], 0\n"
"ds_read_b64_tr_b16 v[160:161], v242 offset:8192\n"
"s_add_u32 m0, s46, 16384\n"
"v_mfma_f32_16x16x32_bf16 v[64:67], a[16:19], v[144:147], v[64:67]\n"
"ds_read_b64_tr_b16 v[162:163], v242 offset:8704\n"
"v_mfma_f32_16x16x32_bf16 v[80:83], a[4:7], v[128:131], 0\n"
"v_mfma_f32_16x16x32_bf16 v[80:83], a[20:23], v[144:147], v[80:83]\n"
"ds_read_b64_tr_b16 v[164:165], v243 offset:8192\n"
"v_mfma_f32_16x16x32_bf16 v[96:99], a[8:11], v[128:131], 0\n"
"ds_read_b64_tr_b16 v[166:167], v243 offset:8704\n"
"s_mov_b32 s92, 1048704\n"
"v_mfma_f32_16x16x32_bf16 v[96:99], a[24:27], v[144:147], v[96:99]\n"
"v_mfma_f32_16x16x32_bf16 v[112:115], a[12:15], v[128:131], 0\n"
"ds_read_b64_tr_b16 v[168:169], v244 offset:8192\n"
"v_cvt_pk_bf16_f32 v192, v0, v1\n"
"v_mfma_f32_16x16x32_bf16 v[112:115], a[28:31], v[144:147], v[112:115]\n"
"ds_read_b64_tr_b16 v[170:171], v244 offset:8704\n"
"v_mfma_f32_16x16x32_bf16 v[68:71], a[0:3], v[132:135], 0\n"
"buffer_load_dwordx4 v238, s[20:23], s92 offen offset:0 lds\n"
"v_mfma_f32_16x16x32_bf16 v[68:71], a[16:19], v[148:151], v[68:71]\n"
"v_cvt_pk_bf16_f32 v194, v4, v5\n"
"ds_read_b64_tr_b16 v[172:173], v245 offset:8192\n"
"v_mfma_f32_16x16x32_bf16 v[84:87], a[4:7], v[132:135], 0\n"
"ds_read_b64_tr_b16 v[174:175], v245 offset:8704\n"
"v_mfma_f32_16x16x32_bf16 v[84:87], a[20:23], v[148:151], v[84:87]\n"
"v_cvt_pk_bf16_f32 v193, v2, v3\n"
"v_mfma_f32_16x16x32_bf16 v[100:103], a[8:11], v[132:135], 0\n"
"ds_read_b64_tr_b16 v[176:177], v242 offset:12288\n"
"buffer_load_dwordx4 v239, s[20:23], s92 offen offset:1024 lds\n"
"v_mfma_f32_16x16x32_bf16 v[100:103], a[24:27], v[148:151], v[100:103]\n"
"v_mfma_f32_16x16x32_bf16 v[116:119], a[12:15], v[132:135], 0\n"
"ds_read_b64_tr_b16 v[178:179], v242 offset:12800\n"
"v_cvt_pk_bf16_f32 v195, v6, v7\n"
"v_mfma_f32_16x16x32_bf16 v[116:119], a[28:31], v[148:151], v[116:119]\n"
"ds_read_b64_tr_b16 v[180:181], v243 offset:12288\n"
"v_mfma_f32_16x16x32_bf16 v[72:75], a[0:3], v[136:139], 0\n"
"s_add_u32 m0, s46, 24576\n"
"v_mfma_f32_16x16x32_bf16 v[72:75], a[16:19], v[152:155], v[72:75]\n"
"ds_read_b64_tr_b16 v[182:183], v243 offset:12800\n"
"s_add_u32 s87, s48, 0\n"
"v_mfma_f32_16x16x32_bf16 v[88:91], a[4:7], v[136:139], 0\n"
"ds_read_b64_tr_b16 v[184:185], v244 offset:12288\n"
"v_mfma_f32_16x16x32_bf16 v[88:91], a[20:23], v[152:155], v[88:91]\n"
"v_mfma_f32_16x16x32_bf16 v[104:107], a[8:11], v[136:139], 0\n"
"ds_read_b64_tr_b16 v[186:187], v244 offset:12800\n"
"s_mov_b32 s92, 1572992\n"
"v_mfma_f32_16x16x32_bf16 v[104:107], a[24:27], v[152:155], v[104:107]\n"
"s_waitcnt vmcnt(13)\n"
"v_permlane16_swap_b32 v192, v194\n"
"v_mfma_f32_16x16x32_bf16 v[120:123], a[12:15], v[136:139], 0\n"
"ds_read_b64_tr_b16 v[188:189], v245 offset:12288\n"
"v_mfma_f32_16x16x32_bf16 v[120:123], a[28:31], v[152:155], v[120:123]\n"
"ds_read_b64_tr_b16 v[190:191], v245 offset:12800\n"
"v_cmp_ne_u32 vcc, s82, v233\n"
"v_mfma_f32_16x16x32_bf16 v[76:79], a[0:3], v[140:143], 0\n"
"v_permlane16_swap_b32 v193, v195\n"
"buffer_load_dwordx4 v238, s[20:23], s92 offen offset:0 lds\n"
"v_mfma_f32_16x16x32_bf16 v[76:79], a[16:19], v[156:159], v[76:79]\n"
"v_mfma_f32_16x16x32_bf16 v[92:95], a[4:7], v[140:143], 0\n"
"s_cbranch_vccnz Lps11_%=\n"
"Lpb10_%=:\n"
"buffer_store_dwordx4 v[192:195], v248, s[36:39], s87 offen offset:0 sc1\n"
"v_mfma_f32_16x16x32_bf16 v[92:95], a[20:23], v[156:159], v[92:95]\n"
"v_mfma_f32_16x16x32_bf16 v[108:111], a[8:11], v[140:143], 0\n"
"buffer_load_dwordx4 v239, s[20:23], s92 offen offset:1024 lds\n"
"v_mfma_f32_16x16x32_bf16 v[108:111], a[24:27], v[156:159], v[108:111]\n"
"v_mfma_f32_16x16x32_bf16 v[124:127], a[12:15], v[140:143], 0\n"
"v_mfma_f32_16x16x32_bf16 v[124:127], a[28:31], v[156:159], v[124:127]\n"
"s_waitcnt lgkmcnt(0)\n"
"v_mfma_f32_16x16x32_bf16 v[64:67], a[32:35], v[160:163], v[64:67]\n"
"ds_read_b64_tr_b16 v[128:129], v242 offset:16384\n"
"buffer_load_dwordx4 a[0:3], v248, s[40:43], s50 offen offset:0 sc1\n"
"v_mfma_f32_16x16x32_bf16 v[64:67], a[48:51], v[176:179], v[64:67]\n"
"ds_read_b64_tr_b16 v[130:131], v242 offset:16896\n"
"v_mfma_f32_16x16x32_bf16 v[80:83], a[36:39], v[160:163], v[80:83]\n"
"v_mfma_f32_16x16x32_bf16 v[80:83], a[52:55], v[176:179], v[80:83]\n"
"ds_read_b64_tr_b16 v[132:133], v243 offset:16384\n"
"v_mfma_f32_16x16x32_bf16 v[96:99], a[40:43], v[160:163], v[96:99]\n"
"ds_read_b64_tr_b16 v[134:135], v243 offset:16896\n"
"v_mfma_f32_16x16x32_bf16 v[96:99], a[56:59], v[176:179], v[96:99]\n"
"v_mfma_f32_16x16x32_bf16 v[112:115], a[44:47], v[160:163], v[112:115]\n"
"ds_read_b64_tr_b16 v[136:137], v244 offset:16384\n"
"buffer_load_dwordx4 a[16:19], v248, s[40:43], s50 offen offset:1024 sc1\n"
"v_mfma_f32_16x16x32_bf16 v[112:115], a[60:63], v[176:179], v[112:115]\n"
"v_cvt_pk_bf16_f32 v196, v8, v9\n"
"v_mfma_f32_16x16x32_bf16 v[68:71], a[32:35], v[164:167], v[68:71]\n"
"ds_read_b64_tr_b16 v[138:139], v244 offset:16896\n"
"v_cvt_pk_bf16_f32 v198, v12, v13\n"
"v_mfma_f32_16x16x32_bf16 v[68:71], a[48:51], v[180:183], v[68:71]\n"
"ds_read_b64_tr_b16 v[140:141], v245 offset:16384\n"
"v_mfma_f32_16x16x32_bf16 v[84:87], a[36:39], v[164:167], v[84:87]\n"
"ds_read_b64_tr_b16 v[142:143], v245 offset:16896\n"
"v_mfma_f32_16x16x32_bf16 v[84:87], a[52:55], v[180:183], v[84:87]\n"
"v_cvt_pk_bf16_f32 v197, v10, v11\n"
"v_mfma_f32_16x16x32_bf16 v[100:103], a[40:43], v[164:167], v[100:103]\n"
"ds_read_b64_tr_b16 v[144:145], v242 offset:20480\n"
"buffer_load_dwordx4 a[4:7], v248, s[40:43], s51 offen offset:0 sc1\n"
"v_mfma_f32_16x16x32_bf16 v[100:103], a[56:59], v[180:183], v[100:103]\n"
"v_mfma_f32_16x16x32_bf16 v[116:119], a[44:47], v[164:167], v[116:119]\n"
"ds_read_b64_tr_b16 v[146:147], v242 offset:20992\n"
"v_cvt_pk_bf16_f32 v199, v14, v15\n"
"v_mfma_f32_16x16x32_bf16 v[116:119], a[60:63], v[180:183], v[116:119]\n"
"ds_read_b64_tr_b16 v[148:149], v243 offset:20480\n"
"v_mfma_f32_16x16x32_bf16 v[72:75], a[32:35], v[168:171], v[72:75]\n"
"v_mfma_f32_16x16x32_bf16 v[72:75], a[48:51], v[184:187], v[72:75]\n"
"ds_read_b64_tr_b16 v[150:151], v243 offset:20992\n"
"s_add_u32 s87, s48, 0\n"
"v_mfma_f32_16x16x32_bf16 v[88:91], a[36:39], v[168:171], v[88:91]\n"
"ds_read_b64_tr_b16 v[152:153], v244 offset:20480\n"
"v_mfma_f32_16x16x32_bf16 v[88:91], a[52:55], v[184:187], v[88:91]\n"
"buffer_load_dwordx4 a[20:23], v248, s[40:43], s51 offen offset:1024 sc1\n"
"v_mfma_f32_16x16x32_bf16 v[104:107], a[40:43], v[168:171], v[104:107]\n"
"ds_read_b64_tr_b16 v[154:155], v244 offset:20992\n"
"v_permlane16_swap_b32 v196, v198\n"
"v_mfma_f32_16x16x32_bf16 v[104:107], a[56:59], v[184:187], v[104:107]\n"
"ds_read_b64_tr_b16 v[156:157], v245 offset:20480\n"
"v_mfma_f32_16x16x32_bf16 v[120:123], a[44:47], v[168:171], v[120:123]\n"
"v_mfma_f32_16x16x32_bf16 v[120:123], a[60:63], v[184:187], v[120:123]\n"
"ds_read_b64_tr_b16 v[158:159], v245 offset:20992\n"
"v_permlane16_swap_b32 v197, v199\n"
"v_mfma_f32_16x16x32_bf16 v[76:79], a[32:35], v[172:175], v[76:79]\n"
"v_mfma_f32_16x16x32_bf16 v[76:79], a[48:51], v[188:191], v[76:79]\n"
"buffer_load_dwordx4 a[8:11], v248, s[40:43], s52 offen offset:0 sc1\n"
"v_mfma_f32_16x16x32_bf16 v[92:95], a[36:39], v[172:175], v[92:95]\n"
"buffer_store_dwordx4 v[196:199], v248, s[36:39], s87 offen offset:1024 sc1\n"
"v_mfma_f32_16x16x32_bf16 v[92:95], a[52:55], v[188:191], v[92:95]\n"
"v_mfma_f32_16x16x32_bf16 v[108:111], a[40:43], v[172:175], v[108:111]\n"
"v_mfma_f32_16x16x32_bf16 v[108:111], a[56:59], v[188:191], v[108:111]\n"
"v_mfma_f32_16x16x32_bf16 v[124:127], a[44:47], v[172:175], v[124:127]\n"
"v_mfma_f32_16x16x32_bf16 v[124:127], a[60:63], v[188:191], v[124:127]\n"
"s_waitcnt lgkmcnt(0)\n"
"v_mfma_f32_16x16x32_bf16 v[64:67], a[64:67], v[128:131], v[64:67]\n"
"ds_read_b64_tr_b16 v[160:161], v242 offset:24576\n"
"buffer_load_dwordx4 a[24:27], v248, s[40:43], s52 offen offset:1024 sc1\n"
"v_mfma_f32_16x16x32_bf16 v[64:67], a[80:83], v[144:147], v[64:67]\n"
"ds_read_b64_tr_b16 v[162:163], v242 offset:25088\n"
"v_mfma_f32_16x16x32_bf16 v[80:83], a[68:71], v[128:131], v[80:83]\n"
"v_mfma_f32_16x16x32_bf16 v[80:83], a[84:87], v[144:147], v[80:83]\n"
"ds_read_b64_tr_b16 v[164:165], v243 offset:24576\n"
"v_mfma_f32_16x16x32_bf16 v[96:99], a[72:75], v[128:131], v[96:99]\n"
"ds_read_b64_tr_b16 v[166:167], v243 offset:25088\n"
"buffer_load_dwordx4 a[12:15], v248, s[40:43], s53 offen offset:0 sc1\n"
"v_mfma_f32_16x16x32_bf16 v[96:99], a[88:91], v[144:147], v[96:99]\n"
"v_mfma_f32_16x16x32_bf16 v[112:115], a[76:79], v[128:131], v[112:115]\n"
"ds_read_b64_tr_b16 v[168:169], v244 offset:24576\n"
"v_cvt_pk_bf16_f32 v200, v16, v17\n"
"v_mfma_f32_16x16x32_bf16 v[112:115], a[92:95], v[144:147], v[112:115]\n"
"ds_read_b64_tr_b16 v[170:171], v244 offset:25088\n"
"v_mfma_f32_16x16x32_bf16 v[68:71], a[64:67], v[132:135], v[68:71]\n"
"buffer_load_dwordx4 a[28:31], v248, s[40:43], s53 offen offset:1024 sc1\n"
"v_mfma_f32_16x16x32_bf16 v[68:71], a[80:83], v[148:151], v[68:71]\n"
"v_cvt_pk_bf16_f32 v202, v20, v21\n"
"ds_read_b64_tr_b16 v[172:173], v245 offset:24576\n"
"v_mfma_f32_16x16x32_bf16 v[84:87], a[68:71], v[132:135], v[84:87]\n"
"ds_read_b64_tr_b16 v[174:175], v245 offset:25088\n"
"v_mfma_f32_16x16x32_bf16 v[84:87], a[84:87], v[148:151], v[84:87]\n"
"v_cvt_pk_bf16_f32 v201, v18, v19\n"
"v_mfma_f32_16x16x32_bf16 v[100:103], a[72:75], v[132:135], v[100:103]\n"
"ds_read_b64_tr_b16 v[176:177], v242 offset:28672\n"
"buffer_load_dwordx4 a[32:35], v248, s[40:43], s50 offen offset:2048 sc1\n"
"v_mfma_f32_16x16x32_bf16 v[100:103], a[88:91], v[148:151], v[100:103]\n"
"v_mfma_f32_16x16x32_bf16 v[116:119], a[76:79], v[132:135], v[116:119]\n"
"ds_read_b64_tr_b16 v[178:179], v242 offset:29184\n"
"v_cvt_pk_bf16_f32 v203, v22, v23\n"
"v_mfma_f32_16x16x32_bf16 v[116:119], a[92:95], v[148:151], v[116:119]\n"
"ds_read_b64_tr_b16 v[180:181], v243 offset:28672\n"
"v_mfma_f32_16x16x32_bf16 v[72:75], a[64:67], v[136:139], v[72:75]\n"
"buffer_load_dwordx4 a[48:51], v248, s[40:43], s50 offen offset:3072 sc1\n"
"v_mfma_f32_16x16x32_bf16 v[72:75], a[80:83], v[152:155], v[72:75]\n"
"ds_read_b64_tr_b16 v[182:183], v243 offset:29184\n"
"s_add_u32 s87, s48, 4096\n"
"v_mfma_f32_16x16x32_bf16 v[88:91], a[68:71], v[136:139], v[88:91]\n"
"ds_read_b64_tr_b16 v[184:185], v244 offset:28672\n"
"v_mfma_f32_16x16x32_bf16 v[88:91], a[84:87], v[152:155], v[88:91]\n"
"v_mfma_f32_16x16x32_bf16 v[104:107], a[72:75], v[136:139], v[104:107]\n"
"ds_read_b64_tr_b16 v[186:187], v244 offset:29184\n"
"buffer_load_dwordx4 a[36:39], v248, s[40:43], s51 offen offset:2048 sc1\n"
"v_mfma_f32_16x16x32_bf16 v[104:107], a[88:91], v[152:155], v[104:107]\n"
"v_permlane16_swap_b32 v200, v202\n"
"v_mfma_f32_16x16x32_bf16 v[120:123], a[76:79], v[136:139], v[120:123]\n"
"ds_read_b64_tr_b16 v[188:189], v245 offset:28672\n"
"v_mfma_f32_16x16x32_bf16 v[120:123], a[92:95], v[152:155], v[120:123]\n"
"ds_read_b64_tr_b16 v[190:191], v245 offset:29184\n"
"v_permlane16_swap_b32 v201, v203\n"
"v_mfma_f32_16x16x32_bf16 v[76:79], a[64:67], v[140:143], v[76:79]\n"
"buffer_load_dwordx4 a[52:55], v248, s[40:43], s51 offen offset:3072 sc1\n"
"v_mfma_f32_16x16x32_bf16 v[76:79], a[80:83], v[156:159], v[76:79]\n"
"v_mfma_f32_16x16x32_bf16 v[92:95], a[68:71], v[140:143], v[92:95]\n"
"buffer_store_dwordx4 v[200:203], v248, s[36:39], s87 offen offset:0 sc1\n"
"v_mfma_f32_16x16x32_bf16 v[92:95], a[84:87], v[156:159], v[92:95]\n"
"v_mfma_f32_16x16x32_bf16 v[108:111], a[72:75], v[140:143], v[108:111]\n"
"buffer_load_dwordx4 a[40:43], v248, s[40:43], s52 offen offset:2048 sc1\n"
"v_mfma_f32_16x16x32_bf16 v[108:111], a[88:91], v[156:159], v[108:111]\n"
"v_mfma_f32_16x16x32_bf16 v[124:127], a[76:79], v[140:143], v[124:127]\n"
"v_mfma_f32_16x16x32_bf16 v[124:127], a[92:95], v[156:159], v[124:127]\n"
"s_waitcnt vmcnt(31) lgkmcnt(0)\n"
"s_barrier\n"
"v_mfma_f32_16x16x32_bf16 v[64:67], a[96:99], v[160:163], v[64:67]\n"
"v_add_u32 v242, 0xffff0000, v242\n"
"buffer_load_dwordx4 a[56:59], v248, s[40:43], s52 offen offset:3072 sc1\n"
"v_mfma_f32_16x16x32_bf16 v[64:67], a[112:115], v[176:179], v[64:67]\n"
"v_add_u32 v243, 0xffff0000, v243\n"
"v_mfma_f32_16x16x32_bf16 v[80:83], a[100:103], v[160:163], v[80:83]\n"
"v_add_u32 v244, 0xffff0000, v244\n"
"buffer_load_dwordx4 a[44:47], v248, s[40:43], s53 offen offset:2048 sc1\n"
"v_mfma_f32_16x16x32_bf16 v[80:83], a[116:119], v[176:179], v[80:83]\n"
"v_add_u32 v245, 0xffff0000, v245\n"
"v_mfma_f32_16x16x32_bf16 v[96:99], a[104:107], v[160:163], v[96:99]\n"
"v_mfma_f32_16x16x32_bf16 v[96:99], a[120:123], v[176:179], v[96:99]\n"
"ds_read_b64_tr_b16 v[128:129], v242 offset:32768\n"
"buffer_load_dwordx4 a[60:63], v248, s[40:43], s53 offen offset:3072 sc1\n"
"v_mfma_f32_16x16x32_bf16 v[112:115], a[108:111], v[160:163], v[112:115]\n"
"ds_read_b64_tr_b16 v[130:131], v242 offset:33280\n"
"v_mfma_f32_16x16x32_bf16 v[112:115], a[124:127], v[176:179], v[112:115]\n"
"v_cvt_pk_bf16_f32 v204, v24, v25\n"
"ds_read_b64_tr_b16 v[132:133], v243 offset:32768\n"
"v_mfma_f32_16x16x32_bf16 v[68:71], a[96:99], v[164:167], v[68:71]\n"
"ds_read_b64_tr_b16 v[134:135], v243 offset:33280\n"
"s_add_u32 s50, s50, 4096\n"
"v_mfma_f32_16x16x32_bf16 v[68:71], a[112:115], v[180:183], v[68:71]\n"
"v_cvt_pk_bf16_f32 v206, v28, v29\n"
"v_mfma_f32_16x16x32_bf16 v[84:87], a[100:103], v[164:167], v[84:87]\n"
"ds_read_b64_tr_b16 v[136:137], v244 offset:32768\n"
"v_mfma_f32_16x16x32_bf16 v[84:87], a[116:119], v[180:183], v[84:87]\n"
"ds_read_b64_tr_b16 v[138:139], v244 offset:33280\n"
"buffer_load_dwordx4 a[64:67], v248, s[40:43], s50 offen offset:0 sc1\n"
"v_mfma_f32_16x16x32_bf16 v[100:103], a[104:107], v[164:167], v[100:103]\n"
"v_cvt_pk_bf16_f32 v205, v26, v27\n"
"v_mfma_f32_16x16x32_bf16 v[100:103], a[120:123], v[180:183], v[100:103]\n"
"ds_read_b64_tr_b16 v[140:141], v245 offset:32768\n"
"ds_read_b64_tr_b16 v[142:143], v245 offset:33280\n"
"v_mfma_f32_16x16x32_bf16 v[116:119], a[108:111], v[164:167], v[116:119]\n"
"buffer_load_dwordx4 a[80:83], v248, s[40:43], s50 offen offset:1024 sc1\n"
"v_mfma_f32_16x16x32_bf16 v[116:119], a[124:127], v[180:183], v[116:119]\n"
"v_cvt_pk_bf16_f32 v207, v30, v31\n"
"ds_read_b64_tr_b16 v[144:145], v242 offset:36864\n"
"v_mfma_f32_16x16x32_bf16 v[72:75], a[96:99], v[168:171], v[72:75]\n"
"ds_read_b64_tr_b16 v[146:147], v242 offset:37376\n"
"v_mfma_f32_16x16x32_bf16 v[72:75], a[112:115], v[184:187], v[72:75]\n"
"ds_read_b64_tr_b16 v[148:149], v243 offset:36864\n"
"s_add_u32 s51, s51, 4096\n"
"v_mfma_f32_16x16x32_bf16 v[88:91], a[100:103], v[168:171], v[88:91]\n"
"s_add_u32 s87, s48, 4096\n"
"ds_read_b64_tr_b16 v[150:151], v243 offset:37376\n"
"v_mfma_f32_16x16x32_bf16 v[88:91], a[116:119], v[184:187], v[88:91]\n"
"v_mfma_f32_16x16x32_bf16 v[104:107], a[104:107], v[168:171], v[104:107]\n"
"ds_read_b64_tr_b16 v[152:153], v244 offset:36864\n"
"buffer_load_dwordx4 a[68:71], v248, s[40:43], s51 offen offset:0 sc1\n"
"v_mfma_f32_16x16x32_bf16 v[104:107], a[120:123], v[184:187], v[104:107]\n"
"v_permlane16_swap_b32 v204, v206\n"
"v_mfma_f32_16x16x32_bf16 v[120:123], a[108:111], v[168:171], v[120:123]\n"
"ds_read_b64_tr_b16 v[154:155], v244 offset:37376\n"
"ds_read_b64_tr_b16 v[156:157], v245 offset:36864\n"
"v_mfma_f32_16x16x32_bf16 v[120:123], a[124:127], v[184:187], v[120:123]\n"
"ds_read_b64_tr_b16 v[158:159], v245 offset:37376\n"
"buffer_load_dwordx4 a[84:87], v248, s[40:43], s51 offen offset:1024 sc1\n"
"v_mfma_f32_16x16x32_bf16 v[76:79], a[96:99], v[172:175], v[76:79]\n"
"v_permlane16_swap_b32 v205, v207\n"
"v_mfma_f32_16x16x32_bf16 v[76:79], a[112:115], v[188:191], v[76:79]\n"
"v_mfma_f32_16x16x32_bf16 v[92:95], a[100:103], v[172:175], v[92:95]\n"
"s_add_u32 s52, s52, 4096\n"
"buffer_store_dwordx4 v[204:207], v248, s[36:39], s87 offen offset:1024 sc1\n"
"v_mfma_f32_16x16x32_bf16 v[92:95], a[116:119], v[188:191], v[92:95]\n"
"v_mfma_f32_16x16x32_bf16 v[108:111], a[104:107], v[172:175], v[108:111]\n"
"v_mfma_f32_16x16x32_bf16 v[108:111], a[120:123], v[188:191], v[108:111]\n"
"buffer_load_dwordx4 a[72:75], v248, s[40:43], s52 offen offset:0 sc1\n"
"v_mfma_f32_16x16x32_bf16 v[124:127], a[108:111], v[172:175], v[124:127]\n"
"v_mfma_f32_16x16x32_bf16 v[124:127], a[124:127], v[188:191], v[124:127]\n"
"s_waitcnt lgkmcnt(0)\n"
"v_mfma_f32_16x16x32_bf16 v[64:67], a[128:131], v[128:131], v[64:67]\n"
"ds_read_b64_tr_b16 v[160:161], v242 offset:40960\n"
"buffer_load_dwordx4 a[88:91], v248, s[40:43], s52 offen offset:1024 sc1\n"
"v_mfma_f32_16x16x32_bf16 v[64:67], a[144:147], v[144:147], v[64:67]\n"
"ds_read_b64_tr_b16 v[162:163], v242 offset:41472\n"
"v_mfma_f32_16x16x32_bf16 v[80:83], a[132:135], v[128:131], v[80:83]\n"
"v_mfma_f32_16x16x32_bf16 v[80:83], a[148:151], v[144:147], v[80:83]\n"
"ds_read_b64_tr_b16 v[164:165], v243 offset:40960\n"
"v_mfma_f32_16x16x32_bf16 v[96:99], a[136:139], v[128:131], v[96:99]\n"
"ds_read_b64_tr_b16 v[166:167], v243 offset:41472\n"
"s_add_u32 s53, s53, 4096\n"
"v_mfma_f32_16x16x32_bf16 v[96:99], a[152:155], v[144:147], v[96:99]\n"
"v_mfma_f32_16x16x32_bf16 v[112:115], a[140:143], v[128:131], v[112:115]\n"
"ds_read_b64_tr_b16 v[168:169], v244 offset:40960\n"
"v_cvt_pk_bf16_f32 v192, v32, v33\n"
"v_mfma_f32_16x16x32_bf16 v[112:115], a[156:159], v[144:147], v[112:115]\n"
"ds_read_b64_tr_b16 v[170:171], v244 offset:41472\n"
"v_mfma_f32_16x16x32_bf16 v[68:71], a[128:131], v[132:135], v[68:71]\n"
"buffer_load_dwordx4 a[76:79], v248, s[40:43], s53 offen offset:0 sc1\n"
"v_mfma_f32_16x16x32_bf16 v[68:71], a[144:147], v[148:151], v[68:71]\n"
"v_cvt_pk_bf16_f32 v194, v36, v37\n"
"ds_read_b64_tr_b16 v[172:173], v245 offset:40960\n"
"v_mfma_f32_16x16x32_bf16 v[84:87], a[132:135], v[132:135], v[84:87]\n"
"ds_read_b64_tr_b16 v[174:175], v245 offset:41472\n"
"v_mfma_f32_16x16x32_bf16 v[84:87], a[148:151], v[148:151], v[84:87]\n"
"v_cvt_pk_bf16_f32 v193, v34, v35\n"
"v_mfma_f32_16x16x32_bf16 v[100:103], a[136:139], v[132:135], v[100:103]\n"
"ds_read_b64_tr_b16 v[176:177], v242 offset:45056\n"
"buffer_load_dwordx4 a[92:95], v248, s[40:43], s53 offen offset:1024 sc1\n"
"v_mfma_f32_16x16x32_bf16 v[100:103], a[152:155], v[148:151], v[100:103]\n"
"v_mfma_f32_16x16x32_bf16 v[116:119], a[140:143], v[132:135], v[116:119]\n"
"ds_read_b64_tr_b16 v[178:179], v242 offset:45568\n"
"v_cvt_pk_bf16_f32 v195, v38, v39\n"
"v_mfma_f32_16x16x32_bf16 v[116:119], a[156:159], v[148:151], v[116:119]\n"
"ds_read_b64_tr_b16 v[180:181], v243 offset:45056\n"
"v_mfma_f32_16x16x32_bf16 v[72:75], a[128:131], v[136:139], v[72:75]\n"
"buffer_load_dwordx4 a[96:99], v248, s[40:43], s50 offen offset:2048 sc1\n"
"v_mfma_f32_16x16x32_bf16 v[72:75], a[144:147], v[152:155], v[72:75]\n"
"ds_read_b64_tr_b16 v[182:183], v243 offset:45568\n"
"s_add_u32 s87, s48, 8192\n"
"v_mfma_f32_16x16x32_bf16 v[88:91], a[132:135], v[136:139], v[88:91]\n"
"ds_read_b64_tr_b16 v[184:185], v244 offset:45056\n"
"v_mfma_f32_16x16x32_bf16 v[88:91], a[148:151], v[152:155], v[88:91]\n"
"v_mfma_f32_16x16x32_bf16 v[104:107], a[136:139], v[136:139], v[104:107]\n"
"ds_read_b64_tr_b16 v[186:187], v244 offset:45568\n"
"buffer_load_dwordx4 a[112:115], v248, s[40:43], s50 offen offset:3072 sc1\n"
"v_mfma_f32_16x16x32_bf16 v[104:107], a[152:155], v[152:155], v[104:107]\n"
"v_permlane16_swap_b32 v192, v194\n"
"v_mfma_f32_16x16x32_bf16 v[120:123], a[140:143], v[136:139], v[120:123]\n"
"ds_read_b64_tr_b16 v[188:189], v245 offset:45056\n"
"v_mfma_f32_16x16x32_bf16 v[120:123], a[156:159], v[152:155], v[120:123]\n"
"ds_read_b64_tr_b16 v[190:191], v245 offset:45568\n"
"v_permlane16_swap_b32 v193, v195\n"
"v_mfma_f32_16x16x32_bf16 v[76:79], a[128:131], v[140:143], v[76:79]\n"
"buffer_load_dwordx4 a[100:103], v248, s[40:43], s51 offen offset:2048 sc1\n"
"v_mfma_f32_16x16x32_bf16 v[76:79], a[144:147], v[156:159], v[76:79]\n"
"v_mfma_f32_16x16x32_bf16 v[92:95], a[132:135], v[140:143], v[92:95]\n"
"buffer_store_dwordx4 v[192:195], v248, s[36:39], s87 offen offset:0 sc1\n"
"v_mfma_f32_16x16x32_bf16 v[92:95], a[148:151], v[156:159], v[92:95]\n"
"v_mfma_f32_16x16x32_bf16 v[108:111], a[136:139], v[140:143], v[108:111]\n"
"buffer_load_dwordx4 a[116:119], v248, s[40:43], s51 offen offset:3072 sc1\n"
"v_mfma_f32_16x16x32_bf16 v[108:111], a[152:155], v[156:159], v[108:111]\n"
"v_mfma_f32_16x16x32_bf16 v[124:127], a[140:143], v[140:143], v[124:127]\n"
"v_mfma_f32_16x16x32_bf16 v[124:127], a[156:159], v[156:159], v[124:127]\n"
"s_waitcnt lgkmcnt(0)\n"
"v_mfma_f32_16x16x32_bf16 v[64:67], a[160:163], v[160:163], v[64:67]\n"
"ds_read_b64_tr_b16 v[128:129], v242 offset:49152\n"
"buffer_load_dwordx4 a[104:107], v248, s[40:43], s52 offen offset:2048 sc1\n"
"v_mfma_f32_16x16x32_bf16 v[64:67], a[176:179], v[176:179], v[64:67]\n"
"ds_read_b64_tr_b16 v[130:131], v242 offset:49664\n"
"v_mfma_f32_16x16x32_bf16 v[80:83], a[164:167], v[160:163], v[80:83]\n"
"v_mfma_f32_16x16x32_bf16 v[80:83], a[180:183], v[176:179], v[80:83]\n"
"ds_read_b64_tr_b16 v[132:133], v243 offset:49152\n"
"buffer_load_dwordx4 a[120:123], v248, s[40:43], s52 offen offset:3072 sc1\n"
"v_mfma_f32_16x16x32_bf16 v[96:99], a[168:171], v[160:163], v[96:99]\n"
"ds_read_b64_tr_b16 v[134:135], v243 offset:49664\n"
"v_mfma_f32_16x16x32_bf16 v[96:99], a[184:187], v[176:179], v[96:99]\n"
"v_mfma_f32_16x16x32_bf16 v[112:115], a[172:175], v[160:163], v[112:115]\n"
"ds_read_b64_tr_b16 v[136:137], v244 offset:49152\n"
"v_cvt_pk_bf16_f32 v196, v40, v41\n"
"v_mfma_f32_16x16x32_bf16 v[112:115], a[188:191], v[176:179], v[112:115]\n"
"ds_read_b64_tr_b16 v[138:139], v244 offset:49664\n"
"buffer_load_dwordx4 a[108:111], v248, s[40:43], s53 offen offset:2048 sc1\n"
"v_mfma_f32_16x16x32_bf16 v[68:71], a[160:163], v[164:167], v[68:71]\n"
"v_cvt_pk_bf16_f32 v198, v44, v45\n"
"v_mfma_f32_16x16x32_bf16 v[68:71], a[176:179], v[180:183], v[68:71]\n"
"ds_read_b64_tr_b16 v[140:141], v245 offset:49152\n"
"v_mfma_f32_16x16x32_bf16 v[84:87], a[164:167], v[164:167], v[84:87]\n"
"ds_read_b64_tr_b16 v[142:143], v245 offset:49664\n"
"buffer_load_dwordx4 a[124:127], v248, s[40:43], s53 offen offset:3072 sc1\n"
"v_mfma_f32_16x16x32_bf16 v[84:87], a[180:183], v[180:183], v[84:87]\n"
"v_cvt_pk_bf16_f32 v197, v42, v43\n"
"v_mfma_f32_16x16x32_bf16 v[100:103], a[168:171], v[164:167], v[100:103]\n"
"ds_read_b64_tr_b16 v[144:145], v242 offset:53248\n"
"v_mfma_f32_16x16x32_bf16 v[100:103], a[184:187], v[180:183], v[100:103]\n"
"v_mfma_f32_16x16x32_bf16 v[116:119], a[172:175], v[164:167], v[116:119]\n"
"ds_read_b64_tr_b16 v[146:147], v242 offset:53760\n"
"s_add_u32 s50, s50, 4096\n"
"v_mfma_f32_16x16x32_bf16 v[116:119], a[188:191], v[180:183], v[116:119]\n"
"v_cvt_pk_bf16_f32 v199, v46, v47\n"
"ds_read_b64_tr_b16 v[148:149], v243 offset:53248\n"
"v_mfma_f32_16x16x32_bf16 v[72:75], a[160:163], v[168:171], v[72:75]\n"
"v_mfma_f32_16x16x32_bf16 v[72:75], a[176:179], v[184:187], v[72:75]\n"
"ds_read_b64_tr_b16 v[150:151], v243 offset:53760\n"
"buffer_load_dwordx4 a[128:131], v248, s[40:43], s50 offen offset:0 sc1\n"
"v_mfma_f32_16x16x32_bf16 v[88:91], a[164:167], v[168:171], v[88:91]\n"
"s_add_u32 s87, s48, 8192\n"
"v_mfma_f32_16x16x32_bf16 v[88:91], a[180:183], v[184:187], v[88:91]\n"
"ds_read_b64_tr_b16 v[152:153], v244 offset:53248\n"
"v_mfma_f32_16x16x32_bf16 v[104:107], a[168:171], v[168:171], v[104:107]\n"
"ds_read_b64_tr_b16 v[154:155], v244 offset:53760\n"
"v_permlane16_swap_b32 v196, v198\n"
"v_mfma_f32_16x16x32_bf16 v[104:107], a[184:187], v[184:187], v[104:107]\n"
"ds_read_b64_tr_b16 v[156:157], v245 offset:53248\n"
"buffer_load_dwordx4 a[144:147], v248, s[40:43], s50 offen offset:1024 sc1\n"
"v_mfma_f32_16x16x32_bf16 v[120:123], a[172:175], v[168:171], v[120:123]\n"
"v_mfma_f32_16x16x32_bf16 v[120:123], a[188:191], v[184:187], v[120:123]\n"
"ds_read_b64_tr_b16 v[158:159], v245 offset:53760\n"
"v_permlane16_swap_b32 v197, v199\n"
"v_mfma_f32_16x16x32_bf16 v[76:79], a[160:163], v[172:175], v[76:79]\n"
"s_add_u32 s51, s51, 4096\n"
"v_mfma_f32_16x16x32_bf16 v[76:79], a[176:179], v[188:191], v[76:79]\n"
"v_mfma_f32_16x16x32_bf16 v[92:95], a[164:167], v[172:175], v[92:95]\n"
"buffer_store_dwordx4 v[196:199], v248, s[36:39], s87 offen offset:1024 sc1\n"
"v_mfma_f32_16x16x32_bf16 v[92:95], a[180:183], v[188:191], v[92:95]\n"
"v_mfma_f32_16x16x32_bf16 v[108:111], a[168:171], v[172:175], v[108:111]\n"
"buffer_load_dwordx4 a[132:135], v248, s[40:43], s51 offen offset:0 sc1\n"
"v_mfma_f32_16x16x32_bf16 v[108:111], a[184:187], v[188:191], v[108:111]\n"
"v_mfma_f32_16x16x32_bf16 v[124:127], a[172:175], v[172:175], v[124:127]\n"
"v_mfma_f32_16x16x32_bf16 v[124:127], a[188:191], v[188:191], v[124:127]\n"
"s_waitcnt vmcnt(31) lgkmcnt(0)\n"
"v_mfma_f32_16x16x32_bf16 v[64:67], a[192:195], v[128:131], v[64:67]\n"
"ds_read_b64_tr_b16 v[160:161], v242 offset:57344\n"
"buffer_load_dwordx4 a[148:151], v248, s[40:43], s51 offen offset:1024 sc1\n"
"v_mfma_f32_16x16x32_bf16 v[64:67], a[208:211], v[144:147], v[64:67]\n"
"ds_read_b64_tr_b16 v[162:163], v242 offset:57856\n"
"v_mfma_f32_16x16x32_bf16 v[80:83], a[196:199], v[128:131], v[80:83]\n"
"v_mfma_f32_16x16x32_bf16 v[80:83], a[212:215], v[144:147], v[80:83]\n"
"ds_read_b64_tr_b16 v[164:165], v243 offset:57344\n"
"s_add_u32 s52, s52, 4096\n"
"v_mfma_f32_16x16x32_bf16 v[96:99], a[200:203], v[128:131], v[96:99]\n"
"ds_read_b64_tr_b16 v[166:167], v243 offset:57856\n"
"v_mfma_f32_16x16x32_bf16 v[96:99], a[216:219], v[144:147], v[96:99]\n"
"v_mfma_f32_16x16x32_bf16 v[112:115], a[204:207], v[128:131], v[112:115]\n"
"ds_read_b64_tr_b16 v[168:169], v244 offset:57344\n"
"v_cvt_pk_bf16_f32 v200, v48, v49\n"
"v_mfma_f32_16x16x32_bf16 v[112:115], a[220:223], v[144:147], v[112:115]\n"
"ds_read_b64_tr_b16 v[170:171], v244 offset:57856\n"
"buffer_load_dwordx4 a[136:139], v248, s[40:43], s52 offen offset:0 sc1\n"
"v_mfma_f32_16x16x32_bf16 v[68:71], a[192:195], v[132:135], v[68:71]\n"
"v_cvt_pk_bf16_f32 v202, v52, v53\n"
"v_mfma_f32_16x16x32_bf16 v[68:71], a[208:211], v[148:151], v[68:71]\n"
"ds_read_b64_tr_b16 v[172:173], v245 offset:57344\n"
"v_mfma_f32_16x16x32_bf16 v[84:87], a[196:199], v[132:135], v[84:87]\n"
"ds_read_b64_tr_b16 v[174:175], v245 offset:57856\n"
"buffer_load_dwordx4 a[152:155], v248, s[40:43], s52 offen offset:1024 sc1\n"
"v_mfma_f32_16x16x32_bf16 v[84:87], a[212:215], v[148:151], v[84:87]\n"
"v_cvt_pk_bf16_f32 v201, v50, v51\n"
"v_mfma_f32_16x16x32_bf16 v[100:103], a[200:203], v[132:135], v[100:103]\n"
"ds_read_b64_tr_b16 v[176:177], v242 offset:61440\n"
"v_mfma_f32_16x16x32_bf16 v[100:103], a[216:219], v[148:151], v[100:103]\n"
"v_mfma_f32_16x16x32_bf16 v[116:119], a[204:207], v[132:135], v[116:119]\n"
"ds_read_b64_tr_b16 v[178:179], v242 offset:61952\n"
"s_add_u32 s53, s53, 4096\n"
"v_mfma_f32_16x16x32_bf16 v[116:119], a[220:223], v[148:151], v[116:119]\n"
"v_cvt_pk_bf16_f32 v203, v54, v55\n"
"ds_read_b64_tr_b16 v[180:181], v243 offset:61440\n"
"v_mfma_f32_16x16x32_bf16 v[72:75], a[192:195], v[136:139], v[72:75]\n"
"v_mfma_f32_16x16x32_bf16 v[72:75], a[208:211], v[152:155], v[72:75]\n"
"ds_read_b64_tr_b16 v[182:183], v243 offset:61952\n"
"buffer_load_dwordx4 a[140:143], v248, s[40:43], s53 offen offset:0 sc1\n"
"v_mfma_f32_16x16x32_bf16 v[88:91], a[196:199], v[136:139], v[88:91]\n"
"s_add_u32 s87, s48, 12288\n"
"v_mfma_f32_16x16x32_bf16 v[88:91], a[212:215], v[152:155], v[88:91]\n"
"ds_read_b64_tr_b16 v[184:185], v244 offset:61440\n"
"v_mfma_f32_16x16x32_bf16 v[104:107], a[200:203], v[136:139], v[104:107]\n"
"ds_read_b64_tr_b16 v[186:187], v244 offset:61952\n"
"v_permlane16_swap_b32 v200, v202\n"
"v_mfma_f32_16x16x32_bf16 v[104:107], a[216:219], v[152:155], v[104:107]\n"
"ds_read_b64_tr_b16 v[188:189], v245 offset:61440\n"
"buffer_load_dwordx4 a[156:159], v248, s[40:43], s53 offen offset:1024 sc1\n"
"v_mfma_f32_16x16x32_bf16 v[120:123], a[204:207], v[136:139], v[120:123]\n"
"v_mfma_f32_16x16x32_bf16 v[120:123], a[220:223], v[152:155], v[120:123]\n"
"ds_read_b64_tr_b16 v[190:191], v245 offset:61952\n"
"v_permlane16_swap_b32 v201, v203\n"
"v_mfma_f32_16x16x32_bf16 v[76:79], a[192:195], v[140:143], v[76:79]\n"
"buffer_load_dwordx4 a[160:163], v248, s[40:43], s50 offen offset:2048 sc1\n"
"v_mfma_f32_16x16x32_bf16 v[76:79], a[208:211], v[156:159], v[76:79]\n"
"v_mfma_f32_16x16x32_bf16 v[92:95], a[196:199], v[140:143], v[92:95]\n"
"buffer_store_dwordx4 v[200:203], v248, s[36:39], s87 offen offset:0 sc1\n"
"v_mfma_f32_16x16x32_bf16 v[92:95], a[212:215], v[156:159], v[92:95]\n"
"v_mfma_f32_16x16x32_bf16 v[108:111], a[200:203], v[140:143], v[108:111]\n"
"buffer_load_dwordx4 a[176:179], v248, s[40:43], s50 offen offset:3072 sc1\n"
"v_mfma_f32_16x16x32_bf16 v[108:111], a[216:219], v[156:159], v[108:111]\n"
"v_mfma_f32_16x16x32_bf16 v[124:127], a[204:207], v[140:143], v[124:127]\n"
"v_mfma_f32_16x16x32_bf16 v[124:127], a[220:223], v[156:159], v[124:127]\n"
"s_waitcnt vmcnt(39) lgkmcnt(0)\n"
"s_barrier\n"
"v_mfma_f32_16x16x32_bf16 v[64:67], a[224:227], v[160:163], v[64:67]\n"
"v_add_u32 v242, 0xffff0000, v242\n"
"buffer_load_dwordx4 a[164:167], v248, s[40:43], s51 offen offset:2048 sc1\n"
"v_mfma_f32_16x16x32_bf16 v[64:67], a[240:243], v[176:179], v[64:67]\n"
"v_add_u32 v243, 0xffff0000, v243\n"
"v_mfma_f32_16x16x32_bf16 v[80:83], a[228:231], v[160:163], v[80:83]\n"
"v_add_u32 v244, 0xffff0000, v244\n"
"v_mfma_f32_16x16x32_bf16 v[80:83], a[244:247], v[176:179], v[80:83]\n"
"v_add_u32 v245, 0xffff0000, v245\n"
"v_mfma_f32_16x16x32_bf16 v[96:99], a[232:235], v[160:163], v[96:99]\n"
"buffer_load_dwordx4 a[180:183], v248, s[40:43], s51 offen offset:3072 sc1\n"
"v_mfma_f32_16x16x32_bf16 v[96:99], a[248:251], v[176:179], v[96:99]\n"
"ds_read_b64_tr_b16 v[128:129], v242 offset:32768\n"
"v_mfma_f32_16x16x32_bf16 v[112:115], a[236:239], v[160:163], v[112:115]\n"
"ds_read_b64_tr_b16 v[130:131], v242 offset:33280\n"
"v_cvt_pk_bf16_f32 v204, v56, v57\n"
"v_mfma_f32_16x16x32_bf16 v[112:115], a[252:255], v[176:179], v[112:115]\n"
"ds_read_b64_tr_b16 v[132:133], v243 offset:32768\n"
"v_mfma_f32_16x16x32_bf16 v[68:71], a[224:227], v[164:167], v[68:71]\n"
"ds_read_b64_tr_b16 v[134:135], v243 offset:33280\n"
"buffer_load_dwordx4 a[168:171], v248, s[40:43], s52 offen offset:2048 sc1\n"
"v_mfma_f32_16x16x32_bf16 v[68:71], a[240:243], v[180:183], v[68:71]\n"
"v_cvt_pk_bf16_f32 v206, v60, v61\n"
"v_mfma_f32_16x16x32_bf16 v[84:87], a[228:231], v[164:167], v[84:87]\n"
"ds_read_b64_tr_b16 v[136:137], v244 offset:32768\n"
"v_mfma_f32_16x16x32_bf16 v[84:87], a[244:247], v[180:183], v[84:87]\n"
"ds_read_b64_tr_b16 v[138:139], v244 offset:33280\n"
"v_cvt_pk_bf16_f32 v205, v58, v59\n"
"v_mfma_f32_16x16x32_bf16 v[100:103], a[232:235], v[164:167], v[100:103]\n"
"ds_read_b64_tr_b16 v[140:141], v245 offset:32768\n"
"buffer_load_dwordx4 a[184:187], v248, s[40:43], s52 offen offset:3072 sc1\n"
"v_mfma_f32_16x16x32_bf16 v[100:103], a[248:251], v[180:183], v[100:103]\n"
"ds_read_b64_tr_b16 v[142:143], v245 offset:33280\n"
"v_mfma_f32_16x16x32_bf16 v[116:119], a[236:239], v[164:167], v[116:119]\n"
"v_cvt_pk_bf16_f32 v207, v62, v63\n"
"v_mfma_f32_16x16x32_bf16 v[116:119], a[252:255], v[180:183], v[116:119]\n"
"ds_read_b64_tr_b16 v[144:145], v242 offset:36864\n"
"v_mfma_f32_16x16x32_bf16 v[72:75], a[224:227], v[168:171], v[72:75]\n"
"ds_read_b64_tr_b16 v[146:147], v242 offset:37376\n"
"buffer_load_dwordx4 a[172:175], v248, s[40:43], s53 offen offset:2048 sc1\n"
"v_mfma_f32_16x16x32_bf16 v[72:75], a[240:243], v[184:187], v[72:75]\n"
"ds_read_b64_tr_b16 v[148:149], v243 offset:36864\n"
"v_mfma_f32_16x16x32_bf16 v[88:91], a[228:231], v[168:171], v[88:91]\n"
"s_add_u32 s87, s48, 12288\n"
"ds_read_b64_tr_b16 v[150:151], v243 offset:37376\n"
"v_mfma_f32_16x16x32_bf16 v[88:91], a[244:247], v[184:187], v[88:91]\n"
"v_mfma_f32_16x16x32_bf16 v[104:107], a[232:235], v[168:171], v[104:107]\n"
"ds_read_b64_tr_b16 v[152:153], v244 offset:36864\n"
"buffer_load_dwordx4 a[188:191], v248, s[40:43], s53 offen offset:3072 sc1\n"
"v_mfma_f32_16x16x32_bf16 v[104:107], a[248:251], v[184:187], v[104:107]\n"
"v_permlane16_swap_b32 v204, v206\n"
"v_mfma_f32_16x16x32_bf16 v[120:123], a[236:239], v[168:171], v[120:123]\n"
"ds_read_b64_tr_b16 v[154:155], v244 offset:37376\n"
"ds_read_b64_tr_b16 v[156:157], v245 offset:36864\n"
"v_mfma_f32_16x16x32_bf16 v[120:123], a[252:255], v[184:187], v[120:123]\n"
"ds_read_b64_tr_b16 v[158:159], v245 offset:37376\n"
"v_permlane16_swap_b32 v205, v207\n"
"v_mfma_f32_16x16x32_bf16 v[76:79], a[224:227], v[172:175], v[76:79]\n"
"s_add_u32 s50, s50, 4096\n"
"v_mfma_f32_16x16x32_bf16 v[76:79], a[240:243], v[188:191], v[76:79]\n"
"v_mfma_f32_16x16x32_bf16 v[92:95], a[228:231], v[172:175], v[92:95]\n"
"buffer_store_dwordx4 v[204:207], v248, s[36:39], s87 offen offset:1024 sc1\n"
"v_mfma_f32_16x16x32_bf16 v[92:95], a[244:247], v[188:191], v[92:95]\n"
"v_mfma_f32_16x16x32_bf16 v[108:111], a[232:235], v[172:175], v[108:111]\n"
"buffer_load_dwordx4 a[192:195], v248, s[40:43], s50 offen offset:0 sc1\n"
"v_mfma_f32_16x16x32_bf16 v[108:111], a[248:251], v[188:191], v[108:111]\n"
"v_mfma_f32_16x16x32_bf16 v[124:127], a[236:239], v[172:175], v[124:127]\n"
"v_mfma_f32_16x16x32_bf16 v[124:127], a[252:255], v[188:191], v[124:127]\n"
"s_waitcnt vmcnt(47) lgkmcnt(0)\n"
"v_mfma_f32_16x16x32_bf16 v[0:3], v[128:131], a[0:3], 0\n"
"ds_read_b64_tr_b16 v[160:161], v242 offset:40960\n"
"buffer_load_dwordx4 a[208:211], v248, s[40:43], s50 offen offset:1024 sc1\n"
"v_mfma_f32_16x16x32_bf16 v[0:3], v[144:147], a[16:19], v[0:3]\n"
"ds_read_b64_tr_b16 v[162:163], v242 offset:41472\n"
"v_mfma_f32_16x16x32_bf16 v[16:19], v[128:131], a[4:7], 0\n"
"v_mfma_f32_16x16x32_bf16 v[16:19], v[144:147], a[20:23], v[16:19]\n"
"ds_read_b64_tr_b16 v[164:165], v243 offset:40960\n"
"s_add_u32 s51, s51, 4096\n"
"v_mfma_f32_16x16x32_bf16 v[32:35], v[128:131], a[8:11], 0\n"
"ds_read_b64_tr_b16 v[166:167], v243 offset:41472\n"
"v_mfma_f32_16x16x32_bf16 v[32:35], v[144:147], a[24:27], v[32:35]\n"
"v_mfma_f32_16x16x32_bf16 v[48:51], v[128:131], a[12:15], 0\n"
"ds_read_b64_tr_b16 v[168:169], v244 offset:40960\n"
"buffer_load_dwordx4 a[196:199], v248, s[40:43], s51 offen offset:0 sc1\n"
"v_mfma_f32_16x16x32_bf16 v[48:51], v[144:147], a[28:31], v[48:51]\n"
"ds_read_b64_tr_b16 v[170:171], v244 offset:41472\n"
"v_mfma_f32_16x16x32_bf16 v[4:7], v[132:135], a[0:3], 0\n"
"v_mfma_f32_16x16x32_bf16 v[4:7], v[148:151], a[16:19], v[4:7]\n"
"ds_read_b64_tr_b16 v[172:173], v245 offset:40960\n"
"buffer_load_dwordx4 a[212:215], v248, s[40:43], s51 offen offset:1024 sc1\n"
"v_mfma_f32_16x16x32_bf16 v[20:23], v[132:135], a[4:7], 0\n"
"ds_read_b64_tr_b16 v[174:175], v245 offset:41472\n"
"v_mfma_f32_16x16x32_bf16 v[20:23], v[148:151], a[20:23], v[20:23]\n"
"v_mfma_f32_16x16x32_bf16 v[36:39], v[132:135], a[8:11], 0\n"
"ds_read_b64_tr_b16 v[176:177], v242 offset:45056\n"
"s_add_u32 s52, s52, 4096\n"
"v_mfma_f32_16x16x32_bf16 v[36:39], v[148:151], a[24:27], v[36:39]\n"
"v_mfma_f32_16x16x32_bf16 v[52:55], v[132:135], a[12:15], 0\n"
"ds_read_b64_tr_b16 v[178:179], v242 offset:45568\n"
"v_mfma_f32_16x16x32_bf16 v[52:55], v[148:151], a[28:31], v[52:55]\n"
"ds_read_b64_tr_b16 v[180:181], v243 offset:45056\n"
"v_mfma_f32_16x16x32_bf16 v[8:11], v[136:139], a[0:3], 0\n"
"buffer_load_dwordx4 a[200:203], v248, s[40:43], s52 offen offset:0 sc1\n"
"v_mfma_f32_16x16x32_bf16 v[8:11], v[152:155], a[16:19], v[8:11]\n"
"ds_read_b64_tr_b16 v[182:183], v243 offset:45568\n"
"v_mfma_f32_16x16x32_bf16 v[24:27], v[136:139], a[4:7], 0\n"
"ds_read_b64_tr_b16 v[184:185], v244 offset:45056\n"
"v_mfma_f32_16x16x32_bf16 v[24:27], v[152:155], a[20:23], v[24:27]\n"
"buffer_load_dwordx4 a[216:219], v248, s[40:43], s52 offen offset:1024 sc1\n"
"v_mfma_f32_16x16x32_bf16 v[40:43], v[136:139], a[8:11], 0\n"
"ds_read_b64_tr_b16 v[186:187], v244 offset:45568\n"
"v_mfma_f32_16x16x32_bf16 v[40:43], v[152:155], a[24:27], v[40:43]\n"
"ds_read_b64_tr_b16 v[188:189], v245 offset:45056\n"
"v_mfma_f32_16x16x32_bf16 v[56:59], v[136:139], a[12:15], 0\n"
"s_add_u32 s53, s53, 4096\n"
"v_mfma_f32_16x16x32_bf16 v[56:59], v[152:155], a[28:31], v[56:59]\n"
"ds_read_b64_tr_b16 v[190:191], v245 offset:45568\n"
"v_mfma_f32_16x16x32_bf16 v[12:15], v[140:143], a[0:3], 0\n"
"v_mfma_f32_16x16x32_bf16 v[12:15], v[156:159], a[16:19], v[12:15]\n"
"buffer_load_dwordx4 a[204:207], v248, s[40:43], s53 offen offset:0 sc1\n"
"v_mfma_f32_16x16x32_bf16 v[28:31], v[140:143], a[4:7], 0\n"
"v_mfma_f32_16x16x32_bf16 v[28:31], v[156:159], a[20:23], v[28:31]\n"
"v_mfma_f32_16x16x32_bf16 v[44:47], v[140:143], a[8:11], 0\n"
"buffer_load_dwordx4 a[220:223], v248, s[40:43], s53 offen offset:1024 sc1\n"
"v_mfma_f32_16x16x32_bf16 v[44:47], v[156:159], a[24:27], v[44:47]\n"
"v_mfma_f32_16x16x32_bf16 v[60:63], v[140:143], a[12:15], 0\n"
"v_mfma_f32_16x16x32_bf16 v[60:63], v[156:159], a[28:31], v[60:63]\n"
"s_waitcnt vmcnt(45) lgkmcnt(0)\n"
"v_mfma_f32_16x16x32_bf16 v[0:3], v[160:163], a[32:35], v[0:3]\n"
"ds_read_b64_tr_b16 v[128:129], v242 offset:49152\n"
"buffer_load_dwordx4 a[224:227], v248, s[40:43], s50 offen offset:2048 sc1\n"
"v_mfma_f32_16x16x32_bf16 v[0:3], v[176:179], a[48:51], v[0:3]\n"
"ds_read_b64_tr_b16 v[130:131], v242 offset:49664\n"
"v_mfma_f32_16x16x32_bf16 v[16:19], v[160:163], a[36:39], v[16:19]\n"
"v_mfma_f32_16x16x32_bf16 v[16:19], v[176:179], a[52:55], v[16:19]\n"
"ds_read_b64_tr_b16 v[132:133], v243 offset:49152\n"
"v_mfma_f32_16x16x32_bf16 v[32:35], v[160:163], a[40:43], v[32:35]\n"
"ds_read_b64_tr_b16 v[134:135], v243 offset:49664\n"
"buffer_load_dwordx4 a[240:243], v248, s[40:43], s50 offen offset:3072 sc1\n"
"v_mfma_f32_16x16x32_bf16 v[32:35], v[176:179], a[56:59], v[32:35]\n"
"v_mfma_f32_16x16x32_bf16 v[48:51], v[160:163], a[44:47], v[48:51]\n"
"ds_read_b64_tr_b16 v[136:137], v244 offset:49152\n"
"v_mfma_f32_16x16x32_bf16 v[48:51], v[176:179], a[60:63], v[48:51]\n"
"ds_read_b64_tr_b16 v[138:139], v244 offset:49664\n"
"v_mfma_f32_16x16x32_bf16 v[4:7], v[164:167], a[32:35], v[4:7]\n"
"buffer_load_dwordx4 a[228:231], v248, s[40:43], s51 offen offset:2048 sc1\n"
"v_mfma_f32_16x16x32_bf16 v[4:7], v[180:183], a[48:51], v[4:7]\n"
"ds_read_b64_tr_b16 v[140:141], v245 offset:49152\n"
"v_mfma_f32_16x16x32_bf16 v[20:23], v[164:167], a[36:39], v[20:23]\n"
"ds_read_b64_tr_b16 v[142:143], v245 offset:49664\n"
"v_mfma_f32_16x16x32_bf16 v[20:23], v[180:183], a[52:55], v[20:23]\n"
"v_mfma_f32_16x16x32_bf16 v[36:39], v[164:167], a[40:43], v[36:39]\n"
"ds_read_b64_tr_b16 v[144:145], v242 offset:53248\n"
"buffer_load_dwordx4 a[244:247], v248, s[40:43], s51 offen offset:3072 sc1\n"
"v_mfma_f32_16x16x32_bf16 v[36:39], v[180:183], a[56:59], v[36:39]\n"
"v_mfma_f32_16x16x32_bf16 v[52:55], v[164:167], a[44:47], v[52:55]\n"
"ds_read_b64_tr_b16 v[146:147], v242 offset:53760\n"
"v_mfma_f32_16x16x32_bf16 v[52:55], v[180:183], a[60:63], v[52:55]\n"
"ds_read_b64_tr_b16 v[148:149], v243 offset:53248\n"
"v_mfma_f32_16x16x32_bf16 v[8:11], v[168:171], a[32:35], v[8:11]\n"
"buffer_load_dwordx4 a[232:235], v248, s[40:43], s52 offen offset:2048 sc1\n"
"v_mfma_f32_16x16x32_bf16 v[8:11], v[184:187], a[48:51], v[8:11]\n"
"ds_read_b64_tr_b16 v[150:151], v243 offset:53760\n"
"v_mfma_f32_16x16x32_bf16 v[24:27], v[168:171], a[36:39], v[24:27]\n"
"ds_read_b64_tr_b16 v[152:153], v244 offset:53248\n"
"v_mfma_f32_16x16x32_bf16 v[24:27], v[184:187], a[52:55], v[24:27]\n"
"v_mfma_f32_16x16x32_bf16 v[40:43], v[168:171], a[40:43], v[40:43]\n"
"ds_read_b64_tr_b16 v[154:155], v244 offset:53760\n"
"buffer_load_dwordx4 a[248:251], v248, s[40:43], s52 offen offset:3072 sc1\n"
"v_mfma_f32_16x16x32_bf16 v[40:43], v[184:187], a[56:59], v[40:43]\n"
"ds_read_b64_tr_b16 v[156:157], v245 offset:53248\n"
"v_mfma_f32_16x16x32_bf16 v[56:59], v[168:171], a[44:47], v[56:59]\n"
"v_mfma_f32_16x16x32_bf16 v[56:59], v[184:187], a[60:63], v[56:59]\n"
"ds_read_b64_tr_b16 v[158:159], v245 offset:53760\n"
"v_mfma_f32_16x16x32_bf16 v[12:15], v[172:175], a[32:35], v[12:15]\n"
"buffer_load_dwordx4 a[236:239], v248, s[40:43], s53 offen offset:2048 sc1\n"
"v_mfma_f32_16x16x32_bf16 v[12:15], v[188:191], a[48:51], v[12:15]\n"
"v_mfma_f32_16x16x32_bf16 v[28:31], v[172:175], a[36:39], v[28:31]\n"
"v_mfma_f32_16x16x32_bf16 v[28:31], v[188:191], a[52:55], v[28:31]\n"
"v_mfma_f32_16x16x32_bf16 v[44:47], v[172:175], a[40:43], v[44:47]\n"
"buffer_load_dwordx4 a[252:255], v248, s[40:43], s53 offen offset:3072 sc1\n"
"v_mfma_f32_16x16x32_bf16 v[44:47], v[188:191], a[56:59], v[44:47]\n"
"v_mfma_f32_16x16x32_bf16 v[60:63], v[172:175], a[44:47], v[60:63]\n"
"v_mfma_f32_16x16x32_bf16 v[60:63], v[188:191], a[60:63], v[60:63]\n"
"s_waitcnt vmcnt(44) lgkmcnt(0)\n"
"v_mfma_f32_16x16x32_bf16 v[0:3], v[128:131], a[64:67], v[0:3]\n"
"ds_read_b64_tr_b16 v[160:161], v242 offset:57344\n"
"s_add_u32 m0, s46, 131072\n"
"v_mfma_f32_16x16x32_bf16 v[0:3], v[144:147], a[80:83], v[0:3]\n"
"v_cvt_pk_bf16_f32 v192, v64, v65\n"
"ds_read_b64_tr_b16 v[162:163], v242 offset:57856\n"
"v_mfma_f32_16x16x32_bf16 v[16:19], v[128:131], a[68:71], v[16:19]\n"
"v_cvt_pk_bf16_f32 v194, v68, v69\n"
"v_cvt_pk_bf16_f32 v193, v66, v67\n"
"v_mfma_f32_16x16x32_bf16 v[16:19], v[144:147], a[84:87], v[16:19]\n"
"ds_read_b64_tr_b16 v[164:165], v243 offset:57344\n"
"v_cvt_pk_bf16_f32 v195, v70, v71\n"
"v_mfma_f32_16x16x32_bf16 v[32:35], v[128:131], a[72:75], v[32:35]\n"
"ds_read_b64_tr_b16 v[166:167], v243 offset:57856\n"
"s_mov_b32 s92, 2097280\n"
"v_mfma_f32_16x16x32_bf16 v[32:35], v[144:147], a[88:91], v[32:35]\n"
"s_add_u32 s87, s48, 0\n"
"v_permlane16_swap_b32 v192, v194\n"
"v_mfma_f32_16x16x32_bf16 v[48:51], v[128:131], a[76:79], v[48:51]\n"
"ds_read_b64_tr_b16 v[168:169], v244 offset:57344\n"
"v_permlane16_swap_b32 v193, v195\n"
"v_mfma_f32_16x16x32_bf16 v[48:51], v[144:147], a[92:95], v[48:51]\n"
"ds_read_b64_tr_b16 v[170:171], v244 offset:57856\n"
"buffer_store_dwordx4 v[192:195], v248, s[36:39], s87 offen offset:2048 sc1\n"
"v_mfma_f32_16x16x32_bf16 v[4:7], v[132:135], a[64:67], v[4:7]\n"
"buffer_load_dwordx4 v238, s[20:23], s92 offen offset:0 lds\n"
"v_mfma_f32_16x16x32_bf16 v[4:7], v[148:151], a[80:83], v[4:7]\n"
"v_cvt_pk_bf16_f32 v196, v72, v73\n"
"v_mfma_f32_16x16x32_bf16 v[20:23], v[132:135], a[68:71], v[20:23]\n"
"ds_read_b64_tr_b16 v[172:173], v245 offset:57344\n"
"v_cvt_pk_bf16_f32 v198, v76, v77\n"
"v_mfma_f32_16x16x32_bf16 v[20:23], v[148:151], a[84:87], v[20:23]\n"
"ds_read_b64_tr_b16 v[174:175], v245 offset:57856\n"
"v_cvt_pk_bf16_f32 v197, v74, v75\n"
"v_mfma_f32_16x16x32_bf16 v[36:39], v[132:135], a[72:75], v[36:39]\n"
"v_cvt_pk_bf16_f32 v199, v78, v79\n"
"ds_read_b64_tr_b16 v[176:177], v242 offset:61440\n"
"v_mfma_f32_16x16x32_bf16 v[36:39], v[148:151], a[88:91], v[36:39]\n"
"buffer_load_dwordx4 v239, s[20:23], s92 offen offset:1024 lds\n"
"v_mfma_f32_16x16x32_bf16 v[52:55], v[132:135], a[76:79], v[52:55]\n"
"s_add_u32 s87, s48, 0\n"
"v_permlane16_swap_b32 v196, v198\n"
"v_mfma_f32_16x16x32_bf16 v[52:55], v[148:151], a[92:95], v[52:55]\n"
"ds_read_b64_tr_b16 v[178:179], v242 offset:61952\n"
"v_permlane16_swap_b32 v197, v199\n"
"v_mfma_f32_16x16x32_bf16 v[8:11], v[136:139], a[64:67], v[8:11]\n"
"ds_read_b64_tr_b16 v[180:181], v243 offset:61440\n"
"buffer_store_dwordx4 v[196:199], v248, s[36:39], s87 offen offset:3072 sc1\n"
"v_mfma_f32_16x16x32_bf16 v[8:11], v[152:155], a[80:83], v[8:11]\n"
"s_add_u32 m0, s46, 139264\n"
"v_mfma_f32_16x16x32_bf16 v[24:27], v[136:139], a[68:71], v[24:27]\n"
"v_cvt_pk_bf16_f32 v200, v80, v81\n"
"ds_read_b64_tr_b16 v[182:183], v243 offset:61952\n"
"v_mfma_f32_16x16x32_bf16 v[24:27], v[152:155], a[84:87], v[24:27]\n"
"v_cvt_pk_bf16_f32 v202, v84, v85\n"
"ds_read_b64_tr_b16 v[184:185], v244 offset:61440\n"
"v_mfma_f32_16x16x32_bf16 v[40:43], v[136:139], a[72:75], v[40:43]\n"
"v_cvt_pk_bf16_f32 v201, v82, v83\n"
"v_cvt_pk_bf16_f32 v203, v86, v87\n"
"v_mfma_f32_16x16x32_bf16 v[40:43], v[152:155], a[88:91], v[40:43]\n"
"ds_read_b64_tr_b16 v[186:187], v244 offset:61952\n"
"s_mov_b32 s92, 2621568\n"
"v_mfma_f32_16x16x32_bf16 v[56:59], v[136:139], a[76:79], v[56:59]\n"
"s_add_u32 s87, s48, 4096\n"
"ds_read_b64_tr_b16 v[188:189], v245 offset:61440\n"
"v_mfma_f32_16x16x32_bf16 v[56:59], v[152:155], a[92:95], v[56:59]\n"
"v_permlane16_swap_b32 v200, v202\n"
"v_permlane16_swap_b32 v201, v203\n"
"v_mfma_f32_16x16x32_bf16 v[12:15], v[140:143], a[64:67], v[12:15]\n"
"ds_read_b64_tr_b16 v[190:191], v245 offset:61952\n"
"buffer_store_dwordx4 v[200:203], v248, s[36:39], s87 offen offset:2048 sc1\n"
"v_mfma_f32_16x16x32_bf16 v[12:15], v[156:159], a[80:83], v[12:15]\n"
"buffer_load_dwordx4 v238, s[20:23], s92 offen offset:0 lds\n"
"v_mfma_f32_16x16x32_bf16 v[28:31], v[140:143], a[68:71], v[28:31]\n"
"v_cvt_pk_bf16_f32 v204, v88, v89\n"
"v_mfma_f32_16x16x32_bf16 v[28:31], v[156:159], a[84:87], v[28:31]\n"
"v_cvt_pk_bf16_f32 v206, v92, v93\n"
"v_cvt_pk_bf16_f32 v205, v90, v91\n"
"v_mfma_f32_16x16x32_bf16 v[44:47], v[140:143], a[72:75], v[44:47]\n"
"v_cvt_pk_bf16_f32 v207, v94, v95\n"
"buffer_load_dwordx4 v239, s[20:23], s92 offen offset:1024 lds\n"
"v_mfma_f32_16x16x32_bf16 v[44:47], v[156:159], a[88:91], v[44:47]\n"
"s_add_u32 s87, s48, 4096\n"
"v_mfma_f32_16x16x32_bf16 v[60:63], v[140:143], a[76:79], v[60:63]\n"
"v_permlane16_swap_b32 v204, v206\n"
"v_permlane16_swap_b32 v205, v207\n"
"v_mfma_f32_16x16x32_bf16 v[60:63], v[156:159], a[92:95], v[60:63]\n"
"buffer_store_dwordx4 v[204:207], v248, s[36:39], s87 offen offset:3072 sc1\n"
"s_waitcnt vmcnt(43) lgkmcnt(0)\n"
"s_barrier\n"
"v_mfma_f32_16x16x32_bf16 v[0:3], v[160:163], a[96:99], v[0:3]\n"
"v_add_u32 v242, 0x10000, v242\n"
"v_cvt_pk_bf16_f32 v192, v96, v97\n"
"v_mfma_f32_16x16x32_bf16 v[0:3], v[176:179], a[112:115], v[0:3]\n"
"v_add_u32 v243, 0x10000, v243\n"
"v_cvt_pk_bf16_f32 v194, v100, v101\n"
"v_mfma_f32_16x16x32_bf16 v[16:19], v[160:163], a[100:103], v[16:19]\n"
"v_add_u32 v244, 0x10000, v244\n"
"v_cvt_pk_bf16_f32 v193, v98, v99\n"
"v_mfma_f32_16x16x32_bf16 v[16:19], v[176:179], a[116:119], v[16:19]\n"
"v_add_u32 v245, 0x10000, v245\n"
"v_cvt_pk_bf16_f32 v195, v102, v103\n"
"v_mfma_f32_16x16x32_bf16 v[32:35], v[160:163], a[104:107], v[32:35]\n"
"s_add_u32 s87, s48, 8192\n"
"v_mfma_f32_16x16x32_bf16 v[32:35], v[176:179], a[120:123], v[32:35]\n"
"ds_read_b64_tr_b16 v[128:129], v242 offset:0\n"
"v_permlane16_swap_b32 v192, v194\n"
"v_mfma_f32_16x16x32_bf16 v[48:51], v[160:163], a[108:111], v[48:51]\n"
"ds_read_b64_tr_b16 v[130:131], v242 offset:512\n"
"v_permlane16_swap_b32 v193, v195\n"
"v_mfma_f32_16x16x32_bf16 v[48:51], v[176:179], a[124:127], v[48:51]\n"
"ds_read_b64_tr_b16 v[132:133], v243 offset:0\n"
"buffer_store_dwordx4 v[192:195], v248, s[36:39], s87 offen offset:2048 sc1\n"
"v_mfma_f32_16x16x32_bf16 v[4:7], v[164:167], a[96:99], v[4:7]\n"
"ds_read_b64_tr_b16 v[134:135], v243 offset:512\n"
"v_mfma_f32_16x16x32_bf16 v[4:7], v[180:183], a[112:115], v[4:7]\n"
"v_cvt_pk_bf16_f32 v196, v104, v105\n"
"v_cvt_pk_bf16_f32 v198, v108, v109\n"
"v_mfma_f32_16x16x32_bf16 v[20:23], v[164:167], a[100:103], v[20:23]\n"
"ds_read_b64_tr_b16 v[136:137], v244 offset:0\n"
"v_cvt_pk_bf16_f32 v197, v106, v107\n"
"v_mfma_f32_16x16x32_bf16 v[20:23], v[180:183], a[116:119], v[20:23]\n"
"ds_read_b64_tr_b16 v[138:139], v244 offset:512\n"
"v_cvt_pk_bf16_f32 v199, v110, v111\n"
"v_mfma_f32_16x16x32_bf16 v[36:39], v[164:167], a[104:107], v[36:39]\n"
"ds_read_b64_tr_b16 v[140:141], v245 offset:0\n"
"s_add_u32 s87, s48, 8192\n"
"v_mfma_f32_16x16x32_bf16 v[36:39], v[180:183], a[120:123], v[36:39]\n"
"ds_read_b64_tr_b16 v[142:143], v245 offset:512\n"
"v_permlane16_swap_b32 v196, v198\n"
"v_mfma_f32_16x16x32_bf16 v[52:55], v[164:167], a[108:111], v[52:55]\n"
"v_permlane16_swap_b32 v197, v199\n"
"v_mfma_f32_16x16x32_bf16 v[52:55], v[180:183], a[124:127], v[52:55]\n"
"ds_read_b64_tr_b16 v[144:145], v242 offset:4096\n"
"buffer_store_dwordx4 v[196:199], v248, s[36:39], s87 offen offset:3072 sc1\n"
"v_mfma_f32_16x16x32_bf16 v[8:11], v[168:171], a[96:99], v[8:11]\n"
"ds_read_b64_tr_b16 v[146:147], v242 offset:4608\n"
"v_mfma_f32_16x16x32_bf16 v[8:11], v[184:187], a[112:115], v[8:11]\n"
"v_cvt_pk_bf16_f32 v200, v112, v113\n"
"ds_read_b64_tr_b16 v[148:149], v243 offset:4096\n"
"v_mfma_f32_16x16x32_bf16 v[24:27], v[168:171], a[100:103], v[24:27]\n"
"v_cvt_pk_bf16_f32 v202, v116, v117\n"
"ds_read_b64_tr_b16 v[150:151], v243 offset:4608\n"
"v_mfma_f32_16x16x32_bf16 v[24:27], v[184:187], a[116:119], v[24:27]\n"
"v_cvt_pk_bf16_f32 v201, v114, v115\n"
"v_cvt_pk_bf16_f32 v203, v118, v119\n"
"v_mfma_f32_16x16x32_bf16 v[40:43], v[168:171], a[104:107], v[40:43]\n"
"ds_read_b64_tr_b16 v[152:153], v244 offset:4096\n"
"s_add_u32 s87, s48, 12288\n"
"v_mfma_f32_16x16x32_bf16 v[40:43], v[184:187], a[120:123], v[40:43]\n"
"ds_read_b64_tr_b16 v[154:155], v244 offset:4608\n"
"v_permlane16_swap_b32 v200, v202\n"
"v_mfma_f32_16x16x32_bf16 v[56:59], v[168:171], a[108:111], v[56:59]\n"
"ds_read_b64_tr_b16 v[156:157], v245 offset:4096\n"
"v_permlane16_swap_b32 v201, v203\n"
"v_mfma_f32_16x16x32_bf16 v[56:59], v[184:187], a[124:127], v[56:59]\n"
"ds_read_b64_tr_b16 v[158:159], v245 offset:4608\n"
"buffer_store_dwordx4 v[200:203], v248, s[36:39], s87 offen offset:2048 sc1\n"
"v_mfma_f32_16x16x32_bf16 v[12:15], v[172:175], a[96:99], v[12:15]\n"
"v_cvt_pk_bf16_f32 v204, v120, v121\n"
"v_mfma_f32_16x16x32_bf16 v[12:15], v[188:191], a[112:115], v[12:15]\n"
"v_cvt_pk_bf16_f32 v206, v124, v125\n"
"v_mfma_f32_16x16x32_bf16 v[28:31], v[172:175], a[100:103], v[28:31]\n"
"v_cvt_pk_bf16_f32 v205, v122, v123\n"
"v_mfma_f32_16x16x32_bf16 v[28:31], v[188:191], a[116:119], v[28:31]\n"
"v_cvt_pk_bf16_f32 v207, v126, v127\n"
"v_mfma_f32_16x16x32_bf16 v[44:47], v[172:175], a[104:107], v[44:47]\n"
"s_add_u32 s87, s48, 12288\n"
"v_mfma_f32_16x16x32_bf16 v[44:47], v[188:191], a[120:123], v[44:47]\n"
"v_permlane16_swap_b32 v204, v206\n"
"v_mfma_f32_16x16x32_bf16 v[60:63], v[172:175], a[108:111], v[60:63]\n"
"v_permlane16_swap_b32 v205, v207\n"
"v_mfma_f32_16x16x32_bf16 v[60:63], v[188:191], a[124:127], v[60:63]\n"
"buffer_store_dwordx4 v[204:207], v248, s[36:39], s87 offen offset:3072 sc1\n"
"s_waitcnt vmcnt(38) lgkmcnt(0)\n"
"v_mfma_f32_16x16x32_bf16 v[0:3], v[128:131], a[128:131], v[0:3]\n"
"ds_read_b64_tr_b16 v[160:161], v242 offset:8192\n"
"s_add_u32 m0, s46, 147456\n"
"v_mfma_f32_16x16x32_bf16 v[0:3], v[144:147], a[144:147], v[0:3]\n"
"ds_read_b64_tr_b16 v[162:163], v242 offset:8704\n"
"v_mfma_f32_16x16x32_bf16 v[16:19], v[128:131], a[132:135], v[16:19]\n"
"v_mfma_f32_16x16x32_bf16 v[16:19], v[144:147], a[148:151], v[16:19]\n"
"ds_read_b64_tr_b16 v[164:165], v243 offset:8192\n"
"v_mfma_f32_16x16x32_bf16 v[32:35], v[128:131], a[136:139], v[32:35]\n"
"ds_read_b64_tr_b16 v[166:167], v243 offset:8704\n"
"s_mov_b32 s92, 3145856\n"
"v_mfma_f32_16x16x32_bf16 v[32:35], v[144:147], a[152:155], v[32:35]\n"
"v_mfma_f32_16x16x32_bf16 v[48:51], v[128:131], a[140:143], v[48:51]\n"
"ds_read_b64_tr_b16 v[168:169], v244 offset:8192\n"
"v_mfma_f32_16x16x32_bf16 v[48:51], v[144:147], a[156:159], v[48:51]\n"
"ds_read_b64_tr_b16 v[170:171], v244 offset:8704\n"
"v_mfma_f32_16x16x32_bf16 v[4:7], v[132:135], a[128:131], v[4:7]\n"
"buffer_load_dwordx4 v238, s[20:23], s92 offen offset:0 lds\n"
"v_mfma_f32_16x16x32_bf16 v[4:7], v[148:151], a[144:147], v[4:7]\n"
"ds_read_b64_tr_b16 v[172:173], v245 offset:8192\n"
"v_mfma_f32_16x16x32_bf16 v[20:23], v[132:135], a[132:135], v[20:23]\n"
"ds_read_b64_tr_b16 v[174:175], v245 offset:8704\n"
"v_mfma_f32_16x16x32_bf16 v[20:23], v[148:151], a[148:151], v[20:23]\n"
"v_mfma_f32_16x16x32_bf16 v[36:39], v[132:135], a[136:139], v[36:39]\n"
"ds_read_b64_tr_b16 v[176:177], v242 offset:12288\n"
"buffer_load_dwordx4 v239, s[20:23], s92 offen offset:1024 lds\n"
"v_mfma_f32_16x16x32_bf16 v[36:39], v[148:151], a[152:155], v[36:39]\n"
"v_mfma_f32_16x16x32_bf16 v[52:55], v[132:135], a[140:143], v[52:55]\n"
"ds_read_b64_tr_b16 v[178:179], v242 offset:12800\n"
"v_mfma_f32_16x16x32_bf16 v[52:55], v[148:151], a[156:159], v[52:55]\n"
"ds_read_b64_tr_b16 v[180:181], v243 offset:12288\n"
"v_mfma_f32_16x16x32_bf16 v[8:11], v[136:139], a[128:131], v[8:11]\n"
"s_add_u32 m0, s46, 155648\n"
"v_mfma_f32_16x16x32_bf16 v[8:11], v[152:155], a[144:147], v[8:11]\n"
"ds_read_b64_tr_b16 v[182:183], v243 offset:12800\n"
"v_mfma_f32_16x16x32_bf16 v[24:27], v[136:139], a[132:135], v[24:27]\n"
"ds_read_b64_tr_b16 v[184:185], v244 offset:12288\n"
"v_mfma_f32_16x16x32_bf16 v[24:27], v[152:155], a[148:151], v[24:27]\n"
"v_mfma_f32_16x16x32_bf16 v[40:43], v[136:139], a[136:139], v[40:43]\n"
"ds_read_b64_tr_b16 v[186:187], v244 offset:12800\n"
"s_mov_b32 s92, 3670144\n"
"v_mfma_f32_16x16x32_bf16 v[40:43], v[152:155], a[152:155], v[40:43]\n"
"ds_read_b64_tr_b16 v[188:189], v245 offset:12288\n"
"v_mfma_f32_16x16x32_bf16 v[56:59], v[136:139], a[140:143], v[56:59]\n"
"v_mfma_f32_16x16x32_bf16 v[56:59], v[152:155], a[156:159], v[56:59]\n"
"ds_read_b64_tr_b16 v[190:191], v245 offset:12800\n"
"v_mfma_f32_16x16x32_bf16 v[12:15], v[140:143], a[128:131], v[12:15]\n"
"buffer_load_dwordx4 v238, s[20:23], s92 offen offset:0 lds\n"
"v_mfma_f32_16x16x32_bf16 v[12:15], v[156:159], a[144:147], v[12:15]\n"
"v_mfma_f32_16x16x32_bf16 v[28:31], v[140:143], a[132:135], v[28:31]\n"
"v_mfma_f32_16x16x32_bf16 v[28:31], v[156:159], a[148:151], v[28:31]\n"
"v_mfma_f32_16x16x32_bf16 v[44:47], v[140:143], a[136:139], v[44:47]\n"
"buffer_load_dwordx4 v239, s[20:23], s92 offen offset:1024 lds\n"
"v_mfma_f32_16x16x32_bf16 v[44:47], v[156:159], a[152:155], v[44:47]\n"
"v_mfma_f32_16x16x32_bf16 v[60:63], v[140:143], a[140:143], v[60:63]\n"
"v_mfma_f32_16x16x32_bf16 v[60:63], v[156:159], a[156:159], v[60:63]\n"
"s_waitcnt vmcnt(33) lgkmcnt(0)\n"
"v_mfma_f32_16x16x32_bf16 v[0:3], v[160:163], a[160:163], v[0:3]\n"
"ds_read_b64_tr_b16 v[128:129], v242 offset:16384\n"
"v_mfma_f32_16x16x32_bf16 v[0:3], v[176:179], a[176:179], v[0:3]\n"
"ds_read_b64_tr_b16 v[130:131], v242 offset:16896\n"
"v_mfma_f32_16x16x32_bf16 v[16:19], v[160:163], a[164:167], v[16:19]\n"
"v_mfma_f32_16x16x32_bf16 v[16:19], v[176:179], a[180:183], v[16:19]\n"
"ds_read_b64_tr_b16 v[132:133], v243 offset:16384\n"
"v_mfma_f32_16x16x32_bf16 v[32:35], v[160:163], a[168:171], v[32:35]\n"
"ds_read_b64_tr_b16 v[134:135], v243 offset:16896\n"
"v_mfma_f32_16x16x32_bf16 v[32:35], v[176:179], a[184:187], v[32:35]\n"
"v_mfma_f32_16x16x32_bf16 v[48:51], v[160:163], a[172:175], v[48:51]\n"
"ds_read_b64_tr_b16 v[136:137], v244 offset:16384\n"
"v_mfma_f32_16x16x32_bf16 v[48:51], v[176:179], a[188:191], v[48:51]\n"
"ds_read_b64_tr_b16 v[138:139], v244 offset:16896\n"
"v_mfma_f32_16x16x32_bf16 v[4:7], v[164:167], a[160:163], v[4:7]\n"
"v_mfma_f32_16x16x32_bf16 v[4:7], v[180:183], a[176:179], v[4:7]\n"
"ds_read_b64_tr_b16 v[140:141], v245 offset:16384\n"
"v_mfma_f32_16x16x32_bf16 v[20:23], v[164:167], a[164:167], v[20:23]\n"
"ds_read_b64_tr_b16 v[142:143], v245 offset:16896\n"
"v_mfma_f32_16x16x32_bf16 v[20:23], v[180:183], a[180:183], v[20:23]\n"
"v_mfma_f32_16x16x32_bf16 v[36:39], v[164:167], a[168:171], v[36:39]\n"
"ds_read_b64_tr_b16 v[144:145], v242 offset:20480\n"
"v_mfma_f32_16x16x32_bf16 v[36:39], v[180:183], a[184:187], v[36:39]\n"
"v_mfma_f32_16x16x32_bf16 v[52:55], v[164:167], a[172:175], v[52:55]\n"
"ds_read_b64_tr_b16 v[146:147], v242 offset:20992\n"
"v_mfma_f32_16x16x32_bf16 v[52:55], v[180:183], a[188:191], v[52:55]\n"
"ds_read_b64_tr_b16 v[148:149], v243 offset:20480\n"
"v_mfma_f32_16x16x32_bf16 v[8:11], v[168:171], a[160:163], v[8:11]\n"
"v_mfma_f32_16x16x32_bf16 v[8:11], v[184:187], a[176:179], v[8:11]\n"
"ds_read_b64_tr_b16 v[150:151], v243 offset:20992\n"
"v_mfma_f32_16x16x32_bf16 v[24:27], v[168:171], a[164:167], v[24:27]\n"
"ds_read_b64_tr_b16 v[152:153], v244 offset:20480\n"
"v_mfma_f32_16x16x32_bf16 v[24:27], v[184:187], a[180:183], v[24:27]\n"
"v_mfma_f32_16x16x32_bf16 v[40:43], v[168:171], a[168:171], v[40:43]\n"
"ds_read_b64_tr_b16 v[154:155], v244 offset:20992\n"
"v_mfma_f32_16x16x32_bf16 v[40:43], v[184:187], a[184:187], v[40:43]\n"
"ds_read_b64_tr_b16 v[156:157], v245 offset:20480\n"
"v_mfma_f32_16x16x32_bf16 v[56:59], v[168:171], a[172:175], v[56:59]\n"
"v_mfma_f32_16x16x32_bf16 v[56:59], v[184:187], a[188:191], v[56:59]\n"
"ds_read_b64_tr_b16 v[158:159], v245 offset:20992\n"
"v_mfma_f32_16x16x32_bf16 v[12:15], v[172:175], a[160:163], v[12:15]\n"
"v_mfma_f32_16x16x32_bf16 v[12:15], v[188:191], a[176:179], v[12:15]\n"
"v_mfma_f32_16x16x32_bf16 v[28:31], v[172:175], a[164:167], v[28:31]\n"
"v_mfma_f32_16x16x32_bf16 v[28:31], v[188:191], a[180:183], v[28:31]\n"
"v_mfma_f32_16x16x32_bf16 v[44:47], v[172:175], a[168:171], v[44:47]\n"
"v_mfma_f32_16x16x32_bf16 v[44:47], v[188:191], a[184:187], v[44:47]\n"
"v_mfma_f32_16x16x32_bf16 v[60:63], v[172:175], a[172:175], v[60:63]\n"
"v_mfma_f32_16x16x32_bf16 v[60:63], v[188:191], a[188:191], v[60:63]\n"
"s_waitcnt vmcnt(24) lgkmcnt(0)\n"
"v_mfma_f32_16x16x32_bf16 v[0:3], v[128:131], a[192:195], v[0:3]\n"
"ds_read_b64_tr_b16 v[160:161], v242 offset:24576\n"
"v_mfma_f32_16x16x32_bf16 v[0:3], v[144:147], a[208:211], v[0:3]\n"
"ds_read_b64_tr_b16 v[162:163], v242 offset:25088\n"
"v_mfma_f32_16x16x32_bf16 v[16:19], v[128:131], a[196:199], v[16:19]\n"
"v_mfma_f32_16x16x32_bf16 v[16:19], v[144:147], a[212:215], v[16:19]\n"
"ds_read_b64_tr_b16 v[164:165], v243 offset:24576\n"
"v_mfma_f32_16x16x32_bf16 v[32:35], v[128:131], a[200:203], v[32:35]\n"
"ds_read_b64_tr_b16 v[166:167], v243 offset:25088\n"
"v_mfma_f32_16x16x32_bf16 v[32:35], v[144:147], a[216:219], v[32:35]\n"
"v_mfma_f32_16x16x32_bf16 v[48:51], v[128:131], a[204:207], v[48:51]\n"
"ds_read_b64_tr_b16 v[168:169], v244 offset:24576\n"
"v_mfma_f32_16x16x32_bf16 v[48:51], v[144:147], a[220:223], v[48:51]\n"
"ds_read_b64_tr_b16 v[170:171], v244 offset:25088\n"
"v_mfma_f32_16x16x32_bf16 v[4:7], v[132:135], a[192:195], v[4:7]\n"
"v_mfma_f32_16x16x32_bf16 v[4:7], v[148:151], a[208:211], v[4:7]\n"
"ds_read_b64_tr_b16 v[172:173], v245 offset:24576\n"
"v_mfma_f32_16x16x32_bf16 v[20:23], v[132:135], a[196:199], v[20:23]\n"
"ds_read_b64_tr_b16 v[174:175], v245 offset:25088\n"
"v_mfma_f32_16x16x32_bf16 v[20:23], v[148:151], a[212:215], v[20:23]\n"
"v_mfma_f32_16x16x32_bf16 v[36:39], v[132:135], a[200:203], v[36:39]\n"
"ds_read_b64_tr_b16 v[176:177], v242 offset:28672\n"
"v_mfma_f32_16x16x32_bf16 v[36:39], v[148:151], a[216:219], v[36:39]\n"
"v_mfma_f32_16x16x32_bf16 v[52:55], v[132:135], a[204:207], v[52:55]\n"
"ds_read_b64_tr_b16 v[178:179], v242 offset:29184\n"
"v_mfma_f32_16x16x32_bf16 v[52:55], v[148:151], a[220:223], v[52:55]\n"
"ds_read_b64_tr_b16 v[180:181], v243 offset:28672\n"
"v_mfma_f32_16x16x32_bf16 v[8:11], v[136:139], a[192:195], v[8:11]\n"
"v_mfma_f32_16x16x32_bf16 v[8:11], v[152:155], a[208:211], v[8:11]\n"
"ds_read_b64_tr_b16 v[182:183], v243 offset:29184\n"
"v_mfma_f32_16x16x32_bf16 v[24:27], v[136:139], a[196:199], v[24:27]\n"
"ds_read_b64_tr_b16 v[184:185], v244 offset:28672\n"
"v_mfma_f32_16x16x32_bf16 v[24:27], v[152:155], a[212:215], v[24:27]\n"
"v_mfma_f32_16x16x32_bf16 v[40:43], v[136:139], a[200:203], v[40:43]\n"
"ds_read_b64_tr_b16 v[186:187], v244 offset:29184\n"
"v_mfma_f32_16x16x32_bf16 v[40:43], v[152:155], a[216:219], v[40:43]\n"
"ds_read_b64_tr_b16 v[188:189], v245 offset:28672\n"
"v_mfma_f32_16x16x32_bf16 v[56:59], v[136:139], a[204:207], v[56:59]\n"
"v_mfma_f32_16x16x32_bf16 v[56:59], v[152:155], a[220:223], v[56:59]\n"
"ds_read_b64_tr_b16 v[190:191], v245 offset:29184\n"
"v_mfma_f32_16x16x32_bf16 v[12:15], v[140:143], a[192:195], v[12:15]\n"
"v_mfma_f32_16x16x32_bf16 v[12:15], v[156:159], a[208:211], v[12:15]\n"
"v_mfma_f32_16x16x32_bf16 v[28:31], v[140:143], a[196:199], v[28:31]\n"
"v_mfma_f32_16x16x32_bf16 v[28:31], v[156:159], a[212:215], v[28:31]\n"
"v_mfma_f32_16x16x32_bf16 v[44:47], v[140:143], a[200:203], v[44:47]\n"
"v_mfma_f32_16x16x32_bf16 v[44:47], v[156:159], a[216:219], v[44:47]\n"
"v_mfma_f32_16x16x32_bf16 v[60:63], v[140:143], a[204:207], v[60:63]\n"
"v_mfma_f32_16x16x32_bf16 v[60:63], v[156:159], a[220:223], v[60:63]\n"
"s_waitcnt vmcnt(16) lgkmcnt(0)\n"
"s_barrier\n"
"v_mfma_f32_16x16x32_bf16 v[0:3], v[160:163], a[224:227], v[0:3]\n"
"v_add_u32 v242, 0xffff0000, v242\n"
"v_mfma_f32_16x16x32_bf16 v[0:3], v[176:179], a[240:243], v[0:3]\n"
"v_add_u32 v243, 0xffff0000, v243\n"
"v_mfma_f32_16x16x32_bf16 v[16:19], v[160:163], a[228:231], v[16:19]\n"
"v_add_u32 v244, 0xffff0000, v244\n"
"v_mfma_f32_16x16x32_bf16 v[16:19], v[176:179], a[244:247], v[16:19]\n"
"v_add_u32 v245, 0xffff0000, v245\n"
"v_mfma_f32_16x16x32_bf16 v[32:35], v[160:163], a[232:235], v[32:35]\n"
"v_mfma_f32_16x16x32_bf16 v[32:35], v[176:179], a[248:251], v[32:35]\n"
"ds_read_b64_tr_b16 v[128:129], v242 offset:0\n"
"v_mfma_f32_16x16x32_bf16 v[48:51], v[160:163], a[236:239], v[48:51]\n"
"ds_read_b64_tr_b16 v[130:131], v242 offset:512\n"
"v_mfma_f32_16x16x32_bf16 v[48:51], v[176:179], a[252:255], v[48:51]\n"
"ds_read_b64_tr_b16 v[132:133], v243 offset:0\n"
"v_mfma_f32_16x16x32_bf16 v[4:7], v[164:167], a[224:227], v[4:7]\n"
"ds_read_b64_tr_b16 v[134:135], v243 offset:512\n"
"v_mfma_f32_16x16x32_bf16 v[4:7], v[180:183], a[240:243], v[4:7]\n"
"v_mfma_f32_16x16x32_bf16 v[20:23], v[164:167], a[228:231], v[20:23]\n"
"ds_read_b64_tr_b16 v[136:137], v244 offset:0\n"
"v_mfma_f32_16x16x32_bf16 v[20:23], v[180:183], a[244:247], v[20:23]\n"
"ds_read_b64_tr_b16 v[138:139], v244 offset:512\n"
"v_mfma_f32_16x16x32_bf16 v[36:39], v[164:167], a[232:235], v[36:39]\n"
"ds_read_b64_tr_b16 v[140:141], v245 offset:0\n"
"v_mfma_f32_16x16x32_bf16 v[36:39], v[180:183], a[248:251], v[36:39]\n"
"ds_read_b64_tr_b16 v[142:143], v245 offset:512\n"
"v_mfma_f32_16x16x32_bf16 v[52:55], v[164:167], a[236:239], v[52:55]\n"
"v_mfma_f32_16x16x32_bf16 v[52:55], v[180:183], a[252:255], v[52:55]\n"
"ds_read_b64_tr_b16 v[144:145], v242 offset:4096\n"
"v_mfma_f32_16x16x32_bf16 v[8:11], v[168:171], a[224:227], v[8:11]\n"
"ds_read_b64_tr_b16 v[146:147], v242 offset:4608\n"
"v_mfma_f32_16x16x32_bf16 v[8:11], v[184:187], a[240:243], v[8:11]\n"
"ds_read_b64_tr_b16 v[148:149], v243 offset:4096\n"
"v_mfma_f32_16x16x32_bf16 v[24:27], v[168:171], a[228:231], v[24:27]\n"
"ds_read_b64_tr_b16 v[150:151], v243 offset:4608\n"
"v_mfma_f32_16x16x32_bf16 v[24:27], v[184:187], a[244:247], v[24:27]\n"
"v_mfma_f32_16x16x32_bf16 v[40:43], v[168:171], a[232:235], v[40:43]\n"
"ds_read_b64_tr_b16 v[152:153], v244 offset:4096\n"
"v_mfma_f32_16x16x32_bf16 v[40:43], v[184:187], a[248:251], v[40:43]\n"
"ds_read_b64_tr_b16 v[154:155], v244 offset:4608\n"
"v_mfma_f32_16x16x32_bf16 v[56:59], v[168:171], a[236:239], v[56:59]\n"
"ds_read_b64_tr_b16 v[156:157], v245 offset:4096\n"
"v_mfma_f32_16x16x32_bf16 v[56:59], v[184:187], a[252:255], v[56:59]\n"
"ds_read_b64_tr_b16 v[158:159], v245 offset:4608\n"
"v_mfma_f32_16x16x32_bf16 v[12:15], v[172:175], a[224:227], v[12:15]\n"
"v_mfma_f32_16x16x32_bf16 v[12:15], v[188:191], a[240:243], v[12:15]\n"
"v_mfma_f32_16x16x32_bf16 v[28:31], v[172:175], a[228:231], v[28:31]\n"
"v_mfma_f32_16x16x32_bf16 v[28:31], v[188:191], a[244:247], v[28:31]\n"
"v_mfma_f32_16x16x32_bf16 v[44:47], v[172:175], a[232:235], v[44:47]\n"
"v_mfma_f32_16x16x32_bf16 v[44:47], v[188:191], a[248:251], v[44:47]\n"
"v_mfma_f32_16x16x32_bf16 v[60:63], v[172:175], a[236:239], v[60:63]\n"
"v_mfma_f32_16x16x32_bf16 v[60:63], v[188:191], a[252:255], v[60:63]\n"
"s_waitcnt lgkmcnt(0)\n"
"v_mfma_f32_16x16x32_bf16 v[64:67], v[128:131], a[0:3], 0\n"
"ds_read_b64_tr_b16 v[160:161], v242 offset:8192\n"
"v_mfma_f32_16x16x32_bf16 v[64:67], v[144:147], a[16:19], v[64:67]\n"
"ds_read_b64_tr_b16 v[162:163], v242 offset:8704\n"
"v_mfma_f32_16x16x32_bf16 v[80:83], v[128:131], a[4:7], 0\n"
"v_mfma_f32_16x16x32_bf16 v[80:83], v[144:147], a[20:23], v[80:83]\n"
"ds_read_b64_tr_b16 v[164:165], v243 offset:8192\n"
"v_mfma_f32_16x16x32_bf16 v[96:99], v[128:131], a[8:11], 0\n"
"ds_read_b64_tr_b16 v[166:167], v243 offset:8704\n"
"v_mfma_f32_16x16x32_bf16 v[96:99], v[144:147], a[24:27], v[96:99]\n"
"v_mfma_f32_16x16x32_bf16 v[112:115], v[128:131], a[12:15], 0\n"
"ds_read_b64_tr_b16 v[168:169], v244 offset:8192\n"
"v_cvt_pk_bf16_f32 v192, v0, v1\n"
"v_mfma_f32_16x16x32_bf16 v[112:115], v[144:147], a[28:31], v[112:115]\n"
"ds_read_b64_tr_b16 v[170:171], v244 offset:8704\n"
"v_mfma_f32_16x16x32_bf16 v[68:71], v[132:135], a[0:3], 0\n"
"s_lshl_b32 s95, s84, 7\n"
"v_mfma_f32_16x16x32_bf16 v[68:71], v[148:151], a[16:19], v[68:71]\n"
"ds_read_b64_tr_b16 v[172:173], v245 offset:8192\n"
"s_lshl_b32 s94, s83, 2\n"
"v_mfma_f32_16x16x32_bf16 v[84:87], v[132:135], a[4:7], 0\n"
"v_cvt_pk_bf16_f32 v194, v16, v17\n"
"ds_read_b64_tr_b16 v[174:175], v245 offset:8704\n"
"v_mfma_f32_16x16x32_bf16 v[84:87], v[148:151], a[20:23], v[84:87]\n"
"s_add_u32 s95, s95, s94\n"
"s_add_u32 s95, s95, 0x10000\n"
"v_mfma_f32_16x16x32_bf16 v[100:103], v[132:135], a[8:11], 0\n"
"ds_read_b64_tr_b16 v[176:177], v242 offset:12288\n"
"v_mfma_f32_16x16x32_bf16 v[100:103], v[148:151], a[24:27], v[100:103]\n"
"s_waitcnt vmcnt(4)\n"
"v_cvt_pk_bf16_f32 v193, v2, v3\n"
"v_mfma_f32_16x16x32_bf16 v[116:119], v[132:135], a[12:15], 0\n"
"ds_read_b64_tr_b16 v[178:179], v242 offset:12800\n"
"v_mov_b32 v235, s82\n"
"v_mfma_f32_16x16x32_bf16 v[116:119], v[148:151], a[28:31], v[116:119]\n"
"ds_read_b64_tr_b16 v[180:181], v243 offset:12288\n"
"s_mov_b64 exec, 1\n"
"buffer_store_dword v235, off, s[88:91], s95 sc1\n"
"s_mov_b64 exec, -1\n"
"v_mfma_f32_16x16x32_bf16 v[72:75], v[136:139], a[0:3], 0\n"
"v_mfma_f32_16x16x32_bf16 v[72:75], v[152:155], a[16:19], v[72:75]\n"
"ds_read_b64_tr_b16 v[182:183], v243 offset:12800\n"
"v_mfma_f32_16x16x32_bf16 v[88:91], v[136:139], a[4:7], 0\n"
"v_cvt_pk_bf16_f32 v195, v18, v19\n"
"ds_read_b64_tr_b16 v[184:185], v244 offset:12288\n"
"v_mfma_f32_16x16x32_bf16 v[88:91], v[152:155], a[20:23], v[88:91]\n"
"v_mfma_f32_16x16x32_bf16 v[104:107], v[136:139], a[8:11], 0\n"
"ds_read_b64_tr_b16 v[186:187], v244 offset:12800\n"
"v_mfma_f32_16x16x32_bf16 v[104:107], v[152:155], a[24:27], v[104:107]\n"
"ds_read_b64_tr_b16 v[188:189], v245 offset:12288\n"
"s_nop 0\n"
"v_permlane16_swap_b32 v192, v194\n"
"v_mfma_f32_16x16x32_bf16 v[120:123], v[136:139], a[12:15], 0\n"
"v_mfma_f32_16x16x32_bf16 v[120:123], v[152:155], a[28:31], v[120:123]\n"
"ds_read_b64_tr_b16 v[190:191], v245 offset:12800\n"
"v_mfma_f32_16x16x32_bf16 v[76:79], v[140:143], a[0:3], 0\n"
"v_mfma_f32_16x16x32_bf16 v[76:79], v[156:159], a[16:19], v[76:79]\n"
"v_permlane16_swap_b32 v193, v195\n"
"v_mfma_f32_16x16x32_bf16 v[92:95], v[140:143], a[4:7], 0\n"
"v_mfma_f32_16x16x32_bf16 v[92:95], v[156:159], a[20:23], v[92:95]\n"
"v_mfma_f32_16x16x32_bf16 v[108:111], v[140:143], a[8:11], 0\n"
"v_mfma_f32_16x16x32_bf16 v[108:111], v[156:159], a[24:27], v[108:111]\n"
"v_mfma_f32_16x16x32_bf16 v[124:127], v[140:143], a[12:15], 0\n"
"v_mfma_f32_16x16x32_bf16 v[124:127], v[156:159], a[28:31], v[124:127]\n"
"s_waitcnt lgkmcnt(0)\n"
"v_mfma_f32_16x16x32_bf16 v[64:67], v[160:163], a[32:35], v[64:67]\n"
"ds_read_b64_tr_b16 v[128:129], v242 offset:16384\n"
"v_mfma_f32_16x16x32_bf16 v[64:67], v[176:179], a[48:51], v[64:67]\n"
"ds_read_b64_tr_b16 v[130:131], v242 offset:16896\n"
"v_mfma_f32_16x16x32_bf16 v[80:83], v[160:163], a[36:39], v[80:83]\n"
"v_mfma_f32_16x16x32_bf16 v[80:83], v[176:179], a[52:55], v[80:83]\n"
"ds_read_b64_tr_b16 v[132:133], v243 offset:16384\n"
"s_and_b32 s95, s84, 31\n"
"v_mfma_f32_16x16x32_bf16 v[96:99], v[160:163], a[40:43], v[96:99]\n"
"ds_read_b64_tr_b16 v[134:135], v243 offset:16896\n"
"s_lshl_b32 s95, s95, 7\n"
"v_mfma_f32_16x16x32_bf16 v[96:99], v[176:179], a[56:59], v[96:99]\n"
"s_add_u32 s97, s95, 0x10000\n"
"v_mfma_f32_16x16x32_bf16 v[112:115], v[160:163], a[44:47], v[112:115]\n"
"ds_read_b64_tr_b16 v[136:137], v244 offset:16384\n"
"v_bfe_u32 v232, v255, 2, 3\n"
"v_mfma_f32_16x16x32_bf16 v[112:115], v[176:179], a[60:63], v[112:115]\n"
"v_cvt_pk_bf16_f32 v196, v4, v5\n"
"ds_read_b64_tr_b16 v[138:139], v244 offset:16896\n"
"v_mfma_f32_16x16x32_bf16 v[68:71], v[164:167], a[32:35], v[68:71]\n"
"v_lshlrev_b32 v232, 12, v232\n"
"v_mfma_f32_16x16x32_bf16 v[68:71], v[180:183], a[48:51], v[68:71]\n"
"ds_read_b64_tr_b16 v[140:141], v245 offset:16384\n"
"v_and_b32 v233, 3, v255\n"
"v_mfma_f32_16x16x32_bf16 v[84:87], v[164:167], a[36:39], v[84:87]\n"
"v_cvt_pk_bf16_f32 v198, v20, v21\n"
"ds_read_b64_tr_b16 v[142:143], v245 offset:16896\n"
"v_mfma_f32_16x16x32_bf16 v[84:87], v[180:183], a[52:55], v[84:87]\n"
"v_lshl_add_u32 v232, v233, 2, v232\n"
"v_mfma_f32_16x16x32_bf16 v[100:103], v[164:167], a[40:43], v[100:103]\n"
"ds_read_b64_tr_b16 v[144:145], v242 offset:20480\n"
"v_mfma_f32_16x16x32_bf16 v[100:103], v[180:183], a[56:59], v[100:103]\n"
"v_cvt_pk_bf16_f32 v197, v6, v7\n"
"v_mfma_f32_16x16x32_bf16 v[116:119], v[164:167], a[44:47], v[116:119]\n"
"ds_read_b64_tr_b16 v[146:147], v242 offset:20992\n"
"v_mfma_f32_16x16x32_bf16 v[116:119], v[180:183], a[60:63], v[116:119]\n"
"ds_read_b64_tr_b16 v[148:149], v243 offset:20480\n"
"v_mfma_f32_16x16x32_bf16 v[72:75], v[168:171], a[32:35], v[72:75]\n"
"v_mfma_f32_16x16x32_bf16 v[72:75], v[184:187], a[48:51], v[72:75]\n"
"ds_read_b64_tr_b16 v[150:151], v243 offset:20992\n"
"v_cvt_pk_bf16_f32 v199, v22, v23\n"
"v_mfma_f32_16x16x32_bf16 v[88:91], v[168:171], a[36:39], v[88:91]\n"
"ds_read_b64_tr_b16 v[152:153], v244 offset:20480\n"
"v_mfma_f32_16x16x32_bf16 v[88:91], v[184:187], a[52:55], v[88:91]\n"
"v_mfma_f32_16x16x32_bf16 v[104:107], v[168:171], a[40:43], v[104:107]\n"
"ds_read_b64_tr_b16 v[154:155], v244 offset:20992\n"
"v_mfma_f32_16x16x32_bf16 v[104:107], v[184:187], a[56:59], v[104:107]\n"
"ds_read_b64_tr_b16 v[156:157], v245 offset:20480\n"
"s_nop 0\n"
"v_permlane16_swap_b32 v196, v198\n"
"v_mfma_f32_16x16x32_bf16 v[120:123], v[168:171], a[44:47], v[120:123]\n"
"v_mfma_f32_16x16x32_bf16 v[120:123], v[184:187], a[60:63], v[120:123]\n"
"ds_read_b64_tr_b16 v[158:159], v245 offset:20992\n"
"v_mfma_f32_16x16x32_bf16 v[76:79], v[172:175], a[32:35], v[76:79]\n"
"v_mfma_f32_16x16x32_bf16 v[76:79], v[188:191], a[48:51], v[76:79]\n"
"v_permlane16_swap_b32 v197, v199\n"
"v_mfma_f32_16x16x32_bf16 v[92:95], v[172:175], a[36:39], v[92:95]\n"
"v_mfma_f32_16x16x32_bf16 v[92:95], v[188:191], a[52:55], v[92:95]\n"
"v_mfma_f32_16x16x32_bf16 v[108:111], v[172:175], a[40:43], v[108:111]\n"
"v_mfma_f32_16x16x32_bf16 v[108:111], v[188:191], a[56:59], v[108:111]\n"
"v_mfma_f32_16x16x32_bf16 v[124:127], v[172:175], a[44:47], v[124:127]\n"
"v_mfma_f32_16x16x32_bf16 v[124:127], v[188:191], a[60:63], v[124:127]\n"
"s_waitcnt lgkmcnt(0)\n"
"v_mfma_f32_16x16x32_bf16 v[64:67], v[128:131], a[64:67], v[64:67]\n"
"ds_read_b64_tr_b16 v[160:161], v242 offset:24576\n"
"v_mfma_f32_16x16x32_bf16 v[64:67], v[144:147], a[80:83], v[64:67]\n"
"ds_read_b64_tr_b16 v[162:163], v242 offset:25088\n"
"v_mfma_f32_16x16x32_bf16 v[80:83], v[128:131], a[68:71], v[80:83]\n"
"v_mfma_f32_16x16x32_bf16 v[80:83], v[144:147], a[84:87], v[80:83]\n"
"ds_read_b64_tr_b16 v[164:165], v243 offset:24576\n"
"v_mfma_f32_16x16x32_bf16 v[96:99], v[128:131], a[72:75], v[96:99]\n"
"ds_read_b64_tr_b16 v[166:167], v243 offset:25088\n"
"v_mfma_f32_16x16x32_bf16 v[96:99], v[144:147], a[88:91], v[96:99]\n"
"v_mfma_f32_16x16x32_bf16 v[112:115], v[128:131], a[76:79], v[112:115]\n"
"ds_read_b64_tr_b16 v[168:169], v244 offset:24576\n"
"v_cvt_pk_bf16_f32 v200, v8, v9\n"
"v_mfma_f32_16x16x32_bf16 v[112:115], v[144:147], a[92:95], v[112:115]\n"
"ds_read_b64_tr_b16 v[170:171], v244 offset:25088\n"
"v_mfma_f32_16x16x32_bf16 v[68:71], v[132:135], a[64:67], v[68:71]\n"
"v_mfma_f32_16x16x32_bf16 v[68:71], v[148:151], a[80:83], v[68:71]\n"
"ds_read_b64_tr_b16 v[172:173], v245 offset:24576\n"
"v_cvt_pk_bf16_f32 v202, v24, v25\n"
"v_mfma_f32_16x16x32_bf16 v[84:87], v[132:135], a[68:71], v[84:87]\n"
"ds_read_b64_tr_b16 v[174:175], v245 offset:25088\n"
"v_mfma_f32_16x16x32_bf16 v[84:87], v[148:151], a[84:87], v[84:87]\n"
"v_mfma_f32_16x16x32_bf16 v[100:103], v[132:135], a[72:75], v[100:103]\n"
"ds_read_b64_tr_b16 v[176:177], v242 offset:28672\n"
"v_mfma_f32_16x16x32_bf16 v[100:103], v[148:151], a[88:91], v[100:103]\n"
"v_cvt_pk_bf16_f32 v201, v10, v11\n"
"v_mfma_f32_16x16x32_bf16 v[116:119], v[132:135], a[76:79], v[116:119]\n"
"ds_read_b64_tr_b16 v[178:179], v242 offset:29184\n"
"v_mfma_f32_16x16x32_bf16 v[116:119], v[148:151], a[92:95], v[116:119]\n"
"ds_read_b64_tr_b16 v[180:181], v243 offset:28672\n"
"v_mfma_f32_16x16x32_bf16 v[72:75], v[136:139], a[64:67], v[72:75]\n"
"v_mfma_f32_16x16x32_bf16 v[72:75], v[152:155], a[80:83], v[72:75]\n"
"ds_read_b64_tr_b16 v[182:183], v243 offset:29184\n"
"v_cvt_pk_bf16_f32 v203, v26, v27\n"
"v_mfma_f32_16x16x32_bf16 v[88:91], v[136:139], a[68:71], v[88:91]\n"
"ds_read_b64_tr_b16 v[184:185], v244 offset:28672\n"
"v_mfma_f32_16x16x32_bf16 v[88:91], v[152:155], a[84:87], v[88:91]\n"
"v_mfma_f32_16x16x32_bf16 v[104:107], v[136:139], a[72:75], v[104:107]\n"
"ds_read_b64_tr_b16 v[186:187], v244 offset:29184\n"
"v_mfma_f32_16x16x32_bf16 v[104:107], v[152:155], a[88:91], v[104:107]\n"
"ds_read_b64_tr_b16 v[188:189], v245 offset:28672\n"
"s_nop 0\n"
"v_permlane16_swap_b32 v200, v202\n"
"v_mfma_f32_16x16x32_bf16 v[120:123], v[136:139], a[76:79], v[120:123]\n"
"v_mfma_f32_16x16x32_bf16 v[120:123], v[152:155], a[92:95], v[120:123]\n"
"ds_read_b64_tr_b16 v[190:191], v245 offset:29184\n"
"v_mfma_f32_16x16x32_bf16 v[76:79], v[140:143], a[64:67], v[76:79]\n"
"v_mfma_f32_16x16x32_bf16 v[76:79], v[156:159], a[80:83], v[76:79]\n"
"v_permlane16_swap_b32 v201, v203\n"
"v_mfma_f32_16x16x32_bf16 v[92:95], v[140:143], a[68:71], v[92:95]\n"
"v_mfma_f32_16x16x32_bf16 v[92:95], v[156:159], a[84:87], v[92:95]\n"
"v_mfma_f32_16x16x32_bf16 v[108:111], v[140:143], a[72:75], v[108:111]\n"
"buffer_load_dword v233, v232, s[88:91], s97 offen sc1\n"
"v_mfma_f32_16x16x32_bf16 v[108:111], v[156:159], a[88:91], v[108:111]\n"
"v_mfma_f32_16x16x32_bf16 v[124:127], v[140:143], a[76:79], v[124:127]\n"
"v_mfma_f32_16x16x32_bf16 v[124:127], v[156:159], a[92:95], v[124:127]\n"
"s_waitcnt vmcnt(2) lgkmcnt(0)\n"
"s_barrier\n"
"v_mfma_f32_16x16x32_bf16 v[64:67], v[160:163], a[96:99], v[64:67]\n"
"v_add_u32 v242, 0x20000, v242\n"
"v_mfma_f32_16x16x32_bf16 v[64:67], v[176:179], a[112:115], v[64:67]\n"
"v_add_u32 v243, 0x20000, v243\n"
"v_mfma_f32_16x16x32_bf16 v[80:83], v[160:163], a[100:103], v[80:83]\n"
"v_add_u32 v244, 0x20000, v244\n"
"v_mfma_f32_16x16x32_bf16 v[80:83], v[176:179], a[116:119], v[80:83]\n"
"v_add_u32 v245, 0x20000, v245\n"
"s_lshl_b32 s95, s86, 15\n"
"v_mfma_f32_16x16x32_bf16 v[96:99], v[160:163], a[104:107], v[96:99]\n"
"v_mfma_f32_16x16x32_bf16 v[96:99], v[176:179], a[120:123], v[96:99]\n"
"ds_read_b64_tr_b16 v[128:129], v242 offset:0\n"
"v_mfma_f32_16x16x32_bf16 v[112:115], v[160:163], a[108:111], v[112:115]\n"
"ds_read_b64_tr_b16 v[130:131], v242 offset:512\n"
"s_sub_u32 s30, 0x80000, s95\n"
"v_mfma_f32_16x16x32_bf16 v[112:115], v[176:179], a[124:127], v[112:115]\n"
"v_cvt_pk_bf16_f32 v204, v12, v13\n"
"ds_read_b64_tr_b16 v[132:133], v243 offset:0\n"
"v_mfma_f32_16x16x32_bf16 v[68:71], v[164:167], a[96:99], v[68:71]\n"
"ds_read_b64_tr_b16 v[134:135], v243 offset:512\n"
"v_mfma_f32_16x16x32_bf16 v[68:71], v[180:183], a[112:115], v[68:71]\n"
"s_mov_b32 s38, s30\n"
"v_cvt_pk_bf16_f32 v206, v28, v29\n"
"v_mfma_f32_16x16x32_bf16 v[84:87], v[164:167], a[100:103], v[84:87]\n"
"ds_read_b64_tr_b16 v[136:137], v244 offset:0\n"
"v_mfma_f32_16x16x32_bf16 v[84:87], v[180:183], a[116:119], v[84:87]\n"
"ds_read_b64_tr_b16 v[138:139], v244 offset:512\n"
"v_mfma_f32_16x16x32_bf16 v[100:103], v[164:167], a[104:107], v[100:103]\n"
"ds_read_b64_tr_b16 v[140:141], v245 offset:0\n"
"v_mfma_f32_16x16x32_bf16 v[100:103], v[180:183], a[120:123], v[100:103]\n"
"ds_read_b64_tr_b16 v[142:143], v245 offset:512\n"
"v_cvt_pk_bf16_f32 v205, v14, v15\n"
"v_mfma_f32_16x16x32_bf16 v[116:119], v[164:167], a[108:111], v[116:119]\n"
"v_mfma_f32_16x16x32_bf16 v[116:119], v[180:183], a[124:127], v[116:119]\n"
"ds_read_b64_tr_b16 v[144:145], v242 offset:4096\n"
"v_mfma_f32_16x16x32_bf16 v[72:75], v[168:171], a[96:99], v[72:75]\n"
"ds_read_b64_tr_b16 v[146:147], v242 offset:4608\n"
"v_mfma_f32_16x16x32_bf16 v[72:75], v[184:187], a[112:115], v[72:75]\n"
"ds_read_b64_tr_b16 v[148:149], v243 offset:4096\n"
"v_cvt_pk_bf16_f32 v207, v30, v31\n"
"v_mfma_f32_16x16x32_bf16 v[88:91], v[168:171], a[100:103], v[88:91]\n"
"ds_read_b64_tr_b16 v[150:151], v243 offset:4608\n"
"v_mfma_f32_16x16x32_bf16 v[88:91], v[184:187], a[116:119], v[88:91]\n"
"v_mfma_f32_16x16x32_bf16 v[104:107], v[168:171], a[104:107], v[104:107]\n"
"ds_read_b64_tr_b16 v[152:153], v244 offset:4096\n"
"v_mfma_f32_16x16x32_bf16 v[104:107], v[184:187], a[120:123], v[104:107]\n"
"ds_read_b64_tr_b16 v[154:155], v244 offset:4608\n"
"s_nop 0\n"
"v_permlane16_swap_b32 v204, v206\n"
"v_mfma_f32_16x16x32_bf16 v[120:123], v[168:171], a[108:111], v[120:123]\n"
"ds_read_b64_tr_b16 v[156:157], v245 offset:4096\n"
"v_mfma_f32_16x16x32_bf16 v[120:123], v[184:187], a[124:127], v[120:123]\n"
"ds_read_b64_tr_b16 v[158:159], v245 offset:4608\n"
"v_mfma_f32_16x16x32_bf16 v[76:79], v[172:175], a[96:99], v[76:79]\n"
"v_mfma_f32_16x16x32_bf16 v[76:79], v[188:191], a[112:115], v[76:79]\n"
"v_permlane16_swap_b32 v205, v207\n"
"v_mfma_f32_16x16x32_bf16 v[92:95], v[172:175], a[100:103], v[92:95]\n"
"v_mfma_f32_16x16x32_bf16 v[92:95], v[188:191], a[116:119], v[92:95]\n"
"v_mfma_f32_16x16x32_bf16 v[108:111], v[172:175], a[104:107], v[108:111]\n"
"v_mfma_f32_16x16x32_bf16 v[108:111], v[188:191], a[120:123], v[108:111]\n"
"v_mfma_f32_16x16x32_bf16 v[124:127], v[172:175], a[108:111], v[124:127]\n"
"v_mfma_f32_16x16x32_bf16 v[124:127], v[188:191], a[124:127], v[124:127]\n"
"s_waitcnt lgkmcnt(0)\n"
"s_waitcnt vmcnt(0)\n"
"v_cmp_ne_u32 vcc, s82, v233\n"
"s_cbranch_vccnz Lps15_%=\n"
"Lpb14_%=:\n"
"v_mfma_f32_16x16x32_bf16 v[64:67], v[128:131], a[128:131], v[64:67]\n"
"ds_read_b64_tr_b16 v[160:161], v242 offset:8192\n"
"s_add_u32 s92, s54, 0\n"
"v_mfma_f32_16x16x32_bf16 v[64:67], v[144:147], a[144:147], v[64:67]\n"
"ds_read_b64_tr_b16 v[162:163], v242 offset:8704\n"
"v_mfma_f32_16x16x32_bf16 v[80:83], v[128:131], a[132:135], v[80:83]\n"
"v_mfma_f32_16x16x32_bf16 v[80:83], v[144:147], a[148:151], v[80:83]\n"
"ds_read_b64_tr_b16 v[164:165], v243 offset:8192\n"
"s_add_u32 m0, s54, 0\n"
"v_mfma_f32_16x16x32_bf16 v[96:99], v[128:131], a[136:139], v[96:99]\n"
"ds_read_b64_tr_b16 v[166:167], v243 offset:8704\n"
"v_mfma_f32_16x16x32_bf16 v[96:99], v[144:147], a[152:155], v[96:99]\n"
"v_mfma_f32_16x16x32_bf16 v[112:115], v[128:131], a[140:143], v[112:115]\n"
"ds_read_b64_tr_b16 v[168:169], v244 offset:8192\n"
"s_nop 0\n"
"buffer_load_dwordx4 v248, s[28:31], s92 offen offset:0 sc1 lds\n"
"v_mfma_f32_16x16x32_bf16 v[112:115], v[144:147], a[156:159], v[112:115]\n"
"v_mfma_f32_16x16x32_bf16 v[68:71], v[132:135], a[128:131], v[68:71]\n"
"v_cvt_pk_bf16_f32 v208, v32, v33\n"
"ds_read_b64_tr_b16 v[170:171], v244 offset:8704\n"
"v_mfma_f32_16x16x32_bf16 v[68:71], v[148:151], a[144:147], v[68:71]\n"
"ds_read_b64_tr_b16 v[172:173], v245 offset:8192\n"
"buffer_load_dwordx4 v248, s[28:31], s92 offen offset:1024 sc1 lds\n"
"v_mfma_f32_16x16x32_bf16 v[84:87], v[132:135], a[132:135], v[84:87]\n"
"v_cvt_pk_bf16_f32 v210, v48, v49\n"
"v_mfma_f32_16x16x32_bf16 v[84:87], v[148:151], a[148:151], v[84:87]\n"
"ds_read_b64_tr_b16 v[174:175], v245 offset:8704\n"
"v_mfma_f32_16x16x32_bf16 v[100:103], v[132:135], a[136:139], v[100:103]\n"
"ds_read_b64_tr_b16 v[176:177], v242 offset:12288\n"
"buffer_load_dwordx4 v248, s[28:31], s92 offen offset:2048 sc1 lds\n"
"v_mfma_f32_16x16x32_bf16 v[100:103], v[148:151], a[152:155], v[100:103]\n"
"v_cvt_pk_bf16_f32 v209, v34, v35\n"
"v_mfma_f32_16x16x32_bf16 v[116:119], v[132:135], a[140:143], v[116:119]\n"
"ds_read_b64_tr_b16 v[178:179], v242 offset:12800\n"
"v_mfma_f32_16x16x32_bf16 v[116:119], v[148:151], a[156:159], v[116:119]\n"
"ds_read_b64_tr_b16 v[180:181], v243 offset:12288\n"
"v_mfma_f32_16x16x32_bf16 v[72:75], v[136:139], a[128:131], v[72:75]\n"
"buffer_load_dwordx4 v248, s[28:31], s92 offen offset:3072 sc1 lds\n"
"v_mfma_f32_16x16x32_bf16 v[72:75], v[152:155], a[144:147], v[72:75]\n"
"ds_read_b64_tr_b16 v[182:183], v243 offset:12800\n"
"v_cvt_pk_bf16_f32 v211, v50, v51\n"
"v_mfma_f32_16x16x32_bf16 v[88:91], v[136:139], a[132:135], v[88:91]\n"
"ds_read_b64_tr_b16 v[184:185], v244 offset:12288\n"
"v_mfma_f32_16x16x32_bf16 v[88:91], v[152:155], a[148:151], v[88:91]\n"
"s_add_u32 s92, s46, 0\n"
"v_mfma_f32_16x16x32_bf16 v[104:107], v[136:139], a[136:139], v[104:107]\n"
"ds_read_b64_tr_b16 v[186:187], v244 offset:12800\n"
"v_mfma_f32_16x16x32_bf16 v[104:107], v[152:155], a[152:155], v[104:107]\n"
"ds_read_b64_tr_b16 v[188:189], v245 offset:12288\n"
"s_nop 0\n"
"v_permlane16_swap_b32 v208, v210\n"
"v_mfma_f32_16x16x32_bf16 v[120:123], v[136:139], a[140:143], v[120:123]\n"
"s_add_u32 m0, s46, 65536\n"
"v_mfma_f32_16x16x32_bf16 v[120:123], v[152:155], a[156:159], v[120:123]\n"
"ds_read_b64_tr_b16 v[190:191], v245 offset:12800\n"
"v_mfma_f32_16x16x32_bf16 v[76:79], v[140:143], a[128:131], v[76:79]\n"
"v_mfma_f32_16x16x32_bf16 v[76:79], v[156:159], a[144:147], v[76:79]\n"
"s_nop 0\n"
"buffer_load_dwordx4 v248, s[36:39], s92 offen offset:0 sc1 lds\n"
"v_mfma_f32_16x16x32_bf16 v[92:95], v[140:143], a[132:135], v[92:95]\n"
"v_permlane16_swap_b32 v209, v211\n"
"v_mfma_f32_16x16x32_bf16 v[92:95], v[156:159], a[148:151], v[92:95]\n"
"v_mfma_f32_16x16x32_bf16 v[108:111], v[140:143], a[136:139], v[108:111]\n"
"buffer_load_dwordx4 v248, s[36:39], s92 offen offset:1024 sc1 lds\n"
"v_mfma_f32_16x16x32_bf16 v[108:111], v[156:159], a[152:155], v[108:111]\n"
"v_mfma_f32_16x16x32_bf16 v[124:127], v[140:143], a[140:143], v[124:127]\n"
"v_mfma_f32_16x16x32_bf16 v[124:127], v[156:159], a[156:159], v[124:127]\n"
"s_waitcnt lgkmcnt(0)\n"
"v_mfma_f32_16x16x32_bf16 v[64:67], v[160:163], a[160:163], v[64:67]\n"
"ds_read_b64_tr_b16 v[128:129], v242 offset:16384\n"
"s_add_u32 s92, s54, 16384\n"
"v_mfma_f32_16x16x32_bf16 v[64:67], v[176:179], a[176:179], v[64:67]\n"
"ds_read_b64_tr_b16 v[130:131], v242 offset:16896\n"
"s_add_u32 s16, s80, 0x2280000\n"
"s_addc_u32 s17, s81, 0\n"
"v_mfma_f32_16x16x32_bf16 v[80:83], v[160:163], a[164:167], v[80:83]\n"
"s_and_b32 s17, s17, 0xffff\n"
"v_mfma_f32_16x16x32_bf16 v[80:83], v[176:179], a[180:183], v[80:83]\n"
"s_add_u32 m0, s54, 16384\n"
"s_mov_b32 s18, 0x1000000\n"
"v_mfma_f32_16x16x32_bf16 v[96:99], v[160:163], a[168:171], v[96:99]\n"
"s_mov_b32 s19, 0x20000\n"
"ds_read_b64_tr_b16 v[132:133], v243 offset:16384\n"
"v_mfma_f32_16x16x32_bf16 v[96:99], v[176:179], a[184:187], v[96:99]\n"
"s_sub_u32 s20, s66, 4096\n"
"s_subb_u32 s21, s67, 0\n"
"v_mfma_f32_16x16x32_bf16 v[112:115], v[160:163], a[172:175], v[112:115]\n"
"s_and_b32 s21, s21, 0xffff\n"
"ds_read_b64_tr_b16 v[134:135], v243 offset:16896\n"
"v_mfma_f32_16x16x32_bf16 v[112:115], v[176:179], a[188:191], v[112:115]\n"
"s_mov_b32 s22, 0x401000\n"
"s_nop 0\n"
"buffer_load_dwordx4 v248, s[28:31], s92 offen offset:0 sc1 lds\n"
"v_mfma_f32_16x16x32_bf16 v[68:71], v[164:167], a[160:163], v[68:71]\n"
"v_mfma_f32_16x16x32_bf16 v[68:71], v[180:183], a[176:179], v[68:71]\n"
"s_mov_b32 s23, 0x20000\n"
"s_lshl_b32 s53, s83, 14\n"
"v_mfma_f32_16x16x32_bf16 v[84:87], v[164:167], a[164:167], v[84:87]\n"
"ds_read_b64_tr_b16 v[136:137], v244 offset:16384\n"
"s_bfe_u32 s94, s84, 0x20003\n"
"v_mfma_f32_16x16x32_bf16 v[84:87], v[180:183], a[180:183], v[84:87]\n"
"s_lshl_b32 s94, s94, 8\n"
"v_cvt_pk_bf16_f32 v212, v36, v37\n"
"v_mfma_f32_16x16x32_bf16 v[100:103], v[164:167], a[168:171], v[100:103]\n"
"ds_read_b64_tr_b16 v[138:139], v244 offset:16896\n"
"s_add_u32 s53, s53, s94\n"
"v_mfma_f32_16x16x32_bf16 v[100:103], v[180:183], a[184:187], v[100:103]\n"
"buffer_load_dwordx4 v248, s[28:31], s92 offen offset:1024 sc1 lds\n"
"v_mfma_f32_16x16x32_bf16 v[116:119], v[164:167], a[172:175], v[116:119]\n"
"s_and_b32 s94, s84, 7\n"
"s_lshl_b32 s94, s94, 19\n"
"v_mfma_f32_16x16x32_bf16 v[116:119], v[180:183], a[188:191], v[116:119]\n"
"ds_read_b64_tr_b16 v[140:141], v245 offset:16384\n"
"s_add_u32 s53, s53, s94\n"
"v_mfma_f32_16x16x32_bf16 v[72:75], v[168:171], a[160:163], v[72:75]\n"
"s_lshl_b32 s87, s84, 7\n"
"v_cvt_pk_bf16_f32 v214, v52, v53\n"
"v_mfma_f32_16x16x32_bf16 v[72:75], v[184:187], a[176:179], v[72:75]\n"
"ds_read_b64_tr_b16 v[142:143], v245 offset:16896\n"
"buffer_load_dwordx4 v248, s[28:31], s92 offen offset:2048 sc1 lds\n"
"v_mfma_f32_16x16x32_bf16 v[88:91], v[168:171], a[164:167], v[88:91]\n"
"s_lshl_b32 s94, s83, 2\n"
"v_mfma_f32_16x16x32_bf16 v[88:91], v[184:187], a[180:183], v[88:91]\n"
"s_add_u32 s87, s87, s94\n"
"s_add_u32 s87, s87, 0x18000\n"
"v_mfma_f32_16x16x32_bf16 v[104:107], v[168:171], a[168:171], v[104:107]\n"
"ds_read_b64_tr_b16 v[144:145], v242 offset:20480\n"
"s_lshl_b32 s94, s86, 2\n"
"v_mfma_f32_16x16x32_bf16 v[104:107], v[184:187], a[184:187], v[104:107]\n"
"s_add_u32 s47, s94, s83\n"
"buffer_load_dwordx4 v248, s[28:31], s92 offen offset:3072 sc1 lds\n"
"v_mfma_f32_16x16x32_bf16 v[120:123], v[168:171], a[172:175], v[120:123]\n"
"s_sub_u32 s95, 60, s94\n"
"v_mfma_f32_16x16x32_bf16 v[120:123], v[184:187], a[188:191], v[120:123]\n"
"v_cvt_pk_bf16_f32 v213, v38, v39\n"
"ds_read_b64_tr_b16 v[146:147], v242 offset:20992\n"
"v_mfma_f32_16x16x32_bf16 v[76:79], v[172:175], a[160:163], v[76:79]\n"
"s_add_u32 s48, s95, s83\n"
"s_lshl_b32 s94, s85, 12\n"
"v_mfma_f32_16x16x32_bf16 v[76:79], v[188:191], a[176:179], v[76:79]\n"
"ds_read_b64_tr_b16 v[148:149], v243 offset:20480\n"
"s_lshl_b32 s51, s48, 18\n"
"v_mfma_f32_16x16x32_bf16 v[92:95], v[172:175], a[164:167], v[92:95]\n"
"s_add_u32 s51, s51, s94\n"
"s_add_u32 s92, s54, 8192\n"
"v_mfma_f32_16x16x32_bf16 v[92:95], v[188:191], a[180:183], v[92:95]\n"
"s_lshl_b32 s52, s47, 18\n"
"ds_read_b64_tr_b16 v[150:151], v243 offset:20992\n"
"v_mfma_f32_16x16x32_bf16 v[108:111], v[172:175], a[168:171], v[108:111]\n"
"s_add_u32 s52, s52, s94\n"
"v_accvgpr_write_b32 a0, 0\n"
"v_mfma_f32_16x16x32_bf16 v[108:111], v[188:191], a[184:187], v[108:111]\n"
"v_cvt_pk_bf16_f32 v215, v54, v55\n"
"ds_read_b64_tr_b16 v[152:153], v244 offset:20480\n"
"v_mfma_f32_16x16x32_bf16 v[124:127], v[172:175], a[172:175], v[124:127]\n"
"s_add_u32 m0, s54, 73728\n"
"v_accvgpr_write_b32 a1, 0\n"
"v_mfma_f32_16x16x32_bf16 v[124:127], v[188:191], a[188:191], v[124:127]\n"
"v_accvgpr_write_b32 a2, 0\n"
"v_accvgpr_write_b32 a3, 0\n"
"ds_read_b64_tr_b16 v[154:155], v244 offset:20992\n"
"v_accvgpr_write_b32 a4, 0\n"
"v_accvgpr_write_b32 a5, 0\n"
"ds_read_b64_tr_b16 v[156:157], v245 offset:20480\n"
"s_nop 0\n"
"buffer_load_dwordx4 v248, s[36:39], s92 offen offset:0 sc1 lds\n"
"v_accvgpr_write_b32 a6, 0\n"
"v_accvgpr_write_b32 a7, 0\n"
"s_nop 0\n"
"v_permlane16_swap_b32 v212, v214\n"
"v_accvgpr_write_b32 a8, 0\n"
"ds_read_b64_tr_b16 v[158:159], v245 offset:20992\n"
"v_accvgpr_write_b32 a9, 0\n"
"v_accvgpr_write_b32 a10, 0\n"
"buffer_load_dwordx4 v248, s[36:39], s92 offen offset:1024 sc1 lds\n"
"v_accvgpr_write_b32 a11, 0\n"
"v_accvgpr_write_b32 a12, 0\n"
"v_accvgpr_write_b32 a13, 0\n"
"v_permlane16_swap_b32 v213, v215\n"
"buffer_load_dwordx4 v248, s[36:39], s92 offen offset:2048 sc1 lds\n"
"v_accvgpr_write_b32 a14, 0\n"
"v_accvgpr_write_b32 a15, 0\n"
"v_accvgpr_write_b32 a16, 0\n"
"v_accvgpr_write_b32 a17, 0\n"
"v_accvgpr_write_b32 a18, 0\n"
"buffer_load_dwordx4 v248, s[36:39], s92 offen offset:3072 sc1 lds\n"
"v_accvgpr_write_b32 a19, 0\n"
"v_accvgpr_write_b32 a20, 0\n"
"v_accvgpr_write_b32 a21, 0\n"
"v_accvgpr_write_b32 a22, 0\n"
"v_accvgpr_write_b32 a23, 0\n"
"s_waitcnt lgkmcnt(0)\n"
"v_mfma_f32_16x16x32_bf16 v[64:67], v[128:131], a[192:195], v[64:67]\n"
"ds_read_b64_tr_b16 v[160:161], v242 offset:24576\n"
"s_add_u32 s92, s54, 32768\n"
"v_mfma_f32_16x16x32_bf16 v[64:67], v[144:147], a[208:211], v[64:67]\n"
"ds_read_b64_tr_b16 v[162:163], v242 offset:25088\n"
"s_mov_b32 s40, 0x10000\n"
"v_mfma_f32_16x16x32_bf16 v[80:83], v[128:131], a[196:199], v[80:83]\n"
"s_mov_b32 s41, 0xe000\n"
"s_add_u32 m0, s54, 32768\n"
"v_mfma_f32_16x16x32_bf16 v[80:83], v[144:147], a[212:215], v[80:83]\n"
"s_mov_b32 s42, 0x4000\n"
"s_mov_b32 s43, 0x2000\n"
"v_mfma_f32_16x16x32_bf16 v[96:99], v[128:131], a[200:203], v[96:99]\n"
"ds_read_b64_tr_b16 v[164:165], v243 offset:24576\n"
"s_mov_b32 s49, 0\n"
"v_mfma_f32_16x16x32_bf16 v[96:99], v[144:147], a[216:219], v[96:99]\n"
"s_mov_b32 s50, 0x53800000\n"
"ds_read_b64_tr_b16 v[166:167], v243 offset:25088\n"
"v_mfma_f32_16x16x32_bf16 v[112:115], v[128:131], a[204:207], v[112:115]\n"
"v_mov_b32 v246, 0xff800000\n"
"v_add_u32 v247, 0x10000, v248\n"
"v_mfma_f32_16x16x32_bf16 v[112:115], v[144:147], a[220:223], v[112:115]\n"
"s_nop 0\n"
"buffer_load_dwordx4 v248, s[28:31], s92 offen offset:0 sc1 lds\n"
"v_mfma_f32_16x16x32_bf16 v[68:71], v[132:135], a[192:195], v[68:71]\n"
"v_lshrrev_b32 v253, 5, v255\n"
"v_mfma_f32_16x16x32_bf16 v[68:71], v[148:151], a[208:211], v[68:71]\n"
"ds_read_b64_tr_b16 v[168:169], v244 offset:24576\n"
"v_lshlrev_b32 v237, 8, v253\n"
"v_mfma_f32_16x16x32_bf16 v[84:87], v[132:135], a[196:199], v[84:87]\n"
"v_bfe_u32 v253, v255, 4, 1\n"
"v_cvt_pk_bf16_f32 v216, v40, v41\n"
"v_mfma_f32_16x16x32_bf16 v[84:87], v[148:151], a[212:215], v[84:87]\n"
"ds_read_b64_tr_b16 v[170:171], v244 offset:25088\n"
"v_lshl_add_u32 v237, v253, 17, v237\n"
"v_mfma_f32_16x16x32_bf16 v[100:103], v[132:135], a[200:203], v[100:103]\n"
"v_and_b32 v253, 0xf0, v248\n"
"buffer_load_dwordx4 v248, s[28:31], s92 offen offset:1024 sc1 lds\n"
"v_mfma_f32_16x16x32_bf16 v[100:103], v[148:151], a[216:219], v[100:103]\n"
"v_add_u32 v237, v253, v237\n"
"v_mfma_f32_16x16x32_bf16 v[116:119], v[132:135], a[204:207], v[116:119]\n"
"v_lshrrev_b32 v253, 3, v255\n"
"ds_read_b64_tr_b16 v[172:173], v245 offset:24576\n"
"v_mfma_f32_16x16x32_bf16 v[116:119], v[148:151], a[220:223], v[116:119]\n"
"v_and_b32 v252, 7, v255\n"
"v_cvt_pk_bf16_f32 v218, v56, v57\n"
"v_mfma_f32_16x16x32_bf16 v[72:75], v[136:139], a[192:195], v[72:75]\n"
"ds_read_b64_tr_b16 v[174:175], v245 offset:25088\n"
"buffer_load_dwordx4 v248, s[28:31], s92 offen offset:2048 sc1 lds\n"
"v_mfma_f32_16x16x32_bf16 v[72:75], v[152:155], a[208:211], v[72:75]\n"
"v_lshrrev_b32 v251, 4, v255\n"
"v_mfma_f32_16x16x32_bf16 v[88:91], v[136:139], a[196:199], v[88:91]\n"
"v_lshlrev_b32 v251, 1, v251\n"
"v_xor_b32 v251, v251, v252\n"
"v_mfma_f32_16x16x32_bf16 v[88:91], v[152:155], a[212:215], v[88:91]\n"
"v_lshlrev_b32 v251, 4, v251\n"
"ds_read_b64_tr_b16 v[176:177], v242 offset:28672\n"
"v_mfma_f32_16x16x32_bf16 v[104:107], v[136:139], a[200:203], v[104:107]\n"
"v_lshl_add_u32 v250, v253, 10, v251\n"
"v_mul_u32_u24 v253, 3584, v253\n"
"v_mfma_f32_16x16x32_bf16 v[104:107], v[152:155], a[216:219], v[104:107]\n"
"buffer_load_dwordx4 v248, s[28:31], s92 offen offset:3072 sc1 lds\n"
"v_mfma_f32_16x16x32_bf16 v[120:123], v[136:139], a[204:207], v[120:123]\n"
"v_add_u32 v251, v253, v251\n"
"v_accvgpr_write_b32 a24, 0\n"
"v_mfma_f32_16x16x32_bf16 v[120:123], v[152:155], a[220:223], v[120:123]\n"
"v_cvt_pk_bf16_f32 v217, v42, v43\n"
"ds_read_b64_tr_b16 v[178:179], v242 offset:29184\n"
"v_mfma_f32_16x16x32_bf16 v[76:79], v[140:143], a[192:195], v[76:79]\n"
"v_accvgpr_write_b32 a25, 0\n"
"ds_read_b64_tr_b16 v[180:181], v243 offset:28672\n"
"v_mfma_f32_16x16x32_bf16 v[76:79], v[156:159], a[208:211], v[76:79]\n"
"v_accvgpr_write_b32 a26, 0\n"
"v_accvgpr_write_b32 a27, 0\n"
"v_mfma_f32_16x16x32_bf16 v[92:95], v[140:143], a[196:199], v[92:95]\n"
"s_add_u32 s92, s54, 24576\n"
"v_accvgpr_write_b32 a28, 0\n"
"v_mfma_f32_16x16x32_bf16 v[92:95], v[156:159], a[212:215], v[92:95]\n"
"v_accvgpr_write_b32 a29, 0\n"
"ds_read_b64_tr_b16 v[182:183], v243 offset:29184\n"
"v_mfma_f32_16x16x32_bf16 v[108:111], v[140:143], a[200:203], v[108:111]\n"
"v_accvgpr_write_b32 a30, 0\n"
"v_accvgpr_write_b32 a31, 0\n"
"v_mfma_f32_16x16x32_bf16 v[108:111], v[156:159], a[216:219], v[108:111]\n"
"v_cvt_pk_bf16_f32 v219, v58, v59\n"
"ds_read_b64_tr_b16 v[184:185], v244 offset:28672\n"
"v_mfma_f32_16x16x32_bf16 v[124:127], v[140:143], a[204:207], v[124:127]\n"
"s_add_u32 m0, s54, 90112\n"
"v_accvgpr_write_b32 a32, 0\n"
"v_mfma_f32_16x16x32_bf16 v[124:127], v[156:159], a[220:223], v[124:127]\n"
"v_accvgpr_write_b32 a33, 0\n"
"v_accvgpr_write_b32 a34, 0\n"
"ds_read_b64_tr_b16 v[186:187], v244 offset:29184\n"
"v_accvgpr_write_b32 a35, 0\n"
"v_accvgpr_write_b32 a36, 0\n"
"ds_read_b64_tr_b16 v[188:189], v245 offset:28672\n"
"s_nop 0\n"
"buffer_load_dwordx4 v248, s[36:39], s92 offen offset:0 sc1 lds\n"
"v_accvgpr_write_b32 a37, 0\n"
"v_accvgpr_write_b32 a38, 0\n"
"s_nop 0\n"
"v_permlane16_swap_b32 v216, v218\n"
"v_accvgpr_write_b32 a39, 0\n"
"v_accvgpr_write_b32 a40, 0\n"
"ds_read_b64_tr_b16 v[190:191], v245 offset:29184\n"
"v_accvgpr_write_b32 a41, 0\n"
"buffer_load_dwordx4 v248, s[36:39], s92 offen offset:1024 sc1 lds\n"
"v_accvgpr_write_b32 a42, 0\n"
"v_accvgpr_write_b32 a43, 0\n"
"v_accvgpr_write_b32 a44, 0\n"
"v_accvgpr_write_b32 a45, 0\n"
"v_permlane16_swap_b32 v217, v219\n"
"buffer_load_dwordx4 v248, s[36:39], s92 offen offset:2048 sc1 lds\n"
"v_accvgpr_write_b32 a46, 0\n"
"v_accvgpr_write_b32 a47, 0\n"
"v_accvgpr_write_b32 a48, 0\n"
"v_accvgpr_write_b32 a49, 0\n"
"v_accvgpr_write_b32 a50, 0\n"
"buffer_load_dwordx4 v248, s[36:39], s92 offen offset:3072 sc1 lds\n"
"v_accvgpr_write_b32 a51, 0\n"
"v_accvgpr_write_b32 a52, 0\n"
"v_accvgpr_write_b32 a53, 0\n"
"v_accvgpr_write_b32 a54, 0\n"
"v_accvgpr_write_b32 a55, 0\n"
"s_waitcnt lgkmcnt(0)\n"
"v_mfma_f32_16x16x32_bf16 v[64:67], v[160:163], a[224:227], v[64:67]\n"
"s_add_u32 s92, s54, 49152\n"
"v_mfma_f32_16x16x32_bf16 v[64:67], v[176:179], a[240:243], v[64:67]\n"
"v_accvgpr_write_b32 a56, 0\n"
"v_accvgpr_write_b32 a57, 0\n"
"v_mfma_f32_16x16x32_bf16 v[80:83], v[160:163], a[228:231], v[80:83]\n"
"s_add_u32 m0, s54, 49152\n"
"v_accvgpr_write_b32 a58, 0\n"
"v_mfma_f32_16x16x32_bf16 v[80:83], v[176:179], a[244:247], v[80:83]\n"
"v_accvgpr_write_b32 a59, 0\n"
"v_mfma_f32_16x16x32_bf16 v[96:99], v[160:163], a[232:235], v[96:99]\n"
"v_accvgpr_write_b32 a60, 0\n"
"v_accvgpr_write_b32 a61, 0\n"
"v_mfma_f32_16x16x32_bf16 v[96:99], v[176:179], a[248:251], v[96:99]\n"
"s_nop 0\n"
"buffer_load_dwordx4 v248, s[28:31], s92 offen offset:0 sc1 lds\n"
"v_mfma_f32_16x16x32_bf16 v[112:115], v[160:163], a[236:239], v[112:115]\n"
"v_accvgpr_write_b32 a62, 0\n"
"v_mfma_f32_16x16x32_bf16 v[112:115], v[176:179], a[252:255], v[112:115]\n"
"v_accvgpr_write_b32 a63, 0\n"
"v_cvt_pk_bf16_f32 v220, v44, v45\n"
"v_mfma_f32_16x16x32_bf16 v[68:71], v[164:167], a[224:227], v[68:71]\n"
"v_accvgpr_write_b32 a64, 0\n"
"v_accvgpr_write_b32 a65, 0\n"
"v_mfma_f32_16x16x32_bf16 v[68:71], v[180:183], a[240:243], v[68:71]\n"
"buffer_load_dwordx4 v248, s[28:31], s92 offen offset:1024 sc1 lds\n"
"v_mfma_f32_16x16x32_bf16 v[84:87], v[164:167], a[228:231], v[84:87]\n"
"v_accvgpr_write_b32 a66, 0\n"
"v_accvgpr_write_b32 a67, 0\n"
"v_mfma_f32_16x16x32_bf16 v[84:87], v[180:183], a[244:247], v[84:87]\n"
"v_cvt_pk_bf16_f32 v222, v60, v61\n"
"buffer_load_dwordx4 v248, s[28:31], s92 offen offset:2048 sc1 lds\n"
"v_mfma_f32_16x16x32_bf16 v[100:103], v[164:167], a[232:235], v[100:103]\n"
"v_accvgpr_write_b32 a68, 0\n"
"v_mfma_f32_16x16x32_bf16 v[100:103], v[180:183], a[248:251], v[100:103]\n"
"v_accvgpr_write_b32 a69, 0\n"
"v_accvgpr_write_b32 a70, 0\n"
"v_mfma_f32_16x16x32_bf16 v[116:119], v[164:167], a[236:239], v[116:119]\n"
"v_accvgpr_write_b32 a71, 0\n"
"buffer_load_dwordx4 v248, s[28:31], s92 offen offset:3072 sc1 lds\n"
"v_mfma_f32_16x16x32_bf16 v[116:119], v[180:183], a[252:255], v[116:119]\n"
"v_accvgpr_write_b32 a72, 0\n"
"v_mfma_f32_16x16x32_bf16 v[72:75], v[168:171], a[224:227], v[72:75]\n"
"v_cvt_pk_bf16_f32 v221, v46, v47\n"
"v_accvgpr_write_b32 a73, 0\n"
"v_mfma_f32_16x16x32_bf16 v[72:75], v[184:187], a[240:243], v[72:75]\n"
"v_accvgpr_write_b32 a74, 0\n"
"v_accvgpr_write_b32 a75, 0\n"
"v_mfma_f32_16x16x32_bf16 v[88:91], v[168:171], a[228:231], v[88:91]\n"
"s_add_u32 s92, s54, 40960\n"
"v_accvgpr_write_b32 a76, 0\n"
"v_mfma_f32_16x16x32_bf16 v[88:91], v[184:187], a[244:247], v[88:91]\n"
"v_accvgpr_write_b32 a77, 0\n"
"v_cvt_pk_bf16_f32 v223, v62, v63\n"
"v_mfma_f32_16x16x32_bf16 v[104:107], v[168:171], a[232:235], v[104:107]\n"
"s_add_u32 m0, s54, 106496\n"
"v_accvgpr_write_b32 a78, 0\n"
"v_mfma_f32_16x16x32_bf16 v[104:107], v[184:187], a[248:251], v[104:107]\n"
"v_accvgpr_write_b32 a79, 0\n"
"v_accvgpr_write_b32 a80, 0\n"
"v_mfma_f32_16x16x32_bf16 v[120:123], v[168:171], a[236:239], v[120:123]\n"
"v_accvgpr_write_b32 a81, 0\n"
"s_nop 0\n"
"buffer_load_dwordx4 v248, s[36:39], s92 offen offset:0 sc1 lds\n"
"v_mfma_f32_16x16x32_bf16 v[120:123], v[184:187], a[252:255], v[120:123]\n"
"v_mfma_f32_16x16x32_bf16 v[76:79], v[172:175], a[224:227], v[76:79]\n"
"v_accvgpr_write_b32 a82, 0\n"
"v_accvgpr_write_b32 a83, 0\n"
"v_mfma_f32_16x16x32_bf16 v[76:79], v[188:191], a[240:243], v[76:79]\n"
"s_nop 0\n"
"v_permlane16_swap_b32 v220, v222\n"
"v_mfma_f32_16x16x32_bf16 v[92:95], v[172:175], a[228:231], v[92:95]\n"
"v_accvgpr_write_b32 a84, 0\n"
"v_accvgpr_write_b32 a85, 0\n"
"v_mfma_f32_16x16x32_bf16 v[92:95], v[188:191], a[244:247], v[92:95]\n"
"buffer_load_dwordx4 v248, s[36:39], s92 offen offset:1024 sc1 lds\n"
"v_mfma_f32_16x16x32_bf16 v[108:111], v[172:175], a[232:235], v[108:111]\n"
"v_accvgpr_write_b32 a86, 0\n"
"v_accvgpr_write_b32 a87, 0\n"
"v_mfma_f32_16x16x32_bf16 v[108:111], v[188:191], a[248:251], v[108:111]\n"
"v_accvgpr_write_b32 a88, 0\n"
"v_permlane16_swap_b32 v221, v223\n"
"v_mfma_f32_16x16x32_bf16 v[124:127], v[172:175], a[236:239], v[124:127]\n"
"buffer_load_dwordx4 v248, s[36:39], s92 offen offset:2048 sc1 lds\n"
"v_mfma_f32_16x16x32_bf16 v[124:127], v[188:191], a[252:255], v[124:127]\n"
"v_accvgpr_write_b32 a89, 0\n"
"v_accvgpr_write_b32 a90, 0\n"
"v_accvgpr_write_b32 a91, 0\n"
"v_accvgpr_write_b32 a92, 0\n"
"buffer_load_dwordx4 v248, s[36:39], s92 offen offset:3072 sc1 lds\n"
"v_accvgpr_write_b32 a93, 0\n"
"v_accvgpr_write_b32 a94, 0\n"
"v_accvgpr_write_b32 a95, 0\n"
"v_accvgpr_write_b32 a96, 0\n"
"v_accvgpr_write_b32 a97, 0\n"
"v_accvgpr_write_b32 a98, 0\n"
"v_accvgpr_write_b32 a99, 0\n"
"v_accvgpr_write_b32 a100, 0\n"
"v_accvgpr_write_b32 a101, 0\n"
"v_accvgpr_write_b32 a102, 0\n"
"v_accvgpr_write_b32 a103, 0\n"
"v_accvgpr_write_b32 a104, 0\n"
"v_accvgpr_write_b32 a105, 0\n"
"v_accvgpr_write_b32 a106, 0\n"
"v_accvgpr_write_b32 a107, 0\n"
"v_accvgpr_write_b32 a108, 0\n"
"v_accvgpr_write_b32 a109, 0\n"
"v_accvgpr_write_b32 a110, 0\n"
"v_accvgpr_write_b32 a111, 0\n"
"v_accvgpr_write_b32 a112, 0\n"
"v_accvgpr_write_b32 a113, 0\n"
"v_accvgpr_write_b32 a114, 0\n"
"v_accvgpr_write_b32 a115, 0\n"
"v_accvgpr_write_b32 a116, 0\n"
"v_accvgpr_write_b32 a117, 0\n"
"v_accvgpr_write_b32 a118, 0\n"
"v_accvgpr_write_b32 a119, 0\n"
"v_accvgpr_write_b32 a120, 0\n"
"v_accvgpr_write_b32 a121, 0\n"
"v_accvgpr_write_b32 a122, 0\n"
"v_accvgpr_write_b32 a123, 0\n"
"v_accvgpr_write_b32 a124, 0\n"
"v_accvgpr_write_b32 a125, 0\n"
"v_accvgpr_write_b32 a126, 0\n"
"v_accvgpr_write_b32 a127, 0\n"
"s_waitcnt vmcnt(24)\n"
"s_barrier\n"
"ds_read_b128 a[128:131], v248 offset:0\n"
"ds_read_b128 a[132:135], v248 offset:1024\n"
"ds_read_b128 a[136:139], v248 offset:2048\n"
"ds_read_b128 a[140:143], v248 offset:3072\n"
"ds_read_b128 a[144:147], v248 offset:4096\n"
"ds_read_b128 a[148:151], v248 offset:5120\n"
"ds_read_b128 a[152:155], v248 offset:6144\n"
"ds_read_b128 a[156:159], v248 offset:7168\n"
"v_and_b32 v249, 31, v255\n"
"v_lshrrev_b32 v253, 5, v255\n"
"v_lshlrev_b32 v252, 2, v253\n"
"v_sub_u32 v249, v249, v252\n"
"v_cvt_pk_bf16_f32 v0, v64, v65\n"
"v_cvt_pk_bf16_f32 v2, v80, v81\n"
"v_cvt_pk_bf16_f32 v1, v66, v67\n"
"v_cvt_pk_bf16_f32 v3, v82, v83\n"
"s_nop 1\n"
"v_permlane16_swap_b32 v0, v2\n"
"v_permlane16_swap_b32 v1, v3\n"
"v_cvt_pk_bf16_f32 v4, v68, v69\n"
"v_cvt_pk_bf16_f32 v6, v84, v85\n"
"v_cvt_pk_bf16_f32 v5, v70, v71\n"
"v_cvt_pk_bf16_f32 v7, v86, v87\n"
"s_nop 1\n"
"v_permlane16_swap_b32 v4, v6\n"
"v_permlane16_swap_b32 v5, v7\n"
"v_cvt_pk_bf16_f32 v8, v72, v73\n"
"v_cvt_pk_bf16_f32 v10, v88, v89\n"
"v_cvt_pk_bf16_f32 v9, v74, v75\n"
"v_cvt_pk_bf16_f32 v11, v90, v91\n"
"s_nop 1\n"
"v_permlane16_swap_b32 v8, v10\n"
"v_permlane16_swap_b32 v9, v11\n"
"v_cvt_pk_bf16_f32 v12, v76, v77\n"
"v_cvt_pk_bf16_f32 v14, v92, v93\n"
"v_cvt_pk_bf16_f32 v13, v78, v79\n"
"v_cvt_pk_bf16_f32 v15, v94, v95\n"
"s_nop 1\n"
"v_permlane16_swap_b32 v12, v14\n"
"v_permlane16_swap_b32 v13, v15\n"
"s_waitcnt lgkmcnt(0)\n"
"v_mfma_f32_32x32x16_bf16 v[64:79], a[128:131], v[192:195], 0\n"
"v_cvt_pk_bf16_f32 v16, v96, v97\n"
"v_cvt_pk_bf16_f32 v18, v112, v113\n"
"v_cvt_pk_bf16_f32 v17, v98, v99\n"
"v_cvt_pk_bf16_f32 v19, v114, v115\n"
"v_mfma_f32_32x32x16_bf16 v[64:79], a[132:135], v[196:199], v[64:79]\n"
"s_nop 0\n"
"v_permlane16_swap_b32 v16, v18\n"
"v_permlane16_swap_b32 v17, v19\n"
"v_cvt_pk_bf16_f32 v20, v100, v101\n"
"v_mfma_f32_32x32x16_bf16 v[64:79], a[136:139], v[200:203], v[64:79]\n"
"v_cvt_pk_bf16_f32 v22, v116, v117\n"
"v_cvt_pk_bf16_f32 v21, v102, v103\n"
"v_cvt_pk_bf16_f32 v23, v118, v119\n"
"s_nop 0\n"
"v_permlane16_swap_b32 v20, v22\n"
"v_mfma_f32_32x32x16_bf16 v[64:79], a[140:143], v[204:207], v[64:79]\n"
"v_permlane16_swap_b32 v21, v23\n"
"v_cvt_pk_bf16_f32 v24, v104, v105\n"
"v_cvt_pk_bf16_f32 v26, v120, v121\n"
"v_mfma_f32_32x32x16_bf16 v[64:79], a[144:147], v[0:3], v[64:79]\n"
"v_cvt_pk_bf16_f32 v25, v106, v107\n"
"v_cvt_pk_bf16_f32 v27, v122, v123\n"
"s_nop 0\n"
"v_permlane16_swap_b32 v24, v26\n"
"v_permlane16_swap_b32 v25, v27\n"
"v_mfma_f32_32x32x16_bf16 v[64:79], a[148:151], v[4:7], v[64:79]\n"
"v_cvt_pk_bf16_f32 v28, v108, v109\n"
"v_cvt_pk_bf16_f32 v30, v124, v125\n"
"v_cvt_pk_bf16_f32 v29, v110, v111\n"
"v_mfma_f32_32x32x16_bf16 v[64:79], a[152:155], v[8:11], v[64:79]\n"
"v_cvt_pk_bf16_f32 v31, v126, v127\n"
"s_nop 0\n"
"v_permlane16_swap_b32 v28, v30\n"
"v_permlane16_swap_b32 v29, v31\n"
"v_mfma_f32_32x32x16_bf16 v[64:79], a[156:159], v[12:15], v[64:79]\n"
"v_mfma_f32_32x32x16_bf16 v[80:95], a[128:131], v[208:211], 0\n"
"v_mfma_f32_32x32x16_bf16 v[80:95], a[132:135], v[212:215], v[80:95]\n"
"v_mfma_f32_32x32x16_bf16 v[80:95], a[136:139], v[216:219], v[80:95]\n"
"v_mfma_f32_32x32x16_bf16 v[80:95], a[140:143], v[220:223], v[80:95]\n"
"v_mfma_f32_32x32x16_bf16 v[80:95], a[144:147], v[16:19], v[80:95]\n"
"v_mfma_f32_32x32x16_bf16 v[80:95], a[148:151], v[20:23], v[80:95]\n"
"v_mfma_f32_32x32x16_bf16 v[80:95], a[152:155], v[24:27], v[80:95]\n"
"v_mfma_f32_32x32x16_bf16 v[80:95], a[156:159], v[28:31], v[80:95]\n"
"ds_read_b128 a[128:131], v248 offset:8192\n"
"ds_read_b128 a[132:135], v248 offset:9216\n"
"ds_read_b128 a[136:139], v248 offset:10240\n"
"ds_read_b128 a[140:143], v248 offset:11264\n"
"ds_read_b128 a[144:147], v248 offset:12288\n"
"ds_read_b128 a[148:151], v248 offset:13312\n"
"ds_read_b128 a[152:155], v248 offset:14336\n"
"ds_read_b128 a[156:159], v248 offset:15360\n"
"s_nop 3\n"
"s_cmp_ge_u32 s49, s47\n"
"s_cbranch_scc1 Lmo19_%=\n"
"Lmb18_%=:\n"
"s_add_u32 s49, s49, 1\n"
"v_max3_f32 v60, v64, v65, v66\n"
"v_max3_f32 v60, v60, v67, v68\n"
"v_max3_f32 v60, v60, v69, v70\n"
"v_max3_f32 v60, v60, v71, v72\n"
"v_max3_f32 v60, v60, v73, v74\n"
"v_max3_f32 v60, v60, v75, v76\n"
"v_max3_f32 v60, v60, v77, v78\n"
"v_max_f32 v60, v60, v79\n"
"v_mov_b32 v62, v60\n"
"s_nop 1\n"
"v_permlane32_swap_b32 v60, v62\n"
"v_max_f32 v60, v60, v62\n"
"v_sub_f32 v160, 0, v60\n"
"v_sub_f32 v161, 0, v60\n"
"v_sub_f32 v162, 0, v60\n"
"v_sub_f32 v163, 0, v60\n"
"v_sub_f32 v164, 0, v60\n"
"v_sub_f32 v165, 0, v60\n"
"v_sub_f32 v166, 0, v60\n"
"v_sub_f32 v167, 0, v60\n"
"v_sub_f32 v168, 0, v60\n"
"v_sub_f32 v169, 0, v60\n"
"v_sub_f32 v170, 0, v60\n"
"v_sub_f32 v171, 0, v60\n"
"v_sub_f32 v172, 0, v60\n"
"v_sub_f32 v173, 0, v60\n"
"v_sub_f32 v174, 0, v60\n"
"v_sub_f32 v175, 0, v60\n"
"v_max3_f32 v61, v80, v81, v82\n"
"v_max3_f32 v61, v61, v83, v84\n"
"v_max3_f32 v61, v61, v85, v86\n"
"v_max3_f32 v61, v61, v87, v88\n"
"v_max3_f32 v61, v61, v89, v90\n"
"v_max3_f32 v61, v61, v91, v92\n"
"v_max3_f32 v61, v61, v93, v94\n"
"v_max_f32 v61, v61, v95\n"
"v_mov_b32 v62, v61\n"
"s_nop 1\n"
"v_permlane32_swap_b32 v61, v62\n"
"v_max_f32 v61, v61, v62\n"
"v_sub_f32 v176, 0, v61\n"
"v_sub_f32 v177, 0, v61\n"
"v_sub_f32 v178, 0, v61\n"
"v_sub_f32 v179, 0, v61\n"
"v_sub_f32 v180, 0, v61\n"
"v_sub_f32 v181, 0, v61\n"
"v_sub_f32 v182, 0, v61\n"
"v_sub_f32 v183, 0, v61\n"
"v_sub_f32 v184, 0, v61\n"
"v_sub_f32 v185, 0, v61\n"
"v_sub_f32 v186, 0, v61\n"
"v_sub_f32 v187, 0, v61\n"
"v_sub_f32 v188, 0, v61\n"
"v_sub_f32 v189, 0, v61\n"
"v_sub_f32 v190, 0, v61\n"
"v_sub_f32 v191, 0, v61\n"
"v_mov_b32 v234, 0\n"
"v_mov_b32 v235, 0\n"
"s_waitcnt lgkmcnt(0)\n"
"v_mfma_f32_32x32x16_bf16 v[96:111], a[128:131], v[192:195], v[160:175]\n"
"ds_read_b128 a[192:195], v247 offset:0\n"
"v_sub_f32 v64, v64, v60\n"
"v_sub_f32 v65, v65, v60\n"
"v_exp_f32 v224, v64\n"
"v_sub_f32 v66, v66, v60\n"
"v_exp_f32 v225, v65\n"
"v_sub_f32 v67, v67, v60\n"
"v_exp_f32 v226, v66\n"
"v_add_f32 v232, v224, v225\n"
"v_cvt_pk_bf16_f32 v128, v224, v225\n"
"v_sub_f32 v68, v68, v60\n"
"v_mfma_f32_32x32x16_bf16 v[96:111], a[132:135], v[196:199], v[96:111]\n"
"ds_read_b128 a[196:199], v247 offset:1024\n"
"v_exp_f32 v227, v67\n"
"v_add_f32 v232, v232, v226\n"
"v_sub_f32 v69, v69, v60\n"
"v_exp_f32 v228, v68\n"
"v_add_f32 v232, v232, v227\n"
"v_cvt_pk_bf16_f32 v129, v226, v227\n"
"v_sub_f32 v70, v70, v60\n"
"v_exp_f32 v229, v69\n"
"v_add_f32 v232, v232, v228\n"
"v_mfma_f32_32x32x16_bf16 v[96:111], a[136:139], v[200:203], v[96:111]\n"
"v_sub_f32 v71, v71, v60\n"
"v_exp_f32 v230, v70\n"
"v_add_f32 v232, v232, v229\n"
"v_cvt_pk_bf16_f32 v130, v228, v229\n"
"v_sub_f32 v72, v72, v60\n"
"v_exp_f32 v231, v71\n"
"v_add_f32 v232, v232, v230\n"
"v_sub_f32 v73, v73, v60\n"
"v_exp_f32 v224, v72\n"
"v_add_f32 v232, v232, v231\n"
"v_mfma_f32_32x32x16_bf16 v[96:111], a[140:143], v[204:207], v[96:111]\n"
"ds_read_b128 a[200:203], v247 offset:2048\n"
"v_cvt_pk_bf16_f32 v131, v230, v231\n"
"v_sub_f32 v74, v74, v60\n"
"v_exp_f32 v225, v73\n"
"v_add_f32 v232, v232, v224\n"
"v_sub_f32 v75, v75, v60\n"
"v_exp_f32 v226, v74\n"
"v_add_f32 v232, v232, v225\n"
"v_cvt_pk_bf16_f32 v132, v224, v225\n"
"v_sub_f32 v76, v76, v60\n"
"v_mfma_f32_32x32x16_bf16 v[96:111], a[144:147], v[0:3], v[96:111]\n"
"ds_read_b128 a[204:207], v247 offset:3072\n"
"v_exp_f32 v227, v75\n"
"v_add_f32 v232, v232, v226\n"
"v_sub_f32 v77, v77, v60\n"
"v_exp_f32 v228, v76\n"
"v_add_f32 v232, v232, v227\n"
"v_cvt_pk_bf16_f32 v133, v226, v227\n"
"v_sub_f32 v78, v78, v60\n"
"v_exp_f32 v229, v77\n"
"v_add_f32 v232, v232, v228\n"
"v_sub_f32 v79, v79, v60\n"
"v_mfma_f32_32x32x16_bf16 v[96:111], a[148:151], v[4:7], v[96:111]\n"
"v_exp_f32 v230, v78\n"
"v_add_f32 v232, v232, v229\n"
"v_cvt_pk_bf16_f32 v134, v228, v229\n"
"v_sub_f32 v80, v80, v61\n"
"v_exp_f32 v231, v79\n"
"v_add_f32 v232, v232, v230\n"
"v_sub_f32 v81, v81, v61\n"
"v_exp_f32 v224, v80\n"
"v_add_f32 v232, v232, v231\n"
"v_mfma_f32_32x32x16_bf16 v[96:111], a[152:155], v[8:11], v[96:111]\n"
"ds_read_b128 a[208:211], v247 offset:4096\n"
"v_cvt_pk_bf16_f32 v135, v230, v231\n"
"v_sub_f32 v82, v82, v61\n"
"v_exp_f32 v225, v81\n"
"v_sub_f32 v83, v83, v61\n"
"v_exp_f32 v226, v82\n"
"v_add_f32 v233, v224, v225\n"
"v_cvt_pk_bf16_f32 v136, v224, v225\n"
"v_sub_f32 v84, v84, v61\n"
"v_exp_f32 v227, v83\n"
"v_mfma_f32_32x32x16_bf16 v[96:111], a[156:159], v[12:15], v[96:111]\n"
"ds_read_b128 a[212:215], v247 offset:5120\n"
"v_add_f32 v233, v233, v226\n"
"v_sub_f32 v85, v85, v61\n"
"v_exp_f32 v228, v84\n"
"v_add_f32 v233, v233, v227\n"
"v_cvt_pk_bf16_f32 v137, v226, v227\n"
"v_sub_f32 v86, v86, v61\n"
"v_exp_f32 v229, v85\n"
"v_add_f32 v233, v233, v228\n"
"v_sub_f32 v87, v87, v61\n"
"v_exp_f32 v230, v86\n"
"v_mfma_f32_32x32x16_bf16 v[112:127], a[128:131], v[208:211], v[176:191]\n"
"v_add_f32 v233, v233, v229\n"
"v_cvt_pk_bf16_f32 v138, v228, v229\n"
"v_sub_f32 v88, v88, v61\n"
"v_exp_f32 v231, v87\n"
"v_add_f32 v233, v233, v230\n"
"v_sub_f32 v89, v89, v61\n"
"v_exp_f32 v224, v88\n"
"v_add_f32 v233, v233, v231\n"
"v_cvt_pk_bf16_f32 v139, v230, v231\n"
"v_mfma_f32_32x32x16_bf16 v[112:127], a[132:135], v[212:215], v[112:127]\n"
"ds_read_b128 a[216:219], v247 offset:6144\n"
"v_sub_f32 v90, v90, v61\n"
"v_exp_f32 v225, v89\n"
"v_add_f32 v233, v233, v224\n"
"v_sub_f32 v91, v91, v61\n"
"v_exp_f32 v226, v90\n"
"v_add_f32 v233, v233, v225\n"
"v_cvt_pk_bf16_f32 v140, v224, v225\n"
"v_sub_f32 v92, v92, v61\n"
"v_exp_f32 v227, v91\n"
"v_add_f32 v233, v233, v226\n"
"v_mfma_f32_32x32x16_bf16 v[112:127], a[136:139], v[216:219], v[112:127]\n"
"ds_read_b128 a[220:223], v247 offset:7168\n"
"v_sub_f32 v93, v93, v61\n"
"v_exp_f32 v228, v92\n"
"v_add_f32 v233, v233, v227\n"
"v_cvt_pk_bf16_f32 v141, v226, v227\n"
"v_sub_f32 v94, v94, v61\n"
"v_exp_f32 v229, v93\n"
"v_add_f32 v233, v233, v228\n"
"v_sub_f32 v95, v95, v61\n"
"v_exp_f32 v230, v94\n"
"v_mfma_f32_32x32x16_bf16 v[112:127], a[140:143], v[220:223], v[112:127]\n"
"v_add_f32 v233, v233, v229\n"
"v_cvt_pk_bf16_f32 v142, v228, v229\n"
"v_exp_f32 v231, v95\n"
"v_add_f32 v233, v233, v230\n"
"s_nop 0\n"
"v_add_f32 v233, v233, v231\n"
"v_cvt_pk_bf16_f32 v143, v230, v231\n"
"v_add_f32 v234, v234, v232\n"
"v_add_f32 v235, v235, v233\n"
"v_mfma_f32_32x32x16_bf16 v[112:127], a[144:147], v[16:19], v[112:127]\n"
"v_exp_f32 v224, v96\n"
"s_lshl_b32 s95, s84, 2\n"
"s_add_u32 s95, s95, s83\n"
"s_lshl_b32 s95, s95, 12\n"
"v_and_b32 v59, 31, v255\n"
"v_mfma_f32_32x32x16_bf16 v[112:127], a[148:151], v[20:23], v[112:127]\n"
"v_exp_f32 v225, v97\n"
"v_lshlrev_b32 v59, 7, v59\n"
"v_add_u32 v59, s95, v59\n"
"global_load_dword v58, v59, s[66:67]\n"
"s_getpc_b64 s[24:25]\n"
"LCP1_%=:\n"
"v_mfma_f32_32x32x16_bf16 v[112:127], a[152:155], v[24:27], v[112:127]\n"
"v_add_f32 v232, v224, v225\n"
"v_cvt_pk_bf16_f32 v144, v224, v225\n"
"v_exp_f32 v226, v98\n"
"s_and_b32 s94, s42, 0xffff\n"
"v_add_u32 v238, s94, v248\n"
"s_bfe_u32 s95, s84, 0x20003\n"
"s_lshl_b32 s95, s95, 2\n"
"s_add_u32 s95, s95, s83\n"
"s_lshl_b32 s95, s95, 13\n"
"v_mfma_f32_32x32x16_bf16 v[112:127], a[156:159], v[28:31], v[112:127]\n"
"v_add_f32 v232, v232, v226\n"
"v_exp_f32 v227, v99\n"
"s_min_u32 s95, s95, LHOTEND_%=-LCP1_%=-8192\n"
"v_lshlrev_b32 v57, 3, v248\n"
"v_add_u32 v57, s95, v57\n"
"global_load_dword v56, v57, s[24:25]\n"
"s_waitcnt vmcnt(18) lgkmcnt(0)\n"
"s_barrier\n"
"s_mov_b32 s44, 1\n"
"s_cmp_eq_u32 s86, 0\n"
"s_cbranch_scc1 LD_%=\n"
"s_lshl_b32 s44, s86, 1\n"
"s_sub_u32 s44, s44, 1\n"
"s_lshl_b32 s49, s44, 1\n"
"s_add_u32 s49, s49, 1\n"
"LDU_%=:\n"
"s_waitcnt lgkmcnt(0)\n"
"v_mfma_f32_32x32x16_bf16 a[0:15], a[192:195], v[128:131], a[0:15]\n"
"ds_read_b128 a[128:131], v238 offset:0\n"
"v_add_f32 v232, v232, v227\n"
"v_cvt_pk_bf16_f32 v145, v226, v227\n"
"v_exp_f32 v228, v100\n"
"v_mfma_f32_32x32x16_bf16 a[16:31], a[196:199], v[128:131], a[16:31]\n"
"ds_read_b128 a[132:135], v238 offset:1024\n"
"v_exp_f32 v229, v101\n"
"v_add_f32 v232, v232, v228\n"
"v_mfma_f32_32x32x16_bf16 a[32:47], a[200:203], v[128:131], a[32:47]\n"
"v_exp_f32 v230, v102\n"
"v_add_f32 v232, v232, v229\n"
"v_cvt_pk_bf16_f32 v146, v228, v229\n"
"v_mfma_f32_32x32x16_bf16 a[48:63], a[204:207], v[128:131], a[48:63]\n"
"ds_read_b128 a[136:139], v238 offset:2048\n"
"v_exp_f32 v231, v103\n"
"v_add_f32 v232, v232, v230\n"
"v_mfma_f32_32x32x16_bf16 a[0:15], a[208:211], v[132:135], a[0:15]\n"
"ds_read_b128 a[140:143], v238 offset:3072\n"
"v_exp_f32 v224, v104\n"
"v_add_f32 v232, v232, v231\n"
"v_cvt_pk_bf16_f32 v147, v230, v231\n"
"v_mfma_f32_32x32x16_bf16 a[16:31], a[212:215], v[132:135], a[16:31]\n"
"v_exp_f32 v225, v105\n"
"v_add_f32 v232, v232, v224\n"
"v_mfma_f32_32x32x16_bf16 a[32:47], a[216:219], v[132:135], a[32:47]\n"
"ds_read_b128 a[144:147], v238 offset:4096\n"
"v_exp_f32 v226, v106\n"
"v_add_f32 v232, v232, v225\n"
"v_cvt_pk_bf16_f32 v148, v224, v225\n"
"v_mfma_f32_32x32x16_bf16 a[48:63], a[220:223], v[132:135], a[48:63]\n"
"ds_read_b128 a[148:151], v238 offset:5120\n"
"v_exp_f32 v227, v107\n"
"v_add_f32 v232, v232, v226\n"
"v_mfma_f32_32x32x16_bf16 a[64:79], a[192:195], v[136:139], a[64:79]\n"
"v_exp_f32 v228, v108\n"
"v_add_f32 v232, v232, v227\n"
"v_cvt_pk_bf16_f32 v149, v226, v227\n"
"s_add_u32 s92, s40, s54\n"
"s_and_b32 m0, s92, 0xffff\n"
"v_mfma_f32_32x32x16_bf16 a[80:95], a[196:199], v[136:139], a[80:95]\n"
"ds_read_b128 a[152:155], v238 offset:6144\n"
"v_exp_f32 v229, v109\n"
"v_add_f32 v232, v232, v228\n"
"buffer_load_dwordx4 v248, s[28:31], s92 offen offset:0 sc1 lds\n"
"v_mfma_f32_32x32x16_bf16 a[96:111], a[200:203], v[136:139], a[96:111]\n"
"ds_read_b128 a[156:159], v238 offset:7168\n"
"v_exp_f32 v230, v110\n"
"v_add_f32 v232, v232, v229\n"
"v_cvt_pk_bf16_f32 v150, v228, v229\n"
"v_mfma_f32_32x32x16_bf16 a[112:127], a[204:207], v[136:139], a[112:127]\n"
"v_exp_f32 v231, v111\n"
"v_add_f32 v232, v232, v230\n"
"buffer_load_dwordx4 v248, s[28:31], s92 offen offset:1024 sc1 lds\n"
"v_mfma_f32_32x32x16_bf16 a[64:79], a[208:211], v[140:143], a[64:79]\n"
"v_exp_f32 v224, v112\n"
"v_exp_f32 v225, v113\n"
"v_add_f32 v232, v232, v231\n"
"v_cvt_pk_bf16_f32 v151, v230, v231\n"
"s_add_u32 s42, s42, 8192\n"
"v_mfma_f32_32x32x16_bf16 a[80:95], a[212:215], v[140:143], a[80:95]\n"
"v_exp_f32 v226, v114\n"
"v_exp_f32 v227, v115\n"
"v_add_f32 v233, v224, v225\n"
"v_cvt_pk_bf16_f32 v152, v224, v225\n"
"v_mfma_f32_32x32x16_bf16 a[96:111], a[216:219], v[140:143], a[96:111]\n"
"v_exp_f32 v228, v116\n"
"v_add_f32 v233, v233, v226\n"
"v_add_f32 v233, v233, v227\n"
"v_cvt_pk_bf16_f32 v153, v226, v227\n"
"s_and_b32 s94, s43, 0xffff\n"
"v_add_u32 v239, s94, v247\n"
"buffer_load_dwordx4 v248, s[28:31], s92 offen offset:2048 sc1 lds\n"
"v_mfma_f32_32x32x16_bf16 a[112:127], a[220:223], v[140:143], a[112:127]\n"
"v_exp_f32 v229, v117\n"
"v_add_f32 v233, v233, v228\n"
"buffer_load_dwordx4 v248, s[28:31], s92 offen offset:3072 sc1 lds\n"
"s_waitcnt lgkmcnt(0)\n"
"v_mfma_f32_32x32x16_bf16 v[64:79], a[128:131], v[192:195], v[160:175]\n"
"ds_read_b128 a[192:195], v239 offset:0\n"
"v_add_f32 v233, v233, v229\n"
"v_cvt_pk_bf16_f32 v154, v228, v229\n"
"v_exp_f32 v230, v118\n"
"v_mfma_f32_32x32x16_bf16 v[64:79], a[132:135], v[196:199], v[64:79]\n"
"ds_read_b128 a[196:199], v239 offset:1024\n"
"v_add_f32 v233, v233, v230\n"
"v_exp_f32 v231, v119\n"
"v_mfma_f32_32x32x16_bf16 v[64:79], a[136:139], v[200:203], v[64:79]\n"
"v_add_f32 v233, v233, v231\n"
"v_cvt_pk_bf16_f32 v155, v230, v231\n"
"v_exp_f32 v224, v120\n"
"v_mfma_f32_32x32x16_bf16 v[64:79], a[140:143], v[204:207], v[64:79]\n"
"ds_read_b128 a[200:203], v239 offset:2048\n"
"v_add_f32 v233, v233, v224\n"
"v_exp_f32 v225, v121\n"
"v_mfma_f32_32x32x16_bf16 v[64:79], a[144:147], v[0:3], v[64:79]\n"
"ds_read_b128 a[204:207], v239 offset:3072\n"
"v_add_f32 v233, v233, v225\n"
"v_cvt_pk_bf16_f32 v156, v224, v225\n"
"v_exp_f32 v226, v122\n"
"v_mfma_f32_32x32x16_bf16 v[64:79], a[148:151], v[4:7], v[64:79]\n"
"v_add_f32 v233, v233, v226\n"
"v_exp_f32 v227, v123\n"
"v_mfma_f32_32x32x16_bf16 v[64:79], a[152:155], v[8:11], v[64:79]\n"
"ds_read_b128 a[208:211], v239 offset:4096\n"
"v_add_f32 v233, v233, v227\n"
"v_cvt_pk_bf16_f32 v157, v226, v227\n"
"v_exp_f32 v228, v124\n"
"v_mfma_f32_32x32x16_bf16 v[64:79], a[156:159], v[12:15], v[64:79]\n"
"ds_read_b128 a[212:215], v239 offset:5120\n"
"v_add_f32 v233, v233, v228\n"
"v_exp_f32 v229, v125\n"
"v_mfma_f32_32x32x16_bf16 v[80:95], a[128:131], v[208:211], v[176:191]\n"
"v_add_f32 v233, v233, v229\n"
"v_cvt_pk_bf16_f32 v158, v228, v229\n"
"v_exp_f32 v230, v126\n"
"s_add_u32 s92, s41, s54\n"
"s_and_b32 s93, s92, 0xffff\n"
"s_or_b32 m0, s93, 0x10000\n"
"v_mfma_f32_32x32x16_bf16 v[80:95], a[132:135], v[212:215], v[80:95]\n"
"ds_read_b128 a[216:219], v239 offset:6144\n"
"v_add_f32 v233, v233, v230\n"
"v_exp_f32 v231, v127\n"
"buffer_load_dwordx4 v248, s[36:39], s92 offen offset:0 sc1 lds\n"
"v_mfma_f32_32x32x16_bf16 v[80:95], a[136:139], v[216:219], v[80:95]\n"
"ds_read_b128 a[220:223], v239 offset:7168\n"
"v_add_f32 v233, v233, v231\n"
"v_cvt_pk_bf16_f32 v159, v230, v231\n"
"v_add_f32 v236, v232, v233\n"
"v_cmp_nge_f32 vcc, s50, v236\n"
"v_mfma_f32_32x32x16_bf16 v[80:95], a[140:143], v[220:223], v[80:95]\n"
"s_cbranch_vccnz Lfb105_%=\n"
"Lfr106_%=:\n"
"v_add_f32 v234, v234, v232\n"
"v_add_f32 v235, v235, v233\n"
"v_mfma_f32_32x32x16_bf16 v[80:95], a[144:147], v[16:19], v[80:95]\n"
"v_exp_f32 v224, v64\n"
"s_add_u32 s43, s43, 8192\n"
"v_mfma_f32_32x32x16_bf16 v[80:95], a[148:151], v[20:23], v[80:95]\n"
"v_exp_f32 v225, v65\n"
"buffer_load_dwordx4 v248, s[36:39], s92 offen offset:1024 sc1 lds\n"
"v_mfma_f32_32x32x16_bf16 v[80:95], a[152:155], v[24:27], v[80:95]\n"
"v_add_f32 v232, v224, v225\n"
"v_cvt_pk_bf16_f32 v128, v224, v225\n"
"v_exp_f32 v226, v66\n"
"s_and_b32 s94, s42, 0xffff\n"
"v_add_u32 v238, s94, v248\n"
"buffer_load_dwordx4 v248, s[36:39], s92 offen offset:2048 sc1 lds\n"
"v_mfma_f32_32x32x16_bf16 v[80:95], a[156:159], v[28:31], v[80:95]\n"
"v_add_f32 v232, v232, v226\n"
"v_exp_f32 v227, v67\n"
"buffer_load_dwordx4 v248, s[36:39], s92 offen offset:3072 sc1 lds\n"
"s_add_u32 s40, s40, 16384\n"
"s_add_u32 s41, s41, 16384\n"
"s_waitcnt lgkmcnt(0)\n"
"v_mfma_f32_32x32x16_bf16 a[0:15], a[192:195], v[144:147], a[0:15]\n"
"ds_read_b128 a[128:131], v238 offset:0\n"
"v_add_f32 v232, v232, v227\n"
"v_cvt_pk_bf16_f32 v129, v226, v227\n"
"v_exp_f32 v228, v68\n"
"v_mfma_f32_32x32x16_bf16 a[16:31], a[196:199], v[144:147], a[16:31]\n"
"ds_read_b128 a[132:135], v238 offset:1024\n"
"v_exp_f32 v229, v69\n"
"v_add_f32 v232, v232, v228\n"
"v_mfma_f32_32x32x16_bf16 a[32:47], a[200:203], v[144:147], a[32:47]\n"
"v_exp_f32 v230, v70\n"
"v_add_f32 v232, v232, v229\n"
"v_cvt_pk_bf16_f32 v130, v228, v229\n"
"v_mfma_f32_32x32x16_bf16 a[48:63], a[204:207], v[144:147], a[48:63]\n"
"ds_read_b128 a[136:139], v238 offset:2048\n"
"v_exp_f32 v231, v71\n"
"v_add_f32 v232, v232, v230\n"
"v_mfma_f32_32x32x16_bf16 a[0:15], a[208:211], v[148:151], a[0:15]\n"
"ds_read_b128 a[140:143], v238 offset:3072\n"
"v_exp_f32 v224, v72\n"
"v_add_f32 v232, v232, v231\n"
"v_cvt_pk_bf16_f32 v131, v230, v231\n"
"v_mfma_f32_32x32x16_bf16 a[16:31], a[212:215], v[148:151], a[16:31]\n"
"v_exp_f32 v225, v73\n"
"v_add_f32 v232, v232, v224\n"
"v_mfma_f32_32x32x16_bf16 a[32:47], a[216:219], v[148:151], a[32:47]\n"
"ds_read_b128 a[144:147], v238 offset:4096\n"
"v_exp_f32 v226, v74\n"
"v_add_f32 v232, v232, v225\n"
"v_cvt_pk_bf16_f32 v132, v224, v225\n"
"v_mfma_f32_32x32x16_bf16 a[48:63], a[220:223], v[148:151], a[48:63]\n"
"ds_read_b128 a[148:151], v238 offset:5120\n"
"v_exp_f32 v227, v75\n"
"v_add_f32 v232, v232, v226\n"
"v_mfma_f32_32x32x16_bf16 a[64:79], a[192:195], v[152:155], a[64:79]\n"
"v_exp_f32 v228, v76\n"
"v_add_f32 v232, v232, v227\n"
"v_cvt_pk_bf16_f32 v133, v226, v227\n"
"v_mfma_f32_32x32x16_bf16 a[80:95], a[196:199], v[152:155], a[80:95]\n"
"ds_read_b128 a[152:155], v238 offset:6144\n"
"v_exp_f32 v229, v77\n"
"v_add_f32 v232, v232, v228\n"
"v_mfma_f32_32x32x16_bf16 a[96:111], a[200:203], v[152:155], a[96:111]\n"
"ds_read_b128 a[156:159], v238 offset:7168\n"
"v_exp_f32 v230, v78\n"
"v_add_f32 v232, v232, v229\n"
"v_cvt_pk_bf16_f32 v134, v228, v229\n"
"v_mfma_f32_32x32x16_bf16 a[112:127], a[204:207], v[152:155], a[112:127]\n"
"v_exp_f32 v231, v79\n"
"v_add_f32 v232, v232, v230\n"
"v_mfma_f32_32x32x16_bf16 a[64:79], a[208:211], v[156:159], a[64:79]\n"
"v_exp_f32 v224, v80\n"
"v_exp_f32 v225, v81\n"
"v_add_f32 v232, v232, v231\n"
"v_cvt_pk_bf16_f32 v135, v230, v231\n"
"s_add_u32 s42, s42, 8192\n"
"v_mfma_f32_32x32x16_bf16 a[80:95], a[212:215], v[156:159], a[80:95]\n"
"v_exp_f32 v226, v82\n"
"v_exp_f32 v227, v83\n"
"v_add_f32 v233, v224, v225\n"
"v_cvt_pk_bf16_f32 v136, v224, v225\n"
"v_mfma_f32_32x32x16_bf16 a[96:111], a[216:219], v[156:159], a[96:111]\n"
"v_exp_f32 v228, v84\n"
"v_add_f32 v233, v233, v226\n"
"v_add_f32 v233, v233, v227\n"
"v_cvt_pk_bf16_f32 v137, v226, v227\n"
"s_and_b32 s94, s43, 0xffff\n"
"v_add_u32 v239, s94, v247\n"
"v_mfma_f32_32x32x16_bf16 a[112:127], a[220:223], v[156:159], a[112:127]\n"
"v_exp_f32 v229, v85\n"
"v_add_f32 v233, v233, v228\n"
"s_waitcnt lgkmcnt(0)\n"
"v_mfma_f32_32x32x16_bf16 v[96:111], a[128:131], v[192:195], v[160:175]\n"
"ds_read_b128 a[192:195], v239 offset:0\n"
"v_add_f32 v233, v233, v229\n"
"v_cvt_pk_bf16_f32 v138, v228, v229\n"
"v_exp_f32 v230, v86\n"
"v_mfma_f32_32x32x16_bf16 v[96:111], a[132:135], v[196:199], v[96:111]\n"
"ds_read_b128 a[196:199], v239 offset:1024\n"
"v_add_f32 v233, v233, v230\n"
"v_exp_f32 v231, v87\n"
"v_mfma_f32_32x32x16_bf16 v[96:111], a[136:139], v[200:203], v[96:111]\n"
"v_add_f32 v233, v233, v231\n"
"v_cvt_pk_bf16_f32 v139, v230, v231\n"
"v_exp_f32 v224, v88\n"
"v_mfma_f32_32x32x16_bf16 v[96:111], a[140:143], v[204:207], v[96:111]\n"
"ds_read_b128 a[200:203], v239 offset:2048\n"
"v_add_f32 v233, v233, v224\n"
"v_exp_f32 v225, v89\n"
"v_mfma_f32_32x32x16_bf16 v[96:111], a[144:147], v[0:3], v[96:111]\n"
"ds_read_b128 a[204:207], v239 offset:3072\n"
"v_add_f32 v233, v233, v225\n"
"v_cvt_pk_bf16_f32 v140, v224, v225\n"
"v_exp_f32 v226, v90\n"
"v_mfma_f32_32x32x16_bf16 v[96:111], a[148:151], v[4:7], v[96:111]\n"
"v_add_f32 v233, v233, v226\n"
"v_exp_f32 v227, v91\n"
"v_mfma_f32_32x32x16_bf16 v[96:111], a[152:155], v[8:11], v[96:111]\n"
"ds_read_b128 a[208:211], v239 offset:4096\n"
"v_add_f32 v233, v233, v227\n"
"v_cvt_pk_bf16_f32 v141, v226, v227\n"
"v_exp_f32 v228, v92\n"
"v_mfma_f32_32x32x16_bf16 v[96:111], a[156:159], v[12:15], v[96:111]\n"
"ds_read_b128 a[212:215], v239 offset:5120\n"
"v_add_f32 v233, v233, v228\n"
"v_exp_f32 v229, v93\n"
"v_mfma_f32_32x32x16_bf16 v[112:127], a[128:131], v[208:211], v[176:191]\n"
"v_add_f32 v233, v233, v229\n"
"v_cvt_pk_bf16_f32 v142, v228, v229\n"
"v_exp_f32 v230, v94\n"
"v_mfma_f32_32x32x16_bf16 v[112:127], a[132:135], v[212:215], v[112:127]\n"
"ds_read_b128 a[216:219], v239 offset:6144\n"
"v_add_f32 v233, v233, v230\n"
"v_exp_f32 v231, v95\n"
"v_mfma_f32_32x32x16_bf16 v[112:127], a[136:139], v[216:219], v[112:127]\n"
"ds_read_b128 a[220:223], v239 offset:7168\n"
"v_add_f32 v233, v233, v231\n"
"v_cvt_pk_bf16_f32 v143, v230, v231\n"
"v_add_f32 v236, v232, v233\n"
"v_cmp_nge_f32 vcc, s50, v236\n"
"v_mfma_f32_32x32x16_bf16 v[112:127], a[140:143], v[220:223], v[112:127]\n"
"s_cbranch_vccnz Lfb107_%=\n"
"Lfr108_%=:\n"
"v_add_f32 v234, v234, v232\n"
"v_add_f32 v235, v235, v233\n"
"v_mfma_f32_32x32x16_bf16 v[112:127], a[144:147], v[16:19], v[112:127]\n"
"v_exp_f32 v224, v96\n"
"s_add_u32 s43, s43, 8192\n"
"v_mfma_f32_32x32x16_bf16 v[112:127], a[148:151], v[20:23], v[112:127]\n"
"v_exp_f32 v225, v97\n"
"v_mfma_f32_32x32x16_bf16 v[112:127], a[152:155], v[24:27], v[112:127]\n"
"v_add_f32 v232, v224, v225\n"
"v_cvt_pk_bf16_f32 v144, v224, v225\n"
"v_exp_f32 v226, v98\n"
"s_and_b32 s94, s42, 0xffff\n"
"v_add_u32 v238, s94, v248\n"
"v_mfma_f32_32x32x16_bf16 v[112:127], a[156:159], v[28:31], v[112:127]\n"
"v_add_f32 v232, v232, v226\n"
"v_exp_f32 v227, v99\n"
"s_waitcnt vmcnt(8) lgkmcnt(0)\n"
"s_barrier\n"
"s_sub_u32 s44, s44, 1\n"
"s_cmp_lg_u32 s44, 0\n"
"s_cbranch_scc1 LDU_%=\n"
"s_mov_b32 s44, 2\n"
"LD_%=:\n"
"s_waitcnt lgkmcnt(0)\n"
"v_mfma_f32_32x32x16_bf16 a[0:15], a[192:195], v[128:131], a[0:15]\n"
"ds_read_b128 a[128:131], v238 offset:0\n"
"v_add_f32 v232, v232, v227\n"
"v_cvt_pk_bf16_f32 v145, v226, v227\n"
"v_exp_f32 v228, v100\n"
"v_mfma_f32_32x32x16_bf16 a[16:31], a[196:199], v[128:131], a[16:31]\n"
"ds_read_b128 a[132:135], v238 offset:1024\n"
"v_exp_f32 v229, v101\n"
"v_add_f32 v232, v232, v228\n"
"v_mfma_f32_32x32x16_bf16 a[32:47], a[200:203], v[128:131], a[32:47]\n"
"v_exp_f32 v230, v102\n"
"v_add_f32 v232, v232, v229\n"
"v_cvt_pk_bf16_f32 v146, v228, v229\n"
"v_mfma_f32_32x32x16_bf16 a[48:63], a[204:207], v[128:131], a[48:63]\n"
"ds_read_b128 a[136:139], v238 offset:2048\n"
"v_exp_f32 v231, v103\n"
"v_add_f32 v232, v232, v230\n"
"v_mfma_f32_32x32x16_bf16 a[0:15], a[208:211], v[132:135], a[0:15]\n"
"ds_read_b128 a[140:143], v238 offset:3072\n"
"v_exp_f32 v224, v104\n"
"v_add_f32 v232, v232, v231\n"
"v_cvt_pk_bf16_f32 v147, v230, v231\n"
"v_mfma_f32_32x32x16_bf16 a[16:31], a[212:215], v[132:135], a[16:31]\n"
"v_exp_f32 v225, v105\n"
"v_add_f32 v232, v232, v224\n"
"v_mfma_f32_32x32x16_bf16 a[32:47], a[216:219], v[132:135], a[32:47]\n"
"ds_read_b128 a[144:147], v238 offset:4096\n"
"v_exp_f32 v226, v106\n"
"v_add_f32 v232, v232, v225\n"
"v_cvt_pk_bf16_f32 v148, v224, v225\n"
"v_mfma_f32_32x32x16_bf16 a[48:63], a[220:223], v[132:135], a[48:63]\n"
"ds_read_b128 a[148:151], v238 offset:5120\n"
"v_exp_f32 v227, v107\n"
"v_add_f32 v232, v232, v226\n"
"v_mfma_f32_32x32x16_bf16 a[64:79], a[192:195], v[136:139], a[64:79]\n"
"v_exp_f32 v228, v108\n"
"v_add_f32 v232, v232, v227\n"
"v_cvt_pk_bf16_f32 v149, v226, v227\n"
"s_cmp_ge_u32 s49, s47\n"
"s_cbranch_scc1 Lmo22_%=\n"
"Lmb21_%=:\n"
"s_add_u32 s49, s49, 1\n"
"s_add_u32 s92, s40, s54\n"
"s_and_b32 m0, s92, 0xffff\n"
"v_mfma_f32_32x32x16_bf16 a[80:95], a[196:199], v[136:139], a[80:95]\n"
"ds_read_b128 a[152:155], v238 offset:6144\n"
"v_exp_f32 v229, v109\n"
"v_add_f32 v232, v232, v228\n"
"buffer_load_dwordx4 v248, s[28:31], s92 offen offset:0 sc1 lds\n"
"v_mfma_f32_32x32x16_bf16 a[96:111], a[200:203], v[136:139], a[96:111]\n"
"ds_read_b128 a[156:159], v238 offset:7168\n"
"v_exp_f32 v230, v110\n"
"v_add_f32 v232, v232, v229\n"
"v_cvt_pk_bf16_f32 v150, v228, v229\n"
"v_mfma_f32_32x32x16_bf16 a[112:127], a[204:207], v[136:139], a[112:127]\n"
"v_exp_f32 v231, v111\n"
"v_add_f32 v232, v232, v230\n"
"buffer_load_dwordx4 v248, s[28:31], s92 offen offset:1024 sc1 lds\n"
"v_mfma_f32_32x32x16_bf16 a[64:79], a[208:211], v[140:143], a[64:79]\n"
"v_exp_f32 v224, v112\n"
"v_exp_f32 v225, v113\n"
"v_add_f32 v232, v232, v231\n"
"v_cvt_pk_bf16_f32 v151, v230, v231\n"
"s_add_u32 s42, s42, 8192\n"
"v_mfma_f32_32x32x16_bf16 a[80:95], a[212:215], v[140:143], a[80:95]\n"
"v_exp_f32 v226, v114\n"
"v_exp_f32 v227, v115\n"
"v_add_f32 v233, v224, v225\n"
"v_cvt_pk_bf16_f32 v152, v224, v225\n"
"v_mfma_f32_32x32x16_bf16 a[96:111], a[216:219], v[140:143], a[96:111]\n"
"v_exp_f32 v228, v116\n"
"v_add_f32 v233, v233, v226\n"
"v_add_f32 v233, v233, v227\n"
"v_cvt_pk_bf16_f32 v153, v226, v227\n"
"s_and_b32 s94, s43, 0xffff\n"
"v_add_u32 v239, s94, v247\n"
"buffer_load_dwordx4 v248, s[28:31], s92 offen offset:2048 sc1 lds\n"
"v_mfma_f32_32x32x16_bf16 a[112:127], a[220:223], v[140:143], a[112:127]\n"
"v_exp_f32 v229, v117\n"
"v_add_f32 v233, v233, v228\n"
"buffer_load_dwordx4 v248, s[28:31], s92 offen offset:3072 sc1 lds\n"
"s_waitcnt lgkmcnt(0)\n"
"v_mfma_f32_32x32x16_bf16 v[64:79], a[128:131], v[192:195], v[160:175]\n"
"ds_read_b128 a[192:195], v239 offset:0\n"
"v_add_f32 v233, v233, v229\n"
"v_cvt_pk_bf16_f32 v154, v228, v229\n"
"v_exp_f32 v230, v118\n"
"v_mfma_f32_32x32x16_bf16 v[64:79], a[132:135], v[196:199], v[64:79]\n"
"ds_read_b128 a[196:199], v239 offset:1024\n"
"v_add_f32 v233, v233, v230\n"
"v_exp_f32 v231, v119\n"
"v_mfma_f32_32x32x16_bf16 v[64:79], a[136:139], v[200:203], v[64:79]\n"
"v_add_f32 v233, v233, v231\n"
"v_cvt_pk_bf16_f32 v155, v230, v231\n"
"v_exp_f32 v224, v120\n"
"v_mfma_f32_32x32x16_bf16 v[64:79], a[140:143], v[204:207], v[64:79]\n"
"ds_read_b128 a[200:203], v239 offset:2048\n"
"v_add_f32 v233, v233, v224\n"
"v_exp_f32 v225, v121\n"
"v_mfma_f32_32x32x16_bf16 v[64:79], a[144:147], v[0:3], v[64:79]\n"
"ds_read_b128 a[204:207], v239 offset:3072\n"
"v_add_f32 v233, v233, v225\n"
"v_cvt_pk_bf16_f32 v156, v224, v225\n"
"v_exp_f32 v226, v122\n"
"v_mfma_f32_32x32x16_bf16 v[64:79], a[148:151], v[4:7], v[64:79]\n"
"v_add_f32 v233, v233, v226\n"
"v_exp_f32 v227, v123\n"
"v_mfma_f32_32x32x16_bf16 v[64:79], a[152:155], v[8:11], v[64:79]\n"
"ds_read_b128 a[208:211], v239 offset:4096\n"
"v_add_f32 v233, v233, v227\n"
"v_cvt_pk_bf16_f32 v157, v226, v227\n"
"v_exp_f32 v228, v124\n"
"v_mfma_f32_32x32x16_bf16 v[64:79], a[156:159], v[12:15], v[64:79]\n"
"ds_read_b128 a[212:215], v239 offset:5120\n"
"v_add_f32 v233, v233, v228\n"
"v_exp_f32 v229, v125\n"
"v_mfma_f32_32x32x16_bf16 v[80:95], a[128:131], v[208:211], v[176:191]\n"
"v_add_f32 v233, v233, v229\n"
"v_cvt_pk_bf16_f32 v158, v228, v229\n"
"v_exp_f32 v230, v126\n"
"s_add_u32 s92, s41, s54\n"
"s_and_b32 s93, s92, 0xffff\n"
"s_or_b32 m0, s93, 0x10000\n"
"v_mfma_f32_32x32x16_bf16 v[80:95], a[132:135], v[212:215], v[80:95]\n"
"ds_read_b128 a[216:219], v239 offset:6144\n"
"v_add_f32 v233, v233, v230\n"
"v_exp_f32 v231, v127\n"
"buffer_load_dwordx4 v248, s[36:39], s92 offen offset:0 sc1 lds\n"
"v_mfma_f32_32x32x16_bf16 v[80:95], a[136:139], v[216:219], v[80:95]\n"
"ds_read_b128 a[220:223], v239 offset:7168\n"
"v_add_f32 v233, v233, v231\n"
"v_cvt_pk_bf16_f32 v159, v230, v231\n"
"v_add_f32 v236, v232, v233\n"
"v_cmp_nge_f32 vcc, s50, v236\n"
"v_mfma_f32_32x32x16_bf16 v[80:95], a[140:143], v[220:223], v[80:95]\n"
"s_cbranch_vccnz Lfb24_%=\n"
"Lfr25_%=:\n"
"v_add_f32 v234, v234, v232\n"
"v_add_f32 v235, v235, v233\n"
"v_mfma_f32_32x32x16_bf16 v[80:95], a[144:147], v[16:19], v[80:95]\n"
"v_exp_f32 v224, v64\n"
"s_add_u32 s43, s43, 8192\n"
"v_mfma_f32_32x32x16_bf16 v[80:95], a[148:151], v[20:23], v[80:95]\n"
"v_exp_f32 v225, v65\n"
"buffer_load_dwordx4 v248, s[36:39], s92 offen offset:1024 sc1 lds\n"
"v_mfma_f32_32x32x16_bf16 v[80:95], a[152:155], v[24:27], v[80:95]\n"
"v_add_f32 v232, v224, v225\n"
"v_cvt_pk_bf16_f32 v128, v224, v225\n"
"v_exp_f32 v226, v66\n"
"s_and_b32 s94, s42, 0xffff\n"
"v_add_u32 v238, s94, v248\n"
"buffer_load_dwordx4 v248, s[36:39], s92 offen offset:2048 sc1 lds\n"
"v_mfma_f32_32x32x16_bf16 v[80:95], a[156:159], v[28:31], v[80:95]\n"
"v_add_f32 v232, v232, v226\n"
"v_exp_f32 v227, v67\n"
"buffer_load_dwordx4 v248, s[36:39], s92 offen offset:3072 sc1 lds\n"
"s_add_u32 s40, s40, 16384\n"
"s_add_u32 s41, s41, 16384\n"
"s_waitcnt lgkmcnt(0)\n"
"v_mfma_f32_32x32x16_bf16 a[0:15], a[192:195], v[144:147], a[0:15]\n"
"ds_read_b128 a[128:131], v238 offset:0\n"
"v_add_f32 v232, v232, v227\n"
"v_cvt_pk_bf16_f32 v129, v226, v227\n"
"v_exp_f32 v228, v68\n"
"v_mfma_f32_32x32x16_bf16 a[16:31], a[196:199], v[144:147], a[16:31]\n"
"ds_read_b128 a[132:135], v238 offset:1024\n"
"v_exp_f32 v229, v69\n"
"v_add_f32 v232, v232, v228\n"
"v_mfma_f32_32x32x16_bf16 a[32:47], a[200:203], v[144:147], a[32:47]\n"
"v_exp_f32 v230, v70\n"
"v_add_f32 v232, v232, v229\n"
"v_cvt_pk_bf16_f32 v130, v228, v229\n"
"v_mfma_f32_32x32x16_bf16 a[48:63], a[204:207], v[144:147], a[48:63]\n"
"ds_read_b128 a[136:139], v238 offset:2048\n"
"v_exp_f32 v231, v71\n"
"v_add_f32 v232, v232, v230\n"
"v_mfma_f32_32x32x16_bf16 a[0:15], a[208:211], v[148:151], a[0:15]\n"
"ds_read_b128 a[140:143], v238 offset:3072\n"
"v_exp_f32 v224, v72\n"
"v_add_f32 v232, v232, v231\n"
"v_cvt_pk_bf16_f32 v131, v230, v231\n"
"v_mfma_f32_32x32x16_bf16 a[16:31], a[212:215], v[148:151], a[16:31]\n"
"v_exp_f32 v225, v73\n"
"v_add_f32 v232, v232, v224\n"
"v_mfma_f32_32x32x16_bf16 a[32:47], a[216:219], v[148:151], a[32:47]\n"
"ds_read_b128 a[144:147], v238 offset:4096\n"
"v_exp_f32 v226, v74\n"
"v_add_f32 v232, v232, v225\n"
"v_cvt_pk_bf16_f32 v132, v224, v225\n"
"v_mfma_f32_32x32x16_bf16 a[48:63], a[220:223], v[148:151], a[48:63]\n"
"ds_read_b128 a[148:151], v238 offset:5120\n"
"v_exp_f32 v227, v75\n"
"v_add_f32 v232, v232, v226\n"
"v_mfma_f32_32x32x16_bf16 a[64:79], a[192:195], v[152:155], a[64:79]\n"
"v_exp_f32 v228, v76\n"
"v_add_f32 v232, v232, v227\n"
"v_cvt_pk_bf16_f32 v133, v226, v227\n"
"s_cmp_ge_u32 s49, s47\n"
"s_cbranch_scc1 Lmo27_%=\n"
"Lmb26_%=:\n"
"s_add_u32 s49, s49, 1\n"
"v_mfma_f32_32x32x16_bf16 a[80:95], a[196:199], v[152:155], a[80:95]\n"
"ds_read_b128 a[152:155], v238 offset:6144\n"
"v_exp_f32 v229, v77\n"
"v_add_f32 v232, v232, v228\n"
"s_lshl_b32 s95, s84, 2\n"
"s_add_u32 s95, s95, s83\n"
"v_mfma_f32_32x32x16_bf16 a[96:111], a[200:203], v[152:155], a[96:111]\n"
"ds_read_b128 a[156:159], v238 offset:7168\n"
"v_exp_f32 v230, v78\n"
"v_add_f32 v232, v232, v229\n"
"v_cvt_pk_bf16_f32 v134, v228, v229\n"
"s_mul_i32 s95, s95, 1792\n"
"v_min_u32 v60, 13, v255\n"
"v_mfma_f32_32x32x16_bf16 a[112:127], a[204:207], v[152:155], a[112:127]\n"
"v_exp_f32 v231, v79\n"
"v_add_f32 v232, v232, v230\n"
"v_lshlrev_b32 v60, 7, v60\n"
"v_add_u32 v60, s95, v60\n"
"v_mfma_f32_32x32x16_bf16 a[64:79], a[208:211], v[156:159], a[64:79]\n"
"v_exp_f32 v224, v80\n"
"v_exp_f32 v225, v81\n"
"v_add_f32 v232, v232, v231\n"
"v_cvt_pk_bf16_f32 v135, v230, v231\n"
"s_add_u32 s42, s42, 8192\n"
"global_load_dword v61, v60, s[68:69]\n"
"v_mfma_f32_32x32x16_bf16 a[80:95], a[212:215], v[156:159], a[80:95]\n"
"v_exp_f32 v226, v82\n"
"v_exp_f32 v227, v83\n"
"v_add_f32 v233, v224, v225\n"
"v_cvt_pk_bf16_f32 v136, v224, v225\n"
"global_load_dword v62, v60, s[70:71]\n"
"v_mfma_f32_32x32x16_bf16 a[96:111], a[216:219], v[156:159], a[96:111]\n"
"v_exp_f32 v228, v84\n"
"v_add_f32 v233, v233, v226\n"
"v_add_f32 v233, v233, v227\n"
"v_cvt_pk_bf16_f32 v137, v226, v227\n"
"s_and_b32 s94, s43, 0xffff\n"
"v_add_u32 v239, s94, v247\n"
"v_mfma_f32_32x32x16_bf16 a[112:127], a[220:223], v[156:159], a[112:127]\n"
"v_exp_f32 v229, v85\n"
"v_add_f32 v233, v233, v228\n"
"global_load_dword v63, v60, s[72:73]\n"
"s_waitcnt lgkmcnt(0)\n"
"v_mfma_f32_32x32x16_bf16 v[96:111], a[128:131], v[192:195], v[160:175]\n"
"ds_read_b128 a[192:195], v239 offset:0\n"
"v_add_f32 v233, v233, v229\n"
"v_cvt_pk_bf16_f32 v138, v228, v229\n"
"v_exp_f32 v230, v86\n"
"v_mfma_f32_32x32x16_bf16 v[96:111], a[132:135], v[196:199], v[96:111]\n"
"ds_read_b128 a[196:199], v239 offset:1024\n"
"v_add_f32 v233, v233, v230\n"
"v_exp_f32 v231, v87\n"
"v_mfma_f32_32x32x16_bf16 v[96:111], a[136:139], v[200:203], v[96:111]\n"
"v_add_f32 v233, v233, v231\n"
"v_cvt_pk_bf16_f32 v139, v230, v231\n"
"v_exp_f32 v224, v88\n"
"v_mfma_f32_32x32x16_bf16 v[96:111], a[140:143], v[204:207], v[96:111]\n"
"ds_read_b128 a[200:203], v239 offset:2048\n"
"v_add_f32 v233, v233, v224\n"
"v_exp_f32 v225, v89\n"
"v_mfma_f32_32x32x16_bf16 v[96:111], a[144:147], v[0:3], v[96:111]\n"
"ds_read_b128 a[204:207], v239 offset:3072\n"
"v_add_f32 v233, v233, v225\n"
"v_cvt_pk_bf16_f32 v140, v224, v225\n"
"v_exp_f32 v226, v90\n"
"v_mfma_f32_32x32x16_bf16 v[96:111], a[148:151], v[4:7], v[96:111]\n"
"v_add_f32 v233, v233, v226\n"
"v_exp_f32 v227, v91\n"
"v_mfma_f32_32x32x16_bf16 v[96:111], a[152:155], v[8:11], v[96:111]\n"
"ds_read_b128 a[208:211], v239 offset:4096\n"
"v_add_f32 v233, v233, v227\n"
"v_cvt_pk_bf16_f32 v141, v226, v227\n"
"v_exp_f32 v228, v92\n"
"v_mfma_f32_32x32x16_bf16 v[96:111], a[156:159], v[12:15], v[96:111]\n"
"ds_read_b128 a[212:215], v239 offset:5120\n"
"v_add_f32 v233, v233, v228\n"
"v_exp_f32 v229, v93\n"
"v_mfma_f32_32x32x16_bf16 v[112:127], a[128:131], v[208:211], v[176:191]\n"
"v_add_f32 v233, v233, v229\n"
"v_cvt_pk_bf16_f32 v142, v228, v229\n"
"v_exp_f32 v230, v94\n"
"v_mfma_f32_32x32x16_bf16 v[112:127], a[132:135], v[212:215], v[112:127]\n"
"ds_read_b128 a[216:219], v239 offset:6144\n"
"v_add_f32 v233, v233, v230\n"
"v_exp_f32 v231, v95\n"
"v_mfma_f32_32x32x16_bf16 v[112:127], a[136:139], v[216:219], v[112:127]\n"
"ds_read_b128 a[220:223], v239 offset:7168\n"
"v_add_f32 v233, v233, v231\n"
"v_cvt_pk_bf16_f32 v143, v230, v231\n"
"v_add_f32 v236, v232, v233\n"
"v_cmp_nge_f32 vcc, s50, v236\n"
"v_mfma_f32_32x32x16_bf16 v[112:127], a[140:143], v[220:223], v[112:127]\n"
"s_cbranch_vccnz Lfb29_%=\n"
"Lfr30_%=:\n"
"v_add_f32 v234, v234, v232\n"
"v_add_f32 v235, v235, v233\n"
"v_mfma_f32_32x32x16_bf16 v[112:127], a[144:147], v[16:19], v[112:127]\n"
"v_exp_f32 v224, v96\n"
"s_add_u32 s43, s43, 8192\n"
"v_mfma_f32_32x32x16_bf16 v[112:127], a[148:151], v[20:23], v[112:127]\n"
"v_exp_f32 v225, v97\n"
"v_mfma_f32_32x32x16_bf16 v[112:127], a[152:155], v[24:27], v[112:127]\n"
"v_add_f32 v232, v224, v225\n"
"v_cvt_pk_bf16_f32 v144, v224, v225\n"
"v_exp_f32 v226, v98\n"
"s_and_b32 s94, s42, 0xffff\n"
"v_add_u32 v238, s94, v248\n"
"v_mfma_f32_32x32x16_bf16 v[112:127], a[156:159], v[28:31], v[112:127]\n"
"v_add_f32 v232, v232, v226\n"
"v_exp_f32 v227, v99\n"
"s_waitcnt vmcnt(8) lgkmcnt(0)\n"
"s_barrier\n"
"s_sub_u32 s44, s44, 1\n"
"s_cmp_lg_u32 s44, 0\n"
"s_cbranch_scc1 LD_%=\n"
"s_waitcnt lgkmcnt(0)\n"
"v_mfma_f32_32x32x16_bf16 a[0:15], a[192:195], v[128:131], a[0:15]\n"
"ds_read_b128 a[132:135], v238 offset:1024\n"
"ds_read_b128 a[128:131], v238 offset:0\n"
"v_add_f32 v232, v232, v227\n"
"v_cvt_pk_bf16_f32 v145, v226, v227\n"
"v_exp_f32 v228, v100\n"
"v_mfma_f32_32x32x16_bf16 a[16:31], a[196:199], v[128:131], a[16:31]\n"
"ds_read_b128 a[136:139], v238 offset:2048\n"
"v_exp_f32 v229, v101\n"
"v_add_f32 v232, v232, v228\n"
"v_mfma_f32_32x32x16_bf16 a[32:47], a[200:203], v[128:131], a[32:47]\n"
"ds_read_b128 a[140:143], v238 offset:3072\n"
"v_exp_f32 v230, v102\n"
"v_add_f32 v232, v232, v229\n"
"v_cvt_pk_bf16_f32 v146, v228, v229\n"
"s_add_u32 s92, s40, s54\n"
"s_and_b32 m0, s92, 0xffff\n"
"v_mfma_f32_32x32x16_bf16 a[48:63], a[204:207], v[128:131], a[48:63]\n"
"ds_read_b128 a[148:151], v238 offset:5120\n"
"ds_read_b128 a[144:147], v238 offset:4096\n"
"v_exp_f32 v231, v103\n"
"v_add_f32 v232, v232, v230\n"
"buffer_load_dwordx4 v248, s[28:31], s92 offen offset:0 sc1 lds\n"
"v_mfma_f32_32x32x16_bf16 a[0:15], a[208:211], v[132:135], a[0:15]\n"
"ds_read_b128 a[152:155], v238 offset:6144\n"
"v_exp_f32 v224, v104\n"
"v_add_f32 v232, v232, v231\n"
"v_cvt_pk_bf16_f32 v147, v230, v231\n"
"v_mfma_f32_32x32x16_bf16 a[16:31], a[212:215], v[132:135], a[16:31]\n"
"ds_read_b128 a[156:159], v238 offset:7168\n"
"v_exp_f32 v225, v105\n"
"v_add_f32 v232, v232, v224\n"
"v_mfma_f32_32x32x16_bf16 a[32:47], a[216:219], v[132:135], a[32:47]\n"
"ds_read_b128 a[164:167], v238 offset:9216\n"
"ds_read_b128 a[160:163], v238 offset:8192\n"
"v_exp_f32 v226, v106\n"
"v_add_f32 v232, v232, v225\n"
"v_cvt_pk_bf16_f32 v148, v224, v225\n"
"v_mfma_f32_32x32x16_bf16 a[48:63], a[220:223], v[132:135], a[48:63]\n"
"ds_read_b128 a[168:171], v238 offset:10240\n"
"v_exp_f32 v227, v107\n"
"v_add_f32 v232, v232, v226\n"
"buffer_load_dwordx4 v248, s[28:31], s92 offen offset:1024 sc1 lds\n"
"v_mfma_f32_32x32x16_bf16 a[64:79], a[192:195], v[136:139], a[64:79]\n"
"ds_read_b128 a[172:175], v238 offset:11264\n"
"v_exp_f32 v228, v108\n"
"v_add_f32 v232, v232, v227\n"
"v_cvt_pk_bf16_f32 v149, v226, v227\n"
"s_cmp_ge_u32 s49, s47\n"
"s_cbranch_scc1 Lmo32_%=\n"
"Lmb31_%=:\n"
"s_add_u32 s49, s49, 1\n"
"v_mfma_f32_32x32x16_bf16 a[80:95], a[196:199], v[136:139], a[80:95]\n"
"ds_read_b128 a[180:183], v238 offset:13312\n"
"ds_read_b128 a[176:179], v238 offset:12288\n"
"v_exp_f32 v229, v109\n"
"v_add_f32 v232, v232, v228\n"
"v_mfma_f32_32x32x16_bf16 a[96:111], a[200:203], v[136:139], a[96:111]\n"
"ds_read_b128 a[184:187], v238 offset:14336\n"
"v_exp_f32 v230, v110\n"
"v_add_f32 v232, v232, v229\n"
"v_cvt_pk_bf16_f32 v150, v228, v229\n"
"v_mfma_f32_32x32x16_bf16 a[112:127], a[204:207], v[136:139], a[112:127]\n"
"ds_read_b128 a[188:191], v238 offset:15360\n"
"v_exp_f32 v231, v111\n"
"v_add_f32 v232, v232, v230\n"
"v_mfma_f32_32x32x16_bf16 a[64:79], a[208:211], v[140:143], a[64:79]\n"
"v_exp_f32 v224, v112\n"
"v_exp_f32 v225, v113\n"
"v_add_f32 v232, v232, v231\n"
"v_cvt_pk_bf16_f32 v151, v230, v231\n"
"s_add_u32 s42, s42, 16384\n"
"v_mfma_f32_32x32x16_bf16 a[80:95], a[212:215], v[140:143], a[80:95]\n"
"v_exp_f32 v226, v114\n"
"v_exp_f32 v227, v115\n"
"v_add_f32 v233, v224, v225\n"
"v_cvt_pk_bf16_f32 v152, v224, v225\n"
"v_mfma_f32_32x32x16_bf16 a[96:111], a[216:219], v[140:143], a[96:111]\n"
"v_exp_f32 v228, v116\n"
"v_add_f32 v233, v233, v226\n"
"v_add_f32 v233, v233, v227\n"
"v_cvt_pk_bf16_f32 v153, v226, v227\n"
"s_and_b32 s94, s43, 0xffff\n"
"v_add_u32 v239, s94, v247\n"
"buffer_load_dwordx4 v248, s[28:31], s92 offen offset:2048 sc1 lds\n"
"v_mfma_f32_32x32x16_bf16 a[112:127], a[220:223], v[140:143], a[112:127]\n"
"v_exp_f32 v229, v117\n"
"v_add_f32 v233, v233, v228\n"
"buffer_load_dwordx4 v248, s[28:31], s92 offen offset:3072 sc1 lds\n"
"s_waitcnt lgkmcnt(0)\n"
"v_mfma_f32_32x32x16_bf16 v[64:79], a[128:131], v[192:195], v[160:175]\n"
"ds_read_b128 a[192:195], v239 offset:0\n"
"v_add_f32 v233, v233, v229\n"
"v_cvt_pk_bf16_f32 v154, v228, v229\n"
"v_exp_f32 v230, v118\n"
"v_mfma_f32_32x32x16_bf16 v[64:79], a[132:135], v[196:199], v[64:79]\n"
"ds_read_b128 a[196:199], v239 offset:1024\n"
"v_add_f32 v233, v233, v230\n"
"v_exp_f32 v231, v119\n"
"s_getpc_b64 s[24:25]\n"
"LCP2_%=:\n"
"v_mfma_f32_32x32x16_bf16 v[64:79], a[136:139], v[200:203], v[64:79]\n"
"v_add_f32 v233, v233, v231\n"
"v_cvt_pk_bf16_f32 v155, v230, v231\n"
"v_exp_f32 v224, v120\n"
"s_bfe_u32 s95, s84, 0x20003\n"
"s_lshl_b32 s95, s95, 2\n"
"v_mfma_f32_32x32x16_bf16 v[64:79], a[140:143], v[204:207], v[64:79]\n"
"ds_read_b128 a[200:203], v239 offset:2048\n"
"v_add_f32 v233, v233, v224\n"
"v_exp_f32 v225, v121\n"
"s_add_u32 s95, s95, s83\n"
"s_lshl_b32 s95, s95, 13\n"
"v_mfma_f32_32x32x16_bf16 v[64:79], a[144:147], v[0:3], v[64:79]\n"
"ds_read_b128 a[204:207], v239 offset:3072\n"
"v_add_f32 v233, v233, v225\n"
"v_cvt_pk_bf16_f32 v156, v224, v225\n"
"v_exp_f32 v226, v122\n"
"s_min_u32 s95, s95, LHOTEND_%=-LCP2_%=-8192\n"
"v_lshlrev_b32 v60, 3, v248\n"
"v_mfma_f32_32x32x16_bf16 v[64:79], a[148:151], v[4:7], v[64:79]\n"
"v_add_f32 v233, v233, v226\n"
"v_exp_f32 v227, v123\n"
"v_add_u32 v60, s95, v60\n"
"global_load_dword v63, v60, s[24:25]\n"
"v_mfma_f32_32x32x16_bf16 v[64:79], a[152:155], v[8:11], v[64:79]\n"
"ds_read_b128 a[208:211], v239 offset:4096\n"
"v_add_f32 v233, v233, v227\n"
"v_cvt_pk_bf16_f32 v157, v226, v227\n"
"v_exp_f32 v228, v124\n"
"v_mfma_f32_32x32x16_bf16 v[64:79], a[156:159], v[12:15], v[64:79]\n"
"ds_read_b128 a[212:215], v239 offset:5120\n"
"v_add_f32 v233, v233, v228\n"
"v_exp_f32 v229, v125\n"
"v_mfma_f32_32x32x16_bf16 v[80:95], a[160:163], v[192:195], v[160:175]\n"
"v_add_f32 v233, v233, v229\n"
"v_cvt_pk_bf16_f32 v158, v228, v229\n"
"v_exp_f32 v230, v126\n"
"s_add_u32 s92, s41, s46\n"
"s_and_b32 s93, s92, 0xffff\n"
"s_or_b32 m0, s93, 0x10000\n"
"v_mfma_f32_32x32x16_bf16 v[80:95], a[164:167], v[196:199], v[80:95]\n"
"ds_read_b128 a[216:219], v239 offset:6144\n"
"v_add_f32 v233, v233, v230\n"
"v_exp_f32 v231, v127\n"
"buffer_load_dwordx4 v248, s[36:39], s92 offen offset:0 sc1 lds\n"
"v_mfma_f32_32x32x16_bf16 v[80:95], a[168:171], v[200:203], v[80:95]\n"
"ds_read_b128 a[220:223], v239 offset:7168\n"
"v_add_f32 v233, v233, v231\n"
"v_cvt_pk_bf16_f32 v159, v230, v231\n"
"v_add_f32 v236, v232, v233\n"
"v_cmp_nge_f32 vcc, s50, v236\n"
"v_mfma_f32_32x32x16_bf16 v[80:95], a[172:175], v[204:207], v[80:95]\n"
"s_cbranch_vccnz Lfb34_%=\n"
"Lfr35_%=:\n"
"v_add_f32 v234, v234, v232\n"
"v_add_f32 v235, v235, v233\n"
"s_cmp_ge_u32 s49, s48\n"
"s_cbranch_scc1 Lmo37_%=\n"
"Lmb36_%=:\n"
"s_add_u32 s49, s49, 1\n"
"v_mfma_f32_32x32x16_bf16 v[80:95], a[176:179], v[0:3], v[80:95]\n"
"v_exp_f32 v224, v64\n"
"s_add_u32 s43, s43, 8192\n"
"v_mfma_f32_32x32x16_bf16 v[80:95], a[180:183], v[4:7], v[80:95]\n"
"v_exp_f32 v225, v65\n"
"buffer_load_dwordx4 v248, s[36:39], s92 offen offset:1024 sc1 lds\n"
"v_mfma_f32_32x32x16_bf16 v[80:95], a[184:187], v[8:11], v[80:95]\n"
"v_add_f32 v232, v224, v225\n"
"v_cvt_pk_bf16_f32 v128, v224, v225\n"
"v_exp_f32 v226, v66\n"
"s_and_b32 s94, s42, 0xffff\n"
"v_add_u32 v238, s94, v248\n"
"v_mfma_f32_32x32x16_bf16 v[80:95], a[188:191], v[12:15], v[80:95]\n"
"v_add_f32 v232, v232, v226\n"
"v_exp_f32 v227, v67\n"
"s_add_u32 s40, s40, 16384\n"
"s_add_u32 s41, s41, 8192\n"
"s_waitcnt lgkmcnt(0)\n"
"v_mfma_f32_32x32x16_bf16 a[0:15], a[192:195], v[144:147], a[0:15]\n"
"ds_read_b128 a[132:135], v238 offset:1024\n"
"ds_read_b128 a[128:131], v238 offset:0\n"
"v_add_f32 v232, v232, v227\n"
"v_cvt_pk_bf16_f32 v129, v226, v227\n"
"v_exp_f32 v228, v68\n"
"v_mfma_f32_32x32x16_bf16 a[16:31], a[196:199], v[144:147], a[16:31]\n"
"ds_read_b128 a[136:139], v238 offset:2048\n"
"v_exp_f32 v229, v69\n"
"v_add_f32 v232, v232, v228\n"
"s_add_u32 m0, s54, 131072\n"
"s_add_u32 s92, s53, 4096\n"
"buffer_load_dwordx4 v250, s[20:23], s92 offen offset:0 lds\n"
"v_mfma_f32_32x32x16_bf16 a[32:47], a[200:203], v[144:147], a[32:47]\n"
"ds_read_b128 a[140:143], v238 offset:3072\n"
"v_exp_f32 v230, v70\n"
"v_add_f32 v232, v232, v229\n"
"v_cvt_pk_bf16_f32 v130, v228, v229\n"
"v_mfma_f32_32x32x16_bf16 a[48:63], a[204:207], v[144:147], a[48:63]\n"
"ds_read_b128 a[148:151], v238 offset:5120\n"
"ds_read_b128 a[144:147], v238 offset:4096\n"
"v_exp_f32 v231, v71\n"
"v_add_f32 v232, v232, v230\n"
"s_add_u32 s92, s53, 3200\n"
"buffer_load_dwordx4 v250, s[20:23], s92 offen offset:1024 lds\n"
"v_mfma_f32_32x32x16_bf16 a[0:15], a[208:211], v[148:151], a[0:15]\n"
"ds_read_b128 a[152:155], v238 offset:6144\n"
"v_exp_f32 v224, v72\n"
"v_add_f32 v232, v232, v231\n"
"v_cvt_pk_bf16_f32 v131, v230, v231\n"
"v_mfma_f32_32x32x16_bf16 a[16:31], a[212:215], v[148:151], a[16:31]\n"
"ds_read_b128 a[156:159], v238 offset:7168\n"
"v_exp_f32 v225, v73\n"
"v_add_f32 v232, v232, v224\n"
"s_add_u32 s92, s53, 10240\n"
"buffer_load_dwordx4 v250, s[20:23], s92 offen offset:2048 lds\n"
"v_mfma_f32_32x32x16_bf16 a[32:47], a[216:219], v[148:151], a[32:47]\n"
"ds_read_b128 a[164:167], v238 offset:9216\n"
"ds_read_b128 a[160:163], v238 offset:8192\n"
"v_exp_f32 v226, v74\n"
"v_add_f32 v232, v232, v225\n"
"v_cvt_pk_bf16_f32 v132, v224, v225\n"
"v_mfma_f32_32x32x16_bf16 a[48:63], a[220:223], v[148:151], a[48:63]\n"
"ds_read_b128 a[168:171], v238 offset:10240\n"
"v_exp_f32 v227, v75\n"
"v_add_f32 v232, v232, v226\n"
"s_add_u32 s92, s53, 9344\n"
"buffer_load_dwordx4 v250, s[20:23], s92 offen offset:3072 lds\n"
"v_mfma_f32_32x32x16_bf16 a[64:79], a[192:195], v[152:155], a[64:79]\n"
"ds_read_b128 a[172:175], v238 offset:11264\n"
"v_exp_f32 v228, v76\n"
"v_add_f32 v232, v232, v227\n"
"v_cvt_pk_bf16_f32 v133, v226, v227\n"
"s_cmp_ge_u32 s49, s48\n"
"s_cbranch_scc1 Lmo40_%=\n"
"Lmb39_%=:\n"
"s_add_u32 s49, s49, 1\n"
"v_mfma_f32_32x32x16_bf16 a[80:95], a[196:199], v[152:155], a[80:95]\n"
"ds_read_b128 a[180:183], v238 offset:13312\n"
"ds_read_b128 a[176:179], v238 offset:12288\n"
"v_exp_f32 v229, v77\n"
"v_add_f32 v232, v232, v228\n"
"s_add_u32 m0, s54, 147456\n"
"s_add_u32 s92, s53, 69632\n"
"buffer_load_dwordx4 v250, s[20:23], s92 offen offset:0 lds\n"
"v_mfma_f32_32x32x16_bf16 a[96:111], a[200:203], v[152:155], a[96:111]\n"
"ds_read_b128 a[184:187], v238 offset:14336\n"
"v_exp_f32 v230, v78\n"
"v_add_f32 v232, v232, v229\n"
"v_cvt_pk_bf16_f32 v134, v228, v229\n"
"v_mfma_f32_32x32x16_bf16 a[112:127], a[204:207], v[152:155], a[112:127]\n"
"ds_read_b128 a[188:191], v238 offset:15360\n"
"v_exp_f32 v231, v79\n"
"v_add_f32 v232, v232, v230\n"
"s_add_u32 s92, s53, 68736\n"
"buffer_load_dwordx4 v250, s[20:23], s92 offen offset:1024 lds\n"
"v_mfma_f32_32x32x16_bf16 a[64:79], a[208:211], v[156:159], a[64:79]\n"
"v_exp_f32 v224, v80\n"
"v_exp_f32 v225, v81\n"
"v_add_f32 v232, v232, v231\n"
"v_cvt_pk_bf16_f32 v135, v230, v231\n"
"s_add_u32 s42, s42, 16384\n"
"v_mfma_f32_32x32x16_bf16 a[80:95], a[212:215], v[156:159], a[80:95]\n"
"v_exp_f32 v226, v82\n"
"v_exp_f32 v227, v83\n"
"v_add_f32 v233, v224, v225\n"
"v_cvt_pk_bf16_f32 v136, v224, v225\n"
"s_add_u32 s92, s53, 75776\n"
"buffer_load_dwordx4 v250, s[20:23], s92 offen offset:2048 lds\n"
"v_mfma_f32_32x32x16_bf16 a[96:111], a[216:219], v[156:159], a[96:111]\n"
"v_exp_f32 v228, v84\n"
"v_add_f32 v233, v233, v226\n"
"v_add_f32 v233, v233, v227\n"
"v_cvt_pk_bf16_f32 v137, v226, v227\n"
"s_and_b32 s94, s43, 0xffff\n"
"v_add_u32 v239, s94, v247\n"
"v_mfma_f32_32x32x16_bf16 a[112:127], a[220:223], v[156:159], a[112:127]\n"
"v_exp_f32 v229, v85\n"
"v_add_f32 v233, v233, v228\n"
"s_add_u32 s92, s53, 74880\n"
"buffer_load_dwordx4 v250, s[20:23], s92 offen offset:3072 lds\n"
"s_waitcnt lgkmcnt(0)\n"
"v_mfma_f32_32x32x16_bf16 v[96:111], a[128:131], v[192:195], v[160:175]\n"
"ds_read_b128 a[196:199], v239 offset:1024\n"
"ds_read_b128 a[192:195], v239 offset:0\n"
"v_add_f32 v233, v233, v229\n"
"v_cvt_pk_bf16_f32 v138, v228, v229\n"
"v_exp_f32 v230, v86\n"
"v_mfma_f32_32x32x16_bf16 v[96:111], a[132:135], v[196:199], v[96:111]\n"
"ds_read_b128 a[200:203], v239 offset:2048\n"
"v_add_f32 v233, v233, v230\n"
"v_exp_f32 v231, v87\n"
"v_mfma_f32_32x32x16_bf16 v[96:111], a[136:139], v[200:203], v[96:111]\n"
"ds_read_b128 a[204:207], v239 offset:3072\n"
"v_add_f32 v233, v233, v231\n"
"v_cvt_pk_bf16_f32 v139, v230, v231\n"
"v_exp_f32 v224, v88\n"
"v_mfma_f32_32x32x16_bf16 v[96:111], a[140:143], v[204:207], v[96:111]\n"
"ds_read_b128 a[212:215], v239 offset:5120\n"
"ds_read_b128 a[208:211], v239 offset:4096\n"
"v_add_f32 v233, v233, v224\n"
"v_exp_f32 v225, v89\n"
"v_mov_b32 v32, v235\n"
"v_mov_b32 v33, v235\n"
"s_nop 1\n"
"v_permlane32_swap_b32 v32, v33\n"
"v_add_f32 v32, v32, v33\n"
"v_rcp_f32 v34, v32\n"
"s_nop 0\n"
"v_accvgpr_read_b32 v36, a64\n"
"v_accvgpr_read_b32 v37, a65\n"
"v_accvgpr_read_b32 v38, a66\n"
"v_accvgpr_read_b32 v39, a67\n"
"v_accvgpr_read_b32 v40, a68\n"
"v_accvgpr_read_b32 v41, a69\n"
"v_accvgpr_read_b32 v42, a70\n"
"v_mfma_f32_32x32x16_bf16 v[96:111], a[144:147], v[0:3], v[96:111]\n"
"ds_read_b128 a[216:219], v239 offset:6144\n"
"v_add_f32 v233, v233, v225\n"
"v_cvt_pk_bf16_f32 v140, v224, v225\n"
"v_exp_f32 v226, v90\n"
"v_accvgpr_read_b32 v43, a71\n"
"v_mul_f32 v36, v36, v34\n"
"v_mul_f32 v37, v37, v34\n"
"v_mul_f32 v38, v38, v34\n"
"v_mul_f32 v39, v39, v34\n"
"v_mul_f32 v40, v40, v34\n"
"v_mul_f32 v41, v41, v34\n"
"v_mul_f32 v42, v42, v34\n"
"v_mul_f32 v43, v43, v34\n"
"v_cvt_pk_bf16_f32 v44, v36, v37\n"
"v_cvt_pk_bf16_f32 v45, v38, v39\n"
"v_cvt_pk_bf16_f32 v46, v40, v41\n"
"v_cvt_pk_bf16_f32 v47, v42, v43\n"
"v_mfma_f32_32x32x16_bf16 v[96:111], a[148:151], v[4:7], v[96:111]\n"
"ds_read_b128 a[220:223], v239 offset:7168\n"
"v_add_f32 v233, v233, v226\n"
"v_exp_f32 v227, v91\n"
"buffer_store_dwordx4 v[44:47], v237, s[16:19], s52 offen offset:0 sc1\n"
"v_accvgpr_read_b32 v48, a72\n"
"v_accvgpr_read_b32 v49, a73\n"
"v_accvgpr_read_b32 v50, a74\n"
"v_accvgpr_read_b32 v51, a75\n"
"v_accvgpr_read_b32 v52, a76\n"
"v_accvgpr_read_b32 v53, a77\n"
"v_accvgpr_read_b32 v54, a78\n"
"v_accvgpr_read_b32 v55, a79\n"
"v_mul_f32 v48, v48, v34\n"
"v_mul_f32 v49, v49, v34\n"
"v_mul_f32 v50, v50, v34\n"
"v_mul_f32 v51, v51, v34\n"
"v_mul_f32 v52, v52, v34\n"
"v_mfma_f32_32x32x16_bf16 v[96:111], a[152:155], v[8:11], v[96:111]\n"
"ds_read_b128 a[228:231], v239 offset:9216\n"
"ds_read_b128 a[224:227], v239 offset:8192\n"
"v_add_f32 v233, v233, v227\n"
"v_cvt_pk_bf16_f32 v141, v226, v227\n"
"v_exp_f32 v228, v92\n"
"v_mul_f32 v53, v53, v34\n"
"v_mul_f32 v54, v54, v34\n"
"v_mul_f32 v55, v55, v34\n"
"v_cvt_pk_bf16_f32 v56, v48, v49\n"
"v_cvt_pk_bf16_f32 v57, v50, v51\n"
"v_cvt_pk_bf16_f32 v58, v52, v53\n"
"v_cvt_pk_bf16_f32 v59, v54, v55\n"
"buffer_store_dwordx4 v[56:59], v237, s[16:19], s52 offen offset:512 sc1\n"
"v_accvgpr_read_b32 v36, a80\n"
"v_accvgpr_read_b32 v37, a81\n"
"v_accvgpr_read_b32 v38, a82\n"
"v_accvgpr_read_b32 v39, a83\n"
"v_accvgpr_read_b32 v40, a84\n"
"v_mfma_f32_32x32x16_bf16 v[96:111], a[156:159], v[12:15], v[96:111]\n"
"ds_read_b128 a[232:235], v239 offset:10240\n"
"v_add_f32 v233, v233, v228\n"
"v_exp_f32 v229, v93\n"
"v_accvgpr_read_b32 v41, a85\n"
"v_accvgpr_read_b32 v42, a86\n"
"v_accvgpr_read_b32 v43, a87\n"
"v_mul_f32 v36, v36, v34\n"
"v_mul_f32 v37, v37, v34\n"
"v_mul_f32 v38, v38, v34\n"
"v_mul_f32 v39, v39, v34\n"
"v_mul_f32 v40, v40, v34\n"
"v_mul_f32 v41, v41, v34\n"
"v_mul_f32 v42, v42, v34\n"
"v_mul_f32 v43, v43, v34\n"
"v_cvt_pk_bf16_f32 v44, v36, v37\n"
"v_cvt_pk_bf16_f32 v45, v38, v39\n"
"v_cvt_pk_bf16_f32 v46, v40, v41\n"
"v_mfma_f32_32x32x16_bf16 v[112:127], a[160:163], v[192:195], v[160:175]\n"
"ds_read_b128 a[236:239], v239 offset:11264\n"
"v_add_f32 v233, v233, v229\n"
"v_cvt_pk_bf16_f32 v142, v228, v229\n"
"v_exp_f32 v230, v94\n"
"v_cvt_pk_bf16_f32 v47, v42, v43\n"
"buffer_store_dwordx4 v[44:47], v237, s[16:19], s52 offen offset:1024 sc1\n"
"v_accvgpr_read_b32 v48, a88\n"
"v_accvgpr_read_b32 v49, a89\n"
"v_accvgpr_read_b32 v50, a90\n"
"v_accvgpr_read_b32 v51, a91\n"
"v_accvgpr_read_b32 v52, a92\n"
"v_accvgpr_read_b32 v53, a93\n"
"v_accvgpr_read_b32 v54, a94\n"
"v_accvgpr_read_b32 v55, a95\n"
"v_mul_f32 v48, v48, v34\n"
"v_mul_f32 v49, v49, v34\n"
"v_mul_f32 v50, v50, v34\n"
"v_mfma_f32_32x32x16_bf16 v[112:127], a[164:167], v[196:199], v[112:127]\n"
"ds_read_b128 a[244:247], v239 offset:13312\n"
"ds_read_b128 a[240:243], v239 offset:12288\n"
"v_add_f32 v233, v233, v230\n"
"v_exp_f32 v231, v95\n"
"v_mul_f32 v51, v51, v34\n"
"v_mul_f32 v52, v52, v34\n"
"v_mul_f32 v53, v53, v34\n"
"v_mul_f32 v54, v54, v34\n"
"v_mul_f32 v55, v55, v34\n"
"v_cvt_pk_bf16_f32 v56, v48, v49\n"
"v_cvt_pk_bf16_f32 v57, v50, v51\n"
"v_cvt_pk_bf16_f32 v58, v52, v53\n"
"v_cvt_pk_bf16_f32 v59, v54, v55\n"
"buffer_store_dwordx4 v[56:59], v237, s[16:19], s52 offen offset:1536 sc1\n"
"v_accvgpr_read_b32 v36, a96\n"
"v_accvgpr_read_b32 v37, a97\n"
"v_accvgpr_read_b32 v38, a98\n"
"v_accvgpr_read_b32 v39, a99\n"
"v_mfma_f32_32x32x16_bf16 v[112:127], a[168:171], v[200:203], v[112:127]\n"
"ds_read_b128 a[248:251], v239 offset:14336\n"
"v_add_f32 v233, v233, v231\n"
"v_cvt_pk_bf16_f32 v143, v230, v231\n"
"v_add_f32 v236, v232, v233\n"
"v_cmp_nge_f32 vcc, s50, v236\n"
"v_accvgpr_read_b32 v40, a100\n"
"v_accvgpr_read_b32 v41, a101\n"
"v_accvgpr_read_b32 v42, a102\n"
"v_accvgpr_read_b32 v43, a103\n"
"v_mul_f32 v36, v36, v34\n"
"v_mul_f32 v37, v37, v34\n"
"v_mul_f32 v38, v38, v34\n"
"v_mul_f32 v39, v39, v34\n"
"v_mul_f32 v40, v40, v34\n"
"v_mul_f32 v41, v41, v34\n"
"v_mul_f32 v42, v42, v34\n"
"v_mul_f32 v43, v43, v34\n"
"v_cvt_pk_bf16_f32 v44, v36, v37\n"
"v_mfma_f32_32x32x16_bf16 v[112:127], a[172:175], v[204:207], v[112:127]\n"
"ds_read_b128 a[252:255], v239 offset:15360\n"
"s_cbranch_vccnz Lfb42_%=\n"
"Lfr43_%=:\n"
"v_add_f32 v234, v234, v236\n"
"s_cmp_ge_u32 s49, s48\n"
"s_cbranch_scc1 Lmo45_%=\n"
"Lmb44_%=:\n"
"s_add_u32 s49, s49, 1\n"
"v_cvt_pk_bf16_f32 v45, v38, v39\n"
"v_cvt_pk_bf16_f32 v46, v40, v41\n"
"v_cvt_pk_bf16_f32 v47, v42, v43\n"
"buffer_store_dwordx4 v[44:47], v237, s[16:19], s52 offen offset:2048 sc1\n"
"v_accvgpr_read_b32 v48, a104\n"
"v_accvgpr_read_b32 v49, a105\n"
"v_accvgpr_read_b32 v50, a106\n"
"v_accvgpr_read_b32 v51, a107\n"
"v_accvgpr_read_b32 v52, a108\n"
"v_accvgpr_read_b32 v53, a109\n"
"v_accvgpr_read_b32 v54, a110\n"
"v_accvgpr_read_b32 v55, a111\n"
"v_mul_f32 v48, v48, v34\n"
"v_mul_f32 v49, v49, v34\n"
"v_mfma_f32_32x32x16_bf16 v[112:127], a[176:179], v[0:3], v[112:127]\n"
"v_exp_f32 v224, v96\n"
"s_add_u32 s43, s43, 16384\n"
"v_mul_f32 v50, v50, v34\n"
"v_mul_f32 v51, v51, v34\n"
"v_mul_f32 v52, v52, v34\n"
"v_mul_f32 v53, v53, v34\n"
"v_mul_f32 v54, v54, v34\n"
"v_mul_f32 v55, v55, v34\n"
"v_cvt_pk_bf16_f32 v56, v48, v49\n"
"v_cvt_pk_bf16_f32 v57, v50, v51\n"
"v_cvt_pk_bf16_f32 v58, v52, v53\n"
"v_cvt_pk_bf16_f32 v59, v54, v55\n"
"buffer_store_dwordx4 v[56:59], v237, s[16:19], s52 offen offset:2560 sc1\n"
"v_accvgpr_read_b32 v36, a112\n"
"v_accvgpr_read_b32 v37, a113\n"
"v_mfma_f32_32x32x16_bf16 v[112:127], a[180:183], v[4:7], v[112:127]\n"
"v_exp_f32 v225, v97\n"
"v_accvgpr_read_b32 v38, a114\n"
"v_accvgpr_read_b32 v39, a115\n"
"v_accvgpr_read_b32 v40, a116\n"
"v_accvgpr_read_b32 v41, a117\n"
"v_accvgpr_read_b32 v42, a118\n"
"v_accvgpr_read_b32 v43, a119\n"
"v_mul_f32 v36, v36, v34\n"
"v_mul_f32 v37, v37, v34\n"
"v_mul_f32 v38, v38, v34\n"
"v_mul_f32 v39, v39, v34\n"
"v_mul_f32 v40, v40, v34\n"
"v_mul_f32 v41, v41, v34\n"
"v_mul_f32 v42, v42, v34\n"
"v_mul_f32 v43, v43, v34\n"
"v_mfma_f32_32x32x16_bf16 v[112:127], a[184:187], v[8:11], v[112:127]\n"
"v_add_f32 v232, v224, v225\n"
"v_cvt_pk_bf16_f32 v144, v224, v225\n"
"v_exp_f32 v226, v98\n"
"s_and_b32 s94, s42, 0xffff\n"
"v_add_u32 v238, s94, v248\n"
"v_cvt_pk_bf16_f32 v44, v36, v37\n"
"v_cvt_pk_bf16_f32 v45, v38, v39\n"
"v_cvt_pk_bf16_f32 v46, v40, v41\n"
"v_cvt_pk_bf16_f32 v47, v42, v43\n"
"buffer_store_dwordx4 v[44:47], v237, s[16:19], s52 offen offset:3072 sc1\n"
"v_accvgpr_read_b32 v48, a120\n"
"v_accvgpr_read_b32 v49, a121\n"
"v_accvgpr_read_b32 v50, a122\n"
"v_accvgpr_read_b32 v51, a123\n"
"v_accvgpr_read_b32 v52, a124\n"
"v_accvgpr_read_b32 v53, a125\n"
"v_accvgpr_read_b32 v54, a126\n"
"v_accvgpr_read_b32 v55, a127\n"
"v_mfma_f32_32x32x16_bf16 v[112:127], a[188:191], v[12:15], v[112:127]\n"
"v_add_f32 v232, v232, v226\n"
"v_exp_f32 v227, v99\n"
"v_mul_f32 v48, v48, v34\n"
"v_mul_f32 v49, v49, v34\n"
"v_mul_f32 v50, v50, v34\n"
"v_mul_f32 v51, v51, v34\n"
"v_mul_f32 v52, v52, v34\n"
"v_mul_f32 v53, v53, v34\n"
"v_mul_f32 v54, v54, v34\n"
"v_mul_f32 v55, v55, v34\n"
"v_cvt_pk_bf16_f32 v56, v48, v49\n"
"v_cvt_pk_bf16_f32 v57, v50, v51\n"
"v_cvt_pk_bf16_f32 v58, v52, v53\n"
"v_cvt_pk_bf16_f32 v59, v54, v55\n"
"buffer_store_dwordx4 v[56:59], v237, s[16:19], s52 offen offset:3584 sc1\n"
"s_waitcnt vmcnt(8) lgkmcnt(0)\n"
"s_barrier\n"
"s_cmp_eq_u32 s86, 7\n"
"s_cbranch_scc1 LTE_%=\n"
"s_lshl_b32 s44, s86, 1\n"
"s_sub_u32 s44, 13, s44\n"
"s_lshl_b32 s94, s44, 2\n"
"s_add_u32 s49, s49, s94\n"
"LSU_%=:\n"
"s_waitcnt lgkmcnt(0)\n"
"v_mfma_f32_32x32x16_bf16 a[0:15], a[192:195], v[128:131], a[0:15]\n"
"ds_read_b128 a[132:135], v238 offset:1024\n"
"ds_read_b128 a[128:131], v238 offset:0\n"
"v_add_f32 v232, v232, v227\n"
"v_cvt_pk_bf16_f32 v145, v226, v227\n"
"v_exp_f32 v228, v100\n"
"s_add_u32 s92, s40, s45\n"
"s_and_b32 m0, s92, 0xffff\n"
"v_mfma_f32_32x32x16_bf16 a[16:31], a[196:199], v[128:131], a[16:31]\n"
"ds_read_b128 a[136:139], v238 offset:2048\n"
"v_exp_f32 v229, v101\n"
"v_add_f32 v232, v232, v228\n"
"buffer_load_dwordx4 v248, s[28:31], s92 offen offset:0 sc1 lds\n"
"v_mfma_f32_32x32x16_bf16 a[32:47], a[200:203], v[128:131], a[32:47]\n"
"ds_read_b128 a[140:143], v238 offset:3072\n"
"v_exp_f32 v230, v102\n"
"v_add_f32 v232, v232, v229\n"
"v_cvt_pk_bf16_f32 v146, v228, v229\n"
"v_mfma_f32_32x32x16_bf16 a[48:63], a[204:207], v[128:131], a[48:63]\n"
"ds_read_b128 a[148:151], v238 offset:5120\n"
"ds_read_b128 a[144:147], v238 offset:4096\n"
"v_exp_f32 v231, v103\n"
"v_add_f32 v232, v232, v230\n"
"buffer_load_dwordx4 v248, s[28:31], s92 offen offset:1024 sc1 lds\n"
"v_mfma_f32_32x32x16_bf16 a[0:15], a[208:211], v[132:135], a[0:15]\n"
"ds_read_b128 a[152:155], v238 offset:6144\n"
"v_exp_f32 v224, v104\n"
"v_add_f32 v232, v232, v231\n"
"v_cvt_pk_bf16_f32 v147, v230, v231\n"
"v_mfma_f32_32x32x16_bf16 a[16:31], a[212:215], v[132:135], a[16:31]\n"
"ds_read_b128 a[156:159], v238 offset:7168\n"
"v_exp_f32 v225, v105\n"
"v_add_f32 v232, v232, v224\n"
"buffer_load_dwordx4 v248, s[28:31], s92 offen offset:2048 sc1 lds\n"
"v_mfma_f32_32x32x16_bf16 a[32:47], a[216:219], v[132:135], a[32:47]\n"
"ds_read_b128 a[164:167], v238 offset:9216\n"
"ds_read_b128 a[160:163], v238 offset:8192\n"
"v_exp_f32 v226, v106\n"
"v_add_f32 v232, v232, v225\n"
"v_cvt_pk_bf16_f32 v148, v224, v225\n"
"v_mfma_f32_32x32x16_bf16 a[48:63], a[220:223], v[132:135], a[48:63]\n"
"ds_read_b128 a[168:171], v238 offset:10240\n"
"v_exp_f32 v227, v107\n"
"v_add_f32 v232, v232, v226\n"
"buffer_load_dwordx4 v248, s[28:31], s92 offen offset:3072 sc1 lds\n"
"v_mfma_f32_32x32x16_bf16 a[0:15], a[224:227], v[136:139], a[0:15]\n"
"ds_read_b128 a[172:175], v238 offset:11264\n"
"v_exp_f32 v228, v108\n"
"v_add_f32 v232, v232, v227\n"
"v_cvt_pk_bf16_f32 v149, v226, v227\n"
"s_add_u32 s92, s40, s45\n"
"s_add_u32 s92, s92, 4096\n"
"s_and_b32 m0, s92, 0xffff\n"
"v_mfma_f32_32x32x16_bf16 a[16:31], a[228:231], v[136:139], a[16:31]\n"
"ds_read_b128 a[180:183], v238 offset:13312\n"
"ds_read_b128 a[176:179], v238 offset:12288\n"
"v_exp_f32 v229, v109\n"
"v_add_f32 v232, v232, v228\n"
"buffer_load_dwordx4 v248, s[28:31], s92 offen offset:0 sc1 lds\n"
"v_mfma_f32_32x32x16_bf16 a[32:47], a[232:235], v[136:139], a[32:47]\n"
"ds_read_b128 a[184:187], v238 offset:14336\n"
"v_exp_f32 v230, v110\n"
"v_add_f32 v232, v232, v229\n"
"v_cvt_pk_bf16_f32 v150, v228, v229\n"
"v_mfma_f32_32x32x16_bf16 a[48:63], a[236:239], v[136:139], a[48:63]\n"
"ds_read_b128 a[188:191], v238 offset:15360\n"
"v_exp_f32 v231, v111\n"
"v_add_f32 v232, v232, v230\n"
"buffer_load_dwordx4 v248, s[28:31], s92 offen offset:1024 sc1 lds\n"
"v_mfma_f32_32x32x16_bf16 a[0:15], a[240:243], v[140:143], a[0:15]\n"
"v_exp_f32 v224, v112\n"
"v_exp_f32 v225, v113\n"
"v_add_f32 v232, v232, v231\n"
"v_cvt_pk_bf16_f32 v151, v230, v231\n"
"s_add_u32 s42, s42, 16384\n"
"v_mfma_f32_32x32x16_bf16 a[16:31], a[244:247], v[140:143], a[16:31]\n"
"v_exp_f32 v226, v114\n"
"v_exp_f32 v227, v115\n"
"v_add_f32 v233, v224, v225\n"
"v_cvt_pk_bf16_f32 v152, v224, v225\n"
"v_mfma_f32_32x32x16_bf16 a[32:47], a[248:251], v[140:143], a[32:47]\n"
"v_exp_f32 v228, v116\n"
"v_add_f32 v233, v233, v226\n"
"v_add_f32 v233, v233, v227\n"
"v_cvt_pk_bf16_f32 v153, v226, v227\n"
"s_and_b32 s94, s43, 0xffff\n"
"v_add_u32 v239, s94, v247\n"
"buffer_load_dwordx4 v248, s[28:31], s92 offen offset:2048 sc1 lds\n"
"v_mfma_f32_32x32x16_bf16 a[48:63], a[252:255], v[140:143], a[48:63]\n"
"v_exp_f32 v229, v117\n"
"v_add_f32 v233, v233, v228\n"
"buffer_load_dwordx4 v248, s[28:31], s92 offen offset:3072 sc1 lds\n"
"s_waitcnt lgkmcnt(0)\n"
"v_mfma_f32_32x32x16_bf16 v[64:79], a[128:131], v[192:195], v[160:175]\n"
"ds_read_b128 a[196:199], v239 offset:1024\n"
"ds_read_b128 a[192:195], v239 offset:0\n"
"v_add_f32 v233, v233, v229\n"
"v_cvt_pk_bf16_f32 v154, v228, v229\n"
"v_exp_f32 v230, v118\n"
"s_add_u32 s92, s41, s45\n"
"s_and_b32 s93, s92, 0xffff\n"
"s_or_b32 m0, s93, 0x10000\n"
"v_mfma_f32_32x32x16_bf16 v[64:79], a[132:135], v[196:199], v[64:79]\n"
"ds_read_b128 a[200:203], v239 offset:2048\n"
"v_add_f32 v233, v233, v230\n"
"v_exp_f32 v231, v119\n"
"buffer_load_dwordx4 v248, s[36:39], s92 offen offset:0 sc1 lds\n"
"v_mfma_f32_32x32x16_bf16 v[64:79], a[136:139], v[200:203], v[64:79]\n"
"ds_read_b128 a[204:207], v239 offset:3072\n"
"v_add_f32 v233, v233, v231\n"
"v_cvt_pk_bf16_f32 v155, v230, v231\n"
"v_exp_f32 v224, v120\n"
"v_mfma_f32_32x32x16_bf16 v[64:79], a[140:143], v[204:207], v[64:79]\n"
"ds_read_b128 a[212:215], v239 offset:5120\n"
"ds_read_b128 a[208:211], v239 offset:4096\n"
"v_add_f32 v233, v233, v224\n"
"v_exp_f32 v225, v121\n"
"buffer_load_dwordx4 v248, s[36:39], s92 offen offset:1024 sc1 lds\n"
"v_mfma_f32_32x32x16_bf16 v[64:79], a[144:147], v[0:3], v[64:79]\n"
"ds_read_b128 a[216:219], v239 offset:6144\n"
"v_add_f32 v233, v233, v225\n"
"v_cvt_pk_bf16_f32 v156, v224, v225\n"
"v_exp_f32 v226, v122\n"
"v_mfma_f32_32x32x16_bf16 v[64:79], a[148:151], v[4:7], v[64:79]\n"
"ds_read_b128 a[220:223], v239 offset:7168\n"
"v_add_f32 v233, v233, v226\n"
"v_exp_f32 v227, v123\n"
"buffer_load_dwordx4 v248, s[36:39], s92 offen offset:2048 sc1 lds\n"
"v_mfma_f32_32x32x16_bf16 v[64:79], a[152:155], v[8:11], v[64:79]\n"
"ds_read_b128 a[228:231], v239 offset:9216\n"
"ds_read_b128 a[224:227], v239 offset:8192\n"
"v_add_f32 v233, v233, v227\n"
"v_cvt_pk_bf16_f32 v157, v226, v227\n"
"v_exp_f32 v228, v124\n"
"v_mfma_f32_32x32x16_bf16 v[64:79], a[156:159], v[12:15], v[64:79]\n"
"ds_read_b128 a[232:235], v239 offset:10240\n"
"v_add_f32 v233, v233, v228\n"
"v_exp_f32 v229, v125\n"
"buffer_load_dwordx4 v248, s[36:39], s92 offen offset:3072 sc1 lds\n"
"v_mfma_f32_32x32x16_bf16 v[80:95], a[160:163], v[192:195], v[160:175]\n"
"ds_read_b128 a[236:239], v239 offset:11264\n"
"v_add_f32 v233, v233, v229\n"
"v_cvt_pk_bf16_f32 v158, v228, v229\n"
"v_exp_f32 v230, v126\n"
"s_add_u32 s92, s41, s45\n"
"s_add_u32 s92, s92, 4096\n"
"s_and_b32 s93, s92, 0xffff\n"
"s_or_b32 m0, s93, 0x10000\n"
"v_mfma_f32_32x32x16_bf16 v[80:95], a[164:167], v[196:199], v[80:95]\n"
"ds_read_b128 a[244:247], v239 offset:13312\n"
"ds_read_b128 a[240:243], v239 offset:12288\n"
"v_add_f32 v233, v233, v230\n"
"v_exp_f32 v231, v127\n"
"buffer_load_dwordx4 v248, s[36:39], s92 offen offset:0 sc1 lds\n"
"v_mfma_f32_32x32x16_bf16 v[80:95], a[168:171], v[200:203], v[80:95]\n"
"ds_read_b128 a[248:251], v239 offset:14336\n"
"v_add_f32 v233, v233, v231\n"
"v_cvt_pk_bf16_f32 v159, v230, v231\n"
"v_add_f32 v236, v232, v233\n"
"v_cmp_nge_f32 vcc, s50, v236\n"
"v_mfma_f32_32x32x16_bf16 v[80:95], a[172:175], v[204:207], v[80:95]\n"
"ds_read_b128 a[252:255], v239 offset:15360\n"
"s_cbranch_vccnz Lfb109_%=\n"
"Lfr110_%=:\n"
"v_add_f32 v234, v234, v236\n"
"v_mfma_f32_32x32x16_bf16 v[80:95], a[176:179], v[0:3], v[80:95]\n"
"v_exp_f32 v224, v64\n"
"s_add_u32 s43, s43, 16384\n"
"v_mfma_f32_32x32x16_bf16 v[80:95], a[180:183], v[4:7], v[80:95]\n"
"v_exp_f32 v225, v65\n"
"buffer_load_dwordx4 v248, s[36:39], s92 offen offset:1024 sc1 lds\n"
"v_mfma_f32_32x32x16_bf16 v[80:95], a[184:187], v[8:11], v[80:95]\n"
"v_add_f32 v232, v224, v225\n"
"v_cvt_pk_bf16_f32 v128, v224, v225\n"
"v_exp_f32 v226, v66\n"
"s_and_b32 s94, s42, 0xffff\n"
"v_add_u32 v238, s94, v248\n"
"buffer_load_dwordx4 v248, s[36:39], s92 offen offset:2048 sc1 lds\n"
"v_mfma_f32_32x32x16_bf16 v[80:95], a[188:191], v[12:15], v[80:95]\n"
"v_add_f32 v232, v232, v226\n"
"v_exp_f32 v227, v67\n"
"buffer_load_dwordx4 v248, s[36:39], s92 offen offset:3072 sc1 lds\n"
"s_add_u32 s40, s40, 32768\n"
"s_add_u32 s41, s41, 32768\n"
"s_waitcnt lgkmcnt(0)\n"
"v_mfma_f32_32x32x16_bf16 a[0:15], a[192:195], v[144:147], a[0:15]\n"
"ds_read_b128 a[132:135], v238 offset:1024\n"
"ds_read_b128 a[128:131], v238 offset:0\n"
"v_add_f32 v232, v232, v227\n"
"v_cvt_pk_bf16_f32 v129, v226, v227\n"
"v_exp_f32 v228, v68\n"
"v_mfma_f32_32x32x16_bf16 a[16:31], a[196:199], v[144:147], a[16:31]\n"
"ds_read_b128 a[136:139], v238 offset:2048\n"
"v_exp_f32 v229, v69\n"
"v_add_f32 v232, v232, v228\n"
"v_mfma_f32_32x32x16_bf16 a[32:47], a[200:203], v[144:147], a[32:47]\n"
"ds_read_b128 a[140:143], v238 offset:3072\n"
"v_exp_f32 v230, v70\n"
"v_add_f32 v232, v232, v229\n"
"v_cvt_pk_bf16_f32 v130, v228, v229\n"
"v_mfma_f32_32x32x16_bf16 a[48:63], a[204:207], v[144:147], a[48:63]\n"
"ds_read_b128 a[148:151], v238 offset:5120\n"
"ds_read_b128 a[144:147], v238 offset:4096\n"
"v_exp_f32 v231, v71\n"
"v_add_f32 v232, v232, v230\n"
"v_mfma_f32_32x32x16_bf16 a[0:15], a[208:211], v[148:151], a[0:15]\n"
"ds_read_b128 a[152:155], v238 offset:6144\n"
"v_exp_f32 v224, v72\n"
"v_add_f32 v232, v232, v231\n"
"v_cvt_pk_bf16_f32 v131, v230, v231\n"
"v_mfma_f32_32x32x16_bf16 a[16:31], a[212:215], v[148:151], a[16:31]\n"
"ds_read_b128 a[156:159], v238 offset:7168\n"
"v_exp_f32 v225, v73\n"
"v_add_f32 v232, v232, v224\n"
"v_mfma_f32_32x32x16_bf16 a[32:47], a[216:219], v[148:151], a[32:47]\n"
"ds_read_b128 a[164:167], v238 offset:9216\n"
"ds_read_b128 a[160:163], v238 offset:8192\n"
"v_exp_f32 v226, v74\n"
"v_add_f32 v232, v232, v225\n"
"v_cvt_pk_bf16_f32 v132, v224, v225\n"
"v_mfma_f32_32x32x16_bf16 a[48:63], a[220:223], v[148:151], a[48:63]\n"
"ds_read_b128 a[168:171], v238 offset:10240\n"
"v_exp_f32 v227, v75\n"
"v_add_f32 v232, v232, v226\n"
"v_mfma_f32_32x32x16_bf16 a[0:15], a[224:227], v[152:155], a[0:15]\n"
"ds_read_b128 a[172:175], v238 offset:11264\n"
"v_exp_f32 v228, v76\n"
"v_add_f32 v232, v232, v227\n"
"v_cvt_pk_bf16_f32 v133, v226, v227\n"
"v_mfma_f32_32x32x16_bf16 a[16:31], a[228:231], v[152:155], a[16:31]\n"
"ds_read_b128 a[180:183], v238 offset:13312\n"
"ds_read_b128 a[176:179], v238 offset:12288\n"
"v_exp_f32 v229, v77\n"
"v_add_f32 v232, v232, v228\n"
"v_mfma_f32_32x32x16_bf16 a[32:47], a[232:235], v[152:155], a[32:47]\n"
"ds_read_b128 a[184:187], v238 offset:14336\n"
"v_exp_f32 v230, v78\n"
"v_add_f32 v232, v232, v229\n"
"v_cvt_pk_bf16_f32 v134, v228, v229\n"
"v_mfma_f32_32x32x16_bf16 a[48:63], a[236:239], v[152:155], a[48:63]\n"
"ds_read_b128 a[188:191], v238 offset:15360\n"
"v_exp_f32 v231, v79\n"
"v_add_f32 v232, v232, v230\n"
"v_mfma_f32_32x32x16_bf16 a[0:15], a[240:243], v[156:159], a[0:15]\n"
"v_exp_f32 v224, v80\n"
"v_exp_f32 v225, v81\n"
"v_add_f32 v232, v232, v231\n"
"v_cvt_pk_bf16_f32 v135, v230, v231\n"
"s_add_u32 s42, s42, 16384\n"
"v_mfma_f32_32x32x16_bf16 a[16:31], a[244:247], v[156:159], a[16:31]\n"
"v_exp_f32 v226, v82\n"
"v_exp_f32 v227, v83\n"
"v_add_f32 v233, v224, v225\n"
"v_cvt_pk_bf16_f32 v136, v224, v225\n"
"v_mfma_f32_32x32x16_bf16 a[32:47], a[248:251], v[156:159], a[32:47]\n"
"v_exp_f32 v228, v84\n"
"v_add_f32 v233, v233, v226\n"
"v_add_f32 v233, v233, v227\n"
"v_cvt_pk_bf16_f32 v137, v226, v227\n"
"s_and_b32 s94, s43, 0xffff\n"
"v_add_u32 v239, s94, v247\n"
"v_mfma_f32_32x32x16_bf16 a[48:63], a[252:255], v[156:159], a[48:63]\n"
"v_exp_f32 v229, v85\n"
"v_add_f32 v233, v233, v228\n"
"s_waitcnt lgkmcnt(0)\n"
"v_mfma_f32_32x32x16_bf16 v[96:111], a[128:131], v[192:195], v[160:175]\n"
"ds_read_b128 a[196:199], v239 offset:1024\n"
"ds_read_b128 a[192:195], v239 offset:0\n"
"v_add_f32 v233, v233, v229\n"
"v_cvt_pk_bf16_f32 v138, v228, v229\n"
"v_exp_f32 v230, v86\n"
"v_mfma_f32_32x32x16_bf16 v[96:111], a[132:135], v[196:199], v[96:111]\n"
"ds_read_b128 a[200:203], v239 offset:2048\n"
"v_add_f32 v233, v233, v230\n"
"v_exp_f32 v231, v87\n"
"v_mfma_f32_32x32x16_bf16 v[96:111], a[136:139], v[200:203], v[96:111]\n"
"ds_read_b128 a[204:207], v239 offset:3072\n"
"v_add_f32 v233, v233, v231\n"
"v_cvt_pk_bf16_f32 v139, v230, v231\n"
"v_exp_f32 v224, v88\n"
"v_mfma_f32_32x32x16_bf16 v[96:111], a[140:143], v[204:207], v[96:111]\n"
"ds_read_b128 a[212:215], v239 offset:5120\n"
"ds_read_b128 a[208:211], v239 offset:4096\n"
"v_add_f32 v233, v233, v224\n"
"v_exp_f32 v225, v89\n"
"v_mfma_f32_32x32x16_bf16 v[96:111], a[144:147], v[0:3], v[96:111]\n"
"ds_read_b128 a[216:219], v239 offset:6144\n"
"v_add_f32 v233, v233, v225\n"
"v_cvt_pk_bf16_f32 v140, v224, v225\n"
"v_exp_f32 v226, v90\n"
"v_mfma_f32_32x32x16_bf16 v[96:111], a[148:151], v[4:7], v[96:111]\n"
"ds_read_b128 a[220:223], v239 offset:7168\n"
"v_add_f32 v233, v233, v226\n"
"v_exp_f32 v227, v91\n"
"v_mfma_f32_32x32x16_bf16 v[96:111], a[152:155], v[8:11], v[96:111]\n"
"ds_read_b128 a[228:231], v239 offset:9216\n"
"ds_read_b128 a[224:227], v239 offset:8192\n"
"v_add_f32 v233, v233, v227\n"
"v_cvt_pk_bf16_f32 v141, v226, v227\n"
"v_exp_f32 v228, v92\n"
"v_mfma_f32_32x32x16_bf16 v[96:111], a[156:159], v[12:15], v[96:111]\n"
"ds_read_b128 a[232:235], v239 offset:10240\n"
"v_add_f32 v233, v233, v228\n"
"v_exp_f32 v229, v93\n"
"v_mfma_f32_32x32x16_bf16 v[112:127], a[160:163], v[192:195], v[160:175]\n"
"ds_read_b128 a[236:239], v239 offset:11264\n"
"v_add_f32 v233, v233, v229\n"
"v_cvt_pk_bf16_f32 v142, v228, v229\n"
"v_exp_f32 v230, v94\n"
"v_mfma_f32_32x32x16_bf16 v[112:127], a[164:167], v[196:199], v[112:127]\n"
"ds_read_b128 a[244:247], v239 offset:13312\n"
"ds_read_b128 a[240:243], v239 offset:12288\n"
"v_add_f32 v233, v233, v230\n"
"v_exp_f32 v231, v95\n"
"v_mfma_f32_32x32x16_bf16 v[112:127], a[168:171], v[200:203], v[112:127]\n"
"ds_read_b128 a[248:251], v239 offset:14336\n"
"v_add_f32 v233, v233, v231\n"
"v_cvt_pk_bf16_f32 v143, v230, v231\n"
"v_add_f32 v236, v232, v233\n"
"v_cmp_nge_f32 vcc, s50, v236\n"
"v_mfma_f32_32x32x16_bf16 v[112:127], a[172:175], v[204:207], v[112:127]\n"
"ds_read_b128 a[252:255], v239 offset:15360\n"
"s_cbranch_vccnz Lfb111_%=\n"
"Lfr112_%=:\n"
"v_add_f32 v234, v234, v236\n"
"v_mfma_f32_32x32x16_bf16 v[112:127], a[176:179], v[0:3], v[112:127]\n"
"v_exp_f32 v224, v96\n"
"s_add_u32 s43, s43, 16384\n"
"v_mfma_f32_32x32x16_bf16 v[112:127], a[180:183], v[4:7], v[112:127]\n"
"v_exp_f32 v225, v97\n"
"v_mfma_f32_32x32x16_bf16 v[112:127], a[184:187], v[8:11], v[112:127]\n"
"v_add_f32 v232, v224, v225\n"
"v_cvt_pk_bf16_f32 v144, v224, v225\n"
"v_exp_f32 v226, v98\n"
"s_and_b32 s94, s42, 0xffff\n"
"v_add_u32 v238, s94, v248\n"
"v_mfma_f32_32x32x16_bf16 v[112:127], a[188:191], v[12:15], v[112:127]\n"
"v_add_f32 v232, v232, v226\n"
"v_exp_f32 v227, v99\n"
"s_waitcnt vmcnt(0) lgkmcnt(0)\n"
"v_mov_b32 v253, s82\n"
"s_mov_b64 exec, 1\n"
"buffer_store_dword v253, off, s[88:91], s87 sc1\n"
"s_mov_b64 exec, -1\n"
"s_barrier\n"
"s_sub_u32 s44, s44, 1\n"
"s_cmp_lg_u32 s44, 0\n"
"s_cbranch_scc1 LSU_%=\n"
"s_waitcnt lgkmcnt(0)\n"
"v_mfma_f32_32x32x16_bf16 a[0:15], a[192:195], v[128:131], a[0:15]\n"
"ds_read_b128 a[132:135], v238 offset:1024\n"
"ds_read_b128 a[128:131], v238 offset:0\n"
"v_add_f32 v232, v232, v227\n"
"v_cvt_pk_bf16_f32 v145, v226, v227\n"
"v_exp_f32 v228, v100\n"
"s_lshr_b32 s95, s84, 3\n"
"s_lshl_b32 s95, s95, 2\n"
"s_add_u32 s24, s95, s83\n"
"v_mfma_f32_32x32x16_bf16 a[16:31], a[196:199], v[128:131], a[16:31]\n"
"ds_read_b128 a[136:139], v238 offset:2048\n"
"v_exp_f32 v229, v101\n"
"v_add_f32 v232, v232, v228\n"
"s_and_b32 s95, s84, 7\n"
"s_lshl_b32 s95, s95, 19\n"
"s_lshl_b32 s25, s24, 12\n"
"s_add_u32 s95, s95, s25\n"
"v_mfma_f32_32x32x16_bf16 a[32:47], a[200:203], v[128:131], a[32:47]\n"
"ds_read_b128 a[140:143], v238 offset:3072\n"
"v_exp_f32 v230, v102\n"
"v_add_f32 v232, v232, v229\n"
"v_cvt_pk_bf16_f32 v146, v228, v229\n"
"v_mfma_f32_32x32x16_bf16 a[48:63], a[204:207], v[128:131], a[48:63]\n"
"ds_read_b128 a[148:151], v238 offset:5120\n"
"ds_read_b128 a[144:147], v238 offset:4096\n"
"v_exp_f32 v231, v103\n"
"v_add_f32 v232, v232, v230\n"
"v_and_b32 v60, 31, v255\n"
"v_lshlrev_b32 v60, 7, v60\n"
"v_add_u32 v60, s95, v60\n"
"v_mfma_f32_32x32x16_bf16 a[0:15], a[208:211], v[132:135], a[0:15]\n"
"ds_read_b128 a[152:155], v238 offset:6144\n"
"v_exp_f32 v224, v104\n"
"v_add_f32 v232, v232, v231\n"
"v_cvt_pk_bf16_f32 v147, v230, v231\n"
"v_mfma_f32_32x32x16_bf16 a[16:31], a[212:215], v[132:135], a[16:31]\n"
"ds_read_b128 a[156:159], v238 offset:7168\n"
"v_exp_f32 v225, v105\n"
"v_add_f32 v232, v232, v224\n"
"global_load_dword v56, v60, s[66:67]\n"
"v_mfma_f32_32x32x16_bf16 a[32:47], a[216:219], v[132:135], a[32:47]\n"
"ds_read_b128 a[164:167], v238 offset:9216\n"
"ds_read_b128 a[160:163], v238 offset:8192\n"
"v_exp_f32 v226, v106\n"
"v_add_f32 v232, v232, v225\n"
"v_cvt_pk_bf16_f32 v148, v224, v225\n"
"v_mfma_f32_32x32x16_bf16 a[48:63], a[220:223], v[132:135], a[48:63]\n"
"ds_read_b128 a[168:171], v238 offset:10240\n"
"v_exp_f32 v227, v107\n"
"v_add_f32 v232, v232, v226\n"
"s_bfe_u32 s95, s84, 0x20001\n"
"s_mul_i32 s95, s95, 896\n"
"s_mul_i32 s25, s24, 14336\n"
"s_add_u32 s95, s95, s25\n"
"v_mfma_f32_32x32x16_bf16 a[0:15], a[224:227], v[136:139], a[0:15]\n"
"ds_read_b128 a[172:175], v238 offset:11264\n"
"v_exp_f32 v228, v108\n"
"v_add_f32 v232, v232, v227\n"
"v_cvt_pk_bf16_f32 v149, v226, v227\n"
"s_cmp_ge_u32 s49, s48\n"
"s_cbranch_scc1 Lmo48_%=\n"
"Lmb47_%=:\n"
"s_add_u32 s49, s49, 1\n"
"v_bfe_u32 v60, v255, 3, 2\n"
"v_mul_u32_u24 v60, 3584, v60\n"
"v_and_b32 v59, 7, v255\n"
"v_mfma_f32_32x32x16_bf16 a[16:31], a[228:231], v[136:139], a[16:31]\n"
"ds_read_b128 a[180:183], v238 offset:13312\n"
"ds_read_b128 a[176:179], v238 offset:12288\n"
"v_exp_f32 v229, v109\n"
"v_add_f32 v232, v232, v228\n"
"v_mfma_f32_32x32x16_bf16 a[32:47], a[232:235], v[136:139], a[32:47]\n"
"ds_read_b128 a[184:187], v238 offset:14336\n"
"v_exp_f32 v230, v110\n"
"v_add_f32 v232, v232, v229\n"
"v_cvt_pk_bf16_f32 v150, v228, v229\n"
"v_min_u32 v59, 6, v59\n"
"v_lshl_add_u32 v60, v59, 7, v60\n"
"v_add_u32 v60, s95, v60\n"
"v_mfma_f32_32x32x16_bf16 a[48:63], a[236:239], v[136:139], a[48:63]\n"
"ds_read_b128 a[188:191], v238 offset:15360\n"
"v_exp_f32 v231, v111\n"
"v_add_f32 v232, v232, v230\n"
"v_mfma_f32_32x32x16_bf16 a[0:15], a[240:243], v[140:143], a[0:15]\n"
"v_exp_f32 v224, v112\n"
"v_exp_f32 v225, v113\n"
"v_add_f32 v232, v232, v231\n"
"v_cvt_pk_bf16_f32 v151, v230, v231\n"
"s_add_u32 s42, s42, 16384\n"
"global_load_dword v57, v60, s[68:69]\n"
"v_mfma_f32_32x32x16_bf16 a[16:31], a[244:247], v[140:143], a[16:31]\n"
"v_exp_f32 v226, v114\n"
"v_exp_f32 v227, v115\n"
"v_add_f32 v233, v224, v225\n"
"v_cvt_pk_bf16_f32 v152, v224, v225\n"
"v_mfma_f32_32x32x16_bf16 a[32:47], a[248:251], v[140:143], a[32:47]\n"
"v_exp_f32 v228, v116\n"
"v_add_f32 v233, v233, v226\n"
"v_add_f32 v233, v233, v227\n"
"v_cvt_pk_bf16_f32 v153, v226, v227\n"
"s_and_b32 s94, s43, 0xffff\n"
"v_add_u32 v239, s94, v247\n"
"global_load_dword v58, v60, s[70:71]\n"
"v_mfma_f32_32x32x16_bf16 a[48:63], a[252:255], v[140:143], a[48:63]\n"
"v_exp_f32 v229, v117\n"
"v_add_f32 v233, v233, v228\n"
"s_waitcnt lgkmcnt(0)\n"
"v_mfma_f32_32x32x16_bf16 v[64:79], a[128:131], v[192:195], v[160:175]\n"
"ds_read_b128 a[196:199], v239 offset:1024\n"
"ds_read_b128 a[192:195], v239 offset:0\n"
"v_add_f32 v233, v233, v229\n"
"v_cvt_pk_bf16_f32 v154, v228, v229\n"
"v_exp_f32 v230, v118\n"
"s_add_u32 s92, s41, s45\n"
"s_and_b32 s93, s92, 0xffff\n"
"s_or_b32 m0, s93, 0x10000\n"
"v_mfma_f32_32x32x16_bf16 v[64:79], a[132:135], v[196:199], v[64:79]\n"
"ds_read_b128 a[200:203], v239 offset:2048\n"
"v_add_f32 v233, v233, v230\n"
"v_exp_f32 v231, v119\n"
"buffer_load_dwordx4 v248, s[36:39], s92 offen offset:0 sc1 lds\n"
"v_mfma_f32_32x32x16_bf16 v[64:79], a[136:139], v[200:203], v[64:79]\n"
"ds_read_b128 a[204:207], v239 offset:3072\n"
"v_add_f32 v233, v233, v231\n"
"v_cvt_pk_bf16_f32 v155, v230, v231\n"
"v_exp_f32 v224, v120\n"
"v_mfma_f32_32x32x16_bf16 v[64:79], a[140:143], v[204:207], v[64:79]\n"
"ds_read_b128 a[212:215], v239 offset:5120\n"
"ds_read_b128 a[208:211], v239 offset:4096\n"
"v_add_f32 v233, v233, v224\n"
"v_exp_f32 v225, v121\n"
"buffer_load_dwordx4 v248, s[36:39], s92 offen offset:1024 sc1 lds\n"
"v_mfma_f32_32x32x16_bf16 v[64:79], a[144:147], v[0:3], v[64:79]\n"
"ds_read_b128 a[216:219], v239 offset:6144\n"
"v_add_f32 v233, v233, v225\n"
"v_cvt_pk_bf16_f32 v156, v224, v225\n"
"v_exp_f32 v226, v122\n"
"v_mfma_f32_32x32x16_bf16 v[64:79], a[148:151], v[4:7], v[64:79]\n"
"ds_read_b128 a[220:223], v239 offset:7168\n"
"v_add_f32 v233, v233, v226\n"
"v_exp_f32 v227, v123\n"
"buffer_load_dwordx4 v248, s[36:39], s92 offen offset:2048 sc1 lds\n"
"v_mfma_f32_32x32x16_bf16 v[64:79], a[152:155], v[8:11], v[64:79]\n"
"ds_read_b128 a[228:231], v239 offset:9216\n"
"ds_read_b128 a[224:227], v239 offset:8192\n"
"v_add_f32 v233, v233, v227\n"
"v_cvt_pk_bf16_f32 v157, v226, v227\n"
"v_exp_f32 v228, v124\n"
"v_mfma_f32_32x32x16_bf16 v[64:79], a[156:159], v[12:15], v[64:79]\n"
"ds_read_b128 a[232:235], v239 offset:10240\n"
"v_add_f32 v233, v233, v228\n"
"v_exp_f32 v229, v125\n"
"buffer_load_dwordx4 v248, s[36:39], s92 offen offset:3072 sc1 lds\n"
"v_mfma_f32_32x32x16_bf16 v[80:95], a[160:163], v[192:195], v[160:175]\n"
"ds_read_b128 a[236:239], v239 offset:11264\n"
"v_add_f32 v233, v233, v229\n"
"v_cvt_pk_bf16_f32 v158, v228, v229\n"
"v_exp_f32 v230, v126\n"
"s_add_u32 s92, s41, s45\n"
"s_add_u32 s92, s92, 4096\n"
"s_and_b32 s93, s92, 0xffff\n"
"s_or_b32 m0, s93, 0x10000\n"
"v_mfma_f32_32x32x16_bf16 v[80:95], a[164:167], v[196:199], v[80:95]\n"
"ds_read_b128 a[244:247], v239 offset:13312\n"
"ds_read_b128 a[240:243], v239 offset:12288\n"
"v_add_f32 v233, v233, v230\n"
"v_exp_f32 v231, v127\n"
"buffer_load_dwordx4 v248, s[36:39], s92 offen offset:0 sc1 lds\n"
"v_mfma_f32_32x32x16_bf16 v[80:95], a[168:171], v[200:203], v[80:95]\n"
"ds_read_b128 a[248:251], v239 offset:14336\n"
"v_add_f32 v233, v233, v231\n"
"v_cvt_pk_bf16_f32 v159, v230, v231\n"
"v_add_f32 v236, v232, v233\n"
"v_cmp_nge_f32 vcc, s50, v236\n"
"v_mfma_f32_32x32x16_bf16 v[80:95], a[172:175], v[204:207], v[80:95]\n"
"ds_read_b128 a[252:255], v239 offset:15360\n"
"s_cbranch_vccnz Lfb50_%=\n"
"Lfr51_%=:\n"
"v_add_f32 v234, v234, v236\n"
"s_cmp_ge_u32 s49, s48\n"
"s_cbranch_scc1 Lmo53_%=\n"
"Lmb52_%=:\n"
"s_add_u32 s49, s49, 1\n"
"v_mfma_f32_32x32x16_bf16 v[80:95], a[176:179], v[0:3], v[80:95]\n"
"v_exp_f32 v224, v64\n"
"s_add_u32 s43, s43, 16384\n"
"v_mfma_f32_32x32x16_bf16 v[80:95], a[180:183], v[4:7], v[80:95]\n"
"v_exp_f32 v225, v65\n"
"buffer_load_dwordx4 v248, s[36:39], s92 offen offset:1024 sc1 lds\n"
"v_mfma_f32_32x32x16_bf16 v[80:95], a[184:187], v[8:11], v[80:95]\n"
"v_add_f32 v232, v224, v225\n"
"v_cvt_pk_bf16_f32 v128, v224, v225\n"
"v_exp_f32 v226, v66\n"
"s_and_b32 s94, s42, 0xffff\n"
"v_add_u32 v238, s94, v248\n"
"buffer_load_dwordx4 v248, s[36:39], s92 offen offset:2048 sc1 lds\n"
"v_mfma_f32_32x32x16_bf16 v[80:95], a[188:191], v[12:15], v[80:95]\n"
"v_add_f32 v232, v232, v226\n"
"v_exp_f32 v227, v67\n"
"buffer_load_dwordx4 v248, s[36:39], s92 offen offset:3072 sc1 lds\n"
"s_waitcnt lgkmcnt(0)\n"
"v_mfma_f32_32x32x16_bf16 a[0:15], a[192:195], v[144:147], a[0:15]\n"
"ds_read_b128 a[132:135], v238 offset:1024\n"
"ds_read_b128 a[128:131], v238 offset:0\n"
"v_add_f32 v232, v232, v227\n"
"v_cvt_pk_bf16_f32 v129, v226, v227\n"
"v_exp_f32 v228, v68\n"
"v_mfma_f32_32x32x16_bf16 a[16:31], a[196:199], v[144:147], a[16:31]\n"
"ds_read_b128 a[136:139], v238 offset:2048\n"
"v_exp_f32 v229, v69\n"
"v_add_f32 v232, v232, v228\n"
"v_mfma_f32_32x32x16_bf16 a[32:47], a[200:203], v[144:147], a[32:47]\n"
"ds_read_b128 a[140:143], v238 offset:3072\n"
"v_exp_f32 v230, v70\n"
"v_add_f32 v232, v232, v229\n"
"v_cvt_pk_bf16_f32 v130, v228, v229\n"
"v_mfma_f32_32x32x16_bf16 a[48:63], a[204:207], v[144:147], a[48:63]\n"
"ds_read_b128 a[148:151], v238 offset:5120\n"
"ds_read_b128 a[144:147], v238 offset:4096\n"
"v_exp_f32 v231, v71\n"
"v_add_f32 v232, v232, v230\n"
"v_mfma_f32_32x32x16_bf16 a[0:15], a[208:211], v[148:151], a[0:15]\n"
"ds_read_b128 a[152:155], v238 offset:6144\n"
"v_exp_f32 v224, v72\n"
"v_add_f32 v232, v232, v231\n"
"v_cvt_pk_bf16_f32 v131, v230, v231\n"
"v_mfma_f32_32x32x16_bf16 a[16:31], a[212:215], v[148:151], a[16:31]\n"
"ds_read_b128 a[156:159], v238 offset:7168\n"
"v_exp_f32 v225, v73\n"
"v_add_f32 v232, v232, v224\n"
"v_mfma_f32_32x32x16_bf16 a[32:47], a[216:219], v[148:151], a[32:47]\n"
"ds_read_b128 a[164:167], v238 offset:9216\n"
"ds_read_b128 a[160:163], v238 offset:8192\n"
"v_exp_f32 v226, v74\n"
"v_add_f32 v232, v232, v225\n"
"v_cvt_pk_bf16_f32 v132, v224, v225\n"
"v_mfma_f32_32x32x16_bf16 a[48:63], a[220:223], v[148:151], a[48:63]\n"
"ds_read_b128 a[168:171], v238 offset:10240\n"
"v_exp_f32 v227, v75\n"
"v_add_f32 v232, v232, v226\n"
"v_mfma_f32_32x32x16_bf16 a[0:15], a[224:227], v[152:155], a[0:15]\n"
"ds_read_b128 a[172:175], v238 offset:11264\n"
"v_exp_f32 v228, v76\n"
"v_add_f32 v232, v232, v227\n"
"v_cvt_pk_bf16_f32 v133, v226, v227\n"
"s_cmp_ge_u32 s49, s48\n"
"s_cbranch_scc1 Lmo56_%=\n"
"Lmb55_%=:\n"
"s_add_u32 s49, s49, 1\n"
"v_mfma_f32_32x32x16_bf16 a[16:31], a[228:231], v[152:155], a[16:31]\n"
"ds_read_b128 a[180:183], v238 offset:13312\n"
"ds_read_b128 a[176:179], v238 offset:12288\n"
"v_exp_f32 v229, v77\n"
"v_add_f32 v232, v232, v228\n"
"v_mfma_f32_32x32x16_bf16 a[32:47], a[232:235], v[152:155], a[32:47]\n"
"ds_read_b128 a[184:187], v238 offset:14336\n"
"v_exp_f32 v230, v78\n"
"v_add_f32 v232, v232, v229\n"
"v_cvt_pk_bf16_f32 v134, v228, v229\n"
"v_mfma_f32_32x32x16_bf16 a[48:63], a[236:239], v[152:155], a[48:63]\n"
"ds_read_b128 a[188:191], v238 offset:15360\n"
"v_exp_f32 v231, v79\n"
"v_add_f32 v232, v232, v230\n"
"v_mfma_f32_32x32x16_bf16 a[0:15], a[240:243], v[156:159], a[0:15]\n"
"v_exp_f32 v224, v80\n"
"v_exp_f32 v225, v81\n"
"v_add_f32 v232, v232, v231\n"
"v_cvt_pk_bf16_f32 v135, v230, v231\n"
"s_add_u32 s42, s42, 16384\n"
"v_mfma_f32_32x32x16_bf16 a[16:31], a[244:247], v[156:159], a[16:31]\n"
"v_exp_f32 v226, v82\n"
"v_exp_f32 v227, v83\n"
"v_add_f32 v233, v224, v225\n"
"v_cvt_pk_bf16_f32 v136, v224, v225\n"
"v_mfma_f32_32x32x16_bf16 a[32:47], a[248:251], v[156:159], a[32:47]\n"
"v_exp_f32 v228, v84\n"
"v_add_f32 v233, v233, v226\n"
"v_add_f32 v233, v233, v227\n"
"v_cvt_pk_bf16_f32 v137, v226, v227\n"
"s_and_b32 s94, s43, 0xffff\n"
"v_add_u32 v239, s94, v247\n"
"v_mfma_f32_32x32x16_bf16 a[48:63], a[252:255], v[156:159], a[48:63]\n"
"v_exp_f32 v229, v85\n"
"v_add_f32 v233, v233, v228\n"
"s_waitcnt lgkmcnt(0)\n"
"v_mfma_f32_32x32x16_bf16 v[96:111], a[128:131], v[192:195], v[160:175]\n"
"ds_read_b128 a[196:199], v239 offset:1024\n"
"ds_read_b128 a[192:195], v239 offset:0\n"
"v_add_f32 v233, v233, v229\n"
"v_cvt_pk_bf16_f32 v138, v228, v229\n"
"v_exp_f32 v230, v86\n"
"v_mfma_f32_32x32x16_bf16 v[96:111], a[132:135], v[196:199], v[96:111]\n"
"ds_read_b128 a[200:203], v239 offset:2048\n"
"v_add_f32 v233, v233, v230\n"
"v_exp_f32 v231, v87\n"
"v_mfma_f32_32x32x16_bf16 v[96:111], a[136:139], v[200:203], v[96:111]\n"
"ds_read_b128 a[204:207], v239 offset:3072\n"
"v_add_f32 v233, v233, v231\n"
"v_cvt_pk_bf16_f32 v139, v230, v231\n"
"v_exp_f32 v224, v88\n"
"v_mfma_f32_32x32x16_bf16 v[96:111], a[140:143], v[204:207], v[96:111]\n"
"ds_read_b128 a[212:215], v239 offset:5120\n"
"ds_read_b128 a[208:211], v239 offset:4096\n"
"v_add_f32 v233, v233, v224\n"
"v_exp_f32 v225, v89\n"
"v_mfma_f32_32x32x16_bf16 v[96:111], a[144:147], v[0:3], v[96:111]\n"
"ds_read_b128 a[216:219], v239 offset:6144\n"
"v_add_f32 v233, v233, v225\n"
"v_cvt_pk_bf16_f32 v140, v224, v225\n"
"v_exp_f32 v226, v90\n"
"v_mfma_f32_32x32x16_bf16 v[96:111], a[148:151], v[4:7], v[96:111]\n"
"ds_read_b128 a[220:223], v239 offset:7168\n"
"v_add_f32 v233, v233, v226\n"
"v_exp_f32 v227, v91\n"
"v_mfma_f32_32x32x16_bf16 v[96:111], a[152:155], v[8:11], v[96:111]\n"
"ds_read_b128 a[228:231], v239 offset:9216\n"
"ds_read_b128 a[224:227], v239 offset:8192\n"
"v_add_f32 v233, v233, v227\n"
"v_cvt_pk_bf16_f32 v141, v226, v227\n"
"v_exp_f32 v228, v92\n"
"v_mfma_f32_32x32x16_bf16 v[96:111], a[156:159], v[12:15], v[96:111]\n"
"ds_read_b128 a[232:235], v239 offset:10240\n"
"v_add_f32 v233, v233, v228\n"
"v_exp_f32 v229, v93\n"
"v_mfma_f32_32x32x16_bf16 v[112:127], a[160:163], v[192:195], v[160:175]\n"
"ds_read_b128 a[236:239], v239 offset:11264\n"
"v_add_f32 v233, v233, v229\n"
"v_cvt_pk_bf16_f32 v142, v228, v229\n"
"v_exp_f32 v230, v94\n"
"v_mfma_f32_32x32x16_bf16 v[112:127], a[164:167], v[196:199], v[112:127]\n"
"ds_read_b128 a[244:247], v239 offset:13312\n"
"ds_read_b128 a[240:243], v239 offset:12288\n"
"v_add_f32 v233, v233, v230\n"
"v_exp_f32 v231, v95\n"
"v_mfma_f32_32x32x16_bf16 v[112:127], a[168:171], v[200:203], v[112:127]\n"
"ds_read_b128 a[248:251], v239 offset:14336\n"
"v_add_f32 v233, v233, v231\n"
"v_cvt_pk_bf16_f32 v143, v230, v231\n"
"v_add_f32 v236, v232, v233\n"
"v_cmp_nge_f32 vcc, s50, v236\n"
"v_mfma_f32_32x32x16_bf16 v[112:127], a[172:175], v[204:207], v[112:127]\n"
"ds_read_b128 a[252:255], v239 offset:15360\n"
"s_cbranch_vccnz Lfb58_%=\n"
"Lfr59_%=:\n"
"v_add_f32 v234, v234, v236\n"
"s_cmp_ge_u32 s49, s48\n"
"s_cbranch_scc1 Lmo61_%=\n"
"Lmb60_%=:\n"
"s_add_u32 s49, s49, 1\n"
"v_mfma_f32_32x32x16_bf16 v[112:127], a[176:179], v[0:3], v[112:127]\n"
"v_exp_f32 v224, v96\n"
"s_add_u32 s43, s43, 16384\n"
"v_mfma_f32_32x32x16_bf16 v[112:127], a[180:183], v[4:7], v[112:127]\n"
"v_exp_f32 v225, v97\n"
"v_mfma_f32_32x32x16_bf16 v[112:127], a[184:187], v[8:11], v[112:127]\n"
"v_add_f32 v232, v224, v225\n"
"v_cvt_pk_bf16_f32 v144, v224, v225\n"
"v_exp_f32 v226, v98\n"
"s_and_b32 s94, s42, 0xffff\n"
"v_add_u32 v238, s94, v248\n"
"v_mfma_f32_32x32x16_bf16 v[112:127], a[188:191], v[12:15], v[112:127]\n"
"v_add_f32 v232, v232, v226\n"
"v_exp_f32 v227, v99\n"
"s_waitcnt vmcnt(0) lgkmcnt(0)\n"
"s_barrier\n"
"LE_%=:\n"
"s_and_b32 s94, s43, 0xffff\n"
"v_add_u32 v239, s94, v247\n"
"s_and_b32 s92, s84, 0xe7\n"
"s_lshl_b32 s92, s92, 7\n"
"s_lshl_b32 s93, s83, 2\n"
"s_add_u32 s92, s92, s93\n"
"s_add_u32 s97, s92, 0x18000\n"
"v_and_b32 v62, 3, v255\n"
"v_lshlrev_b32 v62, 10, v62\n"
"buffer_load_dword v63, v62, s[88:91], s97 offen sc1\n"
"s_lshl_b32 s92, s86, 20\n"
"s_lshl_b32 s93, s83, 18\n"
"s_add_u32 s92, s92, s93\n"
"s_and_b32 s93, s84, 7\n"
"s_lshl_b32 s93, s93, 14\n"
"s_add_u32 s45, s92, s93\n"
"s_add_u32 s46, s45, 0x20000\n"
"s_waitcnt lgkmcnt(0)\n"
"v_mfma_f32_32x32x16_bf16 a[0:15], a[192:195], v[128:131], a[0:15]\n"
"v_add_f32 v232, v232, v227\n"
"v_cvt_pk_bf16_f32 v145, v226, v227\n"
"v_exp_f32 v228, v100\n"
"v_mfma_f32_32x32x16_bf16 a[16:31], a[196:199], v[128:131], a[16:31]\n"
"ds_read_b128 a[192:195], v239 offset:0\n"
"v_exp_f32 v229, v101\n"
"v_add_f32 v232, v232, v228\n"
"s_add_u32 m0, s54, 0\n"
"s_add_u32 s92, s53, 135168\n"
"buffer_load_dwordx4 v250, s[20:23], s92 offen offset:0 lds\n"
"v_mfma_f32_32x32x16_bf16 a[32:47], a[200:203], v[128:131], a[32:47]\n"
"ds_read_b128 a[196:199], v239 offset:1024\n"
"v_exp_f32 v230, v102\n"
"v_add_f32 v232, v232, v229\n"
"v_cvt_pk_bf16_f32 v146, v228, v229\n"
"v_mfma_f32_32x32x16_bf16 a[48:63], a[204:207], v[128:131], a[48:63]\n"
"ds_read_b128 a[200:203], v239 offset:2048\n"
"v_exp_f32 v231, v103\n"
"v_add_f32 v232, v232, v230\n"
"s_add_u32 s92, s53, 134272\n"
"buffer_load_dwordx4 v250, s[20:23], s92 offen offset:1024 lds\n"
"v_mfma_f32_32x32x16_bf16 a[0:15], a[208:211], v[132:135], a[0:15]\n"
"ds_read_b128 a[204:207], v239 offset:3072\n"
"v_exp_f32 v224, v104\n"
"v_add_f32 v232, v232, v231\n"
"v_cvt_pk_bf16_f32 v147, v230, v231\n"
"v_mfma_f32_32x32x16_bf16 a[16:31], a[212:215], v[132:135], a[16:31]\n"
"ds_read_b128 a[208:211], v239 offset:4096\n"
"v_exp_f32 v225, v105\n"
"v_add_f32 v232, v232, v224\n"
"s_add_u32 s92, s53, 141312\n"
"buffer_load_dwordx4 v250, s[20:23], s92 offen offset:2048 lds\n"
"v_mfma_f32_32x32x16_bf16 a[32:47], a[216:219], v[132:135], a[32:47]\n"
"ds_read_b128 a[212:215], v239 offset:5120\n"
"v_exp_f32 v226, v106\n"
"v_add_f32 v232, v232, v225\n"
"v_cvt_pk_bf16_f32 v148, v224, v225\n"
"v_mfma_f32_32x32x16_bf16 a[48:63], a[220:223], v[132:135], a[48:63]\n"
"ds_read_b128 a[216:219], v239 offset:6144\n"
"v_exp_f32 v227, v107\n"
"v_add_f32 v232, v232, v226\n"
"s_add_u32 s92, s53, 140416\n"
"buffer_load_dwordx4 v250, s[20:23], s92 offen offset:3072 lds\n"
"v_mfma_f32_32x32x16_bf16 a[0:15], a[224:227], v[136:139], a[0:15]\n"
"ds_read_b128 a[220:223], v239 offset:7168\n"
"v_exp_f32 v228, v108\n"
"v_add_f32 v232, v232, v227\n"
"v_cvt_pk_bf16_f32 v149, v226, v227\n"
"s_cmp_ge_u32 s49, s48\n"
"s_cbranch_scc1 Lmo64_%=\n"
"Lmb63_%=:\n"
"s_add_u32 s49, s49, 1\n"
"v_mfma_f32_32x32x16_bf16 a[16:31], a[228:231], v[136:139], a[16:31]\n"
"ds_read_b128 a[224:227], v239 offset:8192\n"
"v_exp_f32 v229, v109\n"
"v_add_f32 v232, v232, v228\n"
"s_add_u32 m0, s54, 16384\n"
"s_add_u32 s92, s53, 200704\n"
"buffer_load_dwordx4 v250, s[20:23], s92 offen offset:0 lds\n"
"v_mfma_f32_32x32x16_bf16 a[32:47], a[232:235], v[136:139], a[32:47]\n"
"ds_read_b128 a[228:231], v239 offset:9216\n"
"v_exp_f32 v230, v110\n"
"v_add_f32 v232, v232, v229\n"
"v_cvt_pk_bf16_f32 v150, v228, v229\n"
"v_mfma_f32_32x32x16_bf16 a[48:63], a[236:239], v[136:139], a[48:63]\n"
"ds_read_b128 a[232:235], v239 offset:10240\n"
"v_exp_f32 v231, v111\n"
"v_add_f32 v232, v232, v230\n"
"s_add_u32 s92, s53, 199808\n"
"buffer_load_dwordx4 v250, s[20:23], s92 offen offset:1024 lds\n"
"v_mfma_f32_32x32x16_bf16 a[0:15], a[240:243], v[140:143], a[0:15]\n"
"ds_read_b128 a[236:239], v239 offset:11264\n"
"v_exp_f32 v224, v112\n"
"v_exp_f32 v225, v113\n"
"v_add_f32 v232, v232, v231\n"
"v_cvt_pk_bf16_f32 v151, v230, v231\n"
"v_mfma_f32_32x32x16_bf16 a[16:31], a[244:247], v[140:143], a[16:31]\n"
"ds_read_b128 a[240:243], v239 offset:12288\n"
"v_exp_f32 v226, v114\n"
"v_exp_f32 v227, v115\n"
"v_add_f32 v233, v224, v225\n"
"v_cvt_pk_bf16_f32 v152, v224, v225\n"
"s_add_u32 s92, s53, 206848\n"
"buffer_load_dwordx4 v250, s[20:23], s92 offen offset:2048 lds\n"
"v_mfma_f32_32x32x16_bf16 a[32:47], a[248:251], v[140:143], a[32:47]\n"
"ds_read_b128 a[244:247], v239 offset:13312\n"
"v_exp_f32 v228, v116\n"
"v_add_f32 v233, v233, v226\n"
"v_add_f32 v233, v233, v227\n"
"v_cvt_pk_bf16_f32 v153, v226, v227\n"
"v_mfma_f32_32x32x16_bf16 a[48:63], a[252:255], v[140:143], a[48:63]\n"
"ds_read_b128 a[248:251], v239 offset:14336\n"
"v_exp_f32 v229, v117\n"
"v_add_f32 v233, v233, v228\n"
"s_add_u32 s92, s53, 205952\n"
"buffer_load_dwordx4 v250, s[20:23], s92 offen offset:3072 lds\n"
"ds_read_b128 a[252:255], v239 offset:15360\n"
"v_add_f32 v233, v233, v229\n"
"v_cvt_pk_bf16_f32 v154, v228, v229\n"
"v_exp_f32 v230, v118\n"
"v_exp_f32 v231, v119\n"
"v_add_f32 v233, v233, v230\n"
"s_add_u32 m0, s54, 32768\n"
"s_add_u32 s92, s53, 266240\n"
"buffer_load_dwordx4 v250, s[20:23], s92 offen offset:0 lds\n"
"v_exp_f32 v224, v120\n"
"v_add_f32 v233, v233, v231\n"
"v_cvt_pk_bf16_f32 v155, v230, v231\n"
"v_exp_f32 v225, v121\n"
"v_add_f32 v233, v233, v224\n"
"s_add_u32 s92, s53, 265344\n"
"buffer_load_dwordx4 v250, s[20:23], s92 offen offset:1024 lds\n"
"v_exp_f32 v226, v122\n"
"v_add_f32 v233, v233, v225\n"
"v_cvt_pk_bf16_f32 v156, v224, v225\n"
"v_exp_f32 v227, v123\n"
"v_add_f32 v233, v233, v226\n"
"s_add_u32 s92, s53, 272384\n"
"buffer_load_dwordx4 v250, s[20:23], s92 offen offset:2048 lds\n"
"v_exp_f32 v228, v124\n"
"v_add_f32 v233, v233, v227\n"
"v_cvt_pk_bf16_f32 v157, v226, v227\n"
"v_exp_f32 v229, v125\n"
"v_add_f32 v233, v233, v228\n"
"s_add_u32 s92, s53, 271488\n"
"buffer_load_dwordx4 v250, s[20:23], s92 offen offset:3072 lds\n"
"v_exp_f32 v230, v126\n"
"v_add_f32 v233, v233, v229\n"
"v_cvt_pk_bf16_f32 v158, v228, v229\n"
"v_exp_f32 v231, v127\n"
"v_add_f32 v233, v233, v230\n"
"s_nop 0\n"
"v_add_f32 v233, v233, v231\n"
"v_cvt_pk_bf16_f32 v159, v230, v231\n"
"v_add_f32 v236, v232, v233\n"
"v_cmp_nge_f32 vcc, s50, v236\n"
"s_cbranch_vccnz Lfb66_%=\n"
"Lfr67_%=:\n"
"v_add_f32 v234, v234, v236\n"
"s_waitcnt vmcnt(12)\n"
"v_cmp_ne_u32 vcc, s82, v63\n"
"s_cbranch_vccnz Lps69_%=\n"
"Lpb68_%=:\n"
"s_waitcnt lgkmcnt(0)\n"
"v_mfma_f32_32x32x16_bf16 a[0:15], a[192:195], v[144:147], a[0:15]\n"
"buffer_load_dwordx4 a[64:67], v248, s[16:19], s45 offen offset:0 sc1\n"
"v_mfma_f32_32x32x16_bf16 a[16:31], a[196:199], v[144:147], a[16:31]\n"
"buffer_load_dwordx4 a[72:75], v248, s[16:19], s45 offen offset:1024 sc1\n"
"v_mfma_f32_32x32x16_bf16 a[32:47], a[200:203], v[144:147], a[32:47]\n"
"buffer_load_dwordx4 a[68:71], v248, s[16:19], s46 offen offset:0 sc1\n"
"v_mfma_f32_32x32x16_bf16 a[48:63], a[204:207], v[144:147], a[48:63]\n"
"v_mfma_f32_32x32x16_bf16 a[0:15], a[208:211], v[148:151], a[0:15]\n"
"buffer_load_dwordx4 a[76:79], v248, s[16:19], s46 offen offset:1024 sc1\n"
"v_mfma_f32_32x32x16_bf16 a[16:31], a[212:215], v[148:151], a[16:31]\n"
"buffer_load_dwordx4 a[80:83], v248, s[16:19], s45 offen offset:2048 sc1\n"
"v_mfma_f32_32x32x16_bf16 a[32:47], a[216:219], v[148:151], a[32:47]\n"
"buffer_load_dwordx4 a[88:91], v248, s[16:19], s45 offen offset:3072 sc1\n"
"v_mfma_f32_32x32x16_bf16 a[48:63], a[220:223], v[148:151], a[48:63]\n"
"v_mfma_f32_32x32x16_bf16 a[0:15], a[224:227], v[152:155], a[0:15]\n"
"buffer_load_dwordx4 a[84:87], v248, s[16:19], s46 offen offset:2048 sc1\n"
"v_mfma_f32_32x32x16_bf16 a[16:31], a[228:231], v[152:155], a[16:31]\n"
"buffer_load_dwordx4 a[92:95], v248, s[16:19], s46 offen offset:3072 sc1\n"
"v_mfma_f32_32x32x16_bf16 a[32:47], a[232:235], v[152:155], a[32:47]\n"
"s_add_u32 s45, s45, 4096\n"
"buffer_load_dwordx4 a[96:99], v248, s[16:19], s45 offen offset:0 sc1\n"
"v_mfma_f32_32x32x16_bf16 a[48:63], a[236:239], v[152:155], a[48:63]\n"
"v_mfma_f32_32x32x16_bf16 a[0:15], a[240:243], v[156:159], a[0:15]\n"
"buffer_load_dwordx4 a[104:107], v248, s[16:19], s45 offen offset:1024 sc1\n"
"v_mfma_f32_32x32x16_bf16 a[16:31], a[244:247], v[156:159], a[16:31]\n"
"s_add_u32 s46, s46, 4096\n"
"buffer_load_dwordx4 a[100:103], v248, s[16:19], s46 offen offset:0 sc1\n"
"v_mfma_f32_32x32x16_bf16 a[32:47], a[248:251], v[156:159], a[32:47]\n"
"buffer_load_dwordx4 a[108:111], v248, s[16:19], s46 offen offset:1024 sc1\n"
"v_mfma_f32_32x32x16_bf16 a[48:63], a[252:255], v[156:159], a[48:63]\n"
"v_mov_b32 v32, v234\n"
"v_mov_b32 v33, v234\n"
"s_nop 1\n"
"v_permlane32_swap_b32 v32, v33\n"
"v_add_f32 v32, v32, v33\n"
"v_rcp_f32 v34, v32\n"
"s_nop 0\n"
"s_nop 7\n"
"buffer_load_dwordx4 a[112:115], v248, s[16:19], s45 offen offset:2048 sc1\n"
"buffer_load_dwordx4 a[120:123], v248, s[16:19], s45 offen offset:3072 sc1\n"
"buffer_load_dwordx4 a[116:119], v248, s[16:19], s46 offen offset:2048 sc1\n"
"buffer_load_dwordx4 a[124:127], v248, s[16:19], s46 offen offset:3072 sc1\n"
"s_add_u32 m0, s54, 49152\n"
"s_add_u32 s92, s53, 331776\n"
"buffer_load_dwordx4 v250, s[20:23], s92 offen offset:0 lds\n"
"s_add_u32 s92, s53, 330880\n"
"buffer_load_dwordx4 v250, s[20:23], s92 offen offset:1024 lds\n"
"s_add_u32 s92, s53, 337920\n"
"buffer_load_dwordx4 v250, s[20:23], s92 offen offset:2048 lds\n"
"s_add_u32 s92, s53, 337024\n"
"buffer_load_dwordx4 v250, s[20:23], s92 offen offset:3072 lds\n"
"v_accvgpr_read_b32 v36, a0\n"
"v_accvgpr_read_b32 v37, a1\n"
"v_accvgpr_read_b32 v38, a2\n"
"v_accvgpr_read_b32 v39, a3\n"
"v_accvgpr_read_b32 v40, a4\n"
"v_accvgpr_read_b32 v41, a5\n"
"v_accvgpr_read_b32 v42, a6\n"
"v_accvgpr_read_b32 v43, a7\n"
"v_pk_mul_f32 v[36:37], v[36:37], v[34:35] op_sel_hi:[1,0]\n"
"v_pk_mul_f32 v[38:39], v[38:39], v[34:35] op_sel_hi:[1,0]\n"
"v_pk_mul_f32 v[40:41], v[40:41], v[34:35] op_sel_hi:[1,0]\n"
"v_pk_mul_f32 v[42:43], v[42:43], v[34:35] op_sel_hi:[1,0]\n"
"v_cvt_pk_bf16_f32 v44, v36, v37\n"
"v_cvt_pk_bf16_f32 v45, v38, v39\n"
"v_cvt_pk_bf16_f32 v46, v40, v41\n"
"v_cvt_pk_bf16_f32 v47, v42, v43\n"
"buffer_store_dwordx4 v[44:47], v237, s[16:19], s51 offen offset:0 sc1\n"
"v_accvgpr_read_b32 v48, a8\n"
"v_accvgpr_read_b32 v49, a9\n"
"v_accvgpr_read_b32 v50, a10\n"
"v_accvgpr_read_b32 v51, a11\n"
"v_accvgpr_read_b32 v52, a12\n"
"v_accvgpr_read_b32 v53, a13\n"
"v_accvgpr_read_b32 v54, a14\n"
"v_accvgpr_read_b32 v55, a15\n"
"v_pk_mul_f32 v[48:49], v[48:49], v[34:35] op_sel_hi:[1,0]\n"
"v_pk_mul_f32 v[50:51], v[50:51], v[34:35] op_sel_hi:[1,0]\n"
"v_pk_mul_f32 v[52:53], v[52:53], v[34:35] op_sel_hi:[1,0]\n"
"v_pk_mul_f32 v[54:55], v[54:55], v[34:35] op_sel_hi:[1,0]\n"
"v_cvt_pk_bf16_f32 v56, v48, v49\n"
"v_cvt_pk_bf16_f32 v57, v50, v51\n"
"v_cvt_pk_bf16_f32 v58, v52, v53\n"
"v_cvt_pk_bf16_f32 v59, v54, v55\n"
"buffer_store_dwordx4 v[56:59], v237, s[16:19], s51 offen offset:512 sc1\n"
"v_accvgpr_read_b32 v36, a16\n"
"v_accvgpr_read_b32 v37, a17\n"
"v_accvgpr_read_b32 v38, a18\n"
"v_accvgpr_read_b32 v39, a19\n"
"v_accvgpr_read_b32 v40, a20\n"
"v_accvgpr_read_b32 v41, a21\n"
"v_accvgpr_read_b32 v42, a22\n"
"v_accvgpr_read_b32 v43, a23\n"
"v_pk_mul_f32 v[36:37], v[36:37], v[34:35] op_sel_hi:[1,0]\n"
"v_pk_mul_f32 v[38:39], v[38:39], v[34:35] op_sel_hi:[1,0]\n"
"v_pk_mul_f32 v[40:41], v[40:41], v[34:35] op_sel_hi:[1,0]\n"
"v_pk_mul_f32 v[42:43], v[42:43], v[34:35] op_sel_hi:[1,0]\n"
"v_cvt_pk_bf16_f32 v44, v36, v37\n"
"v_cvt_pk_bf16_f32 v45, v38, v39\n"
"v_cvt_pk_bf16_f32 v46, v40, v41\n"
"v_cvt_pk_bf16_f32 v47, v42, v43\n"
"buffer_store_dwordx4 v[44:47], v237, s[16:19], s51 offen offset:1024 sc1\n"
"v_accvgpr_read_b32 v48, a24\n"
"v_accvgpr_read_b32 v49, a25\n"
"v_accvgpr_read_b32 v50, a26\n"
"v_accvgpr_read_b32 v51, a27\n"
"v_accvgpr_read_b32 v52, a28\n"
"v_accvgpr_read_b32 v53, a29\n"
"v_accvgpr_read_b32 v54, a30\n"
"v_accvgpr_read_b32 v55, a31\n"
"v_pk_mul_f32 v[48:49], v[48:49], v[34:35] op_sel_hi:[1,0]\n"
"v_pk_mul_f32 v[50:51], v[50:51], v[34:35] op_sel_hi:[1,0]\n"
"v_pk_mul_f32 v[52:53], v[52:53], v[34:35] op_sel_hi:[1,0]\n"
"v_pk_mul_f32 v[54:55], v[54:55], v[34:35] op_sel_hi:[1,0]\n"
"v_cvt_pk_bf16_f32 v56, v48, v49\n"
"v_cvt_pk_bf16_f32 v57, v50, v51\n"
"v_cvt_pk_bf16_f32 v58, v52, v53\n"
"v_cvt_pk_bf16_f32 v59, v54, v55\n"
"buffer_store_dwordx4 v[56:59], v237, s[16:19], s51 offen offset:1536 sc1\n"
"v_accvgpr_read_b32 v36, a32\n"
"v_accvgpr_read_b32 v37, a33\n"
"v_accvgpr_read_b32 v38, a34\n"
"v_accvgpr_read_b32 v39, a35\n"
"v_accvgpr_read_b32 v40, a36\n"
"v_accvgpr_read_b32 v41, a37\n"
"v_accvgpr_read_b32 v42, a38\n"
"v_accvgpr_read_b32 v43, a39\n"
"v_pk_mul_f32 v[36:37], v[36:37], v[34:35] op_sel_hi:[1,0]\n"
"v_pk_mul_f32 v[38:39], v[38:39], v[34:35] op_sel_hi:[1,0]\n"
"v_pk_mul_f32 v[40:41], v[40:41], v[34:35] op_sel_hi:[1,0]\n"
"v_pk_mul_f32 v[42:43], v[42:43], v[34:35] op_sel_hi:[1,0]\n"
"v_cvt_pk_bf16_f32 v44, v36, v37\n"
"v_cvt_pk_bf16_f32 v45, v38, v39\n"
"v_cvt_pk_bf16_f32 v46, v40, v41\n"
"v_cvt_pk_bf16_f32 v47, v42, v43\n"
"buffer_store_dwordx4 v[44:47], v237, s[16:19], s51 offen offset:2048 sc1\n"
"v_accvgpr_read_b32 v48, a40\n"
"v_accvgpr_read_b32 v49, a41\n"
"v_accvgpr_read_b32 v50, a42\n"
"v_accvgpr_read_b32 v51, a43\n"
"v_accvgpr_read_b32 v52, a44\n"
"v_accvgpr_read_b32 v53, a45\n"
"v_accvgpr_read_b32 v54, a46\n"
"v_accvgpr_read_b32 v55, a47\n"
"v_pk_mul_f32 v[48:49], v[48:49], v[34:35] op_sel_hi:[1,0]\n"
"v_pk_mul_f32 v[50:51], v[50:51], v[34:35] op_sel_hi:[1,0]\n"
"v_pk_mul_f32 v[52:53], v[52:53], v[34:35] op_sel_hi:[1,0]\n"
"v_pk_mul_f32 v[54:55], v[54:55], v[34:35] op_sel_hi:[1,0]\n"
"v_cvt_pk_bf16_f32 v56, v48, v49\n"
"v_cvt_pk_bf16_f32 v57, v50, v51\n"
"v_cvt_pk_bf16_f32 v58, v52, v53\n"
"v_cvt_pk_bf16_f32 v59, v54, v55\n"
"buffer_store_dwordx4 v[56:59], v237, s[16:19], s51 offen offset:2560 sc1\n"
"v_accvgpr_read_b32 v36, a48\n"
"v_accvgpr_read_b32 v37, a49\n"
"v_accvgpr_read_b32 v38, a50\n"
"v_accvgpr_read_b32 v39, a51\n"
"v_accvgpr_read_b32 v40, a52\n"
"v_accvgpr_read_b32 v41, a53\n"
"v_accvgpr_read_b32 v42, a54\n"
"v_accvgpr_read_b32 v43, a55\n"
"v_pk_mul_f32 v[36:37], v[36:37], v[34:35] op_sel_hi:[1,0]\n"
"v_pk_mul_f32 v[38:39], v[38:39], v[34:35] op_sel_hi:[1,0]\n"
"v_pk_mul_f32 v[40:41], v[40:41], v[34:35] op_sel_hi:[1,0]\n"
"v_pk_mul_f32 v[42:43], v[42:43], v[34:35] op_sel_hi:[1,0]\n"
"v_cvt_pk_bf16_f32 v44, v36, v37\n"
"v_cvt_pk_bf16_f32 v45, v38, v39\n"
"v_cvt_pk_bf16_f32 v46, v40, v41\n"
"v_cvt_pk_bf16_f32 v47, v42, v43\n"
"buffer_store_dwordx4 v[44:47], v237, s[16:19], s51 offen offset:3072 sc1\n"
"v_accvgpr_read_b32 v48, a56\n"
"v_accvgpr_read_b32 v49, a57\n"
"v_accvgpr_read_b32 v50, a58\n"
"v_accvgpr_read_b32 v51, a59\n"
"v_accvgpr_read_b32 v52, a60\n"
"v_accvgpr_read_b32 v53, a61\n"
"v_accvgpr_read_b32 v54, a62\n"
"v_accvgpr_read_b32 v55, a63\n"
"v_pk_mul_f32 v[48:49], v[48:49], v[34:35] op_sel_hi:[1,0]\n"
"v_pk_mul_f32 v[50:51], v[50:51], v[34:35] op_sel_hi:[1,0]\n"
"v_pk_mul_f32 v[52:53], v[52:53], v[34:35] op_sel_hi:[1,0]\n"
"v_pk_mul_f32 v[54:55], v[54:55], v[34:35] op_sel_hi:[1,0]\n"
"v_cvt_pk_bf16_f32 v56, v48, v49\n"
"v_cvt_pk_bf16_f32 v57, v50, v51\n"
"v_cvt_pk_bf16_f32 v58, v52, v53\n"
"v_cvt_pk_bf16_f32 v59, v54, v55\n"
"buffer_store_dwordx4 v[56:59], v237, s[16:19], s51 offen offset:3584 sc1\n"
"s_waitcnt vmcnt(28)\n"
"s_barrier\n"
"v_lshrrev_b32 v253, 5, v255\n"
"v_lshlrev_b32 v242, 12, v253\n"
"v_bfe_u32 v253, v255, 2, 3\n"
"v_lshl_add_u32 v242, v253, 7, v242\n"
"v_bfe_u32 v253, v255, 3, 2\n"
"v_lshlrev_b32 v253, 1, v253\n"
"v_bfe_u32 v252, v255, 1, 1\n"
"v_xor_b32 v253, v253, v252\n"
"v_lshl_add_u32 v242, v253, 4, v242\n"
"v_and_b32 v252, 1, v255\n"
"v_lshl_add_u32 v242, v252, 3, v242\n"
"v_add_u32 v242, 0, v242\n"
"v_xor_b32 v243, 32, v242\n"
"v_xor_b32 v244, 64, v242\n"
"v_xor_b32 v245, 96, v242\n"
"v_add_u32 v216, 0x0, v242\n"
"v_add_u32 v217, 0x0, v243\n"
"v_add_u32 v218, 0x0, v244\n"
"v_add_u32 v219, 0x0, v245\n"
"v_add_u32 v220, 0x10000, v242\n"
"v_add_u32 v221, 0x10000, v243\n"
"v_add_u32 v222, 0x10000, v244\n"
"v_add_u32 v223, 0x10000, v245\n"
"v_add_u32 v224, 0x20000, v242\n"
"v_add_u32 v225, 0x20000, v243\n"
"v_add_u32 v226, 0x20000, v244\n"
"v_add_u32 v227, 0x20000, v245\n"
"v_add_u32 v180, s53, v250\n"
"v_add_u32 v183, 9344, v180\n"
"v_add_u32 v182, 10240, v180\n"
"v_add_u32 v181, 3200, v180\n"
"v_add_u32 v180, 4096, v180\n"
"s_lshl_b32 s92, s86, 1\n"
"s_sub_u32 s92, 15, s92\n"
"s_lshl_b32 s93, s92, 20\n"
"s_add_u32 s47, s45, s93\n"
"s_sub_u32 s47, s47, 4096\n"
"s_add_u32 s48, s47, 0x20000\n"
"s_and_b32 s93, s84, 7\n"
"s_lshl_b32 s49, s93, 21\n"
"s_lshl_b32 s93, s86, 17\n"
"s_add_u32 s49, s49, s93\n"
"s_lshl_b32 s93, s83, 15\n"
"s_add_u32 s49, s49, s93\n"
"s_bfe_u32 s93, s84, 0x20003\n"
"s_lshl_b32 s93, s93, 12\n"
"s_add_u32 s49, s49, s93\n"
"s_lshl_b32 s92, s92, 17\n"
"s_add_u32 s85, s49, s92\n"
"s_add_u32 s36, s80, 0x3f80000\n"
"s_addc_u32 s37, s81, 0\n"
"s_and_b32 s37, s37, 0xffff\n"
"s_mov_b32 s38, 0x1000000\n"
"s_mov_b32 s39, 0x20000\n"
"ds_read_b64_tr_b16 a[128:129], v224 offset:0\n"
"ds_read_b64_tr_b16 a[130:131], v224 offset:2048\n"
"ds_read_b64_tr_b16 a[160:161], v224 offset:8192\n"
"ds_read_b64_tr_b16 a[162:163], v224 offset:10240\n"
"ds_read_b64_tr_b16 a[132:133], v225 offset:0\n"
"ds_read_b64_tr_b16 a[134:135], v225 offset:2048\n"
"ds_read_b64_tr_b16 a[164:165], v225 offset:8192\n"
"ds_read_b64_tr_b16 a[166:167], v225 offset:10240\n"
"ds_read_b64_tr_b16 a[136:137], v226 offset:0\n"
"ds_read_b64_tr_b16 a[138:139], v226 offset:2048\n"
"ds_read_b64_tr_b16 a[168:169], v226 offset:8192\n"
"ds_read_b64_tr_b16 a[170:171], v226 offset:10240\n"
"ds_read_b64_tr_b16 a[140:141], v227 offset:0\n"
"ds_read_b64_tr_b16 a[142:143], v227 offset:2048\n"
"ds_read_b64_tr_b16 a[172:173], v227 offset:8192\n"
"ds_read_b64_tr_b16 a[174:175], v227 offset:10240\n"
"ds_read_b64_tr_b16 a[144:145], v224 offset:1024\n"
"ds_read_b64_tr_b16 a[146:147], v224 offset:3072\n"
"ds_read_b64_tr_b16 a[176:177], v224 offset:9216\n"
"ds_read_b64_tr_b16 a[178:179], v224 offset:11264\n"
"ds_read_b64_tr_b16 a[148:149], v225 offset:1024\n"
"ds_read_b64_tr_b16 a[150:151], v225 offset:3072\n"
"ds_read_b64_tr_b16 a[180:181], v225 offset:9216\n"
"ds_read_b64_tr_b16 a[182:183], v225 offset:11264\n"
"ds_read_b64_tr_b16 a[152:153], v226 offset:1024\n"
"ds_read_b64_tr_b16 a[154:155], v226 offset:3072\n"
"ds_read_b64_tr_b16 a[184:185], v226 offset:9216\n"
"ds_read_b64_tr_b16 a[186:187], v226 offset:11264\n"
"ds_read_b64_tr_b16 a[156:157], v227 offset:1024\n"
"ds_read_b64_tr_b16 a[158:159], v227 offset:3072\n"
"ds_read_b64_tr_b16 a[188:189], v227 offset:9216\n"
"ds_read_b64_tr_b16 a[190:191], v227 offset:11264\n"
"s_waitcnt vmcnt(24) lgkmcnt(0)\n"
"v_mfma_f32_16x16x32_bf16 v[0:3], a[128:131], a[64:67], 0\n"
"ds_read_b64_tr_b16 a[192:193], v224 offset:16384\n"
"ds_read_b64_tr_b16 a[194:195], v224 offset:18432\n"
"v_mfma_f32_16x16x32_bf16 v[0:3], a[160:163], a[72:75], v[0:3]\n"
"s_add_u32 s45, s45, 4096\n"
"ds_read_b64_tr_b16 a[224:225], v224 offset:24576\n"
"v_mfma_f32_16x16x32_bf16 v[32:35], a[128:131], a[68:71], 0\n"
"ds_read_b64_tr_b16 a[226:227], v224 offset:26624\n"
"buffer_load_dwordx4 a[0:3], v248, s[16:19], s45 offen offset:0 sc1\n"
"v_mfma_f32_16x16x32_bf16 v[32:35], a[160:163], a[76:79], v[32:35]\n"
"ds_read_b64_tr_b16 a[196:197], v225 offset:16384\n"
"v_mfma_f32_16x16x32_bf16 v[4:7], a[132:135], a[64:67], 0\n"
"ds_read_b64_tr_b16 a[198:199], v225 offset:18432\n"
"ds_read_b64_tr_b16 a[228:229], v225 offset:24576\n"
"v_mfma_f32_16x16x32_bf16 v[4:7], a[164:167], a[72:75], v[4:7]\n"
"ds_read_b64_tr_b16 a[230:231], v225 offset:26624\n"
"buffer_load_dwordx4 a[8:11], v248, s[16:19], s45 offen offset:1024 sc1\n"
"v_mfma_f32_16x16x32_bf16 v[36:39], a[132:135], a[68:71], 0\n"
"ds_read_b64_tr_b16 a[200:201], v226 offset:16384\n"
"v_mfma_f32_16x16x32_bf16 v[36:39], a[164:167], a[76:79], v[36:39]\n"
"ds_read_b64_tr_b16 a[202:203], v226 offset:18432\n"
"v_mfma_f32_16x16x32_bf16 v[8:11], a[136:139], a[64:67], 0\n"
"ds_read_b64_tr_b16 a[232:233], v226 offset:24576\n"
"ds_read_b64_tr_b16 a[234:235], v226 offset:26624\n"
"v_mfma_f32_16x16x32_bf16 v[8:11], a[168:171], a[72:75], v[8:11]\n"
"s_add_u32 s46, s46, 4096\n"
"ds_read_b64_tr_b16 a[204:205], v227 offset:16384\n"
"v_mfma_f32_16x16x32_bf16 v[40:43], a[136:139], a[68:71], 0\n"
"ds_read_b64_tr_b16 a[206:207], v227 offset:18432\n"
"buffer_load_dwordx4 a[4:7], v248, s[16:19], s46 offen offset:0 sc1\n"
"v_mfma_f32_16x16x32_bf16 v[40:43], a[168:171], a[76:79], v[40:43]\n"
"ds_read_b64_tr_b16 a[236:237], v227 offset:24576\n"
"v_mfma_f32_16x16x32_bf16 v[12:15], a[140:143], a[64:67], 0\n"
"ds_read_b64_tr_b16 a[238:239], v227 offset:26624\n"
"v_mfma_f32_16x16x32_bf16 v[12:15], a[172:175], a[72:75], v[12:15]\n"
"ds_read_b64_tr_b16 a[208:209], v224 offset:17408\n"
"ds_read_b64_tr_b16 a[210:211], v224 offset:19456\n"
"v_mfma_f32_16x16x32_bf16 v[44:47], a[140:143], a[68:71], 0\n"
"buffer_load_dwordx4 a[12:15], v248, s[16:19], s46 offen offset:1024 sc1\n"
"v_mfma_f32_16x16x32_bf16 v[44:47], a[172:175], a[76:79], v[44:47]\n"
"ds_read_b64_tr_b16 a[240:241], v224 offset:25600\n"
"ds_read_b64_tr_b16 a[242:243], v224 offset:27648\n"
"v_mfma_f32_16x16x32_bf16 v[16:19], a[144:147], a[64:67], 0\n"
"ds_read_b64_tr_b16 a[212:213], v225 offset:17408\n"
"s_add_u32 m0, s54, 65536\n"
"v_mfma_f32_16x16x32_bf16 v[16:19], a[176:179], a[72:75], v[16:19]\n"
"ds_read_b64_tr_b16 a[214:215], v225 offset:19456\n"
"ds_read_b64_tr_b16 a[244:245], v225 offset:25600\n"
"v_mfma_f32_16x16x32_bf16 v[48:51], a[144:147], a[68:71], 0\n"
"ds_read_b64_tr_b16 a[246:247], v225 offset:27648\n"
"s_mov_b32 s92, 393216\n"
"v_mfma_f32_16x16x32_bf16 v[48:51], a[176:179], a[76:79], v[48:51]\n"
"ds_read_b64_tr_b16 a[216:217], v226 offset:17408\n"
"v_mfma_f32_16x16x32_bf16 v[20:23], a[148:151], a[64:67], 0\n"
"ds_read_b64_tr_b16 a[218:219], v226 offset:19456\n"
"v_mfma_f32_16x16x32_bf16 v[20:23], a[180:183], a[72:75], v[20:23]\n"
"ds_read_b64_tr_b16 a[248:249], v226 offset:25600\n"
"ds_read_b64_tr_b16 a[250:251], v226 offset:27648\n"
"v_mfma_f32_16x16x32_bf16 v[52:55], a[148:151], a[68:71], 0\n"
"buffer_load_dwordx4 v180, s[20:23], s92 offen offset:0 lds\n"
"v_mfma_f32_16x16x32_bf16 v[52:55], a[180:183], a[76:79], v[52:55]\n"
"ds_read_b64_tr_b16 a[220:221], v227 offset:17408\n"
"ds_read_b64_tr_b16 a[222:223], v227 offset:19456\n"
"v_mfma_f32_16x16x32_bf16 v[24:27], a[152:155], a[64:67], 0\n"
"ds_read_b64_tr_b16 a[252:253], v227 offset:25600\n"
"buffer_load_dwordx4 v181, s[20:23], s92 offen offset:1024 lds\n"
"v_mfma_f32_16x16x32_bf16 v[24:27], a[184:187], a[72:75], v[24:27]\n"
"ds_read_b64_tr_b16 a[254:255], v227 offset:27648\n"
"v_mfma_f32_16x16x32_bf16 v[56:59], a[152:155], a[68:71], 0\n"
"buffer_load_dwordx4 v182, s[20:23], s92 offen offset:2048 lds\n"
"v_mfma_f32_16x16x32_bf16 v[56:59], a[184:187], a[76:79], v[56:59]\n"
"v_mfma_f32_16x16x32_bf16 v[28:31], a[156:159], a[64:67], 0\n"
"v_mfma_f32_16x16x32_bf16 v[28:31], a[188:191], a[72:75], v[28:31]\n"
"buffer_load_dwordx4 v183, s[20:23], s92 offen offset:3072 lds\n"
"v_mfma_f32_16x16x32_bf16 v[60:63], a[156:159], a[68:71], 0\n"
"v_mfma_f32_16x16x32_bf16 v[60:63], a[188:191], a[76:79], v[60:63]\n"
"s_waitcnt vmcnt(28) lgkmcnt(0)\n"
"v_mfma_f32_16x16x32_bf16 v[0:3], a[192:195], a[80:83], v[0:3]\n"
"ds_read_b64_tr_b16 a[128:129], v216 offset:0\n"
"ds_read_b64_tr_b16 a[130:131], v216 offset:2048\n"
"v_mfma_f32_16x16x32_bf16 v[0:3], a[224:227], a[88:91], v[0:3]\n"
"s_add_u32 m0, s54, 81920\n"
"ds_read_b64_tr_b16 a[160:161], v216 offset:8192\n"
"v_mfma_f32_16x16x32_bf16 v[32:35], a[192:195], a[84:87], v[32:35]\n"
"s_waitcnt vmcnt(8)\n"
"ds_read_b64_tr_b16 a[162:163], v216 offset:10240\n"
"v_mfma_f32_16x16x32_bf16 v[32:35], a[224:227], a[92:95], v[32:35]\n"
"ds_read_b64_tr_b16 a[132:133], v217 offset:0\n"
"s_add_u32 s95, s87, 0x8000\n"
"v_mfma_f32_16x16x32_bf16 v[4:7], a[196:199], a[80:83], v[4:7]\n"
"ds_read_b64_tr_b16 a[134:135], v217 offset:2048\n"
"ds_read_b64_tr_b16 a[164:165], v217 offset:8192\n"
"v_mfma_f32_16x16x32_bf16 v[4:7], a[228:231], a[88:91], v[4:7]\n"
"ds_read_b64_tr_b16 a[166:167], v217 offset:10240\n"
"s_mov_b32 s92, 458752\n"
"v_mfma_f32_16x16x32_bf16 v[36:39], a[196:199], a[84:87], v[36:39]\n"
"v_mov_b32 v253, s82\n"
"ds_read_b64_tr_b16 a[136:137], v218 offset:0\n"
"v_mfma_f32_16x16x32_bf16 v[36:39], a[228:231], a[92:95], v[36:39]\n"
"ds_read_b64_tr_b16 a[138:139], v218 offset:2048\n"
"s_mov_b64 exec, 1\n"
"buffer_store_dword v253, off, s[88:91], s95 sc1\n"
"s_mov_b64 exec, -1\n"
"v_mfma_f32_16x16x32_bf16 v[8:11], a[200:203], a[80:83], v[8:11]\n"
"v_mfma_f32_16x16x32_bf16 v[8:11], a[232:235], a[88:91], v[8:11]\n"
"ds_read_b64_tr_b16 a[168:169], v218 offset:8192\n"
"v_mfma_f32_16x16x32_bf16 v[40:43], a[200:203], a[84:87], v[40:43]\n"
"ds_read_b64_tr_b16 a[170:171], v218 offset:10240\n"
"ds_read_b64_tr_b16 a[140:141], v219 offset:0\n"
"v_mfma_f32_16x16x32_bf16 v[40:43], a[232:235], a[92:95], v[40:43]\n"
"ds_read_b64_tr_b16 a[142:143], v219 offset:2048\n"
"buffer_load_dwordx4 v180, s[20:23], s92 offen offset:0 lds\n"
"v_mfma_f32_16x16x32_bf16 v[12:15], a[204:207], a[80:83], v[12:15]\n"
"ds_read_b64_tr_b16 a[172:173], v219 offset:8192\n"
"v_mfma_f32_16x16x32_bf16 v[12:15], a[236:239], a[88:91], v[12:15]\n"
"ds_read_b64_tr_b16 a[174:175], v219 offset:10240\n"
"ds_read_b64_tr_b16 a[144:145], v216 offset:1024\n"
"v_mfma_f32_16x16x32_bf16 v[44:47], a[204:207], a[84:87], v[44:47]\n"
"ds_read_b64_tr_b16 a[146:147], v216 offset:3072\n"
"ds_read_b64_tr_b16 a[176:177], v216 offset:9216\n"
"v_mfma_f32_16x16x32_bf16 v[44:47], a[236:239], a[92:95], v[44:47]\n"
"ds_read_b64_tr_b16 a[178:179], v216 offset:11264\n"
"v_mfma_f32_16x16x32_bf16 v[16:19], a[208:211], a[80:83], v[16:19]\n"
"ds_read_b64_tr_b16 a[148:149], v217 offset:1024\n"
"buffer_load_dwordx4 v181, s[20:23], s92 offen offset:1024 lds\n"
"v_mfma_f32_16x16x32_bf16 v[16:19], a[240:243], a[88:91], v[16:19]\n"
"ds_read_b64_tr_b16 a[150:151], v217 offset:3072\n"
"v_mfma_f32_16x16x32_bf16 v[48:51], a[208:211], a[84:87], v[48:51]\n"
"ds_read_b64_tr_b16 a[180:181], v217 offset:9216\n"
"ds_read_b64_tr_b16 a[182:183], v217 offset:11264\n"
"v_mfma_f32_16x16x32_bf16 v[48:51], a[240:243], a[92:95], v[48:51]\n"
"ds_read_b64_tr_b16 a[152:153], v218 offset:1024\n"
"v_mfma_f32_16x16x32_bf16 v[20:23], a[212:215], a[80:83], v[20:23]\n"
"ds_read_b64_tr_b16 a[154:155], v218 offset:3072\n"
"v_mfma_f32_16x16x32_bf16 v[20:23], a[244:247], a[88:91], v[20:23]\n"
"ds_read_b64_tr_b16 a[184:185], v218 offset:9216\n"
"ds_read_b64_tr_b16 a[186:187], v218 offset:11264\n"
"v_mfma_f32_16x16x32_bf16 v[52:55], a[212:215], a[84:87], v[52:55]\n"
"buffer_load_dwordx4 v182, s[20:23], s92 offen offset:2048 lds\n"
"v_mfma_f32_16x16x32_bf16 v[52:55], a[244:247], a[92:95], v[52:55]\n"
"ds_read_b64_tr_b16 a[156:157], v219 offset:1024\n"
"ds_read_b64_tr_b16 a[158:159], v219 offset:3072\n"
"v_mfma_f32_16x16x32_bf16 v[24:27], a[216:219], a[80:83], v[24:27]\n"
"ds_read_b64_tr_b16 a[188:189], v219 offset:9216\n"
"v_mfma_f32_16x16x32_bf16 v[24:27], a[248:251], a[88:91], v[24:27]\n"
"ds_read_b64_tr_b16 a[190:191], v219 offset:11264\n"
"v_mfma_f32_16x16x32_bf16 v[56:59], a[216:219], a[84:87], v[56:59]\n"
"buffer_load_dwordx4 v183, s[20:23], s92 offen offset:3072 lds\n"
"v_mfma_f32_16x16x32_bf16 v[56:59], a[248:251], a[92:95], v[56:59]\n"
"v_mfma_f32_16x16x32_bf16 v[28:31], a[220:223], a[80:83], v[28:31]\n"
"v_mfma_f32_16x16x32_bf16 v[28:31], a[252:255], a[88:91], v[28:31]\n"
"v_mfma_f32_16x16x32_bf16 v[60:63], a[220:223], a[84:87], v[60:63]\n"
"v_mfma_f32_16x16x32_bf16 v[60:63], a[252:255], a[92:95], v[60:63]\n"
"s_waitcnt vmcnt(13) lgkmcnt(0)\n"
"v_mfma_f32_16x16x32_bf16 v[0:3], a[128:131], a[96:99], v[0:3]\n"
"ds_read_b64_tr_b16 a[192:193], v216 offset:16384\n"
"ds_read_b64_tr_b16 a[194:195], v216 offset:18432\n"
"v_mfma_f32_16x16x32_bf16 v[0:3], a[160:163], a[104:107], v[0:3]\n"
"buffer_load_dwordx4 a[16:19], v248, s[16:19], s45 offen offset:2048 sc1\n"
"v_mfma_f32_16x16x32_bf16 v[32:35], a[128:131], a[100:103], v[32:35]\n"
"ds_read_b64_tr_b16 a[224:225], v216 offset:24576\n"
"ds_read_b64_tr_b16 a[226:227], v216 offset:26624\n"
"v_mfma_f32_16x16x32_bf16 v[32:35], a[160:163], a[108:111], v[32:35]\n"
"ds_read_b64_tr_b16 a[196:197], v217 offset:16384\n"
"buffer_load_dwordx4 a[24:27], v248, s[16:19], s45 offen offset:3072 sc1\n"
"v_mfma_f32_16x16x32_bf16 v[4:7], a[132:135], a[96:99], v[4:7]\n"
"ds_read_b64_tr_b16 a[198:199], v217 offset:18432\n"
"v_mfma_f32_16x16x32_bf16 v[4:7], a[164:167], a[104:107], v[4:7]\n"
"ds_read_b64_tr_b16 a[228:229], v217 offset:24576\n"
"ds_read_b64_tr_b16 a[230:231], v217 offset:26624\n"
"v_mfma_f32_16x16x32_bf16 v[36:39], a[132:135], a[100:103], v[36:39]\n"
"ds_read_b64_tr_b16 a[200:201], v218 offset:16384\n"
"buffer_load_dwordx4 a[20:23], v248, s[16:19], s46 offen offset:2048 sc1\n"
"v_mfma_f32_16x16x32_bf16 v[36:39], a[164:167], a[108:111], v[36:39]\n"
"ds_read_b64_tr_b16 a[202:203], v218 offset:18432\n"
"v_mfma_f32_16x16x32_bf16 v[8:11], a[136:139], a[96:99], v[8:11]\n"
"ds_read_b64_tr_b16 a[232:233], v218 offset:24576\n"
"ds_read_b64_tr_b16 a[234:235], v218 offset:26624\n"
"v_mfma_f32_16x16x32_bf16 v[8:11], a[168:171], a[104:107], v[8:11]\n"
"ds_read_b64_tr_b16 a[204:205], v219 offset:16384\n"
"buffer_load_dwordx4 a[28:31], v248, s[16:19], s46 offen offset:3072 sc1\n"
"v_mfma_f32_16x16x32_bf16 v[40:43], a[136:139], a[100:103], v[40:43]\n"
"ds_read_b64_tr_b16 a[206:207], v219 offset:18432\n"
"v_mfma_f32_16x16x32_bf16 v[40:43], a[168:171], a[108:111], v[40:43]\n"
"ds_read_b64_tr_b16 a[236:237], v219 offset:24576\n"
"v_mfma_f32_16x16x32_bf16 v[12:15], a[140:143], a[96:99], v[12:15]\n"
"ds_read_b64_tr_b16 a[238:239], v219 offset:26624\n"
"s_add_u32 s45, s45, 4096\n"
"v_mfma_f32_16x16x32_bf16 v[12:15], a[172:175], a[104:107], v[12:15]\n"
"ds_read_b64_tr_b16 a[208:209], v216 offset:17408\n"
"ds_read_b64_tr_b16 a[210:211], v216 offset:19456\n"
"v_mfma_f32_16x16x32_bf16 v[44:47], a[140:143], a[100:103], v[44:47]\n"
"ds_read_b64_tr_b16 a[240:241], v216 offset:25600\n"
"v_mfma_f32_16x16x32_bf16 v[44:47], a[172:175], a[108:111], v[44:47]\n"
"ds_read_b64_tr_b16 a[242:243], v216 offset:27648\n"
"v_mfma_f32_16x16x32_bf16 v[16:19], a[144:147], a[96:99], v[16:19]\n"
"ds_read_b64_tr_b16 a[212:213], v217 offset:17408\n"
"buffer_load_dwordx4 a[32:35], v248, s[16:19], s45 offen offset:0 sc1\n"
"v_mfma_f32_16x16x32_bf16 v[16:19], a[176:179], a[104:107], v[16:19]\n"
"ds_read_b64_tr_b16 a[214:215], v217 offset:19456\n"
"v_mfma_f32_16x16x32_bf16 v[48:51], a[144:147], a[100:103], v[48:51]\n"
"ds_read_b64_tr_b16 a[244:245], v217 offset:25600\n"
"ds_read_b64_tr_b16 a[246:247], v217 offset:27648\n"
"v_mfma_f32_16x16x32_bf16 v[48:51], a[176:179], a[108:111], v[48:51]\n"
"ds_read_b64_tr_b16 a[216:217], v218 offset:17408\n"
"buffer_load_dwordx4 a[40:43], v248, s[16:19], s45 offen offset:1024 sc1\n"
"v_mfma_f32_16x16x32_bf16 v[20:23], a[148:151], a[96:99], v[20:23]\n"
"ds_read_b64_tr_b16 a[218:219], v218 offset:19456\n"
"v_mfma_f32_16x16x32_bf16 v[20:23], a[180:183], a[104:107], v[20:23]\n"
"ds_read_b64_tr_b16 a[248:249], v218 offset:25600\n"
"ds_read_b64_tr_b16 a[250:251], v218 offset:27648\n"
"v_mfma_f32_16x16x32_bf16 v[52:55], a[148:151], a[100:103], v[52:55]\n"
"ds_read_b64_tr_b16 a[220:221], v219 offset:17408\n"
"s_add_u32 s46, s46, 4096\n"
"v_mfma_f32_16x16x32_bf16 v[52:55], a[180:183], a[108:111], v[52:55]\n"
"ds_read_b64_tr_b16 a[222:223], v219 offset:19456\n"
"v_mfma_f32_16x16x32_bf16 v[24:27], a[152:155], a[96:99], v[24:27]\n"
"ds_read_b64_tr_b16 a[252:253], v219 offset:25600\n"
"v_mfma_f32_16x16x32_bf16 v[24:27], a[184:187], a[104:107], v[24:27]\n"
"ds_read_b64_tr_b16 a[254:255], v219 offset:27648\n"
"buffer_load_dwordx4 a[36:39], v248, s[16:19], s46 offen offset:0 sc1\n"
"v_mfma_f32_16x16x32_bf16 v[56:59], a[152:155], a[100:103], v[56:59]\n"
"v_mfma_f32_16x16x32_bf16 v[56:59], a[184:187], a[108:111], v[56:59]\n"
"v_mfma_f32_16x16x32_bf16 v[28:31], a[156:159], a[96:99], v[28:31]\n"
"buffer_load_dwordx4 a[44:47], v248, s[16:19], s46 offen offset:1024 sc1\n"
"v_mfma_f32_16x16x32_bf16 v[28:31], a[188:191], a[104:107], v[28:31]\n"
"v_mfma_f32_16x16x32_bf16 v[60:63], a[156:159], a[100:103], v[60:63]\n"
"v_mfma_f32_16x16x32_bf16 v[60:63], a[188:191], a[108:111], v[60:63]\n"
"s_waitcnt vmcnt(21) lgkmcnt(0)\n"
"v_mfma_f32_16x16x32_bf16 v[0:3], a[192:195], a[112:115], v[0:3]\n"
"ds_read_b64_tr_b16 a[128:129], v216 offset:32768\n"
"ds_read_b64_tr_b16 a[130:131], v216 offset:34816\n"
"v_mfma_f32_16x16x32_bf16 v[0:3], a[224:227], a[120:123], v[0:3]\n"
"buffer_load_dwordx4 a[48:51], v248, s[16:19], s45 offen offset:2048 sc1\n"
"v_mfma_f32_16x16x32_bf16 v[32:35], a[192:195], a[116:119], v[32:35]\n"
"ds_read_b64_tr_b16 a[160:161], v216 offset:40960\n"
"ds_read_b64_tr_b16 a[162:163], v216 offset:43008\n"
"v_mfma_f32_16x16x32_bf16 v[32:35], a[224:227], a[124:127], v[32:35]\n"
"ds_read_b64_tr_b16 a[132:133], v217 offset:32768\n"
"v_mfma_f32_16x16x32_bf16 v[4:7], a[196:199], a[112:115], v[4:7]\n"
"ds_read_b64_tr_b16 a[134:135], v217 offset:34816\n"
"ds_read_b64_tr_b16 a[164:165], v217 offset:40960\n"
"v_mfma_f32_16x16x32_bf16 v[4:7], a[228:231], a[120:123], v[4:7]\n"
"ds_read_b64_tr_b16 a[166:167], v217 offset:43008\n"
"v_mfma_f32_16x16x32_bf16 v[36:39], a[196:199], a[116:119], v[36:39]\n"
"ds_read_b64_tr_b16 a[136:137], v218 offset:32768\n"
"v_mfma_f32_16x16x32_bf16 v[36:39], a[228:231], a[124:127], v[36:39]\n"
"ds_read_b64_tr_b16 a[138:139], v218 offset:34816\n"
"v_mfma_f32_16x16x32_bf16 v[8:11], a[200:203], a[112:115], v[8:11]\n"
"ds_read_b64_tr_b16 a[168:169], v218 offset:40960\n"
"ds_read_b64_tr_b16 a[170:171], v218 offset:43008\n"
"v_mfma_f32_16x16x32_bf16 v[8:11], a[232:235], a[120:123], v[8:11]\n"
"buffer_load_dwordx4 a[56:59], v248, s[16:19], s45 offen offset:3072 sc1\n"
"v_mfma_f32_16x16x32_bf16 v[40:43], a[200:203], a[116:119], v[40:43]\n"
"ds_read_b64_tr_b16 a[140:141], v219 offset:32768\n"
"ds_read_b64_tr_b16 a[142:143], v219 offset:34816\n"
"v_mfma_f32_16x16x32_bf16 v[40:43], a[232:235], a[124:127], v[40:43]\n"
"ds_read_b64_tr_b16 a[172:173], v219 offset:40960\n"
"v_mfma_f32_16x16x32_bf16 v[12:15], a[204:207], a[112:115], v[12:15]\n"
"ds_read_b64_tr_b16 a[174:175], v219 offset:43008\n"
"v_mfma_f32_16x16x32_bf16 v[12:15], a[236:239], a[120:123], v[12:15]\n"
"ds_read_b64_tr_b16 a[144:145], v216 offset:33792\n"
"ds_read_b64_tr_b16 a[146:147], v216 offset:35840\n"
"v_mfma_f32_16x16x32_bf16 v[44:47], a[204:207], a[116:119], v[44:47]\n"
"ds_read_b64_tr_b16 a[176:177], v216 offset:41984\n"
"v_mfma_f32_16x16x32_bf16 v[44:47], a[236:239], a[124:127], v[44:47]\n"
"ds_read_b64_tr_b16 a[178:179], v216 offset:44032\n"
"v_mfma_f32_16x16x32_bf16 v[16:19], a[208:211], a[112:115], v[16:19]\n"
"ds_read_b64_tr_b16 a[148:149], v217 offset:33792\n"
"buffer_load_dwordx4 a[52:55], v248, s[16:19], s46 offen offset:2048 sc1\n"
"v_mfma_f32_16x16x32_bf16 v[16:19], a[240:243], a[120:123], v[16:19]\n"
"ds_read_b64_tr_b16 a[150:151], v217 offset:35840\n"
"v_mfma_f32_16x16x32_bf16 v[48:51], a[208:211], a[116:119], v[48:51]\n"
"ds_read_b64_tr_b16 a[180:181], v217 offset:41984\n"
"ds_read_b64_tr_b16 a[182:183], v217 offset:44032\n"
"v_mfma_f32_16x16x32_bf16 v[48:51], a[240:243], a[124:127], v[48:51]\n"
"ds_read_b64_tr_b16 a[152:153], v218 offset:33792\n"
"v_mfma_f32_16x16x32_bf16 v[20:23], a[212:215], a[112:115], v[20:23]\n"
"ds_read_b64_tr_b16 a[154:155], v218 offset:35840\n"
"s_add_u32 s97, s97, 0x8000\n"
"v_mfma_f32_16x16x32_bf16 v[20:23], a[244:247], a[120:123], v[20:23]\n"
"ds_read_b64_tr_b16 a[184:185], v218 offset:41984\n"
"ds_read_b64_tr_b16 a[186:187], v218 offset:44032\n"
"v_mfma_f32_16x16x32_bf16 v[52:55], a[212:215], a[116:119], v[52:55]\n"
"ds_read_b64_tr_b16 a[156:157], v219 offset:33792\n"
"v_and_b32 v240, 3, v255\n"
"v_mfma_f32_16x16x32_bf16 v[52:55], a[244:247], a[124:127], v[52:55]\n"
"ds_read_b64_tr_b16 a[158:159], v219 offset:35840\n"
"v_mfma_f32_16x16x32_bf16 v[24:27], a[216:219], a[112:115], v[24:27]\n"
"ds_read_b64_tr_b16 a[188:189], v219 offset:41984\n"
"buffer_load_dwordx4 a[60:63], v248, s[16:19], s46 offen offset:3072 sc1\n"
"v_mfma_f32_16x16x32_bf16 v[24:27], a[248:251], a[120:123], v[24:27]\n"
"v_lshlrev_b32 v240, 10, v240\n"
"v_mfma_f32_16x16x32_bf16 v[56:59], a[216:219], a[116:119], v[56:59]\n"
"ds_read_b64_tr_b16 a[190:191], v219 offset:44032\n"
"buffer_load_dword v241, v240, s[88:91], s97 offen sc1\n"
"v_mfma_f32_16x16x32_bf16 v[56:59], a[248:251], a[124:127], v[56:59]\n"
"v_mfma_f32_16x16x32_bf16 v[28:31], a[220:223], a[112:115], v[28:31]\n"
"v_mfma_f32_16x16x32_bf16 v[28:31], a[252:255], a[120:123], v[28:31]\n"
"v_mfma_f32_16x16x32_bf16 v[60:63], a[220:223], a[116:119], v[60:63]\n"
"v_mfma_f32_16x16x32_bf16 v[60:63], a[252:255], a[124:127], v[60:63]\n"
"s_waitcnt vmcnt(22) lgkmcnt(0)\n"
"s_barrier\n"
"v_mfma_f32_16x16x32_bf16 v[0:3], a[128:131], a[0:3], v[0:3]\n"
"ds_read_b64_tr_b16 a[192:193], v216 offset:49152\n"
"ds_read_b64_tr_b16 a[194:195], v216 offset:51200\n"
"v_mfma_f32_16x16x32_bf16 v[0:3], a[160:163], a[8:11], v[0:3]\n"
"s_lshl_b32 s94, s83, 7\n"
"ds_read_b64_tr_b16 a[224:225], v216 offset:57344\n"
"v_mfma_f32_16x16x32_bf16 v[32:35], a[128:131], a[4:7], v[32:35]\n"
"ds_read_b64_tr_b16 a[226:227], v216 offset:59392\n"
"v_mfma_f32_16x16x32_bf16 v[32:35], a[160:163], a[12:15], v[32:35]\n"
"ds_read_b64_tr_b16 a[196:197], v217 offset:49152\n"
"v_mfma_f32_16x16x32_bf16 v[4:7], a[132:135], a[0:3], v[4:7]\n"
"ds_read_b64_tr_b16 a[198:199], v217 offset:51200\n"
"ds_read_b64_tr_b16 a[228:229], v217 offset:57344\n"
"v_mfma_f32_16x16x32_bf16 v[4:7], a[164:167], a[8:11], v[4:7]\n"
"ds_read_b64_tr_b16 a[230:231], v217 offset:59392\n"
"v_mfma_f32_16x16x32_bf16 v[36:39], a[132:135], a[4:7], v[36:39]\n"
"ds_read_b64_tr_b16 a[200:201], v218 offset:49152\n"
"v_mfma_f32_16x16x32_bf16 v[36:39], a[164:167], a[12:15], v[36:39]\n"
"ds_read_b64_tr_b16 a[202:203], v218 offset:51200\n"
"v_mfma_f32_16x16x32_bf16 v[8:11], a[136:139], a[0:3], v[8:11]\n"
"ds_read_b64_tr_b16 a[232:233], v218 offset:57344\n"
"ds_read_b64_tr_b16 a[234:235], v218 offset:59392\n"
"v_mfma_f32_16x16x32_bf16 v[8:11], a[168:171], a[8:11], v[8:11]\n"
"s_add_u32 s94, s94, 0x400\n"
"ds_read_b64_tr_b16 a[204:205], v219 offset:49152\n"
"v_mfma_f32_16x16x32_bf16 v[40:43], a[136:139], a[4:7], v[40:43]\n"
"ds_read_b64_tr_b16 a[206:207], v219 offset:51200\n"
"v_mfma_f32_16x16x32_bf16 v[40:43], a[168:171], a[12:15], v[40:43]\n"
"ds_read_b64_tr_b16 a[236:237], v219 offset:57344\n"
"v_mfma_f32_16x16x32_bf16 v[12:15], a[140:143], a[0:3], v[12:15]\n"
"ds_read_b64_tr_b16 a[238:239], v219 offset:59392\n"
"v_mfma_f32_16x16x32_bf16 v[12:15], a[172:175], a[8:11], v[12:15]\n"
"ds_read_b64_tr_b16 a[208:209], v216 offset:50176\n"
"ds_read_b64_tr_b16 a[210:211], v216 offset:52224\n"
"v_mfma_f32_16x16x32_bf16 v[44:47], a[140:143], a[4:7], v[44:47]\n"
"ds_read_b64_tr_b16 a[240:241], v216 offset:58368\n"
"v_mfma_f32_16x16x32_bf16 v[44:47], a[172:175], a[12:15], v[44:47]\n"
"ds_read_b64_tr_b16 a[242:243], v216 offset:60416\n"
"v_mfma_f32_16x16x32_bf16 v[16:19], a[144:147], a[0:3], v[16:19]\n"
"ds_read_b64_tr_b16 a[212:213], v217 offset:50176\n"
"v_mov_b32 v228, 1\n"
"v_mfma_f32_16x16x32_bf16 v[16:19], a[176:179], a[8:11], v[16:19]\n"
"ds_read_b64_tr_b16 a[214:215], v217 offset:52224\n"
"ds_read_b64_tr_b16 a[244:245], v217 offset:58368\n"
"v_mfma_f32_16x16x32_bf16 v[48:51], a[144:147], a[4:7], v[48:51]\n"
"ds_read_b64_tr_b16 a[246:247], v217 offset:60416\n"
"v_mfma_f32_16x16x32_bf16 v[48:51], a[176:179], a[12:15], v[48:51]\n"
"ds_read_b64_tr_b16 a[216:217], v218 offset:50176\n"
"v_mfma_f32_16x16x32_bf16 v[20:23], a[148:151], a[0:3], v[20:23]\n"
"ds_read_b64_tr_b16 a[218:219], v218 offset:52224\n"
"v_mfma_f32_16x16x32_bf16 v[20:23], a[180:183], a[8:11], v[20:23]\n"
"ds_read_b64_tr_b16 a[248:249], v218 offset:58368\n"
"ds_read_b64_tr_b16 a[250:251], v218 offset:60416\n"
"v_mfma_f32_16x16x32_bf16 v[52:55], a[148:151], a[4:7], v[52:55]\n"
"ds_read_b64_tr_b16 a[220:221], v219 offset:50176\n"
"v_mfma_f32_16x16x32_bf16 v[52:55], a[180:183], a[12:15], v[52:55]\n"
"ds_read_b64_tr_b16 a[222:223], v219 offset:52224\n"
"v_mfma_f32_16x16x32_bf16 v[24:27], a[152:155], a[0:3], v[24:27]\n"
"ds_read_b64_tr_b16 a[252:253], v219 offset:58368\n"
"s_mov_b64 exec, 1\n"
"buffer_atomic_add v228, off, s[88:91], s94 sc0 sc1\n"
"s_mov_b64 exec, -1\n"
"v_mfma_f32_16x16x32_bf16 v[24:27], a[184:187], a[8:11], v[24:27]\n"
"v_mfma_f32_16x16x32_bf16 v[56:59], a[152:155], a[4:7], v[56:59]\n"
"ds_read_b64_tr_b16 a[254:255], v219 offset:60416\n"
"v_mfma_f32_16x16x32_bf16 v[56:59], a[184:187], a[12:15], v[56:59]\n"
"v_mfma_f32_16x16x32_bf16 v[28:31], a[156:159], a[0:3], v[28:31]\n"
"v_mfma_f32_16x16x32_bf16 v[28:31], a[188:191], a[8:11], v[28:31]\n"
"v_mfma_f32_16x16x32_bf16 v[60:63], a[156:159], a[4:7], v[60:63]\n"
"v_mfma_f32_16x16x32_bf16 v[60:63], a[188:191], a[12:15], v[60:63]\n"
"s_waitcnt vmcnt(10) lgkmcnt(0)\n"
"s_barrier\n"
"s_waitcnt vmcnt(1)\n"
"v_cmp_ne_u32 vcc, s82, v241\n"
"s_cbranch_vccnz Lps73_%=\n"
"Lpb72_%=:\n"
"v_mfma_f32_16x16x32_bf16 v[0:3], a[192:195], a[16:19], v[0:3]\n"
"ds_read_b64_tr_b16 a[128:129], v220 offset:0\n"
"ds_read_b64_tr_b16 a[130:131], v220 offset:2048\n"
"v_mfma_f32_16x16x32_bf16 v[0:3], a[224:227], a[24:27], v[0:3]\n"
"buffer_load_dwordx4 a[64:67], v248, s[16:19], s47 offen offset:0 sc1\n"
"v_mfma_f32_16x16x32_bf16 v[32:35], a[192:195], a[20:23], v[32:35]\n"
"ds_read_b64_tr_b16 a[160:161], v220 offset:8192\n"
"ds_read_b64_tr_b16 a[162:163], v220 offset:10240\n"
"v_mfma_f32_16x16x32_bf16 v[32:35], a[224:227], a[28:31], v[32:35]\n"
"ds_read_b64_tr_b16 a[132:133], v221 offset:0\n"
"v_mfma_f32_16x16x32_bf16 v[4:7], a[196:199], a[16:19], v[4:7]\n"
"ds_read_b64_tr_b16 a[134:135], v221 offset:2048\n"
"ds_read_b64_tr_b16 a[164:165], v221 offset:8192\n"
"v_mfma_f32_16x16x32_bf16 v[4:7], a[228:231], a[24:27], v[4:7]\n"
"buffer_load_dwordx4 a[72:75], v248, s[16:19], s47 offen offset:1024 sc1\n"
"v_mfma_f32_16x16x32_bf16 v[36:39], a[196:199], a[20:23], v[36:39]\n"
"ds_read_b64_tr_b16 a[166:167], v221 offset:10240\n"
"ds_read_b64_tr_b16 a[136:137], v222 offset:0\n"
"v_mfma_f32_16x16x32_bf16 v[36:39], a[228:231], a[28:31], v[36:39]\n"
"ds_read_b64_tr_b16 a[138:139], v222 offset:2048\n"
"v_mfma_f32_16x16x32_bf16 v[8:11], a[200:203], a[16:19], v[8:11]\n"
"ds_read_b64_tr_b16 a[168:169], v222 offset:8192\n"
"ds_read_b64_tr_b16 a[170:171], v222 offset:10240\n"
"v_mfma_f32_16x16x32_bf16 v[8:11], a[232:235], a[24:27], v[8:11]\n"
"buffer_load_dwordx4 a[68:71], v248, s[16:19], s48 offen offset:0 sc1\n"
"v_mfma_f32_16x16x32_bf16 v[40:43], a[200:203], a[20:23], v[40:43]\n"
"ds_read_b64_tr_b16 a[140:141], v223 offset:0\n"
"ds_read_b64_tr_b16 a[142:143], v223 offset:2048\n"
"v_mfma_f32_16x16x32_bf16 v[40:43], a[232:235], a[28:31], v[40:43]\n"
"ds_read_b64_tr_b16 a[172:173], v223 offset:8192\n"
"v_mfma_f32_16x16x32_bf16 v[12:15], a[204:207], a[16:19], v[12:15]\n"
"ds_read_b64_tr_b16 a[174:175], v223 offset:10240\n"
"buffer_load_dwordx4 a[76:79], v248, s[16:19], s48 offen offset:1024 sc1\n"
"v_mfma_f32_16x16x32_bf16 v[12:15], a[236:239], a[24:27], v[12:15]\n"
"ds_read_b64_tr_b16 a[144:145], v220 offset:1024\n"
"v_mfma_f32_16x16x32_bf16 v[44:47], a[204:207], a[20:23], v[44:47]\n"
"ds_read_b64_tr_b16 a[146:147], v220 offset:3072\n"
"ds_read_b64_tr_b16 a[176:177], v220 offset:9216\n"
"v_mfma_f32_16x16x32_bf16 v[44:47], a[236:239], a[28:31], v[44:47]\n"
"ds_read_b64_tr_b16 a[178:179], v220 offset:11264\n"
"v_mfma_f32_16x16x32_bf16 v[16:19], a[208:211], a[16:19], v[16:19]\n"
"ds_read_b64_tr_b16 a[148:149], v221 offset:1024\n"
"buffer_load_dwordx4 a[80:83], v248, s[16:19], s47 offen offset:2048 sc1\n"
"v_mfma_f32_16x16x32_bf16 v[16:19], a[240:243], a[24:27], v[16:19]\n"
"ds_read_b64_tr_b16 a[150:151], v221 offset:3072\n"
"v_mfma_f32_16x16x32_bf16 v[48:51], a[208:211], a[20:23], v[48:51]\n"
"ds_read_b64_tr_b16 a[180:181], v221 offset:9216\n"
"ds_read_b64_tr_b16 a[182:183], v221 offset:11264\n"
"v_mfma_f32_16x16x32_bf16 v[48:51], a[240:243], a[28:31], v[48:51]\n"
"ds_read_b64_tr_b16 a[152:153], v222 offset:1024\n"
"v_mfma_f32_16x16x32_bf16 v[20:23], a[212:215], a[16:19], v[20:23]\n"
"ds_read_b64_tr_b16 a[154:155], v222 offset:3072\n"
"buffer_load_dwordx4 a[88:91], v248, s[16:19], s47 offen offset:3072 sc1\n"
"v_mfma_f32_16x16x32_bf16 v[20:23], a[244:247], a[24:27], v[20:23]\n"
"ds_read_b64_tr_b16 a[184:185], v222 offset:9216\n"
"v_mfma_f32_16x16x32_bf16 v[52:55], a[212:215], a[20:23], v[52:55]\n"
"ds_read_b64_tr_b16 a[186:187], v222 offset:11264\n"
"ds_read_b64_tr_b16 a[156:157], v223 offset:1024\n"
"v_mfma_f32_16x16x32_bf16 v[52:55], a[244:247], a[28:31], v[52:55]\n"
"ds_read_b64_tr_b16 a[158:159], v223 offset:3072\n"
"v_mfma_f32_16x16x32_bf16 v[24:27], a[216:219], a[16:19], v[24:27]\n"
"ds_read_b64_tr_b16 a[188:189], v223 offset:9216\n"
"buffer_load_dwordx4 a[84:87], v248, s[16:19], s48 offen offset:2048 sc1\n"
"v_mfma_f32_16x16x32_bf16 v[24:27], a[248:251], a[24:27], v[24:27]\n"
"ds_read_b64_tr_b16 a[190:191], v223 offset:11264\n"
"v_mfma_f32_16x16x32_bf16 v[56:59], a[216:219], a[20:23], v[56:59]\n"
"v_mfma_f32_16x16x32_bf16 v[56:59], a[248:251], a[28:31], v[56:59]\n"
"v_mfma_f32_16x16x32_bf16 v[28:31], a[220:223], a[16:19], v[28:31]\n"
"buffer_load_dwordx4 a[92:95], v248, s[16:19], s48 offen offset:3072 sc1\n"
"v_mfma_f32_16x16x32_bf16 v[28:31], a[252:255], a[24:27], v[28:31]\n"
"v_mfma_f32_16x16x32_bf16 v[60:63], a[220:223], a[20:23], v[60:63]\n"
"v_mfma_f32_16x16x32_bf16 v[60:63], a[252:255], a[28:31], v[60:63]\n"
"s_waitcnt vmcnt(9) lgkmcnt(0)\n"
"v_mfma_f32_16x16x32_bf16 v[0:3], a[128:131], a[32:35], v[0:3]\n"
"ds_read_b64_tr_b16 a[192:193], v220 offset:16384\n"
"ds_read_b64_tr_b16 a[194:195], v220 offset:18432\n"
"v_mfma_f32_16x16x32_bf16 v[0:3], a[160:163], a[40:43], v[0:3]\n"
"s_add_u32 s47, s47, 4096\n"
"ds_read_b64_tr_b16 a[224:225], v220 offset:24576\n"
"v_mfma_f32_16x16x32_bf16 v[32:35], a[128:131], a[36:39], v[32:35]\n"
"ds_read_b64_tr_b16 a[226:227], v220 offset:26624\n"
"v_mfma_f32_16x16x32_bf16 v[32:35], a[160:163], a[44:47], v[32:35]\n"
"ds_read_b64_tr_b16 a[196:197], v221 offset:16384\n"
"buffer_load_dwordx4 a[96:99], v248, s[16:19], s47 offen offset:0 sc1\n"
"v_mfma_f32_16x16x32_bf16 v[4:7], a[132:135], a[32:35], v[4:7]\n"
"ds_read_b64_tr_b16 a[198:199], v221 offset:18432\n"
"v_mfma_f32_16x16x32_bf16 v[4:7], a[164:167], a[40:43], v[4:7]\n"
"ds_read_b64_tr_b16 a[228:229], v221 offset:24576\n"
"ds_read_b64_tr_b16 a[230:231], v221 offset:26624\n"
"v_mfma_f32_16x16x32_bf16 v[36:39], a[132:135], a[36:39], v[36:39]\n"
"ds_read_b64_tr_b16 a[200:201], v222 offset:16384\n"
"buffer_load_dwordx4 a[104:107], v248, s[16:19], s47 offen offset:1024 sc1\n"
"v_mfma_f32_16x16x32_bf16 v[36:39], a[164:167], a[44:47], v[36:39]\n"
"ds_read_b64_tr_b16 a[202:203], v222 offset:18432\n"
"v_mfma_f32_16x16x32_bf16 v[8:11], a[136:139], a[32:35], v[8:11]\n"
"ds_read_b64_tr_b16 a[232:233], v222 offset:24576\n"
"ds_read_b64_tr_b16 a[234:235], v222 offset:26624\n"
"v_mfma_f32_16x16x32_bf16 v[8:11], a[168:171], a[40:43], v[8:11]\n"
"ds_read_b64_tr_b16 a[204:205], v223 offset:16384\n"
"s_add_u32 s48, s48, 4096\n"
"v_mfma_f32_16x16x32_bf16 v[40:43], a[136:139], a[36:39], v[40:43]\n"
"ds_read_b64_tr_b16 a[206:207], v223 offset:18432\n"
"v_mfma_f32_16x16x32_bf16 v[40:43], a[168:171], a[44:47], v[40:43]\n"
"ds_read_b64_tr_b16 a[236:237], v223 offset:24576\n"
"v_mfma_f32_16x16x32_bf16 v[12:15], a[140:143], a[32:35], v[12:15]\n"
"ds_read_b64_tr_b16 a[238:239], v223 offset:26624\n"
"buffer_load_dwordx4 a[100:103], v248, s[16:19], s48 offen offset:0 sc1\n"
"v_mfma_f32_16x16x32_bf16 v[12:15], a[172:175], a[40:43], v[12:15]\n"
"ds_read_b64_tr_b16 a[208:209], v220 offset:17408\n"
"v_mfma_f32_16x16x32_bf16 v[44:47], a[140:143], a[36:39], v[44:47]\n"
"ds_read_b64_tr_b16 a[210:211], v220 offset:19456\n"
"ds_read_b64_tr_b16 a[240:241], v220 offset:25600\n"
"v_mfma_f32_16x16x32_bf16 v[44:47], a[172:175], a[44:47], v[44:47]\n"
"ds_read_b64_tr_b16 a[242:243], v220 offset:27648\n"
"v_mfma_f32_16x16x32_bf16 v[16:19], a[144:147], a[32:35], v[16:19]\n"
"ds_read_b64_tr_b16 a[212:213], v221 offset:17408\n"
"buffer_load_dwordx4 a[108:111], v248, s[16:19], s48 offen offset:1024 sc1\n"
"v_mfma_f32_16x16x32_bf16 v[16:19], a[176:179], a[40:43], v[16:19]\n"
"ds_read_b64_tr_b16 a[214:215], v221 offset:19456\n"
"v_mfma_f32_16x16x32_bf16 v[48:51], a[144:147], a[36:39], v[48:51]\n"
"ds_read_b64_tr_b16 a[244:245], v221 offset:25600\n"
"ds_read_b64_tr_b16 a[246:247], v221 offset:27648\n"
"v_mfma_f32_16x16x32_bf16 v[48:51], a[176:179], a[44:47], v[48:51]\n"
"ds_read_b64_tr_b16 a[216:217], v222 offset:17408\n"
"buffer_load_dwordx4 a[112:115], v248, s[16:19], s47 offen offset:2048 sc1\n"
"v_mfma_f32_16x16x32_bf16 v[20:23], a[148:151], a[32:35], v[20:23]\n"
"ds_read_b64_tr_b16 a[218:219], v222 offset:19456\n"
"v_mfma_f32_16x16x32_bf16 v[20:23], a[180:183], a[40:43], v[20:23]\n"
"ds_read_b64_tr_b16 a[248:249], v222 offset:25600\n"
"ds_read_b64_tr_b16 a[250:251], v222 offset:27648\n"
"v_mfma_f32_16x16x32_bf16 v[52:55], a[148:151], a[36:39], v[52:55]\n"
"ds_read_b64_tr_b16 a[220:221], v223 offset:17408\n"
"buffer_load_dwordx4 a[120:123], v248, s[16:19], s47 offen offset:3072 sc1\n"
"v_mfma_f32_16x16x32_bf16 v[52:55], a[180:183], a[44:47], v[52:55]\n"
"ds_read_b64_tr_b16 a[222:223], v223 offset:19456\n"
"v_mfma_f32_16x16x32_bf16 v[24:27], a[152:155], a[32:35], v[24:27]\n"
"ds_read_b64_tr_b16 a[252:253], v223 offset:25600\n"
"v_mfma_f32_16x16x32_bf16 v[24:27], a[184:187], a[40:43], v[24:27]\n"
"ds_read_b64_tr_b16 a[254:255], v223 offset:27648\n"
"buffer_load_dwordx4 a[116:119], v248, s[16:19], s48 offen offset:2048 sc1\n"
"v_mfma_f32_16x16x32_bf16 v[56:59], a[152:155], a[36:39], v[56:59]\n"
"v_mfma_f32_16x16x32_bf16 v[56:59], a[184:187], a[44:47], v[56:59]\n"
"v_mfma_f32_16x16x32_bf16 v[28:31], a[156:159], a[32:35], v[28:31]\n"
"buffer_load_dwordx4 a[124:127], v248, s[16:19], s48 offen offset:3072 sc1\n"
"v_mfma_f32_16x16x32_bf16 v[28:31], a[188:191], a[40:43], v[28:31]\n"
"v_mfma_f32_16x16x32_bf16 v[60:63], a[156:159], a[36:39], v[60:63]\n"
"v_mfma_f32_16x16x32_bf16 v[60:63], a[188:191], a[44:47], v[60:63]\n"
"s_waitcnt vmcnt(17) lgkmcnt(0)\n"
"v_mfma_f32_16x16x32_bf16 v[0:3], a[192:195], a[48:51], v[0:3]\n"
"ds_read_b64_tr_b16 a[128:129], v224 offset:0\n"
"ds_read_b64_tr_b16 a[130:131], v224 offset:2048\n"
"v_mfma_f32_16x16x32_bf16 v[0:3], a[224:227], a[56:59], v[0:3]\n"
"s_add_u32 s47, s47, 4096\n"
"ds_read_b64_tr_b16 a[160:161], v224 offset:8192\n"
"v_mfma_f32_16x16x32_bf16 v[32:35], a[192:195], a[52:55], v[32:35]\n"
"ds_read_b64_tr_b16 a[162:163], v224 offset:10240\n"
"v_mfma_f32_16x16x32_bf16 v[32:35], a[224:227], a[60:63], v[32:35]\n"
"ds_read_b64_tr_b16 a[132:133], v225 offset:0\n"
"buffer_load_dwordx4 a[0:3], v248, s[16:19], s47 offen offset:0 sc1\n"
"v_mfma_f32_16x16x32_bf16 v[4:7], a[196:199], a[48:51], v[4:7]\n"
"ds_read_b64_tr_b16 a[134:135], v225 offset:2048\n"
"v_mfma_f32_16x16x32_bf16 v[4:7], a[228:231], a[56:59], v[4:7]\n"
"ds_read_b64_tr_b16 a[164:165], v225 offset:8192\n"
"ds_read_b64_tr_b16 a[166:167], v225 offset:10240\n"
"v_mfma_f32_16x16x32_bf16 v[36:39], a[196:199], a[52:55], v[36:39]\n"
"ds_read_b64_tr_b16 a[136:137], v226 offset:0\n"
"buffer_load_dwordx4 a[8:11], v248, s[16:19], s47 offen offset:1024 sc1\n"
"v_mfma_f32_16x16x32_bf16 v[36:39], a[228:231], a[60:63], v[36:39]\n"
"ds_read_b64_tr_b16 a[138:139], v226 offset:2048\n"
"v_mfma_f32_16x16x32_bf16 v[8:11], a[200:203], a[48:51], v[8:11]\n"
"ds_read_b64_tr_b16 a[168:169], v226 offset:8192\n"
"ds_read_b64_tr_b16 a[170:171], v226 offset:10240\n"
"v_mfma_f32_16x16x32_bf16 v[8:11], a[232:235], a[56:59], v[8:11]\n"
"ds_read_b64_tr_b16 a[140:141], v227 offset:0\n"
"s_add_u32 s48, s48, 4096\n"
"v_mfma_f32_16x16x32_bf16 v[40:43], a[200:203], a[52:55], v[40:43]\n"
"ds_read_b64_tr_b16 a[142:143], v227 offset:2048\n"
"v_mfma_f32_16x16x32_bf16 v[40:43], a[232:235], a[60:63], v[40:43]\n"
"ds_read_b64_tr_b16 a[172:173], v227 offset:8192\n"
"v_mfma_f32_16x16x32_bf16 v[12:15], a[204:207], a[48:51], v[12:15]\n"
"ds_read_b64_tr_b16 a[174:175], v227 offset:10240\n"
"buffer_load_dwordx4 a[4:7], v248, s[16:19], s48 offen offset:0 sc1\n"
"v_mfma_f32_16x16x32_bf16 v[12:15], a[236:239], a[56:59], v[12:15]\n"
"ds_read_b64_tr_b16 a[144:145], v224 offset:1024\n"
"v_mfma_f32_16x16x32_bf16 v[44:47], a[204:207], a[52:55], v[44:47]\n"
"ds_read_b64_tr_b16 a[146:147], v224 offset:3072\n"
"ds_read_b64_tr_b16 a[176:177], v224 offset:9216\n"
"v_mfma_f32_16x16x32_bf16 v[44:47], a[236:239], a[60:63], v[44:47]\n"
"ds_read_b64_tr_b16 a[178:179], v224 offset:11264\n"
"v_mfma_f32_16x16x32_bf16 v[16:19], a[208:211], a[48:51], v[16:19]\n"
"ds_read_b64_tr_b16 a[148:149], v225 offset:1024\n"
"buffer_load_dwordx4 a[12:15], v248, s[16:19], s48 offen offset:1024 sc1\n"
"v_mfma_f32_16x16x32_bf16 v[16:19], a[240:243], a[56:59], v[16:19]\n"
"ds_read_b64_tr_b16 a[150:151], v225 offset:3072\n"
"v_mfma_f32_16x16x32_bf16 v[48:51], a[208:211], a[52:55], v[48:51]\n"
"ds_read_b64_tr_b16 a[180:181], v225 offset:9216\n"
"ds_read_b64_tr_b16 a[182:183], v225 offset:11264\n"
"v_mfma_f32_16x16x32_bf16 v[48:51], a[240:243], a[60:63], v[48:51]\n"
"ds_read_b64_tr_b16 a[152:153], v226 offset:1024\n"
"buffer_load_dwordx4 a[16:19], v248, s[16:19], s47 offen offset:2048 sc1\n"
"v_mfma_f32_16x16x32_bf16 v[20:23], a[212:215], a[48:51], v[20:23]\n"
"ds_read_b64_tr_b16 a[154:155], v226 offset:3072\n"
"v_mfma_f32_16x16x32_bf16 v[20:23], a[244:247], a[56:59], v[20:23]\n"
"ds_read_b64_tr_b16 a[184:185], v226 offset:9216\n"
"ds_read_b64_tr_b16 a[186:187], v226 offset:11264\n"
"v_mfma_f32_16x16x32_bf16 v[52:55], a[212:215], a[52:55], v[52:55]\n"
"ds_read_b64_tr_b16 a[156:157], v227 offset:1024\n"
"buffer_load_dwordx4 a[24:27], v248, s[16:19], s47 offen offset:3072 sc1\n"
"v_mfma_f32_16x16x32_bf16 v[52:55], a[244:247], a[60:63], v[52:55]\n"
"ds_read_b64_tr_b16 a[158:159], v227 offset:3072\n"
"v_mfma_f32_16x16x32_bf16 v[24:27], a[216:219], a[48:51], v[24:27]\n"
"ds_read_b64_tr_b16 a[188:189], v227 offset:9216\n"
"v_mfma_f32_16x16x32_bf16 v[24:27], a[248:251], a[56:59], v[24:27]\n"
"ds_read_b64_tr_b16 a[190:191], v227 offset:11264\n"
"buffer_load_dwordx4 a[20:23], v248, s[16:19], s48 offen offset:2048 sc1\n"
"v_mfma_f32_16x16x32_bf16 v[56:59], a[216:219], a[52:55], v[56:59]\n"
"v_mfma_f32_16x16x32_bf16 v[56:59], a[248:251], a[60:63], v[56:59]\n"
"v_mfma_f32_16x16x32_bf16 v[28:31], a[220:223], a[48:51], v[28:31]\n"
"buffer_load_dwordx4 a[28:31], v248, s[16:19], s48 offen offset:3072 sc1\n"
"v_mfma_f32_16x16x32_bf16 v[28:31], a[252:255], a[56:59], v[28:31]\n"
"v_mfma_f32_16x16x32_bf16 v[60:63], a[220:223], a[52:55], v[60:63]\n"
"v_mfma_f32_16x16x32_bf16 v[60:63], a[252:255], a[60:63], v[60:63]\n"
"s_waitcnt vmcnt(20) lgkmcnt(0)\n"
"v_mfma_f32_16x16x32_bf16 v[64:67], a[128:131], a[64:67], 0\n"
"ds_read_b64_tr_b16 a[192:193], v224 offset:16384\n"
"ds_read_b64_tr_b16 a[194:195], v224 offset:18432\n"
"v_mfma_f32_16x16x32_bf16 v[64:67], a[160:163], a[72:75], v[64:67]\n"
"s_add_u32 s47, s47, 4096\n"
"ds_read_b64_tr_b16 a[224:225], v224 offset:24576\n"
"v_mfma_f32_16x16x32_bf16 v[96:99], a[128:131], a[68:71], 0\n"
"ds_read_b64_tr_b16 a[226:227], v224 offset:26624\n"
"v_mfma_f32_16x16x32_bf16 v[96:99], a[160:163], a[76:79], v[96:99]\n"
"ds_read_b64_tr_b16 a[196:197], v225 offset:16384\n"
"buffer_load_dwordx4 a[32:35], v248, s[16:19], s47 offen offset:0 sc1\n"
"v_mfma_f32_16x16x32_bf16 v[68:71], a[132:135], a[64:67], 0\n"
"ds_read_b64_tr_b16 a[198:199], v225 offset:18432\n"
"v_mfma_f32_16x16x32_bf16 v[68:71], a[164:167], a[72:75], v[68:71]\n"
"ds_read_b64_tr_b16 a[228:229], v225 offset:24576\n"
"ds_read_b64_tr_b16 a[230:231], v225 offset:26624\n"
"v_mfma_f32_16x16x32_bf16 v[100:103], a[132:135], a[68:71], 0\n"
"ds_read_b64_tr_b16 a[200:201], v226 offset:16384\n"
"buffer_load_dwordx4 a[40:43], v248, s[16:19], s47 offen offset:1024 sc1\n"
"v_mfma_f32_16x16x32_bf16 v[100:103], a[164:167], a[76:79], v[100:103]\n"
"ds_read_b64_tr_b16 a[202:203], v226 offset:18432\n"
"v_mfma_f32_16x16x32_bf16 v[72:75], a[136:139], a[64:67], 0\n"
"ds_read_b64_tr_b16 a[232:233], v226 offset:24576\n"
"ds_read_b64_tr_b16 a[234:235], v226 offset:26624\n"
"v_mfma_f32_16x16x32_bf16 v[72:75], a[168:171], a[72:75], v[72:75]\n"
"ds_read_b64_tr_b16 a[204:205], v227 offset:16384\n"
"s_add_u32 s48, s48, 4096\n"
"v_mfma_f32_16x16x32_bf16 v[104:107], a[136:139], a[68:71], 0\n"
"ds_read_b64_tr_b16 a[206:207], v227 offset:18432\n"
"v_mfma_f32_16x16x32_bf16 v[104:107], a[168:171], a[76:79], v[104:107]\n"
"ds_read_b64_tr_b16 a[236:237], v227 offset:24576\n"
"v_mfma_f32_16x16x32_bf16 v[76:79], a[140:143], a[64:67], 0\n"
"ds_read_b64_tr_b16 a[238:239], v227 offset:26624\n"
"buffer_load_dwordx4 a[36:39], v248, s[16:19], s48 offen offset:0 sc1\n"
"v_mfma_f32_16x16x32_bf16 v[76:79], a[172:175], a[72:75], v[76:79]\n"
"ds_read_b64_tr_b16 a[208:209], v224 offset:17408\n"
"v_mfma_f32_16x16x32_bf16 v[108:111], a[140:143], a[68:71], 0\n"
"ds_read_b64_tr_b16 a[210:211], v224 offset:19456\n"
"ds_read_b64_tr_b16 a[240:241], v224 offset:25600\n"
"v_mfma_f32_16x16x32_bf16 v[108:111], a[172:175], a[76:79], v[108:111]\n"
"ds_read_b64_tr_b16 a[242:243], v224 offset:27648\n"
"v_mfma_f32_16x16x32_bf16 v[80:83], a[144:147], a[64:67], 0\n"
"ds_read_b64_tr_b16 a[212:213], v225 offset:17408\n"
"buffer_load_dwordx4 a[44:47], v248, s[16:19], s48 offen offset:1024 sc1\n"
"v_mfma_f32_16x16x32_bf16 v[80:83], a[176:179], a[72:75], v[80:83]\n"
"ds_read_b64_tr_b16 a[214:215], v225 offset:19456\n"
"v_mfma_f32_16x16x32_bf16 v[112:115], a[144:147], a[68:71], 0\n"
"ds_read_b64_tr_b16 a[244:245], v225 offset:25600\n"
"ds_read_b64_tr_b16 a[246:247], v225 offset:27648\n"
"v_mfma_f32_16x16x32_bf16 v[112:115], a[176:179], a[76:79], v[112:115]\n"
"ds_read_b64_tr_b16 a[216:217], v226 offset:17408\n"
"buffer_load_dwordx4 a[48:51], v248, s[16:19], s47 offen offset:2048 sc1\n"
"v_mfma_f32_16x16x32_bf16 v[84:87], a[148:151], a[64:67], 0\n"
"ds_read_b64_tr_b16 a[218:219], v226 offset:19456\n"
"v_mfma_f32_16x16x32_bf16 v[84:87], a[180:183], a[72:75], v[84:87]\n"
"ds_read_b64_tr_b16 a[248:249], v226 offset:25600\n"
"ds_read_b64_tr_b16 a[250:251], v226 offset:27648\n"
"v_mfma_f32_16x16x32_bf16 v[116:119], a[148:151], a[68:71], 0\n"
"ds_read_b64_tr_b16 a[220:221], v227 offset:17408\n"
"buffer_load_dwordx4 a[56:59], v248, s[16:19], s47 offen offset:3072 sc1\n"
"v_mfma_f32_16x16x32_bf16 v[116:119], a[180:183], a[76:79], v[116:119]\n"
"ds_read_b64_tr_b16 a[222:223], v227 offset:19456\n"
"v_mfma_f32_16x16x32_bf16 v[88:91], a[152:155], a[64:67], 0\n"
"ds_read_b64_tr_b16 a[252:253], v227 offset:25600\n"
"v_mfma_f32_16x16x32_bf16 v[88:91], a[184:187], a[72:75], v[88:91]\n"
"ds_read_b64_tr_b16 a[254:255], v227 offset:27648\n"
"buffer_load_dwordx4 a[52:55], v248, s[16:19], s48 offen offset:2048 sc1\n"
"v_mfma_f32_16x16x32_bf16 v[120:123], a[152:155], a[68:71], 0\n"
"v_mfma_f32_16x16x32_bf16 v[120:123], a[184:187], a[76:79], v[120:123]\n"
"v_mfma_f32_16x16x32_bf16 v[92:95], a[156:159], a[64:67], 0\n"
"buffer_load_dwordx4 a[60:63], v248, s[16:19], s48 offen offset:3072 sc1\n"
"v_mfma_f32_16x16x32_bf16 v[92:95], a[188:191], a[72:75], v[92:95]\n"
"v_mfma_f32_16x16x32_bf16 v[124:127], a[156:159], a[68:71], 0\n"
"v_mfma_f32_16x16x32_bf16 v[124:127], a[188:191], a[76:79], v[124:127]\n"
"s_waitcnt vmcnt(24) lgkmcnt(0)\n"
"v_mfma_f32_16x16x32_bf16 v[64:67], a[192:195], a[80:83], v[64:67]\n"
"ds_read_b64_tr_b16 a[128:129], v216 offset:0\n"
"ds_read_b64_tr_b16 a[130:131], v216 offset:2048\n"
"v_mfma_f32_16x16x32_bf16 v[64:67], a[224:227], a[88:91], v[64:67]\n"
"v_cvt_pk_bf16_f32 v128, v0, v1\n"
"ds_read_b64_tr_b16 a[160:161], v216 offset:8192\n"
"v_mfma_f32_16x16x32_bf16 v[96:99], a[192:195], a[84:87], v[96:99]\n"
"v_cvt_pk_bf16_f32 v129, v2, v3\n"
"ds_read_b64_tr_b16 a[162:163], v216 offset:10240\n"
"v_mfma_f32_16x16x32_bf16 v[96:99], a[224:227], a[92:95], v[96:99]\n"
"ds_read_b64_tr_b16 a[132:133], v217 offset:0\n"
"v_cvt_pk_bf16_f32 v130, v4, v5\n"
"v_mfma_f32_16x16x32_bf16 v[68:71], a[196:199], a[80:83], v[68:71]\n"
"ds_read_b64_tr_b16 a[134:135], v217 offset:2048\n"
"ds_read_b64_tr_b16 a[164:165], v217 offset:8192\n"
"v_mfma_f32_16x16x32_bf16 v[68:71], a[228:231], a[88:91], v[68:71]\n"
"v_cvt_pk_bf16_f32 v131, v6, v7\n"
"ds_read_b64_tr_b16 a[166:167], v217 offset:10240\n"
"v_mfma_f32_16x16x32_bf16 v[100:103], a[196:199], a[84:87], v[100:103]\n"
"ds_read_b64_tr_b16 a[136:137], v218 offset:0\n"
"buffer_store_dwordx4 v[128:131], v248, s[36:39], s49 offen offset:0 sc1\n"
"v_mfma_f32_16x16x32_bf16 v[100:103], a[228:231], a[92:95], v[100:103]\n"
"ds_read_b64_tr_b16 a[138:139], v218 offset:2048\n"
"v_mfma_f32_16x16x32_bf16 v[72:75], a[200:203], a[80:83], v[72:75]\n"
"ds_read_b64_tr_b16 a[168:169], v218 offset:8192\n"
"ds_read_b64_tr_b16 a[170:171], v218 offset:10240\n"
"v_mfma_f32_16x16x32_bf16 v[72:75], a[232:235], a[88:91], v[72:75]\n"
"v_cvt_pk_bf16_f32 v132, v8, v9\n"
"ds_read_b64_tr_b16 a[140:141], v219 offset:0\n"
"v_mfma_f32_16x16x32_bf16 v[104:107], a[200:203], a[84:87], v[104:107]\n"
"v_cvt_pk_bf16_f32 v133, v10, v11\n"
"ds_read_b64_tr_b16 a[142:143], v219 offset:2048\n"
"v_mfma_f32_16x16x32_bf16 v[104:107], a[232:235], a[92:95], v[104:107]\n"
"ds_read_b64_tr_b16 a[172:173], v219 offset:8192\n"
"v_cvt_pk_bf16_f32 v134, v12, v13\n"
"v_mfma_f32_16x16x32_bf16 v[76:79], a[204:207], a[80:83], v[76:79]\n"
"ds_read_b64_tr_b16 a[174:175], v219 offset:10240\n"
"v_cvt_pk_bf16_f32 v135, v14, v15\n"
"v_mfma_f32_16x16x32_bf16 v[76:79], a[236:239], a[88:91], v[76:79]\n"
"ds_read_b64_tr_b16 a[144:145], v216 offset:1024\n"
"ds_read_b64_tr_b16 a[146:147], v216 offset:3072\n"
"v_mfma_f32_16x16x32_bf16 v[108:111], a[204:207], a[84:87], v[108:111]\n"
"ds_read_b64_tr_b16 a[176:177], v216 offset:9216\n"
"buffer_store_dwordx4 v[132:135], v248, s[36:39], s49 offen offset:1024 sc1\n"
"v_mfma_f32_16x16x32_bf16 v[108:111], a[236:239], a[92:95], v[108:111]\n"
"ds_read_b64_tr_b16 a[178:179], v216 offset:11264\n"
"v_mfma_f32_16x16x32_bf16 v[80:83], a[208:211], a[80:83], v[80:83]\n"
"ds_read_b64_tr_b16 a[148:149], v217 offset:1024\n"
"v_cvt_pk_bf16_f32 v136, v16, v17\n"
"v_mfma_f32_16x16x32_bf16 v[80:83], a[240:243], a[88:91], v[80:83]\n"
"ds_read_b64_tr_b16 a[150:151], v217 offset:3072\n"
"ds_read_b64_tr_b16 a[180:181], v217 offset:9216\n"
"v_mfma_f32_16x16x32_bf16 v[112:115], a[208:211], a[84:87], v[112:115]\n"
"v_cvt_pk_bf16_f32 v137, v18, v19\n"
"ds_read_b64_tr_b16 a[182:183], v217 offset:11264\n"
"v_mfma_f32_16x16x32_bf16 v[112:115], a[240:243], a[92:95], v[112:115]\n"
"ds_read_b64_tr_b16 a[152:153], v218 offset:1024\n"
"v_cvt_pk_bf16_f32 v138, v20, v21\n"
"v_mfma_f32_16x16x32_bf16 v[84:87], a[212:215], a[80:83], v[84:87]\n"
"ds_read_b64_tr_b16 a[154:155], v218 offset:3072\n"
"v_cvt_pk_bf16_f32 v139, v22, v23\n"
"v_mfma_f32_16x16x32_bf16 v[84:87], a[244:247], a[88:91], v[84:87]\n"
"ds_read_b64_tr_b16 a[184:185], v218 offset:9216\n"
"ds_read_b64_tr_b16 a[186:187], v218 offset:11264\n"
"v_mfma_f32_16x16x32_bf16 v[116:119], a[212:215], a[84:87], v[116:119]\n"
"ds_read_b64_tr_b16 a[156:157], v219 offset:1024\n"
"buffer_store_dwordx4 v[136:139], v248, s[36:39], s49 offen offset:2048 sc1\n"
"v_mfma_f32_16x16x32_bf16 v[116:119], a[244:247], a[92:95], v[116:119]\n"
"ds_read_b64_tr_b16 a[158:159], v219 offset:3072\n"
"v_mfma_f32_16x16x32_bf16 v[88:91], a[216:219], a[80:83], v[88:91]\n"
"ds_read_b64_tr_b16 a[188:189], v219 offset:9216\n"
"v_cvt_pk_bf16_f32 v140, v24, v25\n"
"v_mfma_f32_16x16x32_bf16 v[88:91], a[248:251], a[88:91], v[88:91]\n"
"ds_read_b64_tr_b16 a[190:191], v219 offset:11264\n"
"v_cvt_pk_bf16_f32 v141, v26, v27\n"
"v_mfma_f32_16x16x32_bf16 v[120:123], a[216:219], a[84:87], v[120:123]\n"
"v_mfma_f32_16x16x32_bf16 v[120:123], a[248:251], a[92:95], v[120:123]\n"
"v_cvt_pk_bf16_f32 v142, v28, v29\n"
"v_mfma_f32_16x16x32_bf16 v[92:95], a[220:223], a[80:83], v[92:95]\n"
"v_cvt_pk_bf16_f32 v143, v30, v31\n"
"v_mfma_f32_16x16x32_bf16 v[92:95], a[252:255], a[88:91], v[92:95]\n"
"v_mfma_f32_16x16x32_bf16 v[124:127], a[220:223], a[84:87], v[124:127]\n"
"buffer_store_dwordx4 v[140:143], v248, s[36:39], s49 offen offset:3072 sc1\n"
"v_mfma_f32_16x16x32_bf16 v[124:127], a[252:255], a[92:95], v[124:127]\n"
"s_waitcnt vmcnt(24) lgkmcnt(0)\n"
"v_mfma_f32_16x16x32_bf16 v[64:67], a[128:131], a[96:99], v[64:67]\n"
"ds_read_b64_tr_b16 a[192:193], v216 offset:16384\n"
"ds_read_b64_tr_b16 a[194:195], v216 offset:18432\n"
"v_mfma_f32_16x16x32_bf16 v[64:67], a[160:163], a[104:107], v[64:67]\n"
"s_sub_u32 s24, s68, 4096\n"
"s_subb_u32 s25, s69, 0\n"
"v_mfma_f32_16x16x32_bf16 v[96:99], a[128:131], a[100:103], v[96:99]\n"
"v_cvt_pk_bf16_f32 v128, v32, v33\n"
"ds_read_b64_tr_b16 a[224:225], v216 offset:24576\n"
"v_mfma_f32_16x16x32_bf16 v[96:99], a[160:163], a[108:111], v[96:99]\n"
"s_and_b32 s25, s25, 0xffff\n"
"v_cvt_pk_bf16_f32 v129, v34, v35\n"
"v_mfma_f32_16x16x32_bf16 v[68:71], a[132:135], a[96:99], v[68:71]\n"
"ds_read_b64_tr_b16 a[226:227], v216 offset:26624\n"
"ds_read_b64_tr_b16 a[196:197], v217 offset:16384\n"
"v_mfma_f32_16x16x32_bf16 v[68:71], a[164:167], a[104:107], v[68:71]\n"
"s_mov_b32 s26, 0x1c1000\n"
"v_cvt_pk_bf16_f32 v130, v36, v37\n"
"v_mfma_f32_16x16x32_bf16 v[100:103], a[132:135], a[100:103], v[100:103]\n"
"ds_read_b64_tr_b16 a[198:199], v217 offset:18432\n"
"ds_read_b64_tr_b16 a[228:229], v217 offset:24576\n"
"v_mfma_f32_16x16x32_bf16 v[100:103], a[164:167], a[108:111], v[100:103]\n"
"s_mov_b32 s27, 0x20000\n"
"v_cvt_pk_bf16_f32 v131, v38, v39\n"
"v_mfma_f32_16x16x32_bf16 v[72:75], a[136:139], a[96:99], v[72:75]\n"
"ds_read_b64_tr_b16 a[230:231], v217 offset:26624\n"
"ds_read_b64_tr_b16 a[200:201], v218 offset:16384\n"
"v_mfma_f32_16x16x32_bf16 v[72:75], a[168:171], a[104:107], v[72:75]\n"
"s_sub_u32 s28, s70, 4096\n"
"s_subb_u32 s29, s71, 0\n"
"v_mfma_f32_16x16x32_bf16 v[104:107], a[136:139], a[100:103], v[104:107]\n"
"s_add_u32 s49, s49, 16384\n"
"ds_read_b64_tr_b16 a[202:203], v218 offset:18432\n"
"v_mfma_f32_16x16x32_bf16 v[104:107], a[168:171], a[108:111], v[104:107]\n"
"buffer_store_dwordx4 v[128:131], v248, s[36:39], s49 offen offset:0 sc1\n"
"v_mfma_f32_16x16x32_bf16 v[76:79], a[140:143], a[96:99], v[76:79]\n"
"ds_read_b64_tr_b16 a[232:233], v218 offset:24576\n"
"ds_read_b64_tr_b16 a[234:235], v218 offset:26624\n"
"v_mfma_f32_16x16x32_bf16 v[76:79], a[172:175], a[104:107], v[76:79]\n"
"s_and_b32 s29, s29, 0xffff\n"
"ds_read_b64_tr_b16 a[204:205], v219 offset:16384\n"
"v_mfma_f32_16x16x32_bf16 v[108:111], a[140:143], a[100:103], v[108:111]\n"
"s_mov_b32 s30, 0x1c1000\n"
"v_cvt_pk_bf16_f32 v132, v40, v41\n"
"v_mfma_f32_16x16x32_bf16 v[108:111], a[172:175], a[108:111], v[108:111]\n"
"ds_read_b64_tr_b16 a[206:207], v219 offset:18432\n"
"v_cvt_pk_bf16_f32 v133, v42, v43\n"
"v_mfma_f32_16x16x32_bf16 v[80:83], a[144:147], a[96:99], v[80:83]\n"
"ds_read_b64_tr_b16 a[236:237], v219 offset:24576\n"
"s_mov_b32 s31, 0x20000\n"
"v_mfma_f32_16x16x32_bf16 v[80:83], a[176:179], a[104:107], v[80:83]\n"
"ds_read_b64_tr_b16 a[238:239], v219 offset:26624\n"
"s_bfe_u32 s94, s84, 0x20001\n"
"v_mfma_f32_16x16x32_bf16 v[112:115], a[144:147], a[100:103], v[112:115]\n"
"v_cvt_pk_bf16_f32 v134, v44, v45\n"
"ds_read_b64_tr_b16 a[208:209], v216 offset:17408\n"
"v_mfma_f32_16x16x32_bf16 v[112:115], a[176:179], a[108:111], v[112:115]\n"
"ds_read_b64_tr_b16 a[210:211], v216 offset:19456\n"
"v_cvt_pk_bf16_f32 v135, v46, v47\n"
"v_mfma_f32_16x16x32_bf16 v[84:87], a[148:151], a[96:99], v[84:87]\n"
"ds_read_b64_tr_b16 a[240:241], v216 offset:25600\n"
"s_mul_i32 s94, s94, 7\n"
"v_mfma_f32_16x16x32_bf16 v[84:87], a[180:183], a[104:107], v[84:87]\n"
"ds_read_b64_tr_b16 a[242:243], v216 offset:27648\n"
"buffer_store_dwordx4 v[132:135], v248, s[36:39], s49 offen offset:1024 sc1\n"
"v_mfma_f32_16x16x32_bf16 v[116:119], a[148:151], a[100:103], v[116:119]\n"
"ds_read_b64_tr_b16 a[212:213], v217 offset:17408\n"
"v_mfma_f32_16x16x32_bf16 v[116:119], a[180:183], a[108:111], v[116:119]\n"
"s_add_u32 s52, s94, s86\n"
"v_cvt_pk_bf16_f32 v136, v48, v49\n"
"v_mfma_f32_16x16x32_bf16 v[88:91], a[152:155], a[96:99], v[88:91]\n"
"ds_read_b64_tr_b16 a[214:215], v217 offset:19456\n"
"ds_read_b64_tr_b16 a[244:245], v217 offset:25600\n"
"v_mfma_f32_16x16x32_bf16 v[88:91], a[184:187], a[104:107], v[88:91]\n"
"s_cmp_eq_u32 s86, 7\n"
"s_cselect_b32 s26, 0, s26\n"
"s_mov_b32 s30, s26\n"
"s_cselect_b32 s52, 31, s52\n"
"v_mfma_f32_16x16x32_bf16 v[120:123], a[152:155], a[100:103], v[120:123]\n"
"v_mfma_f32_16x16x32_bf16 v[120:123], a[184:187], a[108:111], v[120:123]\n"
"ds_read_b64_tr_b16 a[246:247], v217 offset:27648\n"
"v_cvt_pk_bf16_f32 v137, v50, v51\n"
"v_mfma_f32_16x16x32_bf16 v[92:95], a[156:159], a[96:99], v[92:95]\n"
"ds_read_b64_tr_b16 a[216:217], v218 offset:17408\n"
"s_mul_i32 s44, s83, 57344\n"
"v_mfma_f32_16x16x32_bf16 v[92:95], a[188:191], a[104:107], v[92:95]\n"
"v_cvt_pk_bf16_f32 v138, v52, v53\n"
"ds_read_b64_tr_b16 a[218:219], v218 offset:19456\n"
"v_mfma_f32_16x16x32_bf16 v[124:127], a[156:159], a[100:103], v[124:127]\n"
"s_lshl_b32 s94, s52, 7\n"
"ds_read_b64_tr_b16 a[248:249], v218 offset:25600\n"
"v_mfma_f32_16x16x32_bf16 v[124:127], a[188:191], a[108:111], v[124:127]\n"
"ds_read_b64_tr_b16 a[250:251], v218 offset:27648\n"
"v_cvt_pk_bf16_f32 v139, v54, v55\n"
"ds_read_b64_tr_b16 a[220:221], v219 offset:17408\n"
"s_add_u32 s44, s44, s94\n"
"buffer_store_dwordx4 v[136:139], v248, s[36:39], s49 offen offset:2048 sc1\n"
"ds_read_b64_tr_b16 a[222:223], v219 offset:19456\n"
"ds_read_b64_tr_b16 a[252:253], v219 offset:25600\n"
"v_add_u32 v184, s44, v251\n"
"v_cvt_pk_bf16_f32 v140, v56, v57\n"
"ds_read_b64_tr_b16 a[254:255], v219 offset:27648\n"
"v_add_u32 v187, 29696, v184\n"
"v_cvt_pk_bf16_f32 v141, v58, v59\n"
"v_add_u32 v186, 30720, v184\n"
"v_cvt_pk_bf16_f32 v142, v60, v61\n"
"v_add_u32 v185, 3072, v184\n"
"v_cvt_pk_bf16_f32 v143, v62, v63\n"
"v_add_u32 v184, 4096, v184\n"
"buffer_store_dwordx4 v[140:143], v248, s[36:39], s49 offen offset:3072 sc1\n"
"s_waitcnt vmcnt(24) lgkmcnt(0)\n"
"s_barrier\n"
"v_mfma_f32_16x16x32_bf16 v[64:67], a[192:195], a[112:115], v[64:67]\n"
"ds_read_b64_tr_b16 a[128:129], v216 offset:32768\n"
"ds_read_b64_tr_b16 a[130:131], v216 offset:34816\n"
"v_mfma_f32_16x16x32_bf16 v[64:67], a[224:227], a[120:123], v[64:67]\n"
"s_add_u32 m0, s54, 98304\n"
"ds_read_b64_tr_b16 a[160:161], v216 offset:40960\n"
"v_mfma_f32_16x16x32_bf16 v[96:99], a[192:195], a[116:119], v[96:99]\n"
"ds_read_b64_tr_b16 a[162:163], v216 offset:43008\n"
"v_mfma_f32_16x16x32_bf16 v[96:99], a[224:227], a[124:127], v[96:99]\n"
"ds_read_b64_tr_b16 a[132:133], v217 offset:32768\n"
"v_mfma_f32_16x16x32_bf16 v[68:71], a[196:199], a[112:115], v[68:71]\n"
"ds_read_b64_tr_b16 a[134:135], v217 offset:34816\n"
"ds_read_b64_tr_b16 a[164:165], v217 offset:40960\n"
"v_mfma_f32_16x16x32_bf16 v[68:71], a[228:231], a[120:123], v[68:71]\n"
"ds_read_b64_tr_b16 a[166:167], v217 offset:43008\n"
"s_mov_b32 s92, 0\n"
"v_mfma_f32_16x16x32_bf16 v[100:103], a[196:199], a[116:119], v[100:103]\n"
"ds_read_b64_tr_b16 a[136:137], v218 offset:32768\n"
"v_mfma_f32_16x16x32_bf16 v[100:103], a[228:231], a[124:127], v[100:103]\n"
"ds_read_b64_tr_b16 a[138:139], v218 offset:34816\n"
"v_mfma_f32_16x16x32_bf16 v[72:75], a[200:203], a[112:115], v[72:75]\n"
"ds_read_b64_tr_b16 a[168:169], v218 offset:40960\n"
"ds_read_b64_tr_b16 a[170:171], v218 offset:43008\n"
"v_mfma_f32_16x16x32_bf16 v[72:75], a[232:235], a[120:123], v[72:75]\n"
"ds_read_b64_tr_b16 a[140:141], v219 offset:32768\n"
"v_mfma_f32_16x16x32_bf16 v[104:107], a[200:203], a[116:119], v[104:107]\n"
"ds_read_b64_tr_b16 a[142:143], v219 offset:34816\n"
"buffer_load_dwordx4 v184, s[24:27], s92 offen offset:0 lds\n"
"v_mfma_f32_16x16x32_bf16 v[104:107], a[232:235], a[124:127], v[104:107]\n"
"ds_read_b64_tr_b16 a[172:173], v219 offset:40960\n"
"v_mfma_f32_16x16x32_bf16 v[76:79], a[204:207], a[112:115], v[76:79]\n"
"ds_read_b64_tr_b16 a[174:175], v219 offset:43008\n"
"v_mfma_f32_16x16x32_bf16 v[76:79], a[236:239], a[120:123], v[76:79]\n"
"ds_read_b64_tr_b16 a[144:145], v216 offset:33792\n"
"ds_read_b64_tr_b16 a[146:147], v216 offset:35840\n"
"v_mfma_f32_16x16x32_bf16 v[108:111], a[204:207], a[116:119], v[108:111]\n"
"ds_read_b64_tr_b16 a[176:177], v216 offset:41984\n"
"v_mfma_f32_16x16x32_bf16 v[108:111], a[236:239], a[124:127], v[108:111]\n"
"ds_read_b64_tr_b16 a[178:179], v216 offset:44032\n"
"v_mfma_f32_16x16x32_bf16 v[80:83], a[208:211], a[112:115], v[80:83]\n"
"ds_read_b64_tr_b16 a[148:149], v217 offset:33792\n"
"buffer_load_dwordx4 v185, s[28:31], s92 offen offset:1024 lds\n"
"v_mfma_f32_16x16x32_bf16 v[80:83], a[240:243], a[120:123], v[80:83]\n"
"ds_read_b64_tr_b16 a[150:151], v217 offset:35840\n"
"v_mfma_f32_16x16x32_bf16 v[112:115], a[208:211], a[116:119], v[112:115]\n"
"ds_read_b64_tr_b16 a[180:181], v217 offset:41984\n"
"ds_read_b64_tr_b16 a[182:183], v217 offset:44032\n"
"v_mfma_f32_16x16x32_bf16 v[112:115], a[240:243], a[124:127], v[112:115]\n"
"ds_read_b64_tr_b16 a[152:153], v218 offset:33792\n"
"v_mfma_f32_16x16x32_bf16 v[84:87], a[212:215], a[112:115], v[84:87]\n"
"ds_read_b64_tr_b16 a[154:155], v218 offset:35840\n"
"v_mfma_f32_16x16x32_bf16 v[84:87], a[244:247], a[120:123], v[84:87]\n"
"ds_read_b64_tr_b16 a[184:185], v218 offset:41984\n"
"ds_read_b64_tr_b16 a[186:187], v218 offset:44032\n"
"v_mfma_f32_16x16x32_bf16 v[116:119], a[212:215], a[116:119], v[116:119]\n"
"buffer_load_dwordx4 v186, s[24:27], s92 offen offset:2048 lds\n"
"v_mfma_f32_16x16x32_bf16 v[116:119], a[244:247], a[124:127], v[116:119]\n"
"ds_read_b64_tr_b16 a[156:157], v219 offset:33792\n"
"ds_read_b64_tr_b16 a[158:159], v219 offset:35840\n"
"v_mfma_f32_16x16x32_bf16 v[88:91], a[216:219], a[112:115], v[88:91]\n"
"ds_read_b64_tr_b16 a[188:189], v219 offset:41984\n"
"v_mfma_f32_16x16x32_bf16 v[88:91], a[248:251], a[120:123], v[88:91]\n"
"ds_read_b64_tr_b16 a[190:191], v219 offset:44032\n"
"v_mfma_f32_16x16x32_bf16 v[120:123], a[216:219], a[116:119], v[120:123]\n"
"buffer_load_dwordx4 v187, s[28:31], s92 offen offset:3072 lds\n"
"v_mfma_f32_16x16x32_bf16 v[120:123], a[248:251], a[124:127], v[120:123]\n"
"v_mfma_f32_16x16x32_bf16 v[92:95], a[220:223], a[112:115], v[92:95]\n"
"v_mfma_f32_16x16x32_bf16 v[92:95], a[252:255], a[120:123], v[92:95]\n"
"v_mfma_f32_16x16x32_bf16 v[124:127], a[220:223], a[116:119], v[124:127]\n"
"v_mfma_f32_16x16x32_bf16 v[124:127], a[252:255], a[124:127], v[124:127]\n"
"s_waitcnt vmcnt(24) lgkmcnt(0)\n"
"v_mfma_f32_16x16x32_bf16 v[64:67], a[128:131], a[0:3], v[64:67]\n"
"ds_read_b64_tr_b16 a[192:193], v216 offset:49152\n"
"ds_read_b64_tr_b16 a[194:195], v216 offset:51200\n"
"v_mfma_f32_16x16x32_bf16 v[64:67], a[160:163], a[8:11], v[64:67]\n"
"s_add_u32 m0, s54, 114688\n"
"ds_read_b64_tr_b16 a[224:225], v216 offset:57344\n"
"v_mfma_f32_16x16x32_bf16 v[96:99], a[128:131], a[4:7], v[96:99]\n"
"s_mov_b32 s92, 229376\n"
"ds_read_b64_tr_b16 a[226:227], v216 offset:59392\n"
"v_mfma_f32_16x16x32_bf16 v[96:99], a[160:163], a[12:15], v[96:99]\n"
"buffer_load_dwordx4 v184, s[24:27], s92 offen offset:0 lds\n"
"v_mfma_f32_16x16x32_bf16 v[68:71], a[132:135], a[0:3], v[68:71]\n"
"ds_read_b64_tr_b16 a[196:197], v217 offset:49152\n"
"buffer_load_dwordx4 v185, s[28:31], s92 offen offset:1024 lds\n"
"v_mfma_f32_16x16x32_bf16 v[68:71], a[164:167], a[8:11], v[68:71]\n"
"ds_read_b64_tr_b16 a[198:199], v217 offset:51200\n"
"v_mfma_f32_16x16x32_bf16 v[100:103], a[132:135], a[4:7], v[100:103]\n"
"ds_read_b64_tr_b16 a[228:229], v217 offset:57344\n"
"buffer_load_dwordx4 v186, s[24:27], s92 offen offset:2048 lds\n"
"v_mfma_f32_16x16x32_bf16 v[100:103], a[164:167], a[12:15], v[100:103]\n"
"ds_read_b64_tr_b16 a[230:231], v217 offset:59392\n"
"v_mfma_f32_16x16x32_bf16 v[72:75], a[136:139], a[0:3], v[72:75]\n"
"buffer_load_dwordx4 v187, s[28:31], s92 offen offset:3072 lds\n"
"v_mfma_f32_16x16x32_bf16 v[72:75], a[168:171], a[8:11], v[72:75]\n"
"ds_read_b64_tr_b16 a[200:201], v218 offset:49152\n"
"ds_read_b64_tr_b16 a[202:203], v218 offset:51200\n"
"v_mfma_f32_16x16x32_bf16 v[104:107], a[136:139], a[4:7], v[104:107]\n"
"v_and_b32 v236, 7, v255\n"
"ds_read_b64_tr_b16 a[232:233], v218 offset:57344\n"
"v_mfma_f32_16x16x32_bf16 v[104:107], a[168:171], a[12:15], v[104:107]\n"
"ds_read_b64_tr_b16 a[234:235], v218 offset:59392\n"
"v_bfe_u32 v237, v255, 3, 2\n"
"v_mfma_f32_16x16x32_bf16 v[76:79], a[140:143], a[0:3], v[76:79]\n"
"ds_read_b64_tr_b16 a[204:205], v219 offset:49152\n"
"v_lshrrev_b32 v238, 5, v255\n"
"v_mfma_f32_16x16x32_bf16 v[76:79], a[172:175], a[8:11], v[76:79]\n"
"ds_read_b64_tr_b16 a[206:207], v219 offset:51200\n"
"v_lshlrev_b32 v239, 10, v236\n"
"v_mfma_f32_16x16x32_bf16 v[108:111], a[140:143], a[4:7], v[108:111]\n"
"s_waitcnt vmcnt(8)\n"
"ds_read_b64_tr_b16 a[236:237], v219 offset:57344\n"
"v_mfma_f32_16x16x32_bf16 v[108:111], a[172:175], a[12:15], v[108:111]\n"
"v_lshl_add_u32 v239, v238, 6, v239\n"
"ds_read_b64_tr_b16 a[238:239], v219 offset:59392\n"
"v_mfma_f32_16x16x32_bf16 v[80:83], a[144:147], a[0:3], v[80:83]\n"
"s_add_u32 s95, s87, 0x10000\n"
"ds_read_b64_tr_b16 a[208:209], v216 offset:50176\n"
"v_mfma_f32_16x16x32_bf16 v[80:83], a[176:179], a[8:11], v[80:83]\n"
"ds_read_b64_tr_b16 a[210:211], v216 offset:52224\n"
"v_lshl_add_u32 v239, v237, 3, v239\n"
"v_mfma_f32_16x16x32_bf16 v[112:115], a[144:147], a[4:7], v[112:115]\n"
"ds_read_b64_tr_b16 a[240:241], v216 offset:58368\n"
"v_lshlrev_b32 v235, 7, v238\n"
"v_mfma_f32_16x16x32_bf16 v[112:115], a[176:179], a[12:15], v[112:115]\n"
"v_mov_b32 v253, s82\n"
"ds_read_b64_tr_b16 a[242:243], v216 offset:60416\n"
"v_mfma_f32_16x16x32_bf16 v[84:87], a[148:151], a[0:3], v[84:87]\n"
"v_lshl_add_u32 v235, v237, 4, v235\n"
"ds_read_b64_tr_b16 a[212:213], v217 offset:50176\n"
"v_mfma_f32_16x16x32_bf16 v[84:87], a[180:183], a[8:11], v[84:87]\n"
"v_lshlrev_b32 v234, 10, v238\n"
"s_mov_b64 exec, 1\n"
"buffer_store_dword v253, off, s[88:91], s95 sc1\n"
"s_mov_b64 exec, -1\n"
"v_mfma_f32_16x16x32_bf16 v[116:119], a[148:151], a[4:7], v[116:119]\n"
"v_mfma_f32_16x16x32_bf16 v[116:119], a[180:183], a[12:15], v[116:119]\n"
"ds_read_b64_tr_b16 a[214:215], v217 offset:52224\n"
"v_mfma_f32_16x16x32_bf16 v[88:91], a[152:155], a[0:3], v[88:91]\n"
"ds_read_b64_tr_b16 a[244:245], v217 offset:58368\n"
"v_lshl_add_u32 v234, v237, 8, v234\n"
"v_mfma_f32_16x16x32_bf16 v[88:91], a[184:187], a[8:11], v[88:91]\n"
"ds_read_b64_tr_b16 a[246:247], v217 offset:60416\n"
"v_lshl_add_u32 v234, v236, 4, v234\n"
"v_mfma_f32_16x16x32_bf16 v[120:123], a[152:155], a[4:7], v[120:123]\n"
"ds_read_b64_tr_b16 a[216:217], v218 offset:50176\n"
"ds_read_b64_tr_b16 a[218:219], v218 offset:52224\n"
"v_mfma_f32_16x16x32_bf16 v[120:123], a[184:187], a[12:15], v[120:123]\n"
"v_lshlrev_b32 v233, 11, v238\n"
"ds_read_b64_tr_b16 a[248:249], v218 offset:58368\n"
"v_mfma_f32_16x16x32_bf16 v[92:95], a[156:159], a[0:3], v[92:95]\n"
"ds_read_b64_tr_b16 a[250:251], v218 offset:60416\n"
"v_lshl_add_u32 v233, v237, 8, v233\n"
"v_mfma_f32_16x16x32_bf16 v[92:95], a[188:191], a[8:11], v[92:95]\n"
"ds_read_b64_tr_b16 a[220:221], v219 offset:50176\n"
"v_lshl_add_u32 v233, v236, 4, v233\n"
"v_mfma_f32_16x16x32_bf16 v[124:127], a[156:159], a[4:7], v[124:127]\n"
"ds_read_b64_tr_b16 a[222:223], v219 offset:52224\n"
"v_lshlrev_b32 v232, 5, v236\n"
"v_mfma_f32_16x16x32_bf16 v[124:127], a[188:191], a[12:15], v[124:127]\n"
"ds_read_b64_tr_b16 a[252:253], v219 offset:58368\n"
"v_add_u32 v232, 0x10000, v232\n"
"ds_read_b64_tr_b16 a[254:255], v219 offset:60416\n"
"s_lshl_b32 s94, s83, 3\n"
"v_add_u32 v231, s94, v232\n"
"s_lshl_b32 s45, s83, 9\n"
"s_add_u32 s45, s45, 0x2000\n"
"s_add_u32 s46, s45, 4096\n"
"s_waitcnt vmcnt(9) lgkmcnt(0)\n"
"v_mfma_f32_16x16x32_bf16 v[64:67], a[192:195], a[16:19], v[64:67]\n"
"ds_read_b64_tr_b16 a[128:129], v220 offset:0\n"
"ds_read_b64_tr_b16 a[130:131], v220 offset:2048\n"
"v_mfma_f32_16x16x32_bf16 v[64:67], a[224:227], a[24:27], v[64:67]\n"
"s_add_u32 m0, s54, 131072\n"
"ds_read_b64_tr_b16 a[160:161], v220 offset:8192\n"
"v_mfma_f32_16x16x32_bf16 v[96:99], a[192:195], a[20:23], v[96:99]\n"
"ds_read_b64_tr_b16 a[162:163], v220 offset:10240\n"
"s_mov_b32 s92, 458752\n"
"v_mfma_f32_16x16x32_bf16 v[96:99], a[224:227], a[28:31], v[96:99]\n"
"ds_read_b64_tr_b16 a[132:133], v221 offset:0\n"
"v_mfma_f32_16x16x32_bf16 v[68:71], a[196:199], a[16:19], v[68:71]\n"
"ds_read_b64_tr_b16 a[134:135], v221 offset:2048\n"
"ds_read_b64_tr_b16 a[164:165], v221 offset:8192\n"
"v_mfma_f32_16x16x32_bf16 v[68:71], a[228:231], a[24:27], v[68:71]\n"
"ds_read_b64_tr_b16 a[166:167], v221 offset:10240\n"
"buffer_load_dwordx4 v184, s[24:27], s92 offen offset:0 lds\n"
"v_mfma_f32_16x16x32_bf16 v[100:103], a[196:199], a[20:23], v[100:103]\n"
"ds_read_b64_tr_b16 a[136:137], v222 offset:0\n"
"v_mfma_f32_16x16x32_bf16 v[100:103], a[228:231], a[28:31], v[100:103]\n"
"ds_read_b64_tr_b16 a[138:139], v222 offset:2048\n"
"v_mfma_f32_16x16x32_bf16 v[72:75], a[200:203], a[16:19], v[72:75]\n"
"ds_read_b64_tr_b16 a[168:169], v222 offset:8192\n"
"ds_read_b64_tr_b16 a[170:171], v222 offset:10240\n"
"v_mfma_f32_16x16x32_bf16 v[72:75], a[232:235], a[24:27], v[72:75]\n"
"buffer_load_dwordx4 v185, s[28:31], s92 offen offset:1024 lds\n"
"v_mfma_f32_16x16x32_bf16 v[104:107], a[200:203], a[20:23], v[104:107]\n"
"ds_read_b64_tr_b16 a[140:141], v223 offset:0\n"
"ds_read_b64_tr_b16 a[142:143], v223 offset:2048\n"
"v_mfma_f32_16x16x32_bf16 v[104:107], a[232:235], a[28:31], v[104:107]\n"
"buffer_load_dwordx4 v186, s[24:27], s92 offen offset:2048 lds\n"
"v_mfma_f32_16x16x32_bf16 v[76:79], a[204:207], a[16:19], v[76:79]\n"
"ds_read_b64_tr_b16 a[172:173], v223 offset:8192\n"
"ds_read_b64_tr_b16 a[174:175], v223 offset:10240\n"
"v_mfma_f32_16x16x32_bf16 v[76:79], a[236:239], a[24:27], v[76:79]\n"
"ds_read_b64_tr_b16 a[144:145], v220 offset:1024\n"
"ds_read_b64_tr_b16 a[146:147], v220 offset:3072\n"
"v_mfma_f32_16x16x32_bf16 v[108:111], a[204:207], a[20:23], v[108:111]\n"
"buffer_load_dwordx4 v187, s[28:31], s92 offen offset:3072 lds\n"
"v_mfma_f32_16x16x32_bf16 v[108:111], a[236:239], a[28:31], v[108:111]\n"
"ds_read_b64_tr_b16 a[176:177], v220 offset:9216\n"
"ds_read_b64_tr_b16 a[178:179], v220 offset:11264\n"
"v_mfma_f32_16x16x32_bf16 v[80:83], a[208:211], a[16:19], v[80:83]\n"
"ds_read_b64_tr_b16 a[148:149], v221 offset:1024\n"
"buffer_load_dwordx4 v[144:147], v235, s[88:91], s45 offen offset:0 sc1\n"
"v_mfma_f32_16x16x32_bf16 v[80:83], a[240:243], a[24:27], v[80:83]\n"
"ds_read_b64_tr_b16 a[150:151], v221 offset:3072\n"
"v_mfma_f32_16x16x32_bf16 v[112:115], a[208:211], a[20:23], v[112:115]\n"
"ds_read_b64_tr_b16 a[180:181], v221 offset:9216\n"
"ds_read_b64_tr_b16 a[182:183], v221 offset:11264\n"
"v_mfma_f32_16x16x32_bf16 v[112:115], a[240:243], a[28:31], v[112:115]\n"
"buffer_load_dwordx4 v[148:151], v235, s[88:91], s45 offen offset:64 sc1\n"
"v_mfma_f32_16x16x32_bf16 v[84:87], a[212:215], a[16:19], v[84:87]\n"
"ds_read_b64_tr_b16 a[152:153], v222 offset:1024\n"
"ds_read_b64_tr_b16 a[154:155], v222 offset:3072\n"
"v_mfma_f32_16x16x32_bf16 v[84:87], a[244:247], a[24:27], v[84:87]\n"
"s_lshl_b32 s94, s83, 6\n"
"ds_read_b64_tr_b16 a[184:185], v222 offset:9216\n"
"v_mfma_f32_16x16x32_bf16 v[116:119], a[212:215], a[20:23], v[116:119]\n"
"ds_read_b64_tr_b16 a[186:187], v222 offset:11264\n"
"buffer_load_dwordx4 v[152:155], v235, s[88:91], s45 offen offset:256 sc1\n"
"v_mfma_f32_16x16x32_bf16 v[116:119], a[244:247], a[28:31], v[116:119]\n"
"s_add_u32 s94, s94, 0x4000\n"
"v_mfma_f32_16x16x32_bf16 v[88:91], a[216:219], a[16:19], v[88:91]\n"
"ds_read_b64_tr_b16 a[156:157], v223 offset:1024\n"
"ds_read_b64_tr_b16 a[158:159], v223 offset:3072\n"
"v_mfma_f32_16x16x32_bf16 v[88:91], a[248:251], a[24:27], v[88:91]\n"
"v_mov_b32 v229, s94\n"
"ds_read_b64_tr_b16 a[188:189], v223 offset:9216\n"
"v_mfma_f32_16x16x32_bf16 v[120:123], a[216:219], a[20:23], v[120:123]\n"
"buffer_load_dwordx4 v[156:159], v235, s[88:91], s45 offen offset:320 sc1\n"
"v_mfma_f32_16x16x32_bf16 v[120:123], a[248:251], a[28:31], v[120:123]\n"
"s_waitcnt vmcnt(17)\n"
"ds_read_b64_tr_b16 a[190:191], v223 offset:11264\n"
"v_mfma_f32_16x16x32_bf16 v[92:95], a[220:223], a[16:19], v[92:95]\n"
"buffer_load_dwordx4 v[160:163], v235, s[88:91], s45 offen offset:2048 sc1\n"
"v_mfma_f32_16x16x32_bf16 v[92:95], a[252:255], a[24:27], v[92:95]\n"
"s_mov_b64 exec, 1\n"
"ds_write_b32 v229, v228\n"
"s_mov_b64 exec, -1\n"
"v_mfma_f32_16x16x32_bf16 v[124:127], a[220:223], a[20:23], v[124:127]\n"
"v_mov_b32 v229, 0x4000\n"
"v_mfma_f32_16x16x32_bf16 v[124:127], a[252:255], a[28:31], v[124:127]\n"
"buffer_load_dwordx4 v[164:167], v235, s[88:91], s45 offen offset:2112 sc1\n"
"s_waitcnt vmcnt(19) lgkmcnt(0)\n"
"s_barrier\n"
"ds_read_b32 v228, v229\n"
"v_mfma_f32_16x16x32_bf16 v[64:67], a[128:131], a[32:35], v[64:67]\n"
"ds_read_b64_tr_b16 a[192:193], v220 offset:16384\n"
"ds_read_b64_tr_b16 a[194:195], v220 offset:18432\n"
"v_mfma_f32_16x16x32_bf16 v[64:67], a[160:163], a[40:43], v[64:67]\n"
"s_add_u32 m0, s54, 147456\n"
"ds_read_b64_tr_b16 a[224:225], v220 offset:24576\n"
"v_mfma_f32_16x16x32_bf16 v[96:99], a[128:131], a[36:39], v[96:99]\n"
"ds_read_b64_tr_b16 a[226:227], v220 offset:26624\n"
"s_mov_b32 s92, 688128\n"
"v_mfma_f32_16x16x32_bf16 v[96:99], a[160:163], a[44:47], v[96:99]\n"
"ds_read_b64_tr_b16 a[196:197], v221 offset:16384\n"
"v_mfma_f32_16x16x32_bf16 v[68:71], a[132:135], a[32:35], v[68:71]\n"
"ds_read_b64_tr_b16 a[198:199], v221 offset:18432\n"
"ds_read_b64_tr_b16 a[228:229], v221 offset:24576\n"
"v_mfma_f32_16x16x32_bf16 v[68:71], a[164:167], a[40:43], v[68:71]\n"
"ds_read_b64_tr_b16 a[230:231], v221 offset:26624\n"
"buffer_load_dwordx4 v184, s[24:27], s92 offen offset:0 lds\n"
"v_mfma_f32_16x16x32_bf16 v[100:103], a[132:135], a[36:39], v[100:103]\n"
"ds_read_b64_tr_b16 a[200:201], v222 offset:16384\n"
"v_mfma_f32_16x16x32_bf16 v[100:103], a[164:167], a[44:47], v[100:103]\n"
"ds_read_b64_tr_b16 a[202:203], v222 offset:18432\n"
"v_mfma_f32_16x16x32_bf16 v[72:75], a[136:139], a[32:35], v[72:75]\n"
"ds_read_b64_tr_b16 a[232:233], v222 offset:24576\n"
"ds_read_b64_tr_b16 a[234:235], v222 offset:26624\n"
"v_mfma_f32_16x16x32_bf16 v[72:75], a[168:171], a[40:43], v[72:75]\n"
"buffer_load_dwordx4 v185, s[28:31], s92 offen offset:1024 lds\n"
"v_mfma_f32_16x16x32_bf16 v[104:107], a[136:139], a[36:39], v[104:107]\n"
"ds_read_b64_tr_b16 a[204:205], v223 offset:16384\n"
"ds_read_b64_tr_b16 a[206:207], v223 offset:18432\n"
"v_mfma_f32_16x16x32_bf16 v[104:107], a[168:171], a[44:47], v[104:107]\n"
"buffer_load_dwordx4 v186, s[24:27], s92 offen offset:2048 lds\n"
"v_mfma_f32_16x16x32_bf16 v[76:79], a[140:143], a[32:35], v[76:79]\n"
"ds_read_b64_tr_b16 a[236:237], v223 offset:24576\n"
"ds_read_b64_tr_b16 a[238:239], v223 offset:26624\n"
"v_mfma_f32_16x16x32_bf16 v[76:79], a[172:175], a[40:43], v[76:79]\n"
"ds_read_b64_tr_b16 a[208:209], v220 offset:17408\n"
"ds_read_b64_tr_b16 a[210:211], v220 offset:19456\n"
"v_mfma_f32_16x16x32_bf16 v[108:111], a[140:143], a[36:39], v[108:111]\n"
"buffer_load_dwordx4 v187, s[28:31], s92 offen offset:3072 lds\n"
"v_mfma_f32_16x16x32_bf16 v[108:111], a[172:175], a[44:47], v[108:111]\n"
"ds_read_b64_tr_b16 a[240:241], v220 offset:25600\n"
"ds_read_b64_tr_b16 a[242:243], v220 offset:27648\n"
"v_mfma_f32_16x16x32_bf16 v[80:83], a[144:147], a[32:35], v[80:83]\n"
"ds_read_b64_tr_b16 a[212:213], v221 offset:17408\n"
"buffer_load_dwordx4 v[168:171], v235, s[88:91], s45 offen offset:2304 sc1\n"
"v_mfma_f32_16x16x32_bf16 v[80:83], a[176:179], a[40:43], v[80:83]\n"
"ds_read_b64_tr_b16 a[214:215], v221 offset:19456\n"
"v_mfma_f32_16x16x32_bf16 v[112:115], a[144:147], a[36:39], v[112:115]\n"
"ds_read_b64_tr_b16 a[244:245], v221 offset:25600\n"
"ds_read_b64_tr_b16 a[246:247], v221 offset:27648\n"
"v_mfma_f32_16x16x32_bf16 v[112:115], a[176:179], a[44:47], v[112:115]\n"
"buffer_load_dwordx4 v[172:175], v235, s[88:91], s45 offen offset:2368 sc1\n"
"v_mfma_f32_16x16x32_bf16 v[84:87], a[148:151], a[32:35], v[84:87]\n"
"ds_read_b64_tr_b16 a[216:217], v222 offset:17408\n"
"ds_read_b64_tr_b16 a[218:219], v222 offset:19456\n"
"v_mfma_f32_16x16x32_bf16 v[84:87], a[180:183], a[40:43], v[84:87]\n"
"ds_read_b64_tr_b16 a[248:249], v222 offset:25600\n"
"ds_read_b64_tr_b16 a[250:251], v222 offset:27648\n"
"v_mfma_f32_16x16x32_bf16 v[116:119], a[148:151], a[36:39], v[116:119]\n"
"buffer_load_dwordx4 v[192:195], v235, s[88:91], s46 offen offset:0 sc1\n"
"v_mfma_f32_16x16x32_bf16 v[116:119], a[180:183], a[44:47], v[116:119]\n"
"ds_read_b64_tr_b16 a[220:221], v223 offset:17408\n"
"ds_read_b64_tr_b16 a[222:223], v223 offset:19456\n"
"v_mfma_f32_16x16x32_bf16 v[88:91], a[152:155], a[32:35], v[88:91]\n"
"ds_read_b64_tr_b16 a[252:253], v223 offset:25600\n"
"buffer_load_dwordx4 v[196:199], v235, s[88:91], s46 offen offset:64 sc1\n"
"v_mfma_f32_16x16x32_bf16 v[88:91], a[184:187], a[40:43], v[88:91]\n"
"ds_read_b64_tr_b16 a[254:255], v223 offset:27648\n"
"v_mfma_f32_16x16x32_bf16 v[120:123], a[152:155], a[36:39], v[120:123]\n"
"buffer_load_dwordx4 v[200:203], v235, s[88:91], s46 offen offset:256 sc1\n"
"v_mfma_f32_16x16x32_bf16 v[120:123], a[184:187], a[44:47], v[120:123]\n"
"v_mfma_f32_16x16x32_bf16 v[92:95], a[156:159], a[32:35], v[92:95]\n"
"v_mfma_f32_16x16x32_bf16 v[92:95], a[188:191], a[40:43], v[92:95]\n"
"buffer_load_dwordx4 v[204:207], v235, s[88:91], s46 offen offset:320 sc1\n"
"v_mfma_f32_16x16x32_bf16 v[124:127], a[156:159], a[36:39], v[124:127]\n"
"v_mfma_f32_16x16x32_bf16 v[124:127], a[188:191], a[44:47], v[124:127]\n"
"s_waitcnt vmcnt(29) lgkmcnt(0)\n"
"v_mfma_f32_16x16x32_bf16 v[64:67], a[192:195], a[48:51], v[64:67]\n"
"s_add_u32 m0, s54, 0\n"
"v_mfma_f32_16x16x32_bf16 v[64:67], a[224:227], a[56:59], v[64:67]\n"
"v_readfirstlane_b32 s41, v228\n"
"s_nop 0\n"
"s_and_b32 s41, s41, 0xff\n"
"v_mfma_f32_16x16x32_bf16 v[96:99], a[192:195], a[52:55], v[96:99]\n"
"s_lshr_b32 s94, s41, 4\n"
"v_mfma_f32_16x16x32_bf16 v[96:99], a[224:227], a[60:63], v[96:99]\n"
"s_sub_u32 s95, 15, s94\n"
"s_min_u32 s95, s95, s94\n"
"v_mfma_f32_16x16x32_bf16 v[68:71], a[196:199], a[48:51], v[68:71]\n"
"s_lshl_b32 s95, s95, 12\n"
"s_and_b32 s94, s41, 12\n"
"v_mfma_f32_16x16x32_bf16 v[68:71], a[228:231], a[56:59], v[68:71]\n"
"s_add_u32 s95, s95, s94\n"
"s_mov_b32 s94, 0x30000\n"
"v_mfma_f32_16x16x32_bf16 v[100:103], a[196:199], a[52:55], v[100:103]\n"
"s_cmp_lt_u32 s41, 128\n"
"s_cselect_b32 s97, 0x28000, s94\n"
"v_mfma_f32_16x16x32_bf16 v[100:103], a[228:231], a[60:63], v[100:103]\n"
"s_add_u32 s97, s97, s95\n"
"v_and_b32 v240, 31, v255\n"
"v_mfma_f32_16x16x32_bf16 v[72:75], a[200:203], a[48:51], v[72:75]\n"
"v_lshlrev_b32 v240, 7, v240\n"
"buffer_load_dword v241, v240, s[88:91], s97 offen sc1\n"
"v_mfma_f32_16x16x32_bf16 v[72:75], a[232:235], a[56:59], v[72:75]\n"
"s_mov_b32 s92, 917504\n"
"v_mfma_f32_16x16x32_bf16 v[104:107], a[200:203], a[52:55], v[104:107]\n"
"s_lshr_b32 s94, s41, 1\n"
"s_lshl_b32 s94, s94, 14\n"
"v_mfma_f32_16x16x32_bf16 v[104:107], a[232:235], a[60:63], v[104:107]\n"
"s_add_u32 s94, s94, s54\n"
"s_and_b32 s95, s41, 1\n"
"v_mfma_f32_16x16x32_bf16 v[76:79], a[204:207], a[48:51], v[76:79]\n"
"s_lshl_b32 s95, s95, 7\n"
"s_add_u32 s40, s94, s95\n"
"v_mfma_f32_16x16x32_bf16 v[76:79], a[236:239], a[56:59], v[76:79]\n"
"s_lshl_b32 s94, s94, 1\n"
"s_add_u32 s43, s94, s95\n"
"v_mfma_f32_16x16x32_bf16 v[108:111], a[204:207], a[52:55], v[108:111]\n"
"s_add_u32 s42, s40, 0x400000\n"
"s_add_u32 s16, s56, 0\n"
"s_addc_u32 s17, s57, 0\n"
"v_mfma_f32_16x16x32_bf16 v[108:111], a[236:239], a[60:63], v[108:111]\n"
"s_and_b32 s17, s17, 0xffff\n"
"v_mfma_f32_16x16x32_bf16 v[80:83], a[208:211], a[48:51], v[80:83]\n"
"s_mov_b32 s18, 0x200000\n"
"s_mov_b32 s19, 0x20000\n"
"v_mfma_f32_16x16x32_bf16 v[80:83], a[240:243], a[56:59], v[80:83]\n"
"s_lshl_b32 s47, s41, 13\n"
"s_lshl_b32 s95, s83, 8\n"
"v_mfma_f32_16x16x32_bf16 v[112:115], a[208:211], a[52:55], v[112:115]\n"
"s_add_u32 s47, s47, s95\n"
"buffer_load_dwordx4 v184, s[24:27], s92 offen offset:0 lds\n"
"v_mfma_f32_16x16x32_bf16 v[112:115], a[240:243], a[60:63], v[112:115]\n"
"buffer_load_dwordx4 v185, s[28:31], s92 offen offset:1024 lds\n"
"v_mfma_f32_16x16x32_bf16 v[84:87], a[212:215], a[48:51], v[84:87]\n"
"v_mfma_f32_16x16x32_bf16 v[84:87], a[244:247], a[56:59], v[84:87]\n"
"buffer_load_dwordx4 v186, s[24:27], s92 offen offset:2048 lds\n"
"v_mfma_f32_16x16x32_bf16 v[116:119], a[212:215], a[52:55], v[116:119]\n"
"v_mfma_f32_16x16x32_bf16 v[116:119], a[244:247], a[60:63], v[116:119]\n"
"v_mfma_f32_16x16x32_bf16 v[88:91], a[216:219], a[48:51], v[88:91]\n"
"v_mfma_f32_16x16x32_bf16 v[88:91], a[248:251], a[56:59], v[88:91]\n"
"v_mfma_f32_16x16x32_bf16 v[120:123], a[216:219], a[52:55], v[120:123]\n"
"buffer_load_dwordx4 v187, s[28:31], s92 offen offset:3072 lds\n"
"v_mfma_f32_16x16x32_bf16 v[120:123], a[248:251], a[60:63], v[120:123]\n"
"v_mfma_f32_16x16x32_bf16 v[92:95], a[220:223], a[48:51], v[92:95]\n"
"v_mfma_f32_16x16x32_bf16 v[92:95], a[252:255], a[56:59], v[92:95]\n"
"v_mfma_f32_16x16x32_bf16 v[124:127], a[220:223], a[52:55], v[124:127]\n"
"v_mfma_f32_16x16x32_bf16 v[124:127], a[252:255], a[60:63], v[124:127]\n"
"s_barrier\n"
"s_nop 7\n"
"v_cvt_pk_bf16_f32 v128, v64, v65\n"
"v_cvt_pk_bf16_f32 v129, v66, v67\n"
"v_cvt_pk_bf16_f32 v130, v68, v69\n"
"v_cvt_pk_bf16_f32 v131, v70, v71\n"
"buffer_store_dwordx4 v[128:131], v248, s[36:39], s85 offen offset:0 sc1\n"
"v_cvt_pk_bf16_f32 v132, v72, v73\n"
"v_cvt_pk_bf16_f32 v133, v74, v75\n"
"v_cvt_pk_bf16_f32 v134, v76, v77\n"
"v_cvt_pk_bf16_f32 v135, v78, v79\n"
"buffer_store_dwordx4 v[132:135], v248, s[36:39], s85 offen offset:1024 sc1\n"
"v_cvt_pk_bf16_f32 v136, v80, v81\n"
"v_cvt_pk_bf16_f32 v137, v82, v83\n"
"v_cvt_pk_bf16_f32 v138, v84, v85\n"
"v_cvt_pk_bf16_f32 v139, v86, v87\n"
"buffer_store_dwordx4 v[136:139], v248, s[36:39], s85 offen offset:2048 sc1\n"
"v_cvt_pk_bf16_f32 v140, v88, v89\n"
"v_cvt_pk_bf16_f32 v141, v90, v91\n"
"v_cvt_pk_bf16_f32 v142, v92, v93\n"
"v_cvt_pk_bf16_f32 v143, v94, v95\n"
"buffer_store_dwordx4 v[140:143], v248, s[36:39], s85 offen offset:3072 sc1\n"
"v_cvt_pk_bf16_f32 v128, v96, v97\n"
"v_cvt_pk_bf16_f32 v129, v98, v99\n"
"v_cvt_pk_bf16_f32 v130, v100, v101\n"
"v_cvt_pk_bf16_f32 v131, v102, v103\n"
"s_add_u32 s85, s85, 16384\n"
"buffer_store_dwordx4 v[128:131], v248, s[36:39], s85 offen offset:0 sc1\n"
"v_cvt_pk_bf16_f32 v132, v104, v105\n"
"v_cvt_pk_bf16_f32 v133, v106, v107\n"
"v_cvt_pk_bf16_f32 v134, v108, v109\n"
"v_cvt_pk_bf16_f32 v135, v110, v111\n"
"buffer_store_dwordx4 v[132:135], v248, s[36:39], s85 offen offset:1024 sc1\n"
"v_cvt_pk_bf16_f32 v136, v112, v113\n"
"v_cvt_pk_bf16_f32 v137, v114, v115\n"
"v_cvt_pk_bf16_f32 v138, v116, v117\n"
"v_cvt_pk_bf16_f32 v139, v118, v119\n"
"buffer_store_dwordx4 v[136:139], v248, s[36:39], s85 offen offset:2048 sc1\n"
"v_cvt_pk_bf16_f32 v140, v120, v121\n"
"v_cvt_pk_bf16_f32 v141, v122, v123\n"
"v_cvt_pk_bf16_f32 v142, v124, v125\n"
"v_cvt_pk_bf16_f32 v143, v126, v127\n"
"buffer_store_dwordx4 v[140:143], v248, s[36:39], s85 offen offset:3072 sc1\n"
"s_mov_b32 s48, 0\n"
"s_waitcnt vmcnt(8)\n"
"v_xor_b32 v228, s82, v145\n"
"v_bitop3_b32 v228, v147, s82, v228 bitop3:0xbe\n"
"v_bitop3_b32 v228, v149, s82, v228 bitop3:0xbe\n"
"v_bitop3_b32 v228, v151, s82, v228 bitop3:0xbe\n"
"v_bitop3_b32 v228, v153, s82, v228 bitop3:0xbe\n"
"v_bitop3_b32 v228, v155, s82, v228 bitop3:0xbe\n"
"v_bitop3_b32 v228, v157, s82, v228 bitop3:0xbe\n"
"v_bitop3_b32 v228, v159, s82, v228 bitop3:0xbe\n"
"v_bitop3_b32 v228, v161, s82, v228 bitop3:0xbe\n"
"v_bitop3_b32 v228, v163, s82, v228 bitop3:0xbe\n"
"v_bitop3_b32 v228, v165, s82, v228 bitop3:0xbe\n"
"v_bitop3_b32 v228, v167, s82, v228 bitop3:0xbe\n"
"v_bitop3_b32 v228, v169, s82, v228 bitop3:0xbe\n"
"v_bitop3_b32 v228, v171, s82, v228 bitop3:0xbe\n"
"v_bitop3_b32 v228, v173, s82, v228 bitop3:0xbe\n"
"v_bitop3_b32 v228, v175, s82, v228 bitop3:0xbe\n"
"v_bitop3_b32 v228, v193, s82, v228 bitop3:0xbe\n"
"v_bitop3_b32 v228, v195, s82, v228 bitop3:0xbe\n"
"v_bitop3_b32 v228, v197, s82, v228 bitop3:0xbe\n"
"v_bitop3_b32 v228, v199, s82, v228 bitop3:0xbe\n"
"v_bitop3_b32 v228, v201, s82, v228 bitop3:0xbe\n"
"v_bitop3_b32 v228, v203, s82, v228 bitop3:0xbe\n"
"v_bitop3_b32 v228, v205, s82, v228 bitop3:0xbe\n"
"v_bitop3_b32 v228, v207, s82, v228 bitop3:0xbe\n"
"v_cmp_ne_u32 vcc, 0, v228\n"
"s_cbranch_vccnz Lgs81_%=\n"
"Lgb80_%=:\n"
"v_and_b32 v145, 0xffff0000, v144\n"
"v_lshlrev_b32 v144, 16, v144\n"
"v_and_b32 v147, 0xffff0000, v146\n"
"v_lshlrev_b32 v146, 16, v146\n"
"v_and_b32 v149, 0xffff0000, v148\n"
"v_lshlrev_b32 v148, 16, v148\n"
"v_and_b32 v151, 0xffff0000, v150\n"
"v_lshlrev_b32 v150, 16, v150\n"
"v_and_b32 v153, 0xffff0000, v152\n"
"v_lshlrev_b32 v152, 16, v152\n"
"v_and_b32 v155, 0xffff0000, v154\n"
"v_lshlrev_b32 v154, 16, v154\n"
"v_and_b32 v157, 0xffff0000, v156\n"
"v_lshlrev_b32 v156, 16, v156\n"
"v_and_b32 v159, 0xffff0000, v158\n"
"v_lshlrev_b32 v158, 16, v158\n"
"v_and_b32 v161, 0xffff0000, v160\n"
"v_lshlrev_b32 v160, 16, v160\n"
"v_and_b32 v163, 0xffff0000, v162\n"
"v_lshlrev_b32 v162, 16, v162\n"
"v_and_b32 v165, 0xffff0000, v164\n"
"v_lshlrev_b32 v164, 16, v164\n"
"v_and_b32 v167, 0xffff0000, v166\n"
"v_lshlrev_b32 v166, 16, v166\n"
"v_and_b32 v169, 0xffff0000, v168\n"
"v_lshlrev_b32 v168, 16, v168\n"
"v_and_b32 v171, 0xffff0000, v170\n"
"v_lshlrev_b32 v170, 16, v170\n"
"v_and_b32 v173, 0xffff0000, v172\n"
"v_lshlrev_b32 v172, 16, v172\n"
"v_and_b32 v175, 0xffff0000, v174\n"
"v_lshlrev_b32 v174, 16, v174\n"
"v_and_b32 v193, 0xffff0000, v192\n"
"v_lshlrev_b32 v192, 16, v192\n"
"v_and_b32 v195, 0xffff0000, v194\n"
"v_lshlrev_b32 v194, 16, v194\n"
"v_and_b32 v197, 0xffff0000, v196\n"
"v_lshlrev_b32 v196, 16, v196\n"
"v_and_b32 v199, 0xffff0000, v198\n"
"v_lshlrev_b32 v198, 16, v198\n"
"v_and_b32 v201, 0xffff0000, v200\n"
"v_lshlrev_b32 v200, 16, v200\n"
"v_and_b32 v203, 0xffff0000, v202\n"
"v_lshlrev_b32 v202, 16, v202\n"
"v_and_b32 v205, 0xffff0000, v204\n"
"v_lshlrev_b32 v204, 16, v204\n"
"v_and_b32 v207, 0xffff0000, v206\n"
"v_lshlrev_b32 v206, 16, v206\n"
"s_and_b32 s50, s84, 1\n"
"s_lshl_b32 s50, s50, 2\n"
"s_bfe_u32 s92, s84, 0x20003\n"
"s_add_u32 s50, s50, s92\n"
"s_lshl_b32 s92, s50, 3\n"
"s_lshl_b32 s93, s83, 1\n"
"s_add_u32 s51, s92, s93\n"
"s_lshl_b32 s45, s51, 14\n"
"s_add_u32 s46, s45, 0x4000\n"
"s_mul_i32 s49, s51, 57344\n"
"s_lshl_b32 s92, s52, 11\n"
"s_add_u32 s49, s49, s92\n"
"s_add_u32 s85, s49, 3670016\n"
"s_add_u32 s20, s72, 0\n"
"s_addc_u32 s21, s73, 0\n"
"s_and_b32 s21, s21, 0xffff\n"
"s_mov_b32 s22, 0x1c0000\n"
"s_mov_b32 s23, 0x20000\n"
"s_mul_i32 s55, s83, 458752\n"
"s_bfe_u32 s92, s84, 0x30003\n"
"s_lshl_b32 s92, s92, 7\n"
"s_add_u32 s55, s55, s92\n"
"s_lshl_b32 s53, s83, 13\n"
"v_add_u32 v188, s55, v250\n"
"v_add_u32 v189, 7168, v188\n"
"v_add_u32 v190, 14336, v188\n"
"v_add_u32 v191, 21504, v188\n"
"v_lshrrev_b32 v253, 5, v255\n"
"v_lshlrev_b32 v242, 11, v253\n"
"v_bfe_u32 v253, v255, 2, 3\n"
"v_lshl_add_u32 v242, v253, 7, v242\n"
"v_bfe_u32 v253, v255, 3, 2\n"
"v_lshlrev_b32 v253, 1, v253\n"
"v_bfe_u32 v252, v255, 1, 1\n"
"v_xor_b32 v253, v253, v252\n"
"v_lshl_add_u32 v242, v253, 4, v242\n"
"v_and_b32 v252, 1, v255\n"
"v_lshl_add_u32 v242, v252, 3, v242\n"
"v_add_u32 v242, 0, v242\n"
"v_xor_b32 v243, 32, v242\n"
"v_xor_b32 v244, 64, v242\n"
"v_xor_b32 v245, 96, v242\n"
"v_add_u32 v216, 0x0, v242\n"
"v_add_u32 v217, 0x0, v243\n"
"v_add_u32 v218, 0x0, v244\n"
"v_add_u32 v219, 0x0, v245\n"
"v_add_u32 v220, 0x10000, v242\n"
"v_add_u32 v221, 0x10000, v243\n"
"v_add_u32 v222, 0x10000, v244\n"
"v_add_u32 v223, 0x10000, v245\n"
"v_add_u32 v224, 0x20000, v242\n"
"v_add_u32 v225, 0x20000, v243\n"
"v_add_u32 v226, 0x20000, v244\n"
"v_add_u32 v227, 0x20000, v245\n"
"v_mov_b32 v229, 0x3f80\n"
"v_mov_b32 v230, 0x3f800000\n"
"s_waitcnt vmcnt(0)\n"
"s_add_u32 s95, s87, 0x18000\n"
"v_mov_b32 v253, s82\n"
"s_mov_b64 exec, 1\n"
"buffer_store_dword v253, off, s[88:91], s95 sc1\n"
"s_mov_b64 exec, -1\n"
"s_waitcnt vmcnt(0)\n"
"v_cmp_ne_u32 vcc, s82, v241\n"
"s_cbranch_vccnz Lps77_%=\n"
"Lpb76_%=:\n"
"buffer_load_dwordx2 v[64:65], v239, s[16:19], s47 offen offset:0\n"
"buffer_load_dwordx2 v[66:67], v239, s[16:19], s47 offen offset:32\n"
"buffer_load_dwordx2 v[68:69], v239, s[16:19], s47 offen offset:128\n"
"buffer_load_dwordx2 v[70:71], v239, s[16:19], s47 offen offset:160\n"
"s_add_u32 s92, s40, 0\n"
"buffer_load_dwordx4 v[0:3], v234, s[36:39], s92 offen offset:0 sc1\n"
"buffer_load_dwordx4 v[32:35], v234, s[36:39], s92 offen offset:2048 sc1\n"
"s_add_u32 s92, s40, 2097152\n"
"buffer_load_dwordx4 v[4:7], v234, s[36:39], s92 offen offset:0 sc1\n"
"buffer_load_dwordx4 v[36:39], v234, s[36:39], s92 offen offset:2048 sc1\n"
"s_add_u32 s92, s40, 4194304\n"
"buffer_load_dwordx4 v[8:11], v234, s[36:39], s92 offen offset:0 sc1\n"
"buffer_load_dwordx4 v[40:43], v234, s[36:39], s92 offen offset:2048 sc1\n"
"s_add_u32 s92, s40, 6291456\n"
"buffer_load_dwordx4 v[12:15], v234, s[36:39], s92 offen offset:0 sc1\n"
"buffer_load_dwordx4 v[44:47], v234, s[36:39], s92 offen offset:2048 sc1\n"
"s_add_u32 s92, s40, 8388608\n"
"buffer_load_dwordx4 v[16:19], v234, s[36:39], s92 offen offset:0 sc1\n"
"buffer_load_dwordx4 v[48:51], v234, s[36:39], s92 offen offset:2048 sc1\n"
"s_add_u32 s92, s40, 10485760\n"
"buffer_load_dwordx4 v[20:23], v234, s[36:39], s92 offen offset:0 sc1\n"
"buffer_load_dwordx4 v[52:55], v234, s[36:39], s92 offen offset:2048 sc1\n"
"s_add_u32 s92, s40, 12582912\n"
"buffer_load_dwordx4 v[24:27], v234, s[36:39], s92 offen offset:0 sc1\n"
"buffer_load_dwordx4 v[56:59], v234, s[36:39], s92 offen offset:2048 sc1\n"
"s_add_u32 s92, s40, 14680064\n"
"buffer_load_dwordx4 v[28:31], v234, s[36:39], s92 offen offset:0 sc1\n"
"buffer_load_dwordx4 v[60:63], v234, s[36:39], s92 offen offset:2048 sc1\n"
"s_mov_b32 s40, s48\n"
"s_lshl_b32 s92, s50, 11\n"
"s_lshl_b32 s93, s83, 9\n"
"s_add_u32 s92, s92, s93\n"
"s_add_u32 s97, s92, 0x38000\n"
"v_bfe_u32 v240, v255, 2, 2\n"
"v_lshlrev_b32 v240, 7, v240\n"
"v_and_b32 v253, 3, v255\n"
"v_lshl_add_u32 v240, v253, 2, v240\n"
"s_add_u32 s47, s45, 0x100000\n"
"s_add_u32 s48, s46, 0x100000\n"
"s_add_u32 s16, s80, 0x3680000\n"
"s_addc_u32 s17, s81, 0\n"
"s_and_b32 s17, s17, 0xffff\n"
"s_mov_b32 s18, 0x200000\n"
"s_mov_b32 s19, 0x20000\n"
"s_cmp_eq_u32 s26, 0\n"
"s_cselect_b32 s18, 0, s18\n"
"s_add_u32 s36, s80, 0x3280000\n"
"s_addc_u32 s37, s81, 0\n"
"s_and_b32 s37, s37, 0xffff\n"
"s_mov_b32 s38, 0x600000\n"
"s_mov_b32 s39, 0x20000\n"
"s_waitcnt vmcnt(14)\n"
"v_lshlrev_b32 v80, 16, v64\n"
"v_and_b32 v81, 0xffff0000, v64\n"
"v_lshlrev_b32 v82, 16, v65\n"
"v_and_b32 v83, 0xffff0000, v65\n"
"v_lshlrev_b32 v84, 16, v66\n"
"v_and_b32 v85, 0xffff0000, v66\n"
"v_lshlrev_b32 v86, 16, v67\n"
"v_and_b32 v87, 0xffff0000, v67\n"
"v_lshlrev_b32 v88, 16, v68\n"
"v_and_b32 v89, 0xffff0000, v68\n"
"v_lshlrev_b32 v90, 16, v69\n"
"v_and_b32 v91, 0xffff0000, v69\n"
"v_lshlrev_b32 v92, 16, v70\n"
"v_and_b32 v93, 0xffff0000, v70\n"
"v_lshlrev_b32 v94, 16, v71\n"
"v_and_b32 v95, 0xffff0000, v71\n"
"v_lshlrev_b32 v112, 16, v0\n"
"v_and_b32 v113, 0xffff0000, v0\n"
"v_lshlrev_b32 v114, 16, v1\n"
"v_and_b32 v115, 0xffff0000, v1\n"
"v_lshlrev_b32 v116, 16, v2\n"
"v_and_b32 v117, 0xffff0000, v2\n"
"v_lshlrev_b32 v118, 16, v3\n"
"v_and_b32 v119, 0xffff0000, v3\n"
"v_lshlrev_b32 v120, 16, v32\n"
"v_and_b32 v121, 0xffff0000, v32\n"
"v_lshlrev_b32 v122, 16, v33\n"
"v_and_b32 v123, 0xffff0000, v33\n"
"v_lshlrev_b32 v124, 16, v34\n"
"v_and_b32 v125, 0xffff0000, v34\n"
"v_lshlrev_b32 v126, 16, v35\n"
"v_and_b32 v127, 0xffff0000, v35\n"
"s_waitcnt vmcnt(12)\n"
"v_dot2_f32_bf16 v112, v4, v229, v112\n"
"v_dot2_f32_bf16 v113, v4, v230, v113\n"
"v_dot2_f32_bf16 v114, v5, v229, v114\n"
"v_dot2_f32_bf16 v115, v5, v230, v115\n"
"v_dot2_f32_bf16 v116, v6, v229, v116\n"
"v_dot2_f32_bf16 v117, v6, v230, v117\n"
"v_dot2_f32_bf16 v118, v7, v229, v118\n"
"v_dot2_f32_bf16 v119, v7, v230, v119\n"
"v_dot2_f32_bf16 v120, v36, v229, v120\n"
"v_dot2_f32_bf16 v121, v36, v230, v121\n"
"v_dot2_f32_bf16 v122, v37, v229, v122\n"
"v_dot2_f32_bf16 v123, v37, v230, v123\n"
"v_dot2_f32_bf16 v124, v38, v229, v124\n"
"v_dot2_f32_bf16 v125, v38, v230, v125\n"
"v_dot2_f32_bf16 v126, v39, v229, v126\n"
"v_dot2_f32_bf16 v127, v39, v230, v127\n"
"s_waitcnt vmcnt(10)\n"
"v_dot2_f32_bf16 v112, v8, v229, v112\n"
"v_dot2_f32_bf16 v113, v8, v230, v113\n"
"v_dot2_f32_bf16 v114, v9, v229, v114\n"
"v_dot2_f32_bf16 v115, v9, v230, v115\n"
"v_dot2_f32_bf16 v116, v10, v229, v116\n"
"v_dot2_f32_bf16 v117, v10, v230, v117\n"
"v_dot2_f32_bf16 v118, v11, v229, v118\n"
"v_dot2_f32_bf16 v119, v11, v230, v119\n"
"v_dot2_f32_bf16 v120, v40, v229, v120\n"
"v_dot2_f32_bf16 v121, v40, v230, v121\n"
"v_dot2_f32_bf16 v122, v41, v229, v122\n"
"v_dot2_f32_bf16 v123, v41, v230, v123\n"
"v_dot2_f32_bf16 v124, v42, v229, v124\n"
"v_dot2_f32_bf16 v125, v42, v230, v125\n"
"v_dot2_f32_bf16 v126, v43, v229, v126\n"
"v_dot2_f32_bf16 v127, v43, v230, v127\n"
"s_waitcnt vmcnt(8)\n"
"v_dot2_f32_bf16 v112, v12, v229, v112\n"
"v_dot2_f32_bf16 v113, v12, v230, v113\n"
"v_dot2_f32_bf16 v114, v13, v229, v114\n"
"v_dot2_f32_bf16 v115, v13, v230, v115\n"
"v_dot2_f32_bf16 v116, v14, v229, v116\n"
"v_dot2_f32_bf16 v117, v14, v230, v117\n"
"v_dot2_f32_bf16 v118, v15, v229, v118\n"
"v_dot2_f32_bf16 v119, v15, v230, v119\n"
"v_dot2_f32_bf16 v120, v44, v229, v120\n"
"v_dot2_f32_bf16 v121, v44, v230, v121\n"
"v_dot2_f32_bf16 v122, v45, v229, v122\n"
"v_dot2_f32_bf16 v123, v45, v230, v123\n"
"v_dot2_f32_bf16 v124, v46, v229, v124\n"
"v_dot2_f32_bf16 v125, v46, v230, v125\n"
"v_dot2_f32_bf16 v126, v47, v229, v126\n"
"v_dot2_f32_bf16 v127, v47, v230, v127\n"
"s_waitcnt vmcnt(6)\n"
"v_dot2_f32_bf16 v112, v16, v229, v112\n"
"v_dot2_f32_bf16 v113, v16, v230, v113\n"
"v_dot2_f32_bf16 v114, v17, v229, v114\n"
"v_dot2_f32_bf16 v115, v17, v230, v115\n"
"v_dot2_f32_bf16 v116, v18, v229, v116\n"
"v_dot2_f32_bf16 v117, v18, v230, v117\n"
"v_dot2_f32_bf16 v118, v19, v229, v118\n"
"v_dot2_f32_bf16 v119, v19, v230, v119\n"
"v_dot2_f32_bf16 v120, v48, v229, v120\n"
"v_dot2_f32_bf16 v121, v48, v230, v121\n"
"v_dot2_f32_bf16 v122, v49, v229, v122\n"
"v_dot2_f32_bf16 v123, v49, v230, v123\n"
"v_dot2_f32_bf16 v124, v50, v229, v124\n"
"v_dot2_f32_bf16 v125, v50, v230, v125\n"
"v_dot2_f32_bf16 v126, v51, v229, v126\n"
"v_dot2_f32_bf16 v127, v51, v230, v127\n"
"s_waitcnt vmcnt(4)\n"
"v_dot2_f32_bf16 v112, v20, v229, v112\n"
"v_dot2_f32_bf16 v113, v20, v230, v113\n"
"v_dot2_f32_bf16 v114, v21, v229, v114\n"
"v_dot2_f32_bf16 v115, v21, v230, v115\n"
"v_dot2_f32_bf16 v116, v22, v229, v116\n"
"v_dot2_f32_bf16 v117, v22, v230, v117\n"
"v_dot2_f32_bf16 v118, v23, v229, v118\n"
"v_dot2_f32_bf16 v119, v23, v230, v119\n"
"v_dot2_f32_bf16 v120, v52, v229, v120\n"
"v_dot2_f32_bf16 v121, v52, v230, v121\n"
"v_dot2_f32_bf16 v122, v53, v229, v122\n"
"v_dot2_f32_bf16 v123, v53, v230, v123\n"
"v_dot2_f32_bf16 v124, v54, v229, v124\n"
"v_dot2_f32_bf16 v125, v54, v230, v125\n"
"v_dot2_f32_bf16 v126, v55, v229, v126\n"
"v_dot2_f32_bf16 v127, v55, v230, v127\n"
"s_waitcnt vmcnt(2)\n"
"v_dot2_f32_bf16 v112, v24, v229, v112\n"
"v_dot2_f32_bf16 v113, v24, v230, v113\n"
"v_dot2_f32_bf16 v114, v25, v229, v114\n"
"v_dot2_f32_bf16 v115, v25, v230, v115\n"
"v_dot2_f32_bf16 v116, v26, v229, v116\n"
"v_dot2_f32_bf16 v117, v26, v230, v117\n"
"v_dot2_f32_bf16 v118, v27, v229, v118\n"
"v_dot2_f32_bf16 v119, v27, v230, v119\n"
"v_dot2_f32_bf16 v120, v56, v229, v120\n"
"v_dot2_f32_bf16 v121, v56, v230, v121\n"
"v_dot2_f32_bf16 v122, v57, v229, v122\n"
"v_dot2_f32_bf16 v123, v57, v230, v123\n"
"v_dot2_f32_bf16 v124, v58, v229, v124\n"
"v_dot2_f32_bf16 v125, v58, v230, v125\n"
"v_dot2_f32_bf16 v126, v59, v229, v126\n"
"v_dot2_f32_bf16 v127, v59, v230, v127\n"
"s_waitcnt vmcnt(0)\n"
"v_dot2_f32_bf16 v112, v28, v229, v112\n"
"v_dot2_f32_bf16 v113, v28, v230, v113\n"
"v_dot2_f32_bf16 v114, v29, v229, v114\n"
"v_dot2_f32_bf16 v115, v29, v230, v115\n"
"v_dot2_f32_bf16 v116, v30, v229, v116\n"
"v_dot2_f32_bf16 v117, v30, v230, v117\n"
"v_dot2_f32_bf16 v118, v31, v229, v118\n"
"v_dot2_f32_bf16 v119, v31, v230, v119\n"
"v_dot2_f32_bf16 v120, v60, v229, v120\n"
"v_dot2_f32_bf16 v121, v60, v230, v121\n"
"v_dot2_f32_bf16 v122, v61, v229, v122\n"
"v_dot2_f32_bf16 v123, v61, v230, v123\n"
"v_dot2_f32_bf16 v124, v62, v229, v124\n"
"v_dot2_f32_bf16 v125, v62, v230, v125\n"
"v_dot2_f32_bf16 v126, v63, v229, v126\n"
"v_dot2_f32_bf16 v127, v63, v230, v127\n"
"s_nop 2\n"
"v_fmac_f32 v80, v144, v112\n"
"v_fmac_f32 v81, v145, v113\n"
"v_fmac_f32 v82, v146, v114\n"
"v_fmac_f32 v83, v147, v115\n"
"v_fmac_f32 v84, v148, v116\n"
"v_fmac_f32 v85, v149, v117\n"
"v_fmac_f32 v86, v150, v118\n"
"v_fmac_f32 v87, v151, v119\n"
"v_fmac_f32 v88, v152, v120\n"
"v_fmac_f32 v89, v153, v121\n"
"v_fmac_f32 v90, v154, v122\n"
"v_fmac_f32 v91, v155, v123\n"
"v_fmac_f32 v92, v156, v124\n"
"v_fmac_f32 v93, v157, v125\n"
"v_fmac_f32 v94, v158, v126\n"
"v_fmac_f32 v95, v159, v127\n"
"buffer_load_dword v241, v240, s[88:91], s97 offen sc1\n"
"v_add_f32 v176, v80, v81\n"
"v_add_f32 v178, v82, v83\n"
"v_add_f32 v176, v176, v84\n"
"v_add_f32 v178, v178, v85\n"
"v_add_f32 v176, v176, v86\n"
"v_add_f32 v178, v178, v87\n"
"v_add_f32 v176, v176, v88\n"
"v_add_f32 v178, v178, v89\n"
"v_add_f32 v176, v176, v90\n"
"v_add_f32 v178, v178, v91\n"
"v_add_f32 v176, v176, v92\n"
"v_add_f32 v178, v178, v93\n"
"v_add_f32 v176, v176, v94\n"
"v_add_f32 v178, v178, v95\n"
"v_add_f32 v176, v176, v178\n"
"s_nop 1\n"
"v_add_f32_dpp v176, v176, v176 row_ror:8 row_mask:0xf bank_mask:0xf\n"
"v_mov_b32 v178, v176\n"
"s_nop 1\n"
"v_permlane16_swap_b32 v176, v178\n"
"v_add_f32 v176, v176, v178\n"
"v_mov_b32 v178, v176\n"
"s_nop 1\n"
"v_permlane32_swap_b32 v176, v178\n"
"v_add_f32 v176, v176, v178\n"
"v_mul_f32 v176, 0x3c000000, v176\n"
"v_sub_f32 v96, v80, v176\n"
"v_sub_f32 v97, v81, v176\n"
"v_sub_f32 v98, v82, v176\n"
"v_sub_f32 v99, v83, v176\n"
"v_sub_f32 v100, v84, v176\n"
"v_sub_f32 v101, v85, v176\n"
"v_sub_f32 v102, v86, v176\n"
"v_sub_f32 v103, v87, v176\n"
"v_sub_f32 v104, v88, v176\n"
"v_sub_f32 v105, v89, v176\n"
"v_sub_f32 v106, v90, v176\n"
"v_sub_f32 v107, v91, v176\n"
"v_sub_f32 v108, v92, v176\n"
"v_sub_f32 v109, v93, v176\n"
"v_sub_f32 v110, v94, v176\n"
"v_sub_f32 v111, v95, v176\n"
"v_mul_f32 v177, v96, v96\n"
"v_mul_f32 v178, v97, v97\n"
"v_fmac_f32 v177, v98, v98\n"
"v_fmac_f32 v178, v99, v99\n"
"v_fmac_f32 v177, v100, v100\n"
"v_fmac_f32 v178, v101, v101\n"
"v_fmac_f32 v177, v102, v102\n"
"v_fmac_f32 v178, v103, v103\n"
"v_fmac_f32 v177, v104, v104\n"
"v_fmac_f32 v178, v105, v105\n"
"v_fmac_f32 v177, v106, v106\n"
"v_fmac_f32 v178, v107, v107\n"
"v_fmac_f32 v177, v108, v108\n"
"v_fmac_f32 v178, v109, v109\n"
"v_fmac_f32 v177, v110, v110\n"
"v_fmac_f32 v178, v111, v111\n"
"v_add_f32 v177, v177, v178\n"
"s_nop 1\n"
"v_add_f32_dpp v177, v177, v177 row_ror:8 row_mask:0xf bank_mask:0xf\n"
"v_mov_b32 v178, v177\n"
"s_nop 1\n"
"v_permlane16_swap_b32 v177, v178\n"
"v_add_f32 v177, v177, v178\n"
"v_mov_b32 v178, v177\n"
"s_nop 1\n"
"v_permlane32_swap_b32 v177, v178\n"
"v_add_f32 v177, v177, v178\n"
"ds_write_b64 v231, v[176:177]\n"
"buffer_store_dwordx4 v[80:83], v233, s[36:39], s43 offen offset:0 sc1\n"
"buffer_store_dwordx4 v[84:87], v233, s[36:39], s43 offen offset:1024 sc1\n"
"s_add_u32 s93, s43, 4096\n"
"buffer_store_dwordx4 v[88:91], v233, s[36:39], s93 offen offset:0 sc1\n"
"buffer_store_dwordx4 v[92:95], v233, s[36:39], s93 offen offset:1024 sc1\n"
"s_waitcnt lgkmcnt(0)\n"
"s_barrier\n"
"ds_read_b128 v[64:67], v232\n"
"ds_read_b128 v[68:71], v232 offset:16\n"
"s_waitcnt lgkmcnt(0)\n"
"v_add_f32 v177, v64, v66\n"
"v_add_f32 v179, v68, v70\n"
"v_add_f32 v177, v177, v179\n"
"v_mul_f32 v177, 0x3e800000, v177\n"
"v_sub_f32 v64, v64, v177\n"
"v_sub_f32 v66, v66, v177\n"
"v_sub_f32 v68, v68, v177\n"
"v_sub_f32 v70, v70, v177\n"
"v_add_f32 v65, v65, v67\n"
"v_add_f32 v69, v69, v71\n"
"v_mul_f32 v67, v64, v64\n"
"v_fmac_f32 v67, v66, v66\n"
"v_fmac_f32 v67, v68, v68\n"
"v_fmac_f32 v67, v70, v70\n"
"v_add_f32 v65, v65, v69\n"
"v_fmamk_f32 v65, v67, 0x43000000, v65\n"
"v_mov_b32 v67, 0x3727c5ac\n"
"v_fmamk_f32 v65, v65, 0x3b000000, v67\n"
"v_rsq_f32 v178, v65\n"
"v_sub_f32 v179, v176, v177\n"
"s_nop 0\n"
"v_mul_f32 v179, v179, v178\n"
"v_fma_f32 v96, v96, v178, v179\n"
"v_fma_f32 v97, v97, v178, v179\n"
"v_fma_f32 v98, v98, v178, v179\n"
"v_fma_f32 v99, v99, v178, v179\n"
"v_fma_f32 v100, v100, v178, v179\n"
"v_fma_f32 v101, v101, v178, v179\n"
"v_fma_f32 v102, v102, v178, v179\n"
"v_fma_f32 v103, v103, v178, v179\n"
"v_fma_f32 v104, v104, v178, v179\n"
"v_fma_f32 v105, v105, v178, v179\n"
"v_fma_f32 v106, v106, v178, v179\n"
"v_fma_f32 v107, v107, v178, v179\n"
"v_fma_f32 v108, v108, v178, v179\n"
"v_fma_f32 v109, v109, v178, v179\n"
"v_fma_f32 v110, v110, v178, v179\n"
"v_fma_f32 v111, v111, v178, v179\n"
"v_fma_f32 v96, v96, v192, v160\n"
"v_fma_f32 v97, v97, v193, v161\n"
"v_fma_f32 v98, v98, v194, v162\n"
"v_fma_f32 v99, v99, v195, v163\n"
"v_fma_f32 v100, v100, v196, v164\n"
"v_fma_f32 v101, v101, v197, v165\n"
"v_fma_f32 v102, v102, v198, v166\n"
"v_fma_f32 v103, v103, v199, v167\n"
"v_fma_f32 v104, v104, v200, v168\n"
"v_fma_f32 v105, v105, v201, v169\n"
"v_fma_f32 v106, v106, v202, v170\n"
"v_fma_f32 v107, v107, v203, v171\n"
"v_fma_f32 v108, v108, v204, v172\n"
"v_fma_f32 v109, v109, v205, v173\n"
"v_fma_f32 v110, v110, v206, v174\n"
"v_fma_f32 v111, v111, v207, v175\n"
"v_cvt_pk_bf16_f32 v72, v96, v97\n"
"v_cvt_pk_bf16_f32 v73, v98, v99\n"
"v_cvt_pk_bf16_f32 v74, v100, v101\n"
"v_cvt_pk_bf16_f32 v75, v102, v103\n"
"v_cvt_pk_bf16_f32 v76, v104, v105\n"
"v_cvt_pk_bf16_f32 v77, v106, v107\n"
"v_cvt_pk_bf16_f32 v78, v108, v109\n"
"v_cvt_pk_bf16_f32 v79, v110, v111\n"
"s_waitcnt vmcnt(4)\n"
"v_cmp_ne_u32 vcc, s82, v241\n"
"s_cbranch_vccnz Lps87_%=\n"
"buffer_load_dwordx4 a[64:67], v248, s[16:19], s45 offen offset:0 sc1\n"
"buffer_load_dwordx4 a[72:75], v248, s[16:19], s45 offen offset:1024 sc1\n"
"buffer_load_dwordx4 a[68:71], v248, s[16:19], s46 offen offset:0 sc1\n"
"buffer_load_dwordx4 a[76:79], v248, s[16:19], s46 offen offset:1024 sc1\n"
"buffer_load_dwordx4 a[80:83], v248, s[16:19], s45 offen offset:2048 sc1\n"
"buffer_load_dwordx4 a[88:91], v248, s[16:19], s45 offen offset:3072 sc1\n"
"buffer_load_dwordx4 a[84:87], v248, s[16:19], s46 offen offset:2048 sc1\n"
"buffer_load_dwordx4 a[92:95], v248, s[16:19], s46 offen offset:3072 sc1\n"
"s_add_u32 s45, s45, 4096\n"
"buffer_load_dwordx4 a[96:99], v248, s[16:19], s45 offen offset:0 sc1\n"
"buffer_load_dwordx4 a[104:107], v248, s[16:19], s45 offen offset:1024 sc1\n"
"s_add_u32 s46, s46, 4096\n"
"buffer_load_dwordx4 a[100:103], v248, s[16:19], s46 offen offset:0 sc1\n"
"buffer_load_dwordx4 a[108:111], v248, s[16:19], s46 offen offset:1024 sc1\n"
"buffer_store_dwordx4 v[72:75], v234, s[36:39], s42 offen offset:0 sc1\n"
"buffer_store_dwordx4 v[76:79], v234, s[36:39], s42 offen offset:2048 sc1\n"
"Lpb86_%=:\n"
"s_cmp_gt_u32 s40, 0\n"
"s_cbranch_scc1 Lxs90_%=\n"
"s_add_u32 m0, s54, 16384\n"
"s_mov_b32 s92, 1146880\n"
"buffer_load_dwordx4 v184, s[24:27], s92 offen offset:0 lds\n"
"buffer_load_dwordx4 v185, s[28:31], s92 offen offset:1024 lds\n"
"buffer_load_dwordx4 v186, s[24:27], s92 offen offset:2048 lds\n"
"buffer_load_dwordx4 v187, s[28:31], s92 offen offset:3072 lds\n"
"Lxs90_%=:\n"
"s_cmp_gt_u32 s40, 1\n"
"s_cbranch_scc1 Lxs91_%=\n"
"s_add_u32 m0, s54, 32768\n"
"s_mov_b32 s92, 1376256\n"
"buffer_load_dwordx4 v184, s[24:27], s92 offen offset:0 lds\n"
"buffer_load_dwordx4 v185, s[28:31], s92 offen offset:1024 lds\n"
"buffer_load_dwordx4 v186, s[24:27], s92 offen offset:2048 lds\n"
"buffer_load_dwordx4 v187, s[28:31], s92 offen offset:3072 lds\n"
"Lxs91_%=:\n"
"s_cmp_gt_u32 s40, 2\n"
"s_cbranch_scc1 Lxs92_%=\n"
"s_add_u32 m0, s54, 49152\n"
"s_mov_b32 s92, 1605632\n"
"buffer_load_dwordx4 v184, s[24:27], s92 offen offset:0 lds\n"
"buffer_load_dwordx4 v185, s[28:31], s92 offen offset:1024 lds\n"
"buffer_load_dwordx4 v186, s[24:27], s92 offen offset:2048 lds\n"
"buffer_load_dwordx4 v187, s[28:31], s92 offen offset:3072 lds\n"
"Lxs92_%=:\n"
"s_mov_b32 s40, 3\n"
"ds_read_b64_tr_b16 a[128:129], v220 offset:32768\n"
"ds_read_b64_tr_b16 a[130:131], v220 offset:36864\n"
"ds_read_b64_tr_b16 a[160:161], v220 offset:40960\n"
"ds_read_b64_tr_b16 a[162:163], v220 offset:45056\n"
"ds_read_b64_tr_b16 a[132:133], v221 offset:32768\n"
"ds_read_b64_tr_b16 a[134:135], v221 offset:36864\n"
"ds_read_b64_tr_b16 a[164:165], v221 offset:40960\n"
"ds_read_b64_tr_b16 a[166:167], v221 offset:45056\n"
"ds_read_b64_tr_b16 a[136:137], v222 offset:32768\n"
"ds_read_b64_tr_b16 a[138:139], v222 offset:36864\n"
"ds_read_b64_tr_b16 a[168:169], v222 offset:40960\n"
"ds_read_b64_tr_b16 a[170:171], v222 offset:45056\n"
"ds_read_b64_tr_b16 a[140:141], v223 offset:32768\n"
"ds_read_b64_tr_b16 a[142:143], v223 offset:36864\n"
"ds_read_b64_tr_b16 a[172:173], v223 offset:40960\n"
"ds_read_b64_tr_b16 a[174:175], v223 offset:45056\n"
"ds_read_b64_tr_b16 a[144:145], v220 offset:33792\n"
"ds_read_b64_tr_b16 a[146:147], v220 offset:37888\n"
"ds_read_b64_tr_b16 a[176:177], v220 offset:41984\n"
"ds_read_b64_tr_b16 a[178:179], v220 offset:46080\n"
"ds_read_b64_tr_b16 a[148:149], v221 offset:33792\n"
"ds_read_b64_tr_b16 a[150:151], v221 offset:37888\n"
"ds_read_b64_tr_b16 a[180:181], v221 offset:41984\n"
"ds_read_b64_tr_b16 a[182:183], v221 offset:46080\n"
"ds_read_b64_tr_b16 a[152:153], v222 offset:33792\n"
"ds_read_b64_tr_b16 a[154:155], v222 offset:37888\n"
"ds_read_b64_tr_b16 a[184:185], v222 offset:41984\n"
"ds_read_b64_tr_b16 a[186:187], v222 offset:46080\n"
"ds_read_b64_tr_b16 a[156:157], v223 offset:33792\n"
"ds_read_b64_tr_b16 a[158:159], v223 offset:37888\n"
"ds_read_b64_tr_b16 a[188:189], v223 offset:41984\n"
"ds_read_b64_tr_b16 a[190:191], v223 offset:46080\n"
"s_waitcnt vmcnt(8) lgkmcnt(0)\n"
"v_mfma_f32_16x16x32_bf16 v[0:3], a[128:131], a[64:67], 0\n"
"ds_read_b64_tr_b16 a[192:193], v220 offset:49152\n"
"ds_read_b64_tr_b16 a[194:195], v220 offset:53248\n"
"v_mfma_f32_16x16x32_bf16 v[0:3], a[160:163], a[72:75], v[0:3]\n"
"buffer_load_dwordx4 a[112:115], v248, s[16:19], s45 offen offset:2048 sc1\n"
"v_mfma_f32_16x16x32_bf16 v[32:35], a[128:131], a[68:71], 0\n"
"ds_read_b64_tr_b16 a[224:225], v220 offset:57344\n"
"ds_read_b64_tr_b16 a[226:227], v220 offset:61440\n"
"v_mfma_f32_16x16x32_bf16 v[32:35], a[160:163], a[76:79], v[32:35]\n"
"ds_read_b64_tr_b16 a[196:197], v221 offset:49152\n"
"v_mfma_f32_16x16x32_bf16 v[4:7], a[132:135], a[64:67], 0\n"
"ds_read_b64_tr_b16 a[198:199], v221 offset:53248\n"
"ds_read_b64_tr_b16 a[228:229], v221 offset:57344\n"
"v_mfma_f32_16x16x32_bf16 v[4:7], a[164:167], a[72:75], v[4:7]\n"
"buffer_load_dwordx4 a[120:123], v248, s[16:19], s45 offen offset:3072 sc1\n"
"v_mfma_f32_16x16x32_bf16 v[36:39], a[132:135], a[68:71], 0\n"
"ds_read_b64_tr_b16 a[230:231], v221 offset:61440\n"
"ds_read_b64_tr_b16 a[200:201], v222 offset:49152\n"
"v_mfma_f32_16x16x32_bf16 v[36:39], a[164:167], a[76:79], v[36:39]\n"
"ds_read_b64_tr_b16 a[202:203], v222 offset:53248\n"
"v_mfma_f32_16x16x32_bf16 v[8:11], a[136:139], a[64:67], 0\n"
"ds_read_b64_tr_b16 a[232:233], v222 offset:57344\n"
"ds_read_b64_tr_b16 a[234:235], v222 offset:61440\n"
"v_mfma_f32_16x16x32_bf16 v[8:11], a[168:171], a[72:75], v[8:11]\n"
"ds_read_b64_tr_b16 a[204:205], v223 offset:49152\n"
"buffer_load_dwordx4 a[116:119], v248, s[16:19], s46 offen offset:2048 sc1\n"
"v_mfma_f32_16x16x32_bf16 v[40:43], a[136:139], a[68:71], 0\n"
"ds_read_b64_tr_b16 a[206:207], v223 offset:53248\n"
"v_mfma_f32_16x16x32_bf16 v[40:43], a[168:171], a[76:79], v[40:43]\n"
"ds_read_b64_tr_b16 a[236:237], v223 offset:57344\n"
"v_mfma_f32_16x16x32_bf16 v[12:15], a[140:143], a[64:67], 0\n"
"ds_read_b64_tr_b16 a[238:239], v223 offset:61440\n"
"v_mfma_f32_16x16x32_bf16 v[12:15], a[172:175], a[72:75], v[12:15]\n"
"ds_read_b64_tr_b16 a[208:209], v220 offset:50176\n"
"ds_read_b64_tr_b16 a[210:211], v220 offset:54272\n"
"v_mfma_f32_16x16x32_bf16 v[44:47], a[140:143], a[68:71], 0\n"
"buffer_load_dwordx4 a[124:127], v248, s[16:19], s46 offen offset:3072 sc1\n"
"v_mfma_f32_16x16x32_bf16 v[44:47], a[172:175], a[76:79], v[44:47]\n"
"ds_read_b64_tr_b16 a[240:241], v220 offset:58368\n"
"ds_read_b64_tr_b16 a[242:243], v220 offset:62464\n"
"v_mfma_f32_16x16x32_bf16 v[16:19], a[144:147], a[64:67], 0\n"
"ds_read_b64_tr_b16 a[212:213], v221 offset:50176\n"
"v_mfma_f32_16x16x32_bf16 v[16:19], a[176:179], a[72:75], v[16:19]\n"
"ds_read_b64_tr_b16 a[214:215], v221 offset:54272\n"
"ds_read_b64_tr_b16 a[244:245], v221 offset:58368\n"
"v_mfma_f32_16x16x32_bf16 v[48:51], a[144:147], a[68:71], 0\n"
"ds_read_b64_tr_b16 a[246:247], v221 offset:62464\n"
"s_add_u32 s45, s45, 4096\n"
"v_mfma_f32_16x16x32_bf16 v[48:51], a[176:179], a[76:79], v[48:51]\n"
"ds_read_b64_tr_b16 a[216:217], v222 offset:50176\n"
"v_mfma_f32_16x16x32_bf16 v[20:23], a[148:151], a[64:67], 0\n"
"ds_read_b64_tr_b16 a[218:219], v222 offset:54272\n"
"v_mfma_f32_16x16x32_bf16 v[20:23], a[180:183], a[72:75], v[20:23]\n"
"ds_read_b64_tr_b16 a[248:249], v222 offset:58368\n"
"ds_read_b64_tr_b16 a[250:251], v222 offset:62464\n"
"v_mfma_f32_16x16x32_bf16 v[52:55], a[148:151], a[68:71], 0\n"
"ds_read_b64_tr_b16 a[220:221], v223 offset:50176\n"
"buffer_load_dwordx4 a[0:3], v248, s[16:19], s45 offen offset:0 sc1\n"
"v_mfma_f32_16x16x32_bf16 v[52:55], a[180:183], a[76:79], v[52:55]\n"
"ds_read_b64_tr_b16 a[222:223], v223 offset:54272\n"
"v_mfma_f32_16x16x32_bf16 v[24:27], a[152:155], a[64:67], 0\n"
"ds_read_b64_tr_b16 a[252:253], v223 offset:58368\n"
"v_mfma_f32_16x16x32_bf16 v[24:27], a[184:187], a[72:75], v[24:27]\n"
"ds_read_b64_tr_b16 a[254:255], v223 offset:62464\n"
"v_mfma_f32_16x16x32_bf16 v[56:59], a[152:155], a[68:71], 0\n"
"v_mfma_f32_16x16x32_bf16 v[56:59], a[184:187], a[76:79], v[56:59]\n"
"buffer_load_dwordx4 a[8:11], v248, s[16:19], s45 offen offset:1024 sc1\n"
"v_mfma_f32_16x16x32_bf16 v[28:31], a[156:159], a[64:67], 0\n"
"v_mfma_f32_16x16x32_bf16 v[28:31], a[188:191], a[72:75], v[28:31]\n"
"v_mfma_f32_16x16x32_bf16 v[60:63], a[156:159], a[68:71], 0\n"
"v_mfma_f32_16x16x32_bf16 v[60:63], a[188:191], a[76:79], v[60:63]\n"
"s_waitcnt vmcnt(10) lgkmcnt(0)\n"
"v_mfma_f32_16x16x32_bf16 v[0:3], a[192:195], a[80:83], v[0:3]\n"
"ds_read_b64_tr_b16 a[128:129], v224 offset:0\n"
"ds_read_b64_tr_b16 a[130:131], v224 offset:4096\n"
"v_mfma_f32_16x16x32_bf16 v[0:3], a[224:227], a[88:91], v[0:3]\n"
"s_add_u32 s46, s46, 4096\n"
"ds_read_b64_tr_b16 a[160:161], v224 offset:8192\n"
"v_mfma_f32_16x16x32_bf16 v[32:35], a[192:195], a[84:87], v[32:35]\n"
"ds_read_b64_tr_b16 a[162:163], v224 offset:12288\n"
"v_mfma_f32_16x16x32_bf16 v[32:35], a[224:227], a[92:95], v[32:35]\n"
"ds_read_b64_tr_b16 a[132:133], v225 offset:0\n"
"v_mfma_f32_16x16x32_bf16 v[4:7], a[196:199], a[80:83], v[4:7]\n"
"ds_read_b64_tr_b16 a[134:135], v225 offset:4096\n"
"ds_read_b64_tr_b16 a[164:165], v225 offset:8192\n"
"v_mfma_f32_16x16x32_bf16 v[4:7], a[228:231], a[88:91], v[4:7]\n"
"buffer_load_dwordx4 a[4:7], v248, s[16:19], s46 offen offset:0 sc1\n"
"v_mfma_f32_16x16x32_bf16 v[36:39], a[196:199], a[84:87], v[36:39]\n"
"ds_read_b64_tr_b16 a[166:167], v225 offset:12288\n"
"ds_read_b64_tr_b16 a[136:137], v226 offset:0\n"
"v_mfma_f32_16x16x32_bf16 v[36:39], a[228:231], a[92:95], v[36:39]\n"
"ds_read_b64_tr_b16 a[138:139], v226 offset:4096\n"
"v_mfma_f32_16x16x32_bf16 v[8:11], a[200:203], a[80:83], v[8:11]\n"
"ds_read_b64_tr_b16 a[168:169], v226 offset:8192\n"
"ds_read_b64_tr_b16 a[170:171], v226 offset:12288\n"
"v_mfma_f32_16x16x32_bf16 v[8:11], a[232:235], a[88:91], v[8:11]\n"
"ds_read_b64_tr_b16 a[140:141], v227 offset:0\n"
"buffer_load_dwordx4 a[12:15], v248, s[16:19], s46 offen offset:1024 sc1\n"
"v_mfma_f32_16x16x32_bf16 v[40:43], a[200:203], a[84:87], v[40:43]\n"
"ds_read_b64_tr_b16 a[142:143], v227 offset:4096\n"
"v_mfma_f32_16x16x32_bf16 v[40:43], a[232:235], a[92:95], v[40:43]\n"
"ds_read_b64_tr_b16 a[172:173], v227 offset:8192\n"
"v_mfma_f32_16x16x32_bf16 v[12:15], a[204:207], a[80:83], v[12:15]\n"
"ds_read_b64_tr_b16 a[174:175], v227 offset:12288\n"
"v_mfma_f32_16x16x32_bf16 v[12:15], a[236:239], a[88:91], v[12:15]\n"
"ds_read_b64_tr_b16 a[144:145], v224 offset:1024\n"
"ds_read_b64_tr_b16 a[146:147], v224 offset:5120\n"
"v_mfma_f32_16x16x32_bf16 v[44:47], a[204:207], a[84:87], v[44:47]\n"
"buffer_load_dwordx4 a[16:19], v248, s[16:19], s45 offen offset:2048 sc1\n"
"v_mfma_f32_16x16x32_bf16 v[44:47], a[236:239], a[92:95], v[44:47]\n"
"ds_read_b64_tr_b16 a[176:177], v224 offset:9216\n"
"ds_read_b64_tr_b16 a[178:179], v224 offset:13312\n"
"v_mfma_f32_16x16x32_bf16 v[16:19], a[208:211], a[80:83], v[16:19]\n"
"ds_read_b64_tr_b16 a[148:149], v225 offset:1024\n"
"v_mfma_f32_16x16x32_bf16 v[16:19], a[240:243], a[88:91], v[16:19]\n"
"ds_read_b64_tr_b16 a[150:151], v225 offset:5120\n"
"ds_read_b64_tr_b16 a[180:181], v225 offset:9216\n"
"v_mfma_f32_16x16x32_bf16 v[48:51], a[208:211], a[84:87], v[48:51]\n"
"ds_read_b64_tr_b16 a[182:183], v225 offset:13312\n"
"buffer_load_dwordx4 a[24:27], v248, s[16:19], s45 offen offset:3072 sc1\n"
"v_mfma_f32_16x16x32_bf16 v[48:51], a[240:243], a[92:95], v[48:51]\n"
"ds_read_b64_tr_b16 a[152:153], v226 offset:1024\n"
"v_mfma_f32_16x16x32_bf16 v[20:23], a[212:215], a[80:83], v[20:23]\n"
"ds_read_b64_tr_b16 a[154:155], v226 offset:5120\n"
"s_waitcnt vmcnt(6)\n"
"v_mfma_f32_16x16x32_bf16 v[20:23], a[244:247], a[88:91], v[20:23]\n"
"s_lshl_b32 s95, s41, 7\n"
"ds_read_b64_tr_b16 a[184:185], v226 offset:9216\n"
"v_mfma_f32_16x16x32_bf16 v[52:55], a[212:215], a[84:87], v[52:55]\n"
"ds_read_b64_tr_b16 a[186:187], v226 offset:13312\n"
"s_lshl_b32 s94, s83, 2\n"
"v_mfma_f32_16x16x32_bf16 v[52:55], a[244:247], a[92:95], v[52:55]\n"
"ds_read_b64_tr_b16 a[156:157], v227 offset:1024\n"
"buffer_load_dwordx4 a[20:23], v248, s[16:19], s46 offen offset:2048 sc1\n"
"v_mfma_f32_16x16x32_bf16 v[24:27], a[216:219], a[80:83], v[24:27]\n"
"s_add_u32 s95, s95, s94\n"
"v_mfma_f32_16x16x32_bf16 v[24:27], a[248:251], a[88:91], v[24:27]\n"
"ds_read_b64_tr_b16 a[158:159], v227 offset:5120\n"
"s_add_u32 s95, s95, 0x38000\n"
"v_mfma_f32_16x16x32_bf16 v[56:59], a[216:219], a[84:87], v[56:59]\n"
"v_mov_b32 v253, s82\n"
"ds_read_b64_tr_b16 a[188:189], v227 offset:9216\n"
"v_mfma_f32_16x16x32_bf16 v[56:59], a[248:251], a[92:95], v[56:59]\n"
"s_mov_b64 exec, 1\n"
"buffer_store_dword v253, off, s[88:91], s95 sc1\n"
"s_mov_b64 exec, -1\n"
"v_mfma_f32_16x16x32_bf16 v[28:31], a[220:223], a[80:83], v[28:31]\n"
"v_mfma_f32_16x16x32_bf16 v[28:31], a[252:255], a[88:91], v[28:31]\n"
"ds_read_b64_tr_b16 a[190:191], v227 offset:13312\n"
"s_add_u32 s36, s80, 0x3880000\n"
"s_addc_u32 s37, s81, 0\n"
"v_mfma_f32_16x16x32_bf16 v[60:63], a[220:223], a[84:87], v[60:63]\n"
"s_and_b32 s37, s37, 0xffff\n"
"v_mfma_f32_16x16x32_bf16 v[60:63], a[252:255], a[92:95], v[60:63]\n"
"s_mov_b32 s38, 0x700000\n"
"buffer_load_dwordx4 a[28:31], v248, s[16:19], s46 offen offset:3072 sc1\n"
"s_mov_b32 s39, 0x20000\n"
"s_cmp_eq_u32 s26, 0\n"
"s_cselect_b32 s38, 0, s38\n"
"s_waitcnt vmcnt(9) lgkmcnt(0)\n"
"v_mfma_f32_16x16x32_bf16 v[0:3], a[128:131], a[96:99], v[0:3]\n"
"ds_read_b64_tr_b16 a[192:193], v224 offset:16384\n"
"ds_read_b64_tr_b16 a[194:195], v224 offset:20480\n"
"v_mfma_f32_16x16x32_bf16 v[0:3], a[160:163], a[104:107], v[0:3]\n"
"s_add_u32 s45, s45, 4096\n"
"ds_read_b64_tr_b16 a[224:225], v224 offset:24576\n"
"v_mfma_f32_16x16x32_bf16 v[32:35], a[128:131], a[100:103], v[32:35]\n"
"ds_read_b64_tr_b16 a[226:227], v224 offset:28672\n"
"v_mfma_f32_16x16x32_bf16 v[32:35], a[160:163], a[108:111], v[32:35]\n"
"ds_read_b64_tr_b16 a[196:197], v225 offset:16384\n"
"v_mfma_f32_16x16x32_bf16 v[4:7], a[132:135], a[96:99], v[4:7]\n"
"ds_read_b64_tr_b16 a[198:199], v225 offset:20480\n"
"ds_read_b64_tr_b16 a[228:229], v225 offset:24576\n"
"v_mfma_f32_16x16x32_bf16 v[4:7], a[164:167], a[104:107], v[4:7]\n"
"buffer_load_dwordx4 a[32:35], v248, s[16:19], s45 offen offset:0 sc1\n"
"v_mfma_f32_16x16x32_bf16 v[36:39], a[132:135], a[100:103], v[36:39]\n"
"s_add_u32 s40, s80, 0x3280000\n"
"s_addc_u32 s41, s81, 0\n"
"v_mfma_f32_16x16x32_bf16 v[36:39], a[164:167], a[108:111], v[36:39]\n"
"ds_read_b64_tr_b16 a[230:231], v225 offset:28672\n"
"ds_read_b64_tr_b16 a[200:201], v226 offset:16384\n"
"v_mfma_f32_16x16x32_bf16 v[8:11], a[136:139], a[96:99], v[8:11]\n"
"s_and_b32 s41, s41, 0xffff\n"
"ds_read_b64_tr_b16 a[202:203], v226 offset:20480\n"
"v_mfma_f32_16x16x32_bf16 v[8:11], a[168:171], a[104:107], v[8:11]\n"
"ds_read_b64_tr_b16 a[232:233], v226 offset:24576\n"
"ds_read_b64_tr_b16 a[234:235], v226 offset:28672\n"
"v_mfma_f32_16x16x32_bf16 v[40:43], a[136:139], a[100:103], v[40:43]\n"
"buffer_load_dwordx4 a[40:43], v248, s[16:19], s45 offen offset:1024 sc1\n"
"v_mfma_f32_16x16x32_bf16 v[40:43], a[168:171], a[108:111], v[40:43]\n"
"s_mov_b32 s42, 0x400000\n"
"ds_read_b64_tr_b16 a[204:205], v227 offset:16384\n"
"v_mfma_f32_16x16x32_bf16 v[12:15], a[140:143], a[96:99], v[12:15]\n"
"ds_read_b64_tr_b16 a[206:207], v227 offset:20480\n"
"s_mov_b32 s43, 0x20000\n"
"v_mfma_f32_16x16x32_bf16 v[12:15], a[172:175], a[104:107], v[12:15]\n"
"ds_read_b64_tr_b16 a[236:237], v227 offset:24576\n"
"ds_read_b64_tr_b16 a[238:239], v227 offset:28672\n"
"v_mfma_f32_16x16x32_bf16 v[44:47], a[140:143], a[100:103], v[44:47]\n"
"s_add_u32 s46, s46, 4096\n"
"ds_read_b64_tr_b16 a[208:209], v224 offset:17408\n"
"v_mfma_f32_16x16x32_bf16 v[44:47], a[172:175], a[108:111], v[44:47]\n"
"ds_read_b64_tr_b16 a[210:211], v224 offset:21504\n"
"ds_read_b64_tr_b16 a[240:241], v224 offset:25600\n"
"v_mfma_f32_16x16x32_bf16 v[16:19], a[144:147], a[96:99], v[16:19]\n"
"ds_read_b64_tr_b16 a[242:243], v224 offset:29696\n"
"ds_read_b64_tr_b16 a[212:213], v225 offset:17408\n"
"v_mfma_f32_16x16x32_bf16 v[16:19], a[176:179], a[104:107], v[16:19]\n"
"buffer_load_dwordx4 a[36:39], v248, s[16:19], s46 offen offset:0 sc1\n"
"v_mfma_f32_16x16x32_bf16 v[48:51], a[144:147], a[100:103], v[48:51]\n"
"ds_read_b64_tr_b16 a[214:215], v225 offset:21504\n"
"ds_read_b64_tr_b16 a[244:245], v225 offset:25600\n"
"v_mfma_f32_16x16x32_bf16 v[48:51], a[176:179], a[108:111], v[48:51]\n"
"ds_read_b64_tr_b16 a[246:247], v225 offset:29696\n"
"ds_read_b64_tr_b16 a[216:217], v226 offset:17408\n"
"v_mfma_f32_16x16x32_bf16 v[20:23], a[148:151], a[96:99], v[20:23]\n"
"ds_read_b64_tr_b16 a[218:219], v226 offset:21504\n"
"buffer_load_dwordx4 a[44:47], v248, s[16:19], s46 offen offset:1024 sc1\n"
"v_mfma_f32_16x16x32_bf16 v[20:23], a[180:183], a[104:107], v[20:23]\n"
"ds_read_b64_tr_b16 a[248:249], v226 offset:25600\n"
"v_mfma_f32_16x16x32_bf16 v[52:55], a[148:151], a[100:103], v[52:55]\n"
"ds_read_b64_tr_b16 a[250:251], v226 offset:29696\n"
"ds_read_b64_tr_b16 a[220:221], v227 offset:17408\n"
"v_mfma_f32_16x16x32_bf16 v[52:55], a[180:183], a[108:111], v[52:55]\n"
"ds_read_b64_tr_b16 a[222:223], v227 offset:21504\n"
"v_mfma_f32_16x16x32_bf16 v[24:27], a[152:155], a[96:99], v[24:27]\n"
"ds_read_b64_tr_b16 a[252:253], v227 offset:25600\n"
"buffer_load_dwordx4 a[48:51], v248, s[16:19], s45 offen offset:2048 sc1\n"
"v_mfma_f32_16x16x32_bf16 v[24:27], a[184:187], a[104:107], v[24:27]\n"
"ds_read_b64_tr_b16 a[254:255], v227 offset:29696\n"
"v_mfma_f32_16x16x32_bf16 v[56:59], a[152:155], a[100:103], v[56:59]\n"
"v_mfma_f32_16x16x32_bf16 v[56:59], a[184:187], a[108:111], v[56:59]\n"
"v_mfma_f32_16x16x32_bf16 v[28:31], a[156:159], a[96:99], v[28:31]\n"
"buffer_load_dwordx4 a[56:59], v248, s[16:19], s45 offen offset:3072 sc1\n"
"v_mfma_f32_16x16x32_bf16 v[28:31], a[188:191], a[104:107], v[28:31]\n"
"v_mfma_f32_16x16x32_bf16 v[60:63], a[156:159], a[100:103], v[60:63]\n"
"v_mfma_f32_16x16x32_bf16 v[60:63], a[188:191], a[108:111], v[60:63]\n"
"s_waitcnt vmcnt(15) lgkmcnt(0)\n"
"s_barrier\n"
"v_mfma_f32_16x16x32_bf16 v[0:3], a[192:195], a[112:115], v[0:3]\n"
"ds_read_b64_tr_b16 a[128:129], v216 offset:0\n"
"ds_read_b64_tr_b16 a[130:131], v216 offset:4096\n"
"v_mfma_f32_16x16x32_bf16 v[0:3], a[224:227], a[120:123], v[0:3]\n"
"buffer_load_dwordx4 a[52:55], v248, s[16:19], s46 offen offset:2048 sc1\n"
"v_mfma_f32_16x16x32_bf16 v[32:35], a[192:195], a[116:119], v[32:35]\n"
"ds_read_b64_tr_b16 a[160:161], v216 offset:8192\n"
"ds_read_b64_tr_b16 a[162:163], v216 offset:12288\n"
"v_mfma_f32_16x16x32_bf16 v[32:35], a[224:227], a[124:127], v[32:35]\n"
"ds_read_b64_tr_b16 a[132:133], v217 offset:0\n"
"v_mfma_f32_16x16x32_bf16 v[4:7], a[196:199], a[112:115], v[4:7]\n"
"ds_read_b64_tr_b16 a[134:135], v217 offset:4096\n"
"ds_read_b64_tr_b16 a[164:165], v217 offset:8192\n"
"v_mfma_f32_16x16x32_bf16 v[4:7], a[228:231], a[120:123], v[4:7]\n"
"buffer_load_dwordx4 a[60:63], v248, s[16:19], s46 offen offset:3072 sc1\n"
"v_mfma_f32_16x16x32_bf16 v[36:39], a[196:199], a[116:119], v[36:39]\n"
"ds_read_b64_tr_b16 a[166:167], v217 offset:12288\n"
"ds_read_b64_tr_b16 a[136:137], v218 offset:0\n"
"v_mfma_f32_16x16x32_bf16 v[36:39], a[228:231], a[124:127], v[36:39]\n"
"ds_read_b64_tr_b16 a[138:139], v218 offset:4096\n"
"v_mfma_f32_16x16x32_bf16 v[8:11], a[200:203], a[112:115], v[8:11]\n"
"ds_read_b64_tr_b16 a[168:169], v218 offset:8192\n"
"ds_read_b64_tr_b16 a[170:171], v218 offset:12288\n"
"v_mfma_f32_16x16x32_bf16 v[8:11], a[232:235], a[120:123], v[8:11]\n"
"s_add_u32 m0, s53, 65536\n"
"ds_read_b64_tr_b16 a[140:141], v219 offset:0\n"
"v_mfma_f32_16x16x32_bf16 v[40:43], a[200:203], a[116:119], v[40:43]\n"
"ds_read_b64_tr_b16 a[142:143], v219 offset:4096\n"
"v_mfma_f32_16x16x32_bf16 v[40:43], a[232:235], a[124:127], v[40:43]\n"
"ds_read_b64_tr_b16 a[172:173], v219 offset:8192\n"
"v_mfma_f32_16x16x32_bf16 v[12:15], a[204:207], a[112:115], v[12:15]\n"
"ds_read_b64_tr_b16 a[174:175], v219 offset:12288\n"
"s_mov_b32 s92, 0\n"
"v_mfma_f32_16x16x32_bf16 v[12:15], a[236:239], a[120:123], v[12:15]\n"
"s_add_u32 s97, s97, 0x4000\n"
"ds_read_b64_tr_b16 a[144:145], v216 offset:1024\n"
"v_mfma_f32_16x16x32_bf16 v[44:47], a[204:207], a[116:119], v[44:47]\n"
"ds_read_b64_tr_b16 a[146:147], v216 offset:5120\n"
"ds_read_b64_tr_b16 a[176:177], v216 offset:9216\n"
"v_mfma_f32_16x16x32_bf16 v[44:47], a[236:239], a[124:127], v[44:47]\n"
"ds_read_b64_tr_b16 a[178:179], v216 offset:13312\n"
"v_mfma_f32_16x16x32_bf16 v[16:19], a[208:211], a[112:115], v[16:19]\n"
"ds_read_b64_tr_b16 a[148:149], v217 offset:1024\n"
"buffer_load_dwordx4 v188, s[20:23], s92 offen offset:0 lds\n"
"v_mfma_f32_16x16x32_bf16 v[16:19], a[240:243], a[120:123], v[16:19]\n"
"buffer_load_dword v241, v240, s[88:91], s97 offen sc1\n"
"v_mfma_f32_16x16x32_bf16 v[48:51], a[208:211], a[116:119], v[48:51]\n"
"ds_read_b64_tr_b16 a[150:151], v217 offset:5120\n"
"v_mfma_f32_16x16x32_bf16 v[48:51], a[240:243], a[124:127], v[48:51]\n"
"ds_read_b64_tr_b16 a[180:181], v217 offset:9216\n"
"ds_read_b64_tr_b16 a[182:183], v217 offset:13312\n"
"v_mfma_f32_16x16x32_bf16 v[20:23], a[212:215], a[112:115], v[20:23]\n"
"ds_read_b64_tr_b16 a[152:153], v218 offset:1024\n"
"ds_read_b64_tr_b16 a[154:155], v218 offset:5120\n"
"v_mfma_f32_16x16x32_bf16 v[20:23], a[244:247], a[120:123], v[20:23]\n"
"buffer_load_dwordx4 v189, s[20:23], s92 offen offset:1024 lds\n"
"v_mfma_f32_16x16x32_bf16 v[52:55], a[212:215], a[116:119], v[52:55]\n"
"ds_read_b64_tr_b16 a[184:185], v218 offset:9216\n"
"ds_read_b64_tr_b16 a[186:187], v218 offset:13312\n"
"v_mfma_f32_16x16x32_bf16 v[52:55], a[244:247], a[124:127], v[52:55]\n"
"ds_read_b64_tr_b16 a[156:157], v219 offset:1024\n"
"ds_read_b64_tr_b16 a[158:159], v219 offset:5120\n"
"v_mfma_f32_16x16x32_bf16 v[24:27], a[216:219], a[112:115], v[24:27]\n"
"ds_read_b64_tr_b16 a[188:189], v219 offset:9216\n"
"buffer_load_dwordx4 v190, s[20:23], s92 offen offset:2048 lds\n"
"v_mfma_f32_16x16x32_bf16 v[24:27], a[248:251], a[120:123], v[24:27]\n"
"ds_read_b64_tr_b16 a[190:191], v219 offset:13312\n"
"v_mfma_f32_16x16x32_bf16 v[56:59], a[216:219], a[116:119], v[56:59]\n"
"v_mfma_f32_16x16x32_bf16 v[56:59], a[248:251], a[124:127], v[56:59]\n"
"v_mfma_f32_16x16x32_bf16 v[28:31], a[220:223], a[112:115], v[28:31]\n"
"buffer_load_dwordx4 v191, s[20:23], s92 offen offset:3072 lds\n"
"v_mfma_f32_16x16x32_bf16 v[28:31], a[252:255], a[120:123], v[28:31]\n"
"v_mfma_f32_16x16x32_bf16 v[60:63], a[220:223], a[116:119], v[60:63]\n"
"v_mfma_f32_16x16x32_bf16 v[60:63], a[252:255], a[124:127], v[60:63]\n"
"s_waitcnt vmcnt(18) lgkmcnt(0)\n"
"v_mfma_f32_16x16x32_bf16 v[0:3], a[128:131], a[0:3], v[0:3]\n"
"ds_read_b64_tr_b16 a[192:193], v216 offset:16384\n"
"ds_read_b64_tr_b16 a[194:195], v216 offset:20480\n"
"v_mfma_f32_16x16x32_bf16 v[0:3], a[160:163], a[8:11], v[0:3]\n"
"s_add_u32 m0, s53, 69632\n"
"ds_read_b64_tr_b16 a[224:225], v216 offset:24576\n"
"v_mfma_f32_16x16x32_bf16 v[32:35], a[128:131], a[4:7], v[32:35]\n"
"ds_read_b64_tr_b16 a[226:227], v216 offset:28672\n"
"v_mfma_f32_16x16x32_bf16 v[32:35], a[160:163], a[12:15], v[32:35]\n"
"ds_read_b64_tr_b16 a[196:197], v217 offset:16384\n"
"v_mfma_f32_16x16x32_bf16 v[4:7], a[132:135], a[0:3], v[4:7]\n"
"ds_read_b64_tr_b16 a[198:199], v217 offset:20480\n"
"ds_read_b64_tr_b16 a[228:229], v217 offset:24576\n"
"v_mfma_f32_16x16x32_bf16 v[4:7], a[164:167], a[8:11], v[4:7]\n"
"ds_read_b64_tr_b16 a[230:231], v217 offset:28672\n"
"s_mov_b32 s92, 32768\n"
"v_mfma_f32_16x16x32_bf16 v[36:39], a[132:135], a[4:7], v[36:39]\n"
"ds_read_b64_tr_b16 a[200:201], v218 offset:16384\n"
"v_mfma_f32_16x16x32_bf16 v[36:39], a[164:167], a[12:15], v[36:39]\n"
"ds_read_b64_tr_b16 a[202:203], v218 offset:20480\n"
"v_mfma_f32_16x16x32_bf16 v[8:11], a[136:139], a[0:3], v[8:11]\n"
"ds_read_b64_tr_b16 a[232:233], v218 offset:24576\n"
"ds_read_b64_tr_b16 a[234:235], v218 offset:28672\n"
"v_mfma_f32_16x16x32_bf16 v[8:11], a[168:171], a[8:11], v[8:11]\n"
"ds_read_b64_tr_b16 a[204:205], v219 offset:16384\n"
"v_mfma_f32_16x16x32_bf16 v[40:43], a[136:139], a[4:7], v[40:43]\n"
"ds_read_b64_tr_b16 a[206:207], v219 offset:20480\n"
"buffer_load_dwordx4 v188, s[20:23], s92 offen offset:0 lds\n"
"v_mfma_f32_16x16x32_bf16 v[40:43], a[168:171], a[12:15], v[40:43]\n"
"ds_read_b64_tr_b16 a[236:237], v219 offset:24576\n"
"v_mfma_f32_16x16x32_bf16 v[12:15], a[140:143], a[0:3], v[12:15]\n"
"ds_read_b64_tr_b16 a[238:239], v219 offset:28672\n"
"v_mfma_f32_16x16x32_bf16 v[12:15], a[172:175], a[8:11], v[12:15]\n"
"ds_read_b64_tr_b16 a[208:209], v216 offset:17408\n"
"ds_read_b64_tr_b16 a[210:211], v216 offset:21504\n"
"v_mfma_f32_16x16x32_bf16 v[44:47], a[140:143], a[4:7], v[44:47]\n"
"ds_read_b64_tr_b16 a[240:241], v216 offset:25600\n"
"v_mfma_f32_16x16x32_bf16 v[44:47], a[172:175], a[12:15], v[44:47]\n"
"ds_read_b64_tr_b16 a[242:243], v216 offset:29696\n"
"v_mfma_f32_16x16x32_bf16 v[16:19], a[144:147], a[0:3], v[16:19]\n"
"ds_read_b64_tr_b16 a[212:213], v217 offset:17408\n"
"buffer_load_dwordx4 v189, s[20:23], s92 offen offset:1024 lds\n"
"v_mfma_f32_16x16x32_bf16 v[16:19], a[176:179], a[8:11], v[16:19]\n"
"ds_read_b64_tr_b16 a[214:215], v217 offset:21504\n"
"v_mfma_f32_16x16x32_bf16 v[48:51], a[144:147], a[4:7], v[48:51]\n"
"ds_read_b64_tr_b16 a[244:245], v217 offset:25600\n"
"ds_read_b64_tr_b16 a[246:247], v217 offset:29696\n"
"v_mfma_f32_16x16x32_bf16 v[48:51], a[176:179], a[12:15], v[48:51]\n"
"ds_read_b64_tr_b16 a[216:217], v218 offset:17408\n"
"v_mfma_f32_16x16x32_bf16 v[20:23], a[148:151], a[0:3], v[20:23]\n"
"ds_read_b64_tr_b16 a[218:219], v218 offset:21504\n"
"v_mfma_f32_16x16x32_bf16 v[20:23], a[180:183], a[8:11], v[20:23]\n"
"ds_read_b64_tr_b16 a[248:249], v218 offset:25600\n"
"ds_read_b64_tr_b16 a[250:251], v218 offset:29696\n"
"v_mfma_f32_16x16x32_bf16 v[52:55], a[148:151], a[4:7], v[52:55]\n"
"buffer_load_dwordx4 v190, s[20:23], s92 offen offset:2048 lds\n"
"v_mfma_f32_16x16x32_bf16 v[52:55], a[180:183], a[12:15], v[52:55]\n"
"ds_read_b64_tr_b16 a[220:221], v219 offset:17408\n"
"ds_read_b64_tr_b16 a[222:223], v219 offset:21504\n"
"v_mfma_f32_16x16x32_bf16 v[24:27], a[152:155], a[0:3], v[24:27]\n"
"ds_read_b64_tr_b16 a[252:253], v219 offset:25600\n"
"v_mfma_f32_16x16x32_bf16 v[24:27], a[184:187], a[8:11], v[24:27]\n"
"ds_read_b64_tr_b16 a[254:255], v219 offset:29696\n"
"v_mfma_f32_16x16x32_bf16 v[56:59], a[152:155], a[4:7], v[56:59]\n"
"buffer_load_dwordx4 v191, s[20:23], s92 offen offset:3072 lds\n"
"v_mfma_f32_16x16x32_bf16 v[56:59], a[184:187], a[12:15], v[56:59]\n"
"v_mfma_f32_16x16x32_bf16 v[28:31], a[156:159], a[0:3], v[28:31]\n"
"v_mfma_f32_16x16x32_bf16 v[28:31], a[188:191], a[8:11], v[28:31]\n"
"v_mfma_f32_16x16x32_bf16 v[60:63], a[156:159], a[4:7], v[60:63]\n"
"v_mfma_f32_16x16x32_bf16 v[60:63], a[188:191], a[12:15], v[60:63]\n"
"s_waitcnt vmcnt(17) lgkmcnt(0)\n"
"s_waitcnt vmcnt(7)\n"
"v_cmp_ne_u32 vcc, s82, v241\n"
"s_cbranch_vccnz Lps94_%=\n"
"Lpb93_%=:\n"
"v_mfma_f32_16x16x32_bf16 v[0:3], a[192:195], a[16:19], v[0:3]\n"
"ds_read_b64_tr_b16 a[128:129], v216 offset:32768\n"
"ds_read_b64_tr_b16 a[130:131], v216 offset:36864\n"
"v_mfma_f32_16x16x32_bf16 v[0:3], a[224:227], a[24:27], v[0:3]\n"
"buffer_load_dwordx4 a[64:67], v248, s[16:19], s47 offen offset:0 sc1\n"
"v_mfma_f32_16x16x32_bf16 v[32:35], a[192:195], a[20:23], v[32:35]\n"
"ds_read_b64_tr_b16 a[160:161], v216 offset:40960\n"
"ds_read_b64_tr_b16 a[162:163], v216 offset:45056\n"
"v_mfma_f32_16x16x32_bf16 v[32:35], a[224:227], a[28:31], v[32:35]\n"
"ds_read_b64_tr_b16 a[132:133], v217 offset:32768\n"
"v_mfma_f32_16x16x32_bf16 v[4:7], a[196:199], a[16:19], v[4:7]\n"
"ds_read_b64_tr_b16 a[134:135], v217 offset:36864\n"
"ds_read_b64_tr_b16 a[164:165], v217 offset:40960\n"
"v_mfma_f32_16x16x32_bf16 v[4:7], a[228:231], a[24:27], v[4:7]\n"
"buffer_load_dwordx4 a[72:75], v248, s[16:19], s47 offen offset:1024 sc1\n"
"v_mfma_f32_16x16x32_bf16 v[36:39], a[196:199], a[20:23], v[36:39]\n"
"ds_read_b64_tr_b16 a[166:167], v217 offset:45056\n"
"ds_read_b64_tr_b16 a[136:137], v218 offset:32768\n"
"v_mfma_f32_16x16x32_bf16 v[36:39], a[228:231], a[28:31], v[36:39]\n"
"ds_read_b64_tr_b16 a[138:139], v218 offset:36864\n"
"v_mfma_f32_16x16x32_bf16 v[8:11], a[200:203], a[16:19], v[8:11]\n"
"ds_read_b64_tr_b16 a[168:169], v218 offset:40960\n"
"ds_read_b64_tr_b16 a[170:171], v218 offset:45056\n"
"v_mfma_f32_16x16x32_bf16 v[8:11], a[232:235], a[24:27], v[8:11]\n"
"buffer_load_dwordx4 a[68:71], v248, s[16:19], s48 offen offset:0 sc1\n"
"v_mfma_f32_16x16x32_bf16 v[40:43], a[200:203], a[20:23], v[40:43]\n"
"ds_read_b64_tr_b16 a[140:141], v219 offset:32768\n"
"ds_read_b64_tr_b16 a[142:143], v219 offset:36864\n"
"v_mfma_f32_16x16x32_bf16 v[40:43], a[232:235], a[28:31], v[40:43]\n"
"ds_read_b64_tr_b16 a[172:173], v219 offset:40960\n"
"v_mfma_f32_16x16x32_bf16 v[12:15], a[204:207], a[16:19], v[12:15]\n"
"ds_read_b64_tr_b16 a[174:175], v219 offset:45056\n"
"buffer_load_dwordx4 a[76:79], v248, s[16:19], s48 offen offset:1024 sc1\n"
"v_mfma_f32_16x16x32_bf16 v[12:15], a[236:239], a[24:27], v[12:15]\n"
"ds_read_b64_tr_b16 a[144:145], v216 offset:33792\n"
"v_mfma_f32_16x16x32_bf16 v[44:47], a[204:207], a[20:23], v[44:47]\n"
"ds_read_b64_tr_b16 a[146:147], v216 offset:37888\n"
"ds_read_b64_tr_b16 a[176:177], v216 offset:41984\n"
"v_mfma_f32_16x16x32_bf16 v[44:47], a[236:239], a[28:31], v[44:47]\n"
"ds_read_b64_tr_b16 a[178:179], v216 offset:46080\n"
"v_mfma_f32_16x16x32_bf16 v[16:19], a[208:211], a[16:19], v[16:19]\n"
"ds_read_b64_tr_b16 a[148:149], v217 offset:33792\n"
"buffer_load_dwordx4 a[80:83], v248, s[16:19], s47 offen offset:2048 sc1\n"
"v_mfma_f32_16x16x32_bf16 v[16:19], a[240:243], a[24:27], v[16:19]\n"
"ds_read_b64_tr_b16 a[150:151], v217 offset:37888\n"
"v_mfma_f32_16x16x32_bf16 v[48:51], a[208:211], a[20:23], v[48:51]\n"
"ds_read_b64_tr_b16 a[180:181], v217 offset:41984\n"
"ds_read_b64_tr_b16 a[182:183], v217 offset:46080\n"
"v_mfma_f32_16x16x32_bf16 v[48:51], a[240:243], a[28:31], v[48:51]\n"
"ds_read_b64_tr_b16 a[152:153], v218 offset:33792\n"
"v_mfma_f32_16x16x32_bf16 v[20:23], a[212:215], a[16:19], v[20:23]\n"
"ds_read_b64_tr_b16 a[154:155], v218 offset:37888\n"
"buffer_load_dwordx4 a[88:91], v248, s[16:19], s47 offen offset:3072 sc1\n"
"v_mfma_f32_16x16x32_bf16 v[20:23], a[244:247], a[24:27], v[20:23]\n"
"ds_read_b64_tr_b16 a[184:185], v218 offset:41984\n"
"v_mfma_f32_16x16x32_bf16 v[52:55], a[212:215], a[20:23], v[52:55]\n"
"ds_read_b64_tr_b16 a[186:187], v218 offset:46080\n"
"ds_read_b64_tr_b16 a[156:157], v219 offset:33792\n"
"v_mfma_f32_16x16x32_bf16 v[52:55], a[244:247], a[28:31], v[52:55]\n"
"ds_read_b64_tr_b16 a[158:159], v219 offset:37888\n"
"v_mfma_f32_16x16x32_bf16 v[24:27], a[216:219], a[16:19], v[24:27]\n"
"ds_read_b64_tr_b16 a[188:189], v219 offset:41984\n"
"buffer_load_dwordx4 a[84:87], v248, s[16:19], s48 offen offset:2048 sc1\n"
"v_mfma_f32_16x16x32_bf16 v[24:27], a[248:251], a[24:27], v[24:27]\n"
"ds_read_b64_tr_b16 a[190:191], v219 offset:46080\n"
"v_mfma_f32_16x16x32_bf16 v[56:59], a[216:219], a[20:23], v[56:59]\n"
"v_mfma_f32_16x16x32_bf16 v[56:59], a[248:251], a[28:31], v[56:59]\n"
"v_mfma_f32_16x16x32_bf16 v[28:31], a[220:223], a[16:19], v[28:31]\n"
"buffer_load_dwordx4 a[92:95], v248, s[16:19], s48 offen offset:3072 sc1\n"
"v_mfma_f32_16x16x32_bf16 v[28:31], a[252:255], a[24:27], v[28:31]\n"
"v_mfma_f32_16x16x32_bf16 v[60:63], a[220:223], a[20:23], v[60:63]\n"
"v_mfma_f32_16x16x32_bf16 v[60:63], a[252:255], a[28:31], v[60:63]\n"
"s_waitcnt vmcnt(15) lgkmcnt(0)\n"
"v_mfma_f32_16x16x32_bf16 v[0:3], a[128:131], a[32:35], v[0:3]\n"
"ds_read_b64_tr_b16 a[192:193], v216 offset:49152\n"
"ds_read_b64_tr_b16 a[194:195], v216 offset:53248\n"
"v_mfma_f32_16x16x32_bf16 v[0:3], a[160:163], a[40:43], v[0:3]\n"
"s_add_u32 s47, s47, 4096\n"
"ds_read_b64_tr_b16 a[224:225], v216 offset:57344\n"
"v_mfma_f32_16x16x32_bf16 v[32:35], a[128:131], a[36:39], v[32:35]\n"
"ds_read_b64_tr_b16 a[226:227], v216 offset:61440\n"
"v_mfma_f32_16x16x32_bf16 v[32:35], a[160:163], a[44:47], v[32:35]\n"
"ds_read_b64_tr_b16 a[196:197], v217 offset:49152\n"
"buffer_load_dwordx4 a[96:99], v248, s[16:19], s47 offen offset:0 sc1\n"
"v_mfma_f32_16x16x32_bf16 v[4:7], a[132:135], a[32:35], v[4:7]\n"
"ds_read_b64_tr_b16 a[198:199], v217 offset:53248\n"
"v_mfma_f32_16x16x32_bf16 v[4:7], a[164:167], a[40:43], v[4:7]\n"
"ds_read_b64_tr_b16 a[228:229], v217 offset:57344\n"
"ds_read_b64_tr_b16 a[230:231], v217 offset:61440\n"
"v_mfma_f32_16x16x32_bf16 v[36:39], a[132:135], a[36:39], v[36:39]\n"
"ds_read_b64_tr_b16 a[200:201], v218 offset:49152\n"
"buffer_load_dwordx4 a[104:107], v248, s[16:19], s47 offen offset:1024 sc1\n"
"v_mfma_f32_16x16x32_bf16 v[36:39], a[164:167], a[44:47], v[36:39]\n"
"ds_read_b64_tr_b16 a[202:203], v218 offset:53248\n"
"v_mfma_f32_16x16x32_bf16 v[8:11], a[136:139], a[32:35], v[8:11]\n"
"ds_read_b64_tr_b16 a[232:233], v218 offset:57344\n"
"ds_read_b64_tr_b16 a[234:235], v218 offset:61440\n"
"v_mfma_f32_16x16x32_bf16 v[8:11], a[168:171], a[40:43], v[8:11]\n"
"ds_read_b64_tr_b16 a[204:205], v219 offset:49152\n"
"s_add_u32 s48, s48, 4096\n"
"v_mfma_f32_16x16x32_bf16 v[40:43], a[136:139], a[36:39], v[40:43]\n"
"ds_read_b64_tr_b16 a[206:207], v219 offset:53248\n"
"v_mfma_f32_16x16x32_bf16 v[40:43], a[168:171], a[44:47], v[40:43]\n"
"ds_read_b64_tr_b16 a[236:237], v219 offset:57344\n"
"v_mfma_f32_16x16x32_bf16 v[12:15], a[140:143], a[32:35], v[12:15]\n"
"ds_read_b64_tr_b16 a[238:239], v219 offset:61440\n"
"buffer_load_dwordx4 a[100:103], v248, s[16:19], s48 offen offset:0 sc1\n"
"v_mfma_f32_16x16x32_bf16 v[12:15], a[172:175], a[40:43], v[12:15]\n"
"ds_read_b64_tr_b16 a[208:209], v216 offset:50176\n"
"v_mfma_f32_16x16x32_bf16 v[44:47], a[140:143], a[36:39], v[44:47]\n"
"ds_read_b64_tr_b16 a[210:211], v216 offset:54272\n"
"ds_read_b64_tr_b16 a[240:241], v216 offset:58368\n"
"v_mfma_f32_16x16x32_bf16 v[44:47], a[172:175], a[44:47], v[44:47]\n"
"ds_read_b64_tr_b16 a[242:243], v216 offset:62464\n"
"v_mfma_f32_16x16x32_bf16 v[16:19], a[144:147], a[32:35], v[16:19]\n"
"ds_read_b64_tr_b16 a[212:213], v217 offset:50176\n"
"buffer_load_dwordx4 a[108:111], v248, s[16:19], s48 offen offset:1024 sc1\n"
"v_mfma_f32_16x16x32_bf16 v[16:19], a[176:179], a[40:43], v[16:19]\n"
"ds_read_b64_tr_b16 a[214:215], v217 offset:54272\n"
"v_mfma_f32_16x16x32_bf16 v[48:51], a[144:147], a[36:39], v[48:51]\n"
"ds_read_b64_tr_b16 a[244:245], v217 offset:58368\n"
"ds_read_b64_tr_b16 a[246:247], v217 offset:62464\n"
"v_mfma_f32_16x16x32_bf16 v[48:51], a[176:179], a[44:47], v[48:51]\n"
"ds_read_b64_tr_b16 a[216:217], v218 offset:50176\n"
"buffer_load_dwordx4 a[112:115], v248, s[16:19], s47 offen offset:2048 sc1\n"
"v_mfma_f32_16x16x32_bf16 v[20:23], a[148:151], a[32:35], v[20:23]\n"
"ds_read_b64_tr_b16 a[218:219], v218 offset:54272\n"
"v_mfma_f32_16x16x32_bf16 v[20:23], a[180:183], a[40:43], v[20:23]\n"
"ds_read_b64_tr_b16 a[248:249], v218 offset:58368\n"
"ds_read_b64_tr_b16 a[250:251], v218 offset:62464\n"
"v_mfma_f32_16x16x32_bf16 v[52:55], a[148:151], a[36:39], v[52:55]\n"
"ds_read_b64_tr_b16 a[220:221], v219 offset:50176\n"
"buffer_load_dwordx4 a[120:123], v248, s[16:19], s47 offen offset:3072 sc1\n"
"v_mfma_f32_16x16x32_bf16 v[52:55], a[180:183], a[44:47], v[52:55]\n"
"ds_read_b64_tr_b16 a[222:223], v219 offset:54272\n"
"v_mfma_f32_16x16x32_bf16 v[24:27], a[152:155], a[32:35], v[24:27]\n"
"ds_read_b64_tr_b16 a[252:253], v219 offset:58368\n"
"v_mfma_f32_16x16x32_bf16 v[24:27], a[184:187], a[40:43], v[24:27]\n"
"ds_read_b64_tr_b16 a[254:255], v219 offset:62464\n"
"buffer_load_dwordx4 a[116:119], v248, s[16:19], s48 offen offset:2048 sc1\n"
"v_mfma_f32_16x16x32_bf16 v[56:59], a[152:155], a[36:39], v[56:59]\n"
"v_mfma_f32_16x16x32_bf16 v[56:59], a[184:187], a[44:47], v[56:59]\n"
"v_mfma_f32_16x16x32_bf16 v[28:31], a[156:159], a[32:35], v[28:31]\n"
"buffer_load_dwordx4 a[124:127], v248, s[16:19], s48 offen offset:3072 sc1\n"
"v_mfma_f32_16x16x32_bf16 v[28:31], a[188:191], a[40:43], v[28:31]\n"
"v_mfma_f32_16x16x32_bf16 v[60:63], a[156:159], a[36:39], v[60:63]\n"
"v_mfma_f32_16x16x32_bf16 v[60:63], a[188:191], a[44:47], v[60:63]\n"
"s_waitcnt vmcnt(23) lgkmcnt(0)\n"
"v_mfma_f32_16x16x32_bf16 v[0:3], a[192:195], a[48:51], v[0:3]\n"
"ds_read_b64_tr_b16 a[128:129], v220 offset:32768\n"
"ds_read_b64_tr_b16 a[130:131], v220 offset:36864\n"
"v_mfma_f32_16x16x32_bf16 v[0:3], a[224:227], a[56:59], v[0:3]\n"
"s_add_u32 s47, s47, 4096\n"
"ds_read_b64_tr_b16 a[160:161], v220 offset:40960\n"
"v_mfma_f32_16x16x32_bf16 v[32:35], a[192:195], a[52:55], v[32:35]\n"
"ds_read_b64_tr_b16 a[162:163], v220 offset:45056\n"
"v_lshrrev_b32 v253, 5, v255\n"
"v_mfma_f32_16x16x32_bf16 v[32:35], a[224:227], a[60:63], v[32:35]\n"
"v_lshlrev_b32 v176, 10, v253\n"
"v_bfe_u32 v253, v255, 2, 3\n"
"v_mfma_f32_16x16x32_bf16 v[4:7], a[196:199], a[48:51], v[4:7]\n"
"ds_read_b64_tr_b16 a[132:133], v221 offset:32768\n"
"buffer_load_dwordx4 a[0:3], v248, s[16:19], s47 offen offset:0 sc1\n"
"v_mfma_f32_16x16x32_bf16 v[4:7], a[228:231], a[56:59], v[4:7]\n"
"v_lshl_add_u32 v176, v253, 7, v176\n"
"v_mfma_f32_16x16x32_bf16 v[36:39], a[196:199], a[52:55], v[36:39]\n"
"v_bfe_u32 v253, v255, 3, 2\n"
"ds_read_b64_tr_b16 a[134:135], v221 offset:36864\n"
"v_mfma_f32_16x16x32_bf16 v[36:39], a[228:231], a[60:63], v[36:39]\n"
"ds_read_b64_tr_b16 a[164:165], v221 offset:40960\n"
"v_lshlrev_b32 v253, 1, v253\n"
"v_mfma_f32_16x16x32_bf16 v[8:11], a[200:203], a[48:51], v[8:11]\n"
"v_bfe_u32 v252, v255, 1, 1\n"
"ds_read_b64_tr_b16 a[166:167], v221 offset:45056\n"
"v_mfma_f32_16x16x32_bf16 v[8:11], a[232:235], a[56:59], v[8:11]\n"
"v_xor_b32 v253, v253, v252\n"
"v_lshl_add_u32 v176, v253, 4, v176\n"
"v_mfma_f32_16x16x32_bf16 v[40:43], a[200:203], a[52:55], v[40:43]\n"
"ds_read_b64_tr_b16 a[136:137], v222 offset:32768\n"
"buffer_load_dwordx4 a[8:11], v248, s[16:19], s47 offen offset:1024 sc1\n"
"v_mfma_f32_16x16x32_bf16 v[40:43], a[232:235], a[60:63], v[40:43]\n"
"v_and_b32 v252, 1, v255\n"
"v_mfma_f32_16x16x32_bf16 v[12:15], a[204:207], a[48:51], v[12:15]\n"
"v_lshl_add_u32 v176, v252, 3, v176\n"
"ds_read_b64_tr_b16 a[138:139], v222 offset:36864\n"
"v_mfma_f32_16x16x32_bf16 v[12:15], a[236:239], a[56:59], v[12:15]\n"
"v_add_u32 v176, s53, v176\n"
"v_xor_b32 v177, 32, v176\n"
"v_mfma_f32_16x16x32_bf16 v[44:47], a[204:207], a[52:55], v[44:47]\n"
"ds_read_b64_tr_b16 a[168:169], v222 offset:40960\n"
"ds_read_b64_tr_b16 a[170:171], v222 offset:45056\n"
"v_mfma_f32_16x16x32_bf16 v[44:47], a[236:239], a[60:63], v[44:47]\n"
"v_xor_b32 v178, 64, v176\n"
"v_xor_b32 v179, 96, v176\n"
"v_mfma_f32_16x16x32_bf16 v[16:19], a[208:211], a[48:51], v[16:19]\n"
"ds_read_b64_tr_b16 a[140:141], v223 offset:32768\n"
"s_add_u32 s48, s48, 4096\n"
"v_mfma_f32_16x16x32_bf16 v[16:19], a[240:243], a[56:59], v[16:19]\n"
"v_add_u32 v176, 0x10000, v176\n"
"v_add_u32 v177, 0x10000, v177\n"
"v_mfma_f32_16x16x32_bf16 v[48:51], a[208:211], a[52:55], v[48:51]\n"
"ds_read_b64_tr_b16 a[142:143], v223 offset:36864\n"
"v_add_u32 v178, 0x10000, v178\n"
"v_mfma_f32_16x16x32_bf16 v[48:51], a[240:243], a[60:63], v[48:51]\n"
"v_add_u32 v179, 0x10000, v179\n"
"ds_read_b64_tr_b16 a[172:173], v223 offset:40960\n"
"v_mfma_f32_16x16x32_bf16 v[20:23], a[212:215], a[48:51], v[20:23]\n"
"ds_read_b64_tr_b16 a[174:175], v223 offset:45056\n"
"buffer_load_dwordx4 a[4:7], v248, s[16:19], s48 offen offset:0 sc1\n"
"v_mfma_f32_16x16x32_bf16 v[20:23], a[244:247], a[56:59], v[20:23]\n"
"ds_read_b64_tr_b16 a[144:145], v220 offset:33792\n"
"v_mfma_f32_16x16x32_bf16 v[52:55], a[212:215], a[52:55], v[52:55]\n"
"ds_read_b64_tr_b16 a[146:147], v220 offset:37888\n"
"ds_read_b64_tr_b16 a[176:177], v220 offset:41984\n"
"v_mfma_f32_16x16x32_bf16 v[52:55], a[244:247], a[60:63], v[52:55]\n"
"ds_read_b64_tr_b16 a[178:179], v220 offset:46080\n"
"ds_read_b64_tr_b16 a[148:149], v221 offset:33792\n"
"v_mfma_f32_16x16x32_bf16 v[24:27], a[216:219], a[48:51], v[24:27]\n"
"buffer_load_dwordx4 a[12:15], v248, s[16:19], s48 offen offset:1024 sc1\n"
"v_mfma_f32_16x16x32_bf16 v[24:27], a[248:251], a[56:59], v[24:27]\n"
"ds_read_b64_tr_b16 a[150:151], v221 offset:37888\n"
"ds_read_b64_tr_b16 a[180:181], v221 offset:41984\n"
"v_mfma_f32_16x16x32_bf16 v[56:59], a[216:219], a[52:55], v[56:59]\n"
"ds_read_b64_tr_b16 a[182:183], v221 offset:46080\n"
"ds_read_b64_tr_b16 a[152:153], v222 offset:33792\n"
"v_mfma_f32_16x16x32_bf16 v[56:59], a[248:251], a[60:63], v[56:59]\n"
"buffer_load_dwordx4 a[16:19], v248, s[16:19], s47 offen offset:2048 sc1\n"
"v_mfma_f32_16x16x32_bf16 v[28:31], a[220:223], a[48:51], v[28:31]\n"
"ds_read_b64_tr_b16 a[154:155], v222 offset:37888\n"
"ds_read_b64_tr_b16 a[184:185], v222 offset:41984\n"
"v_mfma_f32_16x16x32_bf16 v[28:31], a[252:255], a[56:59], v[28:31]\n"
"ds_read_b64_tr_b16 a[186:187], v222 offset:46080\n"
"ds_read_b64_tr_b16 a[156:157], v223 offset:33792\n"
"v_mfma_f32_16x16x32_bf16 v[60:63], a[220:223], a[52:55], v[60:63]\n"
"buffer_load_dwordx4 a[24:27], v248, s[16:19], s47 offen offset:3072 sc1\n"
"v_mfma_f32_16x16x32_bf16 v[60:63], a[252:255], a[60:63], v[60:63]\n"
"ds_read_b64_tr_b16 a[158:159], v223 offset:37888\n"
"ds_read_b64_tr_b16 a[188:189], v223 offset:41984\n"
"ds_read_b64_tr_b16 a[190:191], v223 offset:46080\n"
"buffer_load_dwordx4 a[20:23], v248, s[16:19], s48 offen offset:2048 sc1\n"
"buffer_load_dwordx4 a[28:31], v248, s[16:19], s48 offen offset:3072 sc1\n"
"s_waitcnt vmcnt(20) lgkmcnt(0)\n"
"v_mfma_f32_16x16x32_bf16 v[64:67], a[128:131], a[64:67], 0\n"
"ds_read_b64_tr_b16 a[192:193], v220 offset:49152\n"
"ds_read_b64_tr_b16 a[194:195], v220 offset:53248\n"
"v_mfma_f32_16x16x32_bf16 v[64:67], a[160:163], a[72:75], v[64:67]\n"
"s_add_u32 s47, s47, 4096\n"
"ds_read_b64_tr_b16 a[224:225], v220 offset:57344\n"
"v_mfma_f32_16x16x32_bf16 v[96:99], a[128:131], a[68:71], 0\n"
"ds_read_b64_tr_b16 a[226:227], v220 offset:61440\n"
"v_mfma_f32_16x16x32_bf16 v[96:99], a[160:163], a[76:79], v[96:99]\n"
"ds_read_b64_tr_b16 a[196:197], v221 offset:49152\n"
"buffer_load_dwordx4 a[32:35], v248, s[16:19], s47 offen offset:0 sc1\n"
"v_mfma_f32_16x16x32_bf16 v[68:71], a[132:135], a[64:67], 0\n"
"ds_read_b64_tr_b16 a[198:199], v221 offset:53248\n"
"v_mfma_f32_16x16x32_bf16 v[68:71], a[164:167], a[72:75], v[68:71]\n"
"ds_read_b64_tr_b16 a[228:229], v221 offset:57344\n"
"ds_read_b64_tr_b16 a[230:231], v221 offset:61440\n"
"v_mfma_f32_16x16x32_bf16 v[100:103], a[132:135], a[68:71], 0\n"
"ds_read_b64_tr_b16 a[200:201], v222 offset:49152\n"
"buffer_load_dwordx4 a[40:43], v248, s[16:19], s47 offen offset:1024 sc1\n"
"v_mfma_f32_16x16x32_bf16 v[100:103], a[164:167], a[76:79], v[100:103]\n"
"ds_read_b64_tr_b16 a[202:203], v222 offset:53248\n"
"v_mfma_f32_16x16x32_bf16 v[72:75], a[136:139], a[64:67], 0\n"
"ds_read_b64_tr_b16 a[232:233], v222 offset:57344\n"
"ds_read_b64_tr_b16 a[234:235], v222 offset:61440\n"
"v_mfma_f32_16x16x32_bf16 v[72:75], a[168:171], a[72:75], v[72:75]\n"
"ds_read_b64_tr_b16 a[204:205], v223 offset:49152\n"
"s_add_u32 s48, s48, 4096\n"
"v_mfma_f32_16x16x32_bf16 v[104:107], a[136:139], a[68:71], 0\n"
"ds_read_b64_tr_b16 a[206:207], v223 offset:53248\n"
"v_mfma_f32_16x16x32_bf16 v[104:107], a[168:171], a[76:79], v[104:107]\n"
"ds_read_b64_tr_b16 a[236:237], v223 offset:57344\n"
"v_mfma_f32_16x16x32_bf16 v[76:79], a[140:143], a[64:67], 0\n"
"ds_read_b64_tr_b16 a[238:239], v223 offset:61440\n"
"buffer_load_dwordx4 a[36:39], v248, s[16:19], s48 offen offset:0 sc1\n"
"v_mfma_f32_16x16x32_bf16 v[76:79], a[172:175], a[72:75], v[76:79]\n"
"ds_read_b64_tr_b16 a[208:209], v220 offset:50176\n"
"v_mfma_f32_16x16x32_bf16 v[108:111], a[140:143], a[68:71], 0\n"
"ds_read_b64_tr_b16 a[210:211], v220 offset:54272\n"
"ds_read_b64_tr_b16 a[240:241], v220 offset:58368\n"
"v_mfma_f32_16x16x32_bf16 v[108:111], a[172:175], a[76:79], v[108:111]\n"
"ds_read_b64_tr_b16 a[242:243], v220 offset:62464\n"
"v_mfma_f32_16x16x32_bf16 v[80:83], a[144:147], a[64:67], 0\n"
"ds_read_b64_tr_b16 a[212:213], v221 offset:50176\n"
"buffer_load_dwordx4 a[44:47], v248, s[16:19], s48 offen offset:1024 sc1\n"
"v_mfma_f32_16x16x32_bf16 v[80:83], a[176:179], a[72:75], v[80:83]\n"
"ds_read_b64_tr_b16 a[214:215], v221 offset:54272\n"
"v_mfma_f32_16x16x32_bf16 v[112:115], a[144:147], a[68:71], 0\n"
"ds_read_b64_tr_b16 a[244:245], v221 offset:58368\n"
"ds_read_b64_tr_b16 a[246:247], v221 offset:62464\n"
"v_mfma_f32_16x16x32_bf16 v[112:115], a[176:179], a[76:79], v[112:115]\n"
"ds_read_b64_tr_b16 a[216:217], v222 offset:50176\n"
"buffer_load_dwordx4 a[48:51], v248, s[16:19], s47 offen offset:2048 sc1\n"
"v_mfma_f32_16x16x32_bf16 v[84:87], a[148:151], a[64:67], 0\n"
"ds_read_b64_tr_b16 a[218:219], v222 offset:54272\n"
"v_mfma_f32_16x16x32_bf16 v[84:87], a[180:183], a[72:75], v[84:87]\n"
"ds_read_b64_tr_b16 a[248:249], v222 offset:58368\n"
"ds_read_b64_tr_b16 a[250:251], v222 offset:62464\n"
"v_mfma_f32_16x16x32_bf16 v[116:119], a[148:151], a[68:71], 0\n"
"ds_read_b64_tr_b16 a[220:221], v223 offset:50176\n"
"buffer_load_dwordx4 a[56:59], v248, s[16:19], s47 offen offset:3072 sc1\n"
"v_mfma_f32_16x16x32_bf16 v[116:119], a[180:183], a[76:79], v[116:119]\n"
"ds_read_b64_tr_b16 a[222:223], v223 offset:54272\n"
"v_mfma_f32_16x16x32_bf16 v[88:91], a[152:155], a[64:67], 0\n"
"ds_read_b64_tr_b16 a[252:253], v223 offset:58368\n"
"v_mfma_f32_16x16x32_bf16 v[88:91], a[184:187], a[72:75], v[88:91]\n"
"ds_read_b64_tr_b16 a[254:255], v223 offset:62464\n"
"buffer_load_dwordx4 a[52:55], v248, s[16:19], s48 offen offset:2048 sc1\n"
"v_mfma_f32_16x16x32_bf16 v[120:123], a[152:155], a[68:71], 0\n"
"v_mfma_f32_16x16x32_bf16 v[120:123], a[184:187], a[76:79], v[120:123]\n"
"v_mfma_f32_16x16x32_bf16 v[92:95], a[156:159], a[64:67], 0\n"
"buffer_load_dwordx4 a[60:63], v248, s[16:19], s48 offen offset:3072 sc1\n"
"v_mfma_f32_16x16x32_bf16 v[92:95], a[188:191], a[72:75], v[92:95]\n"
"v_mfma_f32_16x16x32_bf16 v[124:127], a[156:159], a[68:71], 0\n"
"v_mfma_f32_16x16x32_bf16 v[124:127], a[188:191], a[76:79], v[124:127]\n"
"s_waitcnt vmcnt(24) lgkmcnt(0)\n"
"v_mfma_f32_16x16x32_bf16 v[64:67], a[192:195], a[80:83], v[64:67]\n"
"ds_read_b64_tr_b16 a[128:129], v224 offset:0\n"
"ds_read_b64_tr_b16 a[130:131], v224 offset:4096\n"
"v_mfma_f32_16x16x32_bf16 v[64:67], a[224:227], a[88:91], v[64:67]\n"
"v_lshrrev_b32 v235, 4, v255\n"
"v_mul_f32 v0, v0, v16\n"
"v_mfma_f32_16x16x32_bf16 v[96:99], a[192:195], a[84:87], v[96:99]\n"
"ds_read_b64_tr_b16 a[160:161], v224 offset:8192\n"
"v_mul_f32 v1, v1, v17\n"
"v_mfma_f32_16x16x32_bf16 v[96:99], a[224:227], a[92:95], v[96:99]\n"
"ds_read_b64_tr_b16 a[162:163], v224 offset:12288\n"
"v_mul_f32 v2, v2, v18\n"
"v_mfma_f32_16x16x32_bf16 v[68:71], a[196:199], a[80:83], v[68:71]\n"
"ds_read_b64_tr_b16 a[132:133], v225 offset:0\n"
"v_lshlrev_b32 v235, 4, v235\n"
"v_mfma_f32_16x16x32_bf16 v[68:71], a[228:231], a[88:91], v[68:71]\n"
"v_mul_f32 v3, v3, v19\n"
"ds_read_b64_tr_b16 a[134:135], v225 offset:4096\n"
"v_mfma_f32_16x16x32_bf16 v[100:103], a[196:199], a[84:87], v[100:103]\n"
"ds_read_b64_tr_b16 a[164:165], v225 offset:8192\n"
"v_cvt_pk_bf16_f32 v128, v0, v1\n"
"v_mfma_f32_16x16x32_bf16 v[100:103], a[228:231], a[92:95], v[100:103]\n"
"ds_read_b64_tr_b16 a[166:167], v225 offset:12288\n"
"ds_read_b64_tr_b16 a[136:137], v226 offset:0\n"
"v_mfma_f32_16x16x32_bf16 v[72:75], a[200:203], a[80:83], v[72:75]\n"
"v_cvt_pk_bf16_f32 v129, v2, v3\n"
"ds_read_b64_tr_b16 a[138:139], v226 offset:4096\n"
"v_mfma_f32_16x16x32_bf16 v[72:75], a[232:235], a[88:91], v[72:75]\n"
"s_bfe_u32 s44, s84, 0x30003\n"
"v_mul_f32 v4, v4, v20\n"
"v_mfma_f32_16x16x32_bf16 v[104:107], a[200:203], a[84:87], v[104:107]\n"
"ds_read_b64_tr_b16 a[168:169], v226 offset:8192\n"
"ds_read_b64_tr_b16 a[170:171], v226 offset:12288\n"
"v_mfma_f32_16x16x32_bf16 v[104:107], a[232:235], a[92:95], v[104:107]\n"
"v_mul_f32 v5, v5, v21\n"
"ds_read_b64_tr_b16 a[140:141], v227 offset:0\n"
"v_mfma_f32_16x16x32_bf16 v[76:79], a[204:207], a[80:83], v[76:79]\n"
"v_mul_f32 v6, v6, v22\n"
"ds_read_b64_tr_b16 a[142:143], v227 offset:4096\n"
"v_mfma_f32_16x16x32_bf16 v[76:79], a[236:239], a[88:91], v[76:79]\n"
"s_lshl_b32 s44, s44, 8\n"
"ds_read_b64_tr_b16 a[172:173], v227 offset:8192\n"
"v_mfma_f32_16x16x32_bf16 v[108:111], a[204:207], a[84:87], v[108:111]\n"
"v_mul_f32 v7, v7, v23\n"
"ds_read_b64_tr_b16 a[174:175], v227 offset:12288\n"
"v_mfma_f32_16x16x32_bf16 v[108:111], a[236:239], a[92:95], v[108:111]\n"
"v_cvt_pk_bf16_f32 v130, v4, v5\n"
"ds_read_b64_tr_b16 a[144:145], v224 offset:1024\n"
"v_mfma_f32_16x16x32_bf16 v[80:83], a[208:211], a[80:83], v[80:83]\n"
"ds_read_b64_tr_b16 a[146:147], v224 offset:5120\n"
"v_cvt_pk_bf16_f32 v131, v6, v7\n"
"v_mfma_f32_16x16x32_bf16 v[80:83], a[240:243], a[88:91], v[80:83]\n"
"ds_read_b64_tr_b16 a[176:177], v224 offset:9216\n"
"s_add_u32 s44, s44, 0x3800\n"
"v_mfma_f32_16x16x32_bf16 v[112:115], a[208:211], a[84:87], v[112:115]\n"
"buffer_store_dwordx4 v[128:131], v248, s[36:39], s49 offen offset:0 sc1\n"
"v_mfma_f32_16x16x32_bf16 v[112:115], a[240:243], a[92:95], v[112:115]\n"
"ds_read_b64_tr_b16 a[178:179], v224 offset:13312\n"
"ds_read_b64_tr_b16 a[148:149], v225 offset:1024\n"
"v_mfma_f32_16x16x32_bf16 v[84:87], a[212:215], a[80:83], v[84:87]\n"
"v_mul_f32 v8, v8, v24\n"
"ds_read_b64_tr_b16 a[150:151], v225 offset:5120\n"
"v_mfma_f32_16x16x32_bf16 v[84:87], a[244:247], a[88:91], v[84:87]\n"
"ds_read_b64_tr_b16 a[180:181], v225 offset:9216\n"
"buffer_load_dwordx4 v[160:163], v235, s[88:91], s44 offen offset:0 sc1\n"
"v_mfma_f32_16x16x32_bf16 v[116:119], a[212:215], a[84:87], v[116:119]\n"
"v_mul_f32 v9, v9, v25\n"
"v_mfma_f32_16x16x32_bf16 v[116:119], a[244:247], a[92:95], v[116:119]\n"
"ds_read_b64_tr_b16 a[182:183], v225 offset:13312\n"
"v_mul_f32 v10, v10, v26\n"
"v_mfma_f32_16x16x32_bf16 v[88:91], a[216:219], a[80:83], v[88:91]\n"
"ds_read_b64_tr_b16 a[152:153], v226 offset:1024\n"
"v_mul_f32 v11, v11, v27\n"
"v_mfma_f32_16x16x32_bf16 v[88:91], a[248:251], a[88:91], v[88:91]\n"
"ds_read_b64_tr_b16 a[154:155], v226 offset:5120\n"
"v_cvt_pk_bf16_f32 v132, v8, v9\n"
"v_mfma_f32_16x16x32_bf16 v[120:123], a[216:219], a[84:87], v[120:123]\n"
"ds_read_b64_tr_b16 a[184:185], v226 offset:9216\n"
"ds_read_b64_tr_b16 a[186:187], v226 offset:13312\n"
"v_mfma_f32_16x16x32_bf16 v[120:123], a[248:251], a[92:95], v[120:123]\n"
"buffer_load_dwordx4 v[164:167], v235, s[88:91], s44 offen offset:64 sc1\n"
"v_mfma_f32_16x16x32_bf16 v[92:95], a[220:223], a[80:83], v[92:95]\n"
"ds_read_b64_tr_b16 a[156:157], v227 offset:1024\n"
"v_cvt_pk_bf16_f32 v133, v10, v11\n"
"v_mfma_f32_16x16x32_bf16 v[92:95], a[252:255], a[88:91], v[92:95]\n"
"ds_read_b64_tr_b16 a[158:159], v227 offset:5120\n"
"v_mul_f32 v12, v12, v28\n"
"v_mfma_f32_16x16x32_bf16 v[124:127], a[220:223], a[84:87], v[124:127]\n"
"ds_read_b64_tr_b16 a[188:189], v227 offset:9216\n"
"buffer_load_dwordx4 v[168:171], v235, s[88:91], s44 offen offset:128 sc1\n"
"v_mfma_f32_16x16x32_bf16 v[124:127], a[252:255], a[92:95], v[124:127]\n"
"v_mul_f32 v13, v13, v29\n"
"ds_read_b64_tr_b16 a[190:191], v227 offset:13312\n"
"v_mul_f32 v14, v14, v30\n"
"v_mul_f32 v15, v15, v31\n"
"buffer_load_dwordx4 v[172:175], v235, s[88:91], s44 offen offset:192 sc1\n"
"v_cvt_pk_bf16_f32 v134, v12, v13\n"
"v_cvt_pk_bf16_f32 v135, v14, v15\n"
"buffer_store_dwordx4 v[132:135], v248, s[36:39], s49 offen offset:1024 sc1\n"
"s_waitcnt vmcnt(26) lgkmcnt(0)\n"
"s_barrier\n"
"v_mfma_f32_16x16x32_bf16 v[64:67], a[128:131], a[96:99], v[64:67]\n"
"ds_read_b64_tr_b16 a[192:193], v224 offset:16384\n"
"ds_read_b64_tr_b16 a[194:195], v224 offset:20480\n"
"v_mfma_f32_16x16x32_bf16 v[64:67], a[160:163], a[104:107], v[64:67]\n"
"s_add_u32 m0, s53, 98304\n"
"v_mul_f32 v32, v32, v48\n"
"v_mfma_f32_16x16x32_bf16 v[96:99], a[128:131], a[100:103], v[96:99]\n"
"ds_read_b64_tr_b16 a[224:225], v224 offset:24576\n"
"v_mul_f32 v33, v33, v49\n"
"v_mfma_f32_16x16x32_bf16 v[96:99], a[160:163], a[108:111], v[96:99]\n"
"ds_read_b64_tr_b16 a[226:227], v224 offset:28672\n"
"v_mul_f32 v34, v34, v50\n"
"v_mfma_f32_16x16x32_bf16 v[68:71], a[132:135], a[96:99], v[68:71]\n"
"ds_read_b64_tr_b16 a[196:197], v225 offset:16384\n"
"v_mul_f32 v35, v35, v51\n"
"v_mfma_f32_16x16x32_bf16 v[68:71], a[164:167], a[104:107], v[68:71]\n"
"ds_read_b64_tr_b16 a[198:199], v225 offset:20480\n"
"ds_read_b64_tr_b16 a[228:229], v225 offset:24576\n"
"v_mfma_f32_16x16x32_bf16 v[100:103], a[132:135], a[100:103], v[100:103]\n"
"v_cvt_pk_bf16_f32 v136, v32, v33\n"
"ds_read_b64_tr_b16 a[230:231], v225 offset:28672\n"
"v_mfma_f32_16x16x32_bf16 v[100:103], a[164:167], a[108:111], v[100:103]\n"
"s_mov_b32 s92, 65536\n"
"v_cvt_pk_bf16_f32 v137, v34, v35\n"
"v_mfma_f32_16x16x32_bf16 v[72:75], a[136:139], a[96:99], v[72:75]\n"
"ds_read_b64_tr_b16 a[200:201], v226 offset:16384\n"
"ds_read_b64_tr_b16 a[202:203], v226 offset:20480\n"
"v_mfma_f32_16x16x32_bf16 v[72:75], a[168:171], a[104:107], v[72:75]\n"
"v_mul_f32 v36, v36, v52\n"
"ds_read_b64_tr_b16 a[232:233], v226 offset:24576\n"
"v_mfma_f32_16x16x32_bf16 v[104:107], a[136:139], a[100:103], v[104:107]\n"
"ds_read_b64_tr_b16 a[234:235], v226 offset:28672\n"
"v_mul_f32 v37, v37, v53\n"
"v_mfma_f32_16x16x32_bf16 v[104:107], a[168:171], a[108:111], v[104:107]\n"
"ds_read_b64_tr_b16 a[204:205], v227 offset:16384\n"
"v_mul_f32 v38, v38, v54\n"
"v_mfma_f32_16x16x32_bf16 v[76:79], a[140:143], a[96:99], v[76:79]\n"
"ds_read_b64_tr_b16 a[206:207], v227 offset:20480\n"
"buffer_load_dwordx4 v188, s[20:23], s92 offen offset:0 lds\n"
"v_mfma_f32_16x16x32_bf16 v[76:79], a[172:175], a[104:107], v[76:79]\n"
"v_mul_f32 v39, v39, v55\n"
"v_mfma_f32_16x16x32_bf16 v[108:111], a[140:143], a[100:103], v[108:111]\n"
"ds_read_b64_tr_b16 a[236:237], v227 offset:24576\n"
"v_cvt_pk_bf16_f32 v138, v36, v37\n"
"v_mfma_f32_16x16x32_bf16 v[108:111], a[172:175], a[108:111], v[108:111]\n"
"ds_read_b64_tr_b16 a[238:239], v227 offset:28672\n"
"ds_read_b64_tr_b16 a[208:209], v224 offset:17408\n"
"v_mfma_f32_16x16x32_bf16 v[80:83], a[144:147], a[96:99], v[80:83]\n"
"ds_read_b64_tr_b16 a[210:211], v224 offset:21504\n"
"v_cvt_pk_bf16_f32 v139, v38, v39\n"
"v_mfma_f32_16x16x32_bf16 v[80:83], a[176:179], a[104:107], v[80:83]\n"
"ds_read_b64_tr_b16 a[240:241], v224 offset:25600\n"
"s_add_u32 s49, s49, 57344\n"
"v_mfma_f32_16x16x32_bf16 v[112:115], a[144:147], a[100:103], v[112:115]\n"
"ds_read_b64_tr_b16 a[242:243], v224 offset:29696\n"
"buffer_store_dwordx4 v[136:139], v248, s[36:39], s49 offen offset:0 sc1\n"
"v_mfma_f32_16x16x32_bf16 v[112:115], a[176:179], a[108:111], v[112:115]\n"
"ds_read_b64_tr_b16 a[212:213], v225 offset:17408\n"
"v_mfma_f32_16x16x32_bf16 v[84:87], a[148:151], a[96:99], v[84:87]\n"
"buffer_load_dwordx4 v189, s[20:23], s92 offen offset:1024 lds\n"
"v_mfma_f32_16x16x32_bf16 v[84:87], a[180:183], a[104:107], v[84:87]\n"
"v_mul_f32 v40, v40, v56\n"
"ds_read_b64_tr_b16 a[214:215], v225 offset:21504\n"
"v_mfma_f32_16x16x32_bf16 v[116:119], a[148:151], a[100:103], v[116:119]\n"
"ds_read_b64_tr_b16 a[244:245], v225 offset:25600\n"
"v_mul_f32 v41, v41, v57\n"
"v_mfma_f32_16x16x32_bf16 v[116:119], a[180:183], a[108:111], v[116:119]\n"
"ds_read_b64_tr_b16 a[246:247], v225 offset:29696\n"
"v_mul_f32 v42, v42, v58\n"
"v_mfma_f32_16x16x32_bf16 v[88:91], a[152:155], a[96:99], v[88:91]\n"
"ds_read_b64_tr_b16 a[216:217], v226 offset:17408\n"
"ds_read_b64_tr_b16 a[218:219], v226 offset:21504\n"
"v_mfma_f32_16x16x32_bf16 v[88:91], a[184:187], a[104:107], v[88:91]\n"
"v_mul_f32 v43, v43, v59\n"
"ds_read_b64_tr_b16 a[248:249], v226 offset:25600\n"
"v_mfma_f32_16x16x32_bf16 v[120:123], a[152:155], a[100:103], v[120:123]\n"
"ds_read_b64_tr_b16 a[250:251], v226 offset:29696\n"
"buffer_load_dwordx4 v190, s[20:23], s92 offen offset:2048 lds\n"
"v_mfma_f32_16x16x32_bf16 v[120:123], a[184:187], a[108:111], v[120:123]\n"
"v_cvt_pk_bf16_f32 v140, v40, v41\n"
"v_mfma_f32_16x16x32_bf16 v[92:95], a[156:159], a[96:99], v[92:95]\n"
"ds_read_b64_tr_b16 a[220:221], v227 offset:17408\n"
"v_cvt_pk_bf16_f32 v141, v42, v43\n"
"v_mfma_f32_16x16x32_bf16 v[92:95], a[188:191], a[104:107], v[92:95]\n"
"ds_read_b64_tr_b16 a[222:223], v227 offset:21504\n"
"v_mul_f32 v44, v44, v60\n"
"v_mfma_f32_16x16x32_bf16 v[124:127], a[156:159], a[100:103], v[124:127]\n"
"ds_read_b64_tr_b16 a[252:253], v227 offset:25600\n"
"v_mul_f32 v45, v45, v61\n"
"v_mfma_f32_16x16x32_bf16 v[124:127], a[188:191], a[108:111], v[124:127]\n"
"ds_read_b64_tr_b16 a[254:255], v227 offset:29696\n"
"buffer_load_dwordx4 v191, s[20:23], s92 offen offset:3072 lds\n"
"v_mul_f32 v46, v46, v62\n"
"v_mul_f32 v47, v47, v63\n"
"v_cvt_pk_bf16_f32 v142, v44, v45\n"
"v_cvt_pk_bf16_f32 v143, v46, v47\n"
"buffer_store_dwordx4 v[140:143], v248, s[36:39], s49 offen offset:1024 sc1\n"
"s_waitcnt vmcnt(28) lgkmcnt(0)\n"
"v_mfma_f32_16x16x32_bf16 v[64:67], a[192:195], a[112:115], v[64:67]\n"
"ds_read_b64_tr_b16 a[128:129], v216 offset:0\n"
"ds_read_b64_tr_b16 a[130:131], v216 offset:4096\n"
"v_mfma_f32_16x16x32_bf16 v[64:67], a[224:227], a[120:123], v[64:67]\n"
"s_add_u32 m0, s53, 102400\n"
"ds_read_b64_tr_b16 a[160:161], v216 offset:8192\n"
"v_mfma_f32_16x16x32_bf16 v[96:99], a[192:195], a[116:119], v[96:99]\n"
"ds_read_b64_tr_b16 a[162:163], v216 offset:12288\n"
"v_mfma_f32_16x16x32_bf16 v[96:99], a[224:227], a[124:127], v[96:99]\n"
"ds_read_b64_tr_b16 a[132:133], v217 offset:0\n"
"v_mfma_f32_16x16x32_bf16 v[68:71], a[196:199], a[112:115], v[68:71]\n"
"ds_read_b64_tr_b16 a[134:135], v217 offset:4096\n"
"ds_read_b64_tr_b16 a[164:165], v217 offset:8192\n"
"v_mfma_f32_16x16x32_bf16 v[68:71], a[228:231], a[120:123], v[68:71]\n"
"ds_read_b64_tr_b16 a[166:167], v217 offset:12288\n"
"s_mov_b32 s92, 98304\n"
"v_mfma_f32_16x16x32_bf16 v[100:103], a[196:199], a[116:119], v[100:103]\n"
"ds_read_b64_tr_b16 a[136:137], v218 offset:0\n"
"v_mfma_f32_16x16x32_bf16 v[100:103], a[228:231], a[124:127], v[100:103]\n"
"ds_read_b64_tr_b16 a[138:139], v218 offset:4096\n"
"v_mfma_f32_16x16x32_bf16 v[72:75], a[200:203], a[112:115], v[72:75]\n"
"ds_read_b64_tr_b16 a[168:169], v218 offset:8192\n"
"ds_read_b64_tr_b16 a[170:171], v218 offset:12288\n"
"v_mfma_f32_16x16x32_bf16 v[72:75], a[232:235], a[120:123], v[72:75]\n"
"ds_read_b64_tr_b16 a[140:141], v219 offset:0\n"
"v_mfma_f32_16x16x32_bf16 v[104:107], a[200:203], a[116:119], v[104:107]\n"
"ds_read_b64_tr_b16 a[142:143], v219 offset:4096\n"
"buffer_load_dwordx4 v188, s[20:23], s92 offen offset:0 lds\n"
"v_mfma_f32_16x16x32_bf16 v[104:107], a[232:235], a[124:127], v[104:107]\n"
"ds_read_b64_tr_b16 a[172:173], v219 offset:8192\n"
"v_mfma_f32_16x16x32_bf16 v[76:79], a[204:207], a[112:115], v[76:79]\n"
"ds_read_b64_tr_b16 a[174:175], v219 offset:12288\n"
"v_mfma_f32_16x16x32_bf16 v[76:79], a[236:239], a[120:123], v[76:79]\n"
"ds_read_b64_tr_b16 a[144:145], v216 offset:1024\n"
"ds_read_b64_tr_b16 a[146:147], v216 offset:5120\n"
"v_mfma_f32_16x16x32_bf16 v[108:111], a[204:207], a[116:119], v[108:111]\n"
"ds_read_b64_tr_b16 a[176:177], v216 offset:9216\n"
"v_mfma_f32_16x16x32_bf16 v[108:111], a[236:239], a[124:127], v[108:111]\n"
"ds_read_b64_tr_b16 a[178:179], v216 offset:13312\n"
"v_mfma_f32_16x16x32_bf16 v[80:83], a[208:211], a[112:115], v[80:83]\n"
"ds_read_b64_tr_b16 a[148:149], v217 offset:1024\n"
"buffer_load_dwordx4 v189, s[20:23], s92 offen offset:1024 lds\n"
"v_mfma_f32_16x16x32_bf16 v[80:83], a[240:243], a[120:123], v[80:83]\n"
"ds_read_b64_tr_b16 a[150:151], v217 offset:5120\n"
"v_mfma_f32_16x16x32_bf16 v[112:115], a[208:211], a[116:119], v[112:115]\n"
"ds_read_b64_tr_b16 a[180:181], v217 offset:9216\n"
"ds_read_b64_tr_b16 a[182:183], v217 offset:13312\n"
"v_mfma_f32_16x16x32_bf16 v[112:115], a[240:243], a[124:127], v[112:115]\n"
"ds_read_b64_tr_b16 a[152:153], v218 offset:1024\n"
"v_mfma_f32_16x16x32_bf16 v[84:87], a[212:215], a[112:115], v[84:87]\n"
"ds_read_b64_tr_b16 a[154:155], v218 offset:5120\n"
"v_mfma_f32_16x16x32_bf16 v[84:87], a[244:247], a[120:123], v[84:87]\n"
"ds_read_b64_tr_b16 a[184:185], v218 offset:9216\n"
"ds_read_b64_tr_b16 a[186:187], v218 offset:13312\n"
"v_mfma_f32_16x16x32_bf16 v[116:119], a[212:215], a[116:119], v[116:119]\n"
"buffer_load_dwordx4 v190, s[20:23], s92 offen offset:2048 lds\n"
"v_mfma_f32_16x16x32_bf16 v[116:119], a[244:247], a[124:127], v[116:119]\n"
"ds_read_b64_tr_b16 a[156:157], v219 offset:1024\n"
"ds_read_b64_tr_b16 a[158:159], v219 offset:5120\n"
"v_mfma_f32_16x16x32_bf16 v[88:91], a[216:219], a[112:115], v[88:91]\n"
"ds_read_b64_tr_b16 a[188:189], v219 offset:9216\n"
"v_mfma_f32_16x16x32_bf16 v[88:91], a[248:251], a[120:123], v[88:91]\n"
"ds_read_b64_tr_b16 a[190:191], v219 offset:13312\n"
"v_mfma_f32_16x16x32_bf16 v[120:123], a[216:219], a[116:119], v[120:123]\n"
"buffer_load_dwordx4 v191, s[20:23], s92 offen offset:3072 lds\n"
"v_mfma_f32_16x16x32_bf16 v[120:123], a[248:251], a[124:127], v[120:123]\n"
"v_mfma_f32_16x16x32_bf16 v[92:95], a[220:223], a[112:115], v[92:95]\n"
"v_mfma_f32_16x16x32_bf16 v[92:95], a[252:255], a[120:123], v[92:95]\n"
"v_mfma_f32_16x16x32_bf16 v[124:127], a[220:223], a[116:119], v[124:127]\n"
"v_mfma_f32_16x16x32_bf16 v[124:127], a[252:255], a[124:127], v[124:127]\n"
"s_waitcnt vmcnt(28) lgkmcnt(0)\n"
"s_barrier\n"
"v_mfma_f32_16x16x32_bf16 v[64:67], a[128:131], a[0:3], v[64:67]\n"
"ds_read_b64_tr_b16 a[192:193], v216 offset:16384\n"
"ds_read_b64_tr_b16 a[194:195], v216 offset:20480\n"
"v_mfma_f32_16x16x32_bf16 v[64:67], a[160:163], a[8:11], v[64:67]\n"
"s_add_u32 m0, s53, 131072\n"
"ds_read_b64_tr_b16 a[224:225], v216 offset:24576\n"
"v_mfma_f32_16x16x32_bf16 v[96:99], a[128:131], a[4:7], v[96:99]\n"
"s_waitcnt vmcnt(28)\n"
"ds_read_b64_tr_b16 a[64:65], v176 offset:0\n"
"v_mfma_f32_16x16x32_bf16 v[96:99], a[160:163], a[12:15], v[96:99]\n"
"ds_read_b64_tr_b16 a[226:227], v216 offset:28672\n"
"ds_read_b64_tr_b16 a[66:67], v176 offset:2048\n"
"v_mfma_f32_16x16x32_bf16 v[68:71], a[132:135], a[0:3], v[68:71]\n"
"ds_read_b64_tr_b16 a[80:81], v176 offset:4096\n"
"ds_read_b64_tr_b16 a[196:197], v217 offset:16384\n"
"v_mfma_f32_16x16x32_bf16 v[68:71], a[164:167], a[8:11], v[68:71]\n"
"ds_read_b64_tr_b16 a[82:83], v176 offset:6144\n"
"ds_read_b64_tr_b16 a[68:69], v177 offset:0\n"
"v_mfma_f32_16x16x32_bf16 v[100:103], a[132:135], a[4:7], v[100:103]\n"
"ds_read_b64_tr_b16 a[198:199], v217 offset:20480\n"
"ds_read_b64_tr_b16 a[228:229], v217 offset:24576\n"
"v_mfma_f32_16x16x32_bf16 v[100:103], a[164:167], a[12:15], v[100:103]\n"
"ds_read_b64_tr_b16 a[70:71], v177 offset:2048\n"
"ds_read_b64_tr_b16 a[84:85], v177 offset:4096\n"
"v_mfma_f32_16x16x32_bf16 v[72:75], a[136:139], a[0:3], v[72:75]\n"
"ds_read_b64_tr_b16 a[230:231], v217 offset:28672\n"
"s_mov_b32 s92, 131072\n"
"v_mfma_f32_16x16x32_bf16 v[72:75], a[168:171], a[8:11], v[72:75]\n"
"ds_read_b64_tr_b16 a[86:87], v177 offset:6144\n"
"ds_read_b64_tr_b16 a[72:73], v178 offset:0\n"
"v_mfma_f32_16x16x32_bf16 v[104:107], a[136:139], a[4:7], v[104:107]\n"
"ds_read_b64_tr_b16 a[200:201], v218 offset:16384\n"
"ds_read_b64_tr_b16 a[74:75], v178 offset:2048\n"
"v_mfma_f32_16x16x32_bf16 v[104:107], a[168:171], a[12:15], v[104:107]\n"
"ds_read_b64_tr_b16 a[88:89], v178 offset:4096\n"
"ds_read_b64_tr_b16 a[202:203], v218 offset:20480\n"
"v_mfma_f32_16x16x32_bf16 v[76:79], a[140:143], a[0:3], v[76:79]\n"
"ds_read_b64_tr_b16 a[90:91], v178 offset:6144\n"
"ds_read_b64_tr_b16 a[76:77], v179 offset:0\n"
"v_mfma_f32_16x16x32_bf16 v[76:79], a[172:175], a[8:11], v[76:79]\n"
"ds_read_b64_tr_b16 a[232:233], v218 offset:24576\n"
"ds_read_b64_tr_b16 a[234:235], v218 offset:28672\n"
"v_mfma_f32_16x16x32_bf16 v[108:111], a[140:143], a[4:7], v[108:111]\n"
"ds_read_b64_tr_b16 a[78:79], v179 offset:2048\n"
"ds_read_b64_tr_b16 a[92:93], v179 offset:4096\n"
"v_mfma_f32_16x16x32_bf16 v[108:111], a[172:175], a[12:15], v[108:111]\n"
"ds_read_b64_tr_b16 a[204:205], v219 offset:16384\n"
"ds_read_b64_tr_b16 a[94:95], v179 offset:6144\n"
"v_mfma_f32_16x16x32_bf16 v[80:83], a[144:147], a[0:3], v[80:83]\n"
"ds_read_b64_tr_b16 a[206:207], v219 offset:20480\n"
"buffer_load_dwordx4 v188, s[20:23], s92 offen offset:0 lds\n"
"v_mfma_f32_16x16x32_bf16 v[80:83], a[176:179], a[8:11], v[80:83]\n"
"ds_read_b64_tr_b16 a[236:237], v219 offset:24576\n"
"v_mfma_f32_16x16x32_bf16 v[112:115], a[144:147], a[4:7], v[112:115]\n"
"ds_read_b64_tr_b16 a[238:239], v219 offset:28672\n"
"ds_read_b64_tr_b16 a[208:209], v216 offset:17408\n"
"v_mfma_f32_16x16x32_bf16 v[112:115], a[176:179], a[12:15], v[112:115]\n"
"ds_read_b64_tr_b16 a[210:211], v216 offset:21504\n"
"ds_read_b64_tr_b16 a[240:241], v216 offset:25600\n"
"v_mfma_f32_16x16x32_bf16 v[84:87], a[148:151], a[0:3], v[84:87]\n"
"ds_read_b64_tr_b16 a[242:243], v216 offset:29696\n"
"ds_read_b64_tr_b16 a[212:213], v217 offset:17408\n"
"v_mfma_f32_16x16x32_bf16 v[84:87], a[180:183], a[8:11], v[84:87]\n"
"buffer_load_dwordx4 v189, s[20:23], s92 offen offset:1024 lds\n"
"v_mfma_f32_16x16x32_bf16 v[116:119], a[148:151], a[4:7], v[116:119]\n"
"ds_read_b64_tr_b16 a[214:215], v217 offset:21504\n"
"ds_read_b64_tr_b16 a[244:245], v217 offset:25600\n"
"v_mfma_f32_16x16x32_bf16 v[116:119], a[180:183], a[12:15], v[116:119]\n"
"ds_read_b64_tr_b16 a[246:247], v217 offset:29696\n"
"ds_read_b64_tr_b16 a[216:217], v218 offset:17408\n"
"v_mfma_f32_16x16x32_bf16 v[88:91], a[152:155], a[0:3], v[88:91]\n"
"ds_read_b64_tr_b16 a[218:219], v218 offset:21504\n"
"ds_read_b64_tr_b16 a[248:249], v218 offset:25600\n"
"v_mfma_f32_16x16x32_bf16 v[88:91], a[184:187], a[8:11], v[88:91]\n"
"ds_read_b64_tr_b16 a[250:251], v218 offset:29696\n"
"buffer_load_dwordx4 v190, s[20:23], s92 offen offset:2048 lds\n"
"v_mfma_f32_16x16x32_bf16 v[120:123], a[152:155], a[4:7], v[120:123]\n"
"ds_read_b64_tr_b16 a[220:221], v219 offset:17408\n"
"v_mfma_f32_16x16x32_bf16 v[120:123], a[184:187], a[12:15], v[120:123]\n"
"ds_read_b64_tr_b16 a[222:223], v219 offset:21504\n"
"ds_read_b64_tr_b16 a[252:253], v219 offset:25600\n"
"v_mfma_f32_16x16x32_bf16 v[92:95], a[156:159], a[0:3], v[92:95]\n"
"ds_read_b64_tr_b16 a[254:255], v219 offset:29696\n"
"buffer_load_dwordx4 v191, s[20:23], s92 offen offset:3072 lds\n"
"v_mfma_f32_16x16x32_bf16 v[92:95], a[188:191], a[8:11], v[92:95]\n"
"v_mfma_f32_16x16x32_bf16 v[124:127], a[156:159], a[4:7], v[124:127]\n"
"v_mfma_f32_16x16x32_bf16 v[124:127], a[188:191], a[12:15], v[124:127]\n"
"s_waitcnt vmcnt(28) lgkmcnt(0)\n"
"v_mfma_f32_16x16x32_bf16 v[64:67], a[192:195], a[16:19], v[64:67]\n"
"ds_read_b64_tr_b16 a[128:129], v216 offset:32768\n"
"ds_read_b64_tr_b16 a[130:131], v216 offset:36864\n"
"v_mfma_f32_16x16x32_bf16 v[64:67], a[224:227], a[24:27], v[64:67]\n"
"s_add_u32 m0, s53, 135168\n"
"ds_read_b64_tr_b16 a[160:161], v216 offset:40960\n"
"v_mfma_f32_16x16x32_bf16 v[96:99], a[192:195], a[20:23], v[96:99]\n"
"s_lshl_b32 s95, s50, 5\n"
"ds_read_b64_tr_b16 a[162:163], v216 offset:45056\n"
"v_mfma_f32_16x16x32_bf16 v[96:99], a[224:227], a[28:31], v[96:99]\n"
"s_mov_b32 s92, 163840\n"
"s_add_u32 s95, s95, s52\n"
"v_mfma_f32_16x16x32_bf16 v[68:71], a[196:199], a[16:19], v[68:71]\n"
"ds_read_b64_tr_b16 a[132:133], v217 offset:32768\n"
"s_lshl_b32 s95, s95, 7\n"
"v_mfma_f32_16x16x32_bf16 v[68:71], a[228:231], a[24:27], v[68:71]\n"
"ds_read_b64_tr_b16 a[134:135], v217 offset:36864\n"
"ds_read_b64_tr_b16 a[164:165], v217 offset:40960\n"
"v_mfma_f32_16x16x32_bf16 v[100:103], a[196:199], a[20:23], v[100:103]\n"
"s_lshl_b32 s94, s83, 2\n"
"ds_read_b64_tr_b16 a[166:167], v217 offset:45056\n"
"v_mfma_f32_16x16x32_bf16 v[100:103], a[228:231], a[28:31], v[100:103]\n"
"buffer_load_dwordx4 v188, s[20:23], s92 offen offset:0 lds\n"
"v_mfma_f32_16x16x32_bf16 v[72:75], a[200:203], a[16:19], v[72:75]\n"
"s_add_u32 s95, s95, s94\n"
"ds_read_b64_tr_b16 a[136:137], v218 offset:32768\n"
"v_mfma_f32_16x16x32_bf16 v[72:75], a[232:235], a[24:27], v[72:75]\n"
"s_add_u32 s87, s95, 0x40000\n"
"ds_read_b64_tr_b16 a[138:139], v218 offset:36864\n"
"v_mfma_f32_16x16x32_bf16 v[104:107], a[200:203], a[20:23], v[104:107]\n"
"s_waitcnt vmcnt(9)\n"
"ds_read_b64_tr_b16 a[168:169], v218 offset:40960\n"
"v_mfma_f32_16x16x32_bf16 v[104:107], a[232:235], a[28:31], v[104:107]\n"
"ds_read_b64_tr_b16 a[170:171], v218 offset:45056\n"
"buffer_load_dwordx4 v189, s[20:23], s92 offen offset:1024 lds\n"
"v_mfma_f32_16x16x32_bf16 v[76:79], a[204:207], a[16:19], v[76:79]\n"
"v_mov_b32 v253, s82\n"
"v_mfma_f32_16x16x32_bf16 v[76:79], a[236:239], a[24:27], v[76:79]\n"
"ds_read_b64_tr_b16 a[140:141], v219 offset:32768\n"
"s_mov_b64 exec, 1\n"
"buffer_store_dword v253, off, s[88:91], s87 sc1\n"
"s_mov_b64 exec, -1\n"
"v_mfma_f32_16x16x32_bf16 v[108:111], a[204:207], a[20:23], v[108:111]\n"
"v_mfma_f32_16x16x32_bf16 v[108:111], a[236:239], a[28:31], v[108:111]\n"
"ds_read_b64_tr_b16 a[142:143], v219 offset:36864\n"
"v_mfma_f32_16x16x32_bf16 v[80:83], a[208:211], a[16:19], v[80:83]\n"
"buffer_load_dwordx4 v190, s[20:23], s92 offen offset:2048 lds\n"
"v_mfma_f32_16x16x32_bf16 v[80:83], a[240:243], a[24:27], v[80:83]\n"
"ds_read_b64_tr_b16 a[172:173], v219 offset:40960\n"
"ds_read_b64_tr_b16 a[174:175], v219 offset:45056\n"
"v_mfma_f32_16x16x32_bf16 v[112:115], a[208:211], a[20:23], v[112:115]\n"
"ds_read_b64_tr_b16 a[144:145], v216 offset:33792\n"
"ds_read_b64_tr_b16 a[146:147], v216 offset:37888\n"
"v_mfma_f32_16x16x32_bf16 v[112:115], a[240:243], a[28:31], v[112:115]\n"
"buffer_load_dwordx4 v191, s[20:23], s92 offen offset:3072 lds\n"
"v_mfma_f32_16x16x32_bf16 v[84:87], a[212:215], a[16:19], v[84:87]\n"
"ds_read_b64_tr_b16 a[176:177], v216 offset:41984\n"
"ds_read_b64_tr_b16 a[178:179], v216 offset:46080\n"
"v_mfma_f32_16x16x32_bf16 v[84:87], a[244:247], a[24:27], v[84:87]\n"
"ds_read_b64_tr_b16 a[148:149], v217 offset:33792\n"
"s_add_u32 m0, s53, 65536\n"
"v_mfma_f32_16x16x32_bf16 v[116:119], a[212:215], a[20:23], v[116:119]\n"
"ds_read_b64_tr_b16 a[150:151], v217 offset:37888\n"
"ds_read_b64_tr_b16 a[180:181], v217 offset:41984\n"
"v_mfma_f32_16x16x32_bf16 v[116:119], a[244:247], a[28:31], v[116:119]\n"
"ds_read_b64_tr_b16 a[182:183], v217 offset:46080\n"
"s_mov_b32 s92, 327680\n"
"v_mfma_f32_16x16x32_bf16 v[88:91], a[216:219], a[16:19], v[88:91]\n"
"ds_read_b64_tr_b16 a[152:153], v218 offset:33792\n"
"ds_read_b64_tr_b16 a[154:155], v218 offset:37888\n"
"v_mfma_f32_16x16x32_bf16 v[88:91], a[248:251], a[24:27], v[88:91]\n"
"ds_read_b64_tr_b16 a[184:185], v218 offset:41984\n"
"ds_read_b64_tr_b16 a[186:187], v218 offset:46080\n"
"v_mfma_f32_16x16x32_bf16 v[120:123], a[216:219], a[20:23], v[120:123]\n"
"buffer_load_dwordx4 v188, s[20:23], s92 offen offset:0 lds\n"
"v_mfma_f32_16x16x32_bf16 v[120:123], a[248:251], a[28:31], v[120:123]\n"
"ds_read_b64_tr_b16 a[156:157], v219 offset:33792\n"
"ds_read_b64_tr_b16 a[158:159], v219 offset:37888\n"
"v_mfma_f32_16x16x32_bf16 v[92:95], a[220:223], a[16:19], v[92:95]\n"
"ds_read_b64_tr_b16 a[188:189], v219 offset:41984\n"
"buffer_load_dwordx4 v189, s[20:23], s92 offen offset:1024 lds\n"
"v_mfma_f32_16x16x32_bf16 v[92:95], a[252:255], a[24:27], v[92:95]\n"
"ds_read_b64_tr_b16 a[190:191], v219 offset:46080\n"
"v_mfma_f32_16x16x32_bf16 v[124:127], a[220:223], a[20:23], v[124:127]\n"
"buffer_load_dwordx4 v190, s[20:23], s92 offen offset:2048 lds\n"
"v_mfma_f32_16x16x32_bf16 v[124:127], a[252:255], a[28:31], v[124:127]\n"
"buffer_load_dwordx4 v191, s[20:23], s92 offen offset:3072 lds\n"
"s_waitcnt vmcnt(17) lgkmcnt(0)\n"
"s_barrier\n"
"v_mfma_f32_16x16x32_bf16 v[64:67], a[128:131], a[32:35], v[64:67]\n"
"ds_read_b64_tr_b16 a[192:193], v216 offset:49152\n"
"ds_read_b64_tr_b16 a[194:195], v216 offset:53248\n"
"v_mfma_f32_16x16x32_bf16 v[64:67], a[160:163], a[40:43], v[64:67]\n"
"s_add_u32 m0, s53, 69632\n"
"ds_read_b64_tr_b16 a[224:225], v216 offset:57344\n"
"v_mfma_f32_16x16x32_bf16 v[96:99], a[128:131], a[36:39], v[96:99]\n"
"ds_read_b64_tr_b16 a[226:227], v216 offset:61440\n"
"s_mov_b32 s92, 360448\n"
"v_mfma_f32_16x16x32_bf16 v[96:99], a[160:163], a[44:47], v[96:99]\n"
"ds_read_b64_tr_b16 a[196:197], v217 offset:49152\n"
"v_mfma_f32_16x16x32_bf16 v[68:71], a[132:135], a[32:35], v[68:71]\n"
"ds_read_b64_tr_b16 a[198:199], v217 offset:53248\n"
"ds_read_b64_tr_b16 a[228:229], v217 offset:57344\n"
"v_mfma_f32_16x16x32_bf16 v[68:71], a[164:167], a[40:43], v[68:71]\n"
"ds_read_b64_tr_b16 a[230:231], v217 offset:61440\n"
"buffer_load_dwordx4 v188, s[20:23], s92 offen offset:0 lds\n"
"v_mfma_f32_16x16x32_bf16 v[100:103], a[132:135], a[36:39], v[100:103]\n"
"ds_read_b64_tr_b16 a[200:201], v218 offset:49152\n"
"v_mfma_f32_16x16x32_bf16 v[100:103], a[164:167], a[44:47], v[100:103]\n"
"ds_read_b64_tr_b16 a[202:203], v218 offset:53248\n"
"v_mfma_f32_16x16x32_bf16 v[72:75], a[136:139], a[32:35], v[72:75]\n"
"ds_read_b64_tr_b16 a[232:233], v218 offset:57344\n"
"ds_read_b64_tr_b16 a[234:235], v218 offset:61440\n"
"v_mfma_f32_16x16x32_bf16 v[72:75], a[168:171], a[40:43], v[72:75]\n"
"buffer_load_dwordx4 v189, s[20:23], s92 offen offset:1024 lds\n"
"v_mfma_f32_16x16x32_bf16 v[104:107], a[136:139], a[36:39], v[104:107]\n"
"ds_read_b64_tr_b16 a[204:205], v219 offset:49152\n"
"ds_read_b64_tr_b16 a[206:207], v219 offset:53248\n"
"v_mfma_f32_16x16x32_bf16 v[104:107], a[168:171], a[44:47], v[104:107]\n"
"buffer_load_dwordx4 v190, s[20:23], s92 offen offset:2048 lds\n"
"v_mfma_f32_16x16x32_bf16 v[76:79], a[140:143], a[32:35], v[76:79]\n"
"ds_read_b64_tr_b16 a[236:237], v219 offset:57344\n"
"ds_read_b64_tr_b16 a[238:239], v219 offset:61440\n"
"v_mfma_f32_16x16x32_bf16 v[76:79], a[172:175], a[40:43], v[76:79]\n"
"ds_read_b64_tr_b16 a[208:209], v216 offset:50176\n"
"ds_read_b64_tr_b16 a[210:211], v216 offset:54272\n"
"v_mfma_f32_16x16x32_bf16 v[108:111], a[140:143], a[36:39], v[108:111]\n"
"buffer_load_dwordx4 v191, s[20:23], s92 offen offset:3072 lds\n"
"v_mfma_f32_16x16x32_bf16 v[108:111], a[172:175], a[44:47], v[108:111]\n"
"ds_read_b64_tr_b16 a[240:241], v216 offset:58368\n"
"ds_read_b64_tr_b16 a[242:243], v216 offset:62464\n"
"v_mfma_f32_16x16x32_bf16 v[80:83], a[144:147], a[32:35], v[80:83]\n"
"ds_read_b64_tr_b16 a[212:213], v217 offset:50176\n"
"s_add_u32 m0, s53, 0\n"
"v_mfma_f32_16x16x32_bf16 v[80:83], a[176:179], a[40:43], v[80:83]\n"
"ds_read_b64_tr_b16 a[214:215], v217 offset:54272\n"
"ds_read_b64_tr_b16 a[244:245], v217 offset:58368\n"
"v_mfma_f32_16x16x32_bf16 v[112:115], a[144:147], a[36:39], v[112:115]\n"
"ds_read_b64_tr_b16 a[246:247], v217 offset:62464\n"
"s_mov_b32 s92, 196608\n"
"v_mfma_f32_16x16x32_bf16 v[112:115], a[176:179], a[44:47], v[112:115]\n"
"ds_read_b64_tr_b16 a[216:217], v218 offset:50176\n"
"v_mfma_f32_16x16x32_bf16 v[84:87], a[148:151], a[32:35], v[84:87]\n"
"ds_read_b64_tr_b16 a[218:219], v218 offset:54272\n"
"v_mfma_f32_16x16x32_bf16 v[84:87], a[180:183], a[40:43], v[84:87]\n"
"ds_read_b64_tr_b16 a[248:249], v218 offset:58368\n"
"ds_read_b64_tr_b16 a[250:251], v218 offset:62464\n"
"v_mfma_f32_16x16x32_bf16 v[116:119], a[148:151], a[36:39], v[116:119]\n"
"buffer_load_dwordx4 v188, s[20:23], s92 offen offset:0 lds\n"
"v_mfma_f32_16x16x32_bf16 v[116:119], a[180:183], a[44:47], v[116:119]\n"
"ds_read_b64_tr_b16 a[220:221], v219 offset:50176\n"
"ds_read_b64_tr_b16 a[222:223], v219 offset:54272\n"
"v_mfma_f32_16x16x32_bf16 v[88:91], a[152:155], a[32:35], v[88:91]\n"
"ds_read_b64_tr_b16 a[252:253], v219 offset:58368\n"
"buffer_load_dwordx4 v189, s[20:23], s92 offen offset:1024 lds\n"
"v_mfma_f32_16x16x32_bf16 v[88:91], a[184:187], a[40:43], v[88:91]\n"
"ds_read_b64_tr_b16 a[254:255], v219 offset:62464\n"
"v_mfma_f32_16x16x32_bf16 v[120:123], a[152:155], a[36:39], v[120:123]\n"
"buffer_load_dwordx4 v190, s[20:23], s92 offen offset:2048 lds\n"
"v_mfma_f32_16x16x32_bf16 v[120:123], a[184:187], a[44:47], v[120:123]\n"
"v_mfma_f32_16x16x32_bf16 v[92:95], a[156:159], a[32:35], v[92:95]\n"
"v_mfma_f32_16x16x32_bf16 v[92:95], a[188:191], a[40:43], v[92:95]\n"
"buffer_load_dwordx4 v191, s[20:23], s92 offen offset:3072 lds\n"
"v_mfma_f32_16x16x32_bf16 v[124:127], a[156:159], a[36:39], v[124:127]\n"
"v_mfma_f32_16x16x32_bf16 v[124:127], a[188:191], a[44:47], v[124:127]\n"
"s_waitcnt vmcnt(25) lgkmcnt(0)\n"
"v_mfma_f32_16x16x32_bf16 v[64:67], a[192:195], a[48:51], v[64:67]\n"
"s_add_u32 m0, s53, 4096\n"
"v_mfma_f32_16x16x32_bf16 v[64:67], a[224:227], a[56:59], v[64:67]\n"
"s_waitcnt vmcnt(21)\n"
"ds_read_b64_tr_b16 a[96:97], v176 offset:32768\n"
"v_mfma_f32_16x16x32_bf16 v[96:99], a[192:195], a[52:55], v[96:99]\n"
"ds_read_b64_tr_b16 a[98:99], v176 offset:34816\n"
"ds_read_b64_tr_b16 a[112:113], v176 offset:36864\n"
"v_mfma_f32_16x16x32_bf16 v[96:99], a[224:227], a[60:63], v[96:99]\n"
"ds_read_b64_tr_b16 a[114:115], v176 offset:38912\n"
"ds_read_b64_tr_b16 a[100:101], v177 offset:32768\n"
"v_mfma_f32_16x16x32_bf16 v[68:71], a[196:199], a[48:51], v[68:71]\n"
"ds_read_b64_tr_b16 a[102:103], v177 offset:34816\n"
"ds_read_b64_tr_b16 a[116:117], v177 offset:36864\n"
"v_mfma_f32_16x16x32_bf16 v[68:71], a[228:231], a[56:59], v[68:71]\n"
"s_mov_b32 s92, 229376\n"
"ds_read_b64_tr_b16 a[118:119], v177 offset:38912\n"
"v_mfma_f32_16x16x32_bf16 v[100:103], a[196:199], a[52:55], v[100:103]\n"
"ds_read_b64_tr_b16 a[104:105], v178 offset:32768\n"
"ds_read_b64_tr_b16 a[106:107], v178 offset:34816\n"
"v_mfma_f32_16x16x32_bf16 v[100:103], a[228:231], a[60:63], v[100:103]\n"
"ds_read_b64_tr_b16 a[120:121], v178 offset:36864\n"
"ds_read_b64_tr_b16 a[122:123], v178 offset:38912\n"
"v_mfma_f32_16x16x32_bf16 v[72:75], a[200:203], a[48:51], v[72:75]\n"
"ds_read_b64_tr_b16 a[108:109], v179 offset:32768\n"
"ds_read_b64_tr_b16 a[110:111], v179 offset:34816\n"
"v_mfma_f32_16x16x32_bf16 v[72:75], a[232:235], a[56:59], v[72:75]\n"
"ds_read_b64_tr_b16 a[124:125], v179 offset:36864\n"
"ds_read_b64_tr_b16 a[126:127], v179 offset:38912\n"
"v_mfma_f32_16x16x32_bf16 v[104:107], a[200:203], a[52:55], v[104:107]\n"
"buffer_load_dwordx4 v188, s[20:23], s92 offen offset:0 lds\n"
"v_mfma_f32_16x16x32_bf16 v[104:107], a[232:235], a[60:63], v[104:107]\n"
"v_mfma_f32_16x16x32_bf16 v[76:79], a[204:207], a[48:51], v[76:79]\n"
"v_mfma_f32_16x16x32_bf16 v[76:79], a[236:239], a[56:59], v[76:79]\n"
"v_mfma_f32_16x16x32_bf16 v[108:111], a[204:207], a[52:55], v[108:111]\n"
"v_mfma_f32_16x16x32_bf16 v[108:111], a[236:239], a[60:63], v[108:111]\n"
"v_mfma_f32_16x16x32_bf16 v[80:83], a[208:211], a[48:51], v[80:83]\n"
"buffer_load_dwordx4 v189, s[20:23], s92 offen offset:1024 lds\n"
"v_mfma_f32_16x16x32_bf16 v[80:83], a[240:243], a[56:59], v[80:83]\n"
"v_mfma_f32_16x16x32_bf16 v[112:115], a[208:211], a[52:55], v[112:115]\n"
"v_mfma_f32_16x16x32_bf16 v[112:115], a[240:243], a[60:63], v[112:115]\n"
"v_mfma_f32_16x16x32_bf16 v[84:87], a[212:215], a[48:51], v[84:87]\n"
"v_mfma_f32_16x16x32_bf16 v[84:87], a[244:247], a[56:59], v[84:87]\n"
"buffer_load_dwordx4 v190, s[20:23], s92 offen offset:2048 lds\n"
"v_mfma_f32_16x16x32_bf16 v[116:119], a[212:215], a[52:55], v[116:119]\n"
"v_mfma_f32_16x16x32_bf16 v[116:119], a[244:247], a[60:63], v[116:119]\n"
"v_mfma_f32_16x16x32_bf16 v[88:91], a[216:219], a[48:51], v[88:91]\n"
"v_mfma_f32_16x16x32_bf16 v[88:91], a[248:251], a[56:59], v[88:91]\n"
"v_mfma_f32_16x16x32_bf16 v[120:123], a[216:219], a[52:55], v[120:123]\n"
"buffer_load_dwordx4 v191, s[20:23], s92 offen offset:3072 lds\n"
"v_mfma_f32_16x16x32_bf16 v[120:123], a[248:251], a[60:63], v[120:123]\n"
"v_mfma_f32_16x16x32_bf16 v[92:95], a[220:223], a[48:51], v[92:95]\n"
"v_mfma_f32_16x16x32_bf16 v[92:95], a[252:255], a[56:59], v[92:95]\n"
"v_mfma_f32_16x16x32_bf16 v[124:127], a[220:223], a[52:55], v[124:127]\n"
"v_mfma_f32_16x16x32_bf16 v[124:127], a[252:255], a[60:63], v[124:127]\n"
"s_nop 7\n"
"v_mul_f32 v64, v64, v80\n"
"v_mul_f32 v65, v65, v81\n"
"v_mul_f32 v66, v66, v82\n"
"v_mul_f32 v67, v67, v83\n"
"v_cvt_pk_bf16_f32 v128, v64, v65\n"
"v_cvt_pk_bf16_f32 v129, v66, v67\n"
"v_mul_f32 v68, v68, v84\n"
"v_mul_f32 v69, v69, v85\n"
"v_mul_f32 v70, v70, v86\n"
"v_mul_f32 v71, v71, v87\n"
"v_cvt_pk_bf16_f32 v130, v68, v69\n"
"v_cvt_pk_bf16_f32 v131, v70, v71\n"
"buffer_store_dwordx4 v[128:131], v248, s[36:39], s85 offen offset:0 sc1\n"
"v_mul_f32 v72, v72, v88\n"
"v_mul_f32 v73, v73, v89\n"
"v_mul_f32 v74, v74, v90\n"
"v_mul_f32 v75, v75, v91\n"
"v_cvt_pk_bf16_f32 v132, v72, v73\n"
"v_cvt_pk_bf16_f32 v133, v74, v75\n"
"v_mul_f32 v76, v76, v92\n"
"v_mul_f32 v77, v77, v93\n"
"v_mul_f32 v78, v78, v94\n"
"v_mul_f32 v79, v79, v95\n"
"v_cvt_pk_bf16_f32 v134, v76, v77\n"
"v_cvt_pk_bf16_f32 v135, v78, v79\n"
"buffer_store_dwordx4 v[132:135], v248, s[36:39], s85 offen offset:1024 sc1\n"
"v_mul_f32 v96, v96, v112\n"
"v_mul_f32 v97, v97, v113\n"
"v_mul_f32 v98, v98, v114\n"
"v_mul_f32 v99, v99, v115\n"
"v_cvt_pk_bf16_f32 v136, v96, v97\n"
"v_cvt_pk_bf16_f32 v137, v98, v99\n"
"v_mul_f32 v100, v100, v116\n"
"v_mul_f32 v101, v101, v117\n"
"v_mul_f32 v102, v102, v118\n"
"v_mul_f32 v103, v103, v119\n"
"v_cvt_pk_bf16_f32 v138, v100, v101\n"
"v_cvt_pk_bf16_f32 v139, v102, v103\n"
"s_add_u32 s85, s85, 57344\n"
"buffer_store_dwordx4 v[136:139], v248, s[36:39], s85 offen offset:0 sc1\n"
"v_mul_f32 v104, v104, v120\n"
"v_mul_f32 v105, v105, v121\n"
"v_mul_f32 v106, v106, v122\n"
"v_mul_f32 v107, v107, v123\n"
"v_cvt_pk_bf16_f32 v140, v104, v105\n"
"v_cvt_pk_bf16_f32 v141, v106, v107\n"
"v_mul_f32 v108, v108, v124\n"
"v_mul_f32 v109, v109, v125\n"
"v_mul_f32 v110, v110, v126\n"
"v_mul_f32 v111, v111, v127\n"
"v_cvt_pk_bf16_f32 v142, v108, v109\n"
"v_cvt_pk_bf16_f32 v143, v110, v111\n"
"buffer_store_dwordx4 v[140:143], v248, s[36:39], s85 offen offset:1024 sc1\n"
"s_and_b32 s50, s84, 7\n"
"s_lshl_b32 s50, s50, 2\n"
"s_lshr_b32 s92, s84, 6\n"
"s_add_u32 s50, s50, s92\n"
"s_lshr_b32 s92, s50, 1\n"
"s_lshl_b32 s92, s92, 12\n"
"s_mul_i32 s93, s83, 896\n"
"s_add_u32 s92, s92, s93\n"
"s_and_b32 s93, s50, 1\n"
"s_lshl_b32 s93, s93, 3\n"
"s_add_u32 s92, s92, s93\n"
"s_add_u32 s97, s92, 0x40000\n"
"v_lshrrev_b32 v240, 1, v255\n"
"v_min_u32 v240, 6, v240\n"
"v_lshlrev_b32 v240, 7, v240\n"
"v_and_b32 v253, 1, v255\n"
"v_lshl_add_u32 v240, v253, 2, v240\n"
"s_waitcnt vmcnt(29)\n"
"v_xor_b32 v200, s82, v161\n"
"v_bitop3_b32 v200, v163, s82, v200 bitop3:0xbe\n"
"v_bitop3_b32 v200, v165, s82, v200 bitop3:0xbe\n"
"v_bitop3_b32 v200, v167, s82, v200 bitop3:0xbe\n"
"v_bitop3_b32 v200, v169, s82, v200 bitop3:0xbe\n"
"v_bitop3_b32 v200, v171, s82, v200 bitop3:0xbe\n"
"v_bitop3_b32 v200, v173, s82, v200 bitop3:0xbe\n"
"v_bitop3_b32 v200, v175, s82, v200 bitop3:0xbe\n"
"v_cmp_ne_u32 vcc, 0, v200\n"
"s_cbranch_vccnz Lgs98_%=\n"
"Lgb97_%=:\n"
"v_lshrrev_b32 v253, 5, v255\n"
"v_lshlrev_b32 v242, 10, v253\n"
"v_bfe_u32 v253, v255, 2, 3\n"
"v_lshl_add_u32 v242, v253, 7, v242\n"
"v_bfe_u32 v253, v255, 3, 2\n"
"v_lshlrev_b32 v253, 1, v253\n"
"v_bfe_u32 v252, v255, 1, 1\n"
"v_xor_b32 v253, v253, v252\n"
"v_lshl_add_u32 v242, v253, 4, v242\n"
"v_and_b32 v252, 1, v255\n"
"v_lshl_add_u32 v242, v252, 3, v242\n"
"v_add_u32 v242, s53, v242\n"
"v_xor_b32 v243, 32, v242\n"
"v_xor_b32 v244, 64, v242\n"
"v_xor_b32 v245, 96, v242\n"
"s_add_u32 s16, s80, 0x3880000\n"
"s_addc_u32 s17, s81, 0\n"
"s_and_b32 s17, s17, 0xffff\n"
"s_mov_b32 s18, 0x700000\n"
"s_mov_b32 s19, 0x20000\n"
"s_mul_i32 s45, s50, 229376\n"
"s_mul_i32 s92, s83, 14336\n"
"s_add_u32 s45, s45, s92\n"
"s_add_u32 s46, s45, 57344\n"
"s_add_u32 s47, s45, 114688\n"
"s_add_u32 s48, s45, 172032\n"
"s_lshl_b32 s92, s50, 2\n"
"s_add_u32 s51, s92, s83\n"
"s_bfe_u32 s94, s84, 0x30003\n"
"s_lshl_b32 s93, s51, 15\n"
"s_lshl_b32 s92, s94, 12\n"
"s_add_u32 s44, s93, s92\n"
"s_lshl_b32 s49, s51, 14\n"
"s_lshl_b32 s92, s94, 7\n"
"s_add_u32 s49, s49, s92\n"
"v_and_b32 v252, 15, v255\n"
"v_lshlrev_b32 v239, 10, v252\n"
"v_bfe_u32 v252, v255, 4, 1\n"
"v_lshl_add_u32 v239, v252, 5, v239\n"
"v_lshrrev_b32 v252, 5, v255\n"
"v_lshl_add_u32 v239, v252, 4, v239\n"
"v_lshlrev_b32 v144, 16, v160\n"
"v_and_b32 v145, 0xffff0000, v160\n"
"v_lshlrev_b32 v146, 16, v162\n"
"v_and_b32 v147, 0xffff0000, v162\n"
"v_lshlrev_b32 v148, 16, v164\n"
"v_and_b32 v149, 0xffff0000, v164\n"
"v_lshlrev_b32 v150, 16, v166\n"
"v_and_b32 v151, 0xffff0000, v166\n"
"v_lshlrev_b32 v152, 16, v168\n"
"v_and_b32 v153, 0xffff0000, v168\n"
"v_lshlrev_b32 v154, 16, v170\n"
"v_and_b32 v155, 0xffff0000, v170\n"
"v_lshlrev_b32 v156, 16, v172\n"
"v_and_b32 v157, 0xffff0000, v172\n"
"v_lshlrev_b32 v158, 16, v174\n"
"v_and_b32 v159, 0xffff0000, v174\n"
"s_waitcnt vmcnt(0)\n"
"s_add_u32 s95, s87, 0x8000\n"
"v_mov_b32 v253, s82\n"
"s_mov_b64 exec, 1\n"
"buffer_store_dword v253, off, s[88:91], s95 sc1\n"
"s_mov_b64 exec, -1\n"
"s_add_u32 s36, s78, 0\n"
"s_addc_u32 s37, s79, 0\n"
"s_and_b32 s37, s37, 0xffff\n"
"s_mov_b32 s38, 0x200000\n"
"s_mov_b32 s39, 0x20000\n"
"buffer_load_dword v241, v240, s[88:91], s97 offen sc1\n"
"s_waitcnt lgkmcnt(0)\n"
"s_barrier\n"
"s_add_u32 m0, s53, 32768\n"
"s_mov_b32 s92, 262144\n"
"buffer_load_dwordx4 v188, s[20:23], s92 offen offset:0 lds\n"
"buffer_load_dwordx4 v189, s[20:23], s92 offen offset:1024 lds\n"
"buffer_load_dwordx4 v190, s[20:23], s92 offen offset:2048 lds\n"
"buffer_load_dwordx4 v191, s[20:23], s92 offen offset:3072 lds\n"
"s_add_u32 m0, s53, 36864\n"
"s_mov_b32 s92, 294912\n"
"buffer_load_dwordx4 v188, s[20:23], s92 offen offset:0 lds\n"
"buffer_load_dwordx4 v189, s[20:23], s92 offen offset:1024 lds\n"
"buffer_load_dwordx4 v190, s[20:23], s92 offen offset:2048 lds\n"
"buffer_load_dwordx4 v191, s[20:23], s92 offen offset:3072 lds\n"
"s_add_u32 m0, s53, 98304\n"
"s_mov_b32 s92, 393216\n"
"buffer_load_dwordx4 v188, s[20:23], s92 offen offset:0 lds\n"
"buffer_load_dwordx4 v189, s[20:23], s92 offen offset:1024 lds\n"
"buffer_load_dwordx4 v190, s[20:23], s92 offen offset:2048 lds\n"
"buffer_load_dwordx4 v191, s[20:23], s92 offen offset:3072 lds\n"
"s_add_u32 m0, s53, 102400\n"
"s_mov_b32 s92, 425984\n"
"buffer_load_dwordx4 v188, s[20:23], s92 offen offset:0 lds\n"
"buffer_load_dwordx4 v189, s[20:23], s92 offen offset:1024 lds\n"
"buffer_load_dwordx4 v190, s[20:23], s92 offen offset:2048 lds\n"
"buffer_load_dwordx4 v191, s[20:23], s92 offen offset:3072 lds\n"
"s_waitcnt vmcnt(16)\n"
"v_cmp_ne_u32 vcc, s82, v241\n"
"s_cbranch_vccnz Lps101_%=\n"
"Lpb100_%=:\n"
"buffer_load_dwordx4 a[0:3], v248, s[16:19], s45 offen offset:0 sc1\n"
"buffer_load_dwordx4 a[16:19], v248, s[16:19], s45 offen offset:1024 sc1\n"
"buffer_load_dwordx4 a[4:7], v248, s[16:19], s46 offen offset:0 sc1\n"
"buffer_load_dwordx4 a[20:23], v248, s[16:19], s46 offen offset:1024 sc1\n"
"buffer_load_dwordx4 a[8:11], v248, s[16:19], s47 offen offset:0 sc1\n"
"buffer_load_dwordx4 a[24:27], v248, s[16:19], s47 offen offset:1024 sc1\n"
"buffer_load_dwordx4 a[12:15], v248, s[16:19], s48 offen offset:0 sc1\n"
"buffer_load_dwordx4 a[28:31], v248, s[16:19], s48 offen offset:1024 sc1\n"
"buffer_load_dwordx4 a[32:35], v248, s[16:19], s45 offen offset:2048 sc1\n"
"buffer_load_dwordx4 a[48:51], v248, s[16:19], s45 offen offset:3072 sc1\n"
"buffer_load_dwordx4 a[36:39], v248, s[16:19], s46 offen offset:2048 sc1\n"
"buffer_load_dwordx4 a[52:55], v248, s[16:19], s46 offen offset:3072 sc1\n"
"buffer_load_dwordx4 a[40:43], v248, s[16:19], s47 offen offset:2048 sc1\n"
"buffer_load_dwordx4 a[56:59], v248, s[16:19], s47 offen offset:3072 sc1\n"
"buffer_load_dwordx4 a[44:47], v248, s[16:19], s48 offen offset:2048 sc1\n"
"buffer_load_dwordx4 a[60:63], v248, s[16:19], s48 offen offset:3072 sc1\n"
"s_add_u32 s45, s45, 4096\n"
"buffer_load_dwordx4 a[128:131], v248, s[16:19], s45 offen offset:0 sc1\n"
"buffer_load_dwordx4 a[144:147], v248, s[16:19], s45 offen offset:1024 sc1\n"
"s_add_u32 s46, s46, 4096\n"
"buffer_load_dwordx4 a[132:135], v248, s[16:19], s46 offen offset:0 sc1\n"
"buffer_load_dwordx4 a[148:151], v248, s[16:19], s46 offen offset:1024 sc1\n"
"s_add_u32 s47, s47, 4096\n"
"buffer_load_dwordx4 a[136:139], v248, s[16:19], s47 offen offset:0 sc1\n"
"buffer_load_dwordx4 a[152:155], v248, s[16:19], s47 offen offset:1024 sc1\n"
"s_add_u32 s48, s48, 4096\n"
"buffer_load_dwordx4 a[140:143], v248, s[16:19], s48 offen offset:0 sc1\n"
"buffer_load_dwordx4 a[156:159], v248, s[16:19], s48 offen offset:1024 sc1\n"
"v_add_u32 v252, s54, v248\n"
"s_lshl_b32 s92, s83, 14\n"
"v_add_u32 v253, s92, v248\n"
"s_waitcnt vmcnt(16) lgkmcnt(0)\n"
"v_mfma_f32_16x16x32_bf16 v[0:3], a[64:67], a[0:3], 0\n"
"v_add_u32 v242, 0x20000, v242\n"
"buffer_load_dwordx4 a[160:163], v248, s[16:19], s45 offen offset:2048 sc1\n"
"v_mfma_f32_16x16x32_bf16 v[0:3], a[80:83], a[16:19], v[0:3]\n"
"v_add_u32 v243, 0x20000, v243\n"
"v_mfma_f32_16x16x32_bf16 v[16:19], a[64:67], a[4:7], 0\n"
"v_add_u32 v244, 0x20000, v244\n"
"v_mfma_f32_16x16x32_bf16 v[16:19], a[80:83], a[20:23], v[16:19]\n"
"v_add_u32 v245, 0x20000, v245\n"
"v_mfma_f32_16x16x32_bf16 v[32:35], a[64:67], a[8:11], 0\n"
"buffer_load_dwordx4 a[176:179], v248, s[16:19], s45 offen offset:3072 sc1\n"
"v_mfma_f32_16x16x32_bf16 v[32:35], a[80:83], a[24:27], v[32:35]\n"
"ds_read_b64_tr_b16 v[64:65], v242 offset:0\n"
"v_mfma_f32_16x16x32_bf16 v[48:51], a[64:67], a[12:15], 0\n"
"ds_read_b64_tr_b16 v[66:67], v242 offset:2048\n"
"v_mfma_f32_16x16x32_bf16 v[48:51], a[80:83], a[28:31], v[48:51]\n"
"ds_read_b64_tr_b16 v[80:81], v242 offset:4096\n"
"v_mfma_f32_16x16x32_bf16 v[4:7], a[68:71], a[0:3], 0\n"
"ds_read_b64_tr_b16 v[82:83], v242 offset:6144\n"
"buffer_load_dwordx4 a[164:167], v248, s[16:19], s46 offen offset:2048 sc1\n"
"v_mfma_f32_16x16x32_bf16 v[4:7], a[84:87], a[16:19], v[4:7]\n"
"v_mfma_f32_16x16x32_bf16 v[20:23], a[68:71], a[4:7], 0\n"
"ds_read_b64_tr_b16 v[68:69], v243 offset:0\n"
"v_mfma_f32_16x16x32_bf16 v[20:23], a[84:87], a[20:23], v[20:23]\n"
"ds_read_b64_tr_b16 v[70:71], v243 offset:2048\n"
"v_mfma_f32_16x16x32_bf16 v[36:39], a[68:71], a[8:11], 0\n"
"ds_read_b64_tr_b16 v[84:85], v243 offset:4096\n"
"buffer_load_dwordx4 a[180:183], v248, s[16:19], s46 offen offset:3072 sc1\n"
"v_mfma_f32_16x16x32_bf16 v[36:39], a[84:87], a[24:27], v[36:39]\n"
"ds_read_b64_tr_b16 v[86:87], v243 offset:6144\n"
"v_mfma_f32_16x16x32_bf16 v[52:55], a[68:71], a[12:15], 0\n"
"v_mfma_f32_16x16x32_bf16 v[52:55], a[84:87], a[28:31], v[52:55]\n"
"ds_read_b64_tr_b16 v[72:73], v244 offset:0\n"
"v_mfma_f32_16x16x32_bf16 v[8:11], a[72:75], a[0:3], 0\n"
"ds_read_b64_tr_b16 v[74:75], v244 offset:2048\n"
"buffer_load_dwordx4 a[168:171], v248, s[16:19], s47 offen offset:2048 sc1\n"
"v_mfma_f32_16x16x32_bf16 v[8:11], a[88:91], a[16:19], v[8:11]\n"
"ds_read_b64_tr_b16 v[88:89], v244 offset:4096\n"
"v_mfma_f32_16x16x32_bf16 v[24:27], a[72:75], a[4:7], 0\n"
"ds_read_b64_tr_b16 v[90:91], v244 offset:6144\n"
"v_mfma_f32_16x16x32_bf16 v[24:27], a[88:91], a[20:23], v[24:27]\n"
"v_mfma_f32_16x16x32_bf16 v[40:43], a[72:75], a[8:11], 0\n"
"ds_read_b64_tr_b16 v[76:77], v245 offset:0\n"
"buffer_load_dwordx4 a[184:187], v248, s[16:19], s47 offen offset:3072 sc1\n"
"v_mfma_f32_16x16x32_bf16 v[40:43], a[88:91], a[24:27], v[40:43]\n"
"ds_read_b64_tr_b16 v[78:79], v245 offset:2048\n"
"v_mfma_f32_16x16x32_bf16 v[56:59], a[72:75], a[12:15], 0\n"
"ds_read_b64_tr_b16 v[92:93], v245 offset:4096\n"
"v_mfma_f32_16x16x32_bf16 v[56:59], a[88:91], a[28:31], v[56:59]\n"
"ds_read_b64_tr_b16 v[94:95], v245 offset:6144\n"
"v_mfma_f32_16x16x32_bf16 v[12:15], a[76:79], a[0:3], 0\n"
"buffer_load_dwordx4 a[172:175], v248, s[16:19], s48 offen offset:2048 sc1\n"
"v_mfma_f32_16x16x32_bf16 v[12:15], a[92:95], a[16:19], v[12:15]\n"
"v_mfma_f32_16x16x32_bf16 v[28:31], a[76:79], a[4:7], 0\n"
"v_mfma_f32_16x16x32_bf16 v[28:31], a[92:95], a[20:23], v[28:31]\n"
"v_mfma_f32_16x16x32_bf16 v[44:47], a[76:79], a[8:11], 0\n"
"buffer_load_dwordx4 a[188:191], v248, s[16:19], s48 offen offset:3072 sc1\n"
"v_mfma_f32_16x16x32_bf16 v[44:47], a[92:95], a[24:27], v[44:47]\n"
"v_mfma_f32_16x16x32_bf16 v[60:63], a[76:79], a[12:15], 0\n"
"v_mfma_f32_16x16x32_bf16 v[60:63], a[92:95], a[28:31], v[60:63]\n"
"s_waitcnt vmcnt(16) lgkmcnt(0)\n"
"v_mfma_f32_16x16x32_bf16 v[0:3], a[96:99], a[32:35], v[0:3]\n"
"v_add_u32 v242, 0xfffe0000, v242\n"
"s_add_u32 s45, s45, 4096\n"
"v_mfma_f32_16x16x32_bf16 v[0:3], a[112:115], a[48:51], v[0:3]\n"
"v_add_u32 v243, 0xfffe0000, v243\n"
"v_mfma_f32_16x16x32_bf16 v[16:19], a[96:99], a[36:39], v[16:19]\n"
"v_add_u32 v244, 0xfffe0000, v244\n"
"buffer_load_dwordx4 a[192:195], v248, s[16:19], s45 offen offset:0 sc1\n"
"v_mfma_f32_16x16x32_bf16 v[16:19], a[112:115], a[52:55], v[16:19]\n"
"v_add_u32 v245, 0xfffe0000, v245\n"
"v_mfma_f32_16x16x32_bf16 v[32:35], a[96:99], a[40:43], v[32:35]\n"
"v_mfma_f32_16x16x32_bf16 v[32:35], a[112:115], a[56:59], v[32:35]\n"
"ds_read_b64_tr_b16 v[96:97], v242 offset:0\n"
"buffer_load_dwordx4 a[208:211], v248, s[16:19], s45 offen offset:1024 sc1\n"
"v_mfma_f32_16x16x32_bf16 v[48:51], a[96:99], a[44:47], v[48:51]\n"
"ds_read_b64_tr_b16 v[98:99], v242 offset:2048\n"
"v_mfma_f32_16x16x32_bf16 v[48:51], a[112:115], a[60:63], v[48:51]\n"
"ds_read_b64_tr_b16 v[112:113], v242 offset:4096\n"
"v_mfma_f32_16x16x32_bf16 v[4:7], a[100:103], a[32:35], v[4:7]\n"
"ds_read_b64_tr_b16 v[114:115], v242 offset:6144\n"
"s_add_u32 s46, s46, 4096\n"
"v_mfma_f32_16x16x32_bf16 v[4:7], a[116:119], a[48:51], v[4:7]\n"
"v_mfma_f32_16x16x32_bf16 v[20:23], a[100:103], a[36:39], v[20:23]\n"
"ds_read_b64_tr_b16 v[100:101], v243 offset:0\n"
"buffer_load_dwordx4 a[196:199], v248, s[16:19], s46 offen offset:0 sc1\n"
"v_mfma_f32_16x16x32_bf16 v[20:23], a[116:119], a[52:55], v[20:23]\n"
"ds_read_b64_tr_b16 v[102:103], v243 offset:2048\n"
"v_mfma_f32_16x16x32_bf16 v[36:39], a[100:103], a[40:43], v[36:39]\n"
"ds_read_b64_tr_b16 v[116:117], v243 offset:4096\n"
"v_mfma_f32_16x16x32_bf16 v[36:39], a[116:119], a[56:59], v[36:39]\n"
"ds_read_b64_tr_b16 v[118:119], v243 offset:6144\n"
"buffer_load_dwordx4 a[212:215], v248, s[16:19], s46 offen offset:1024 sc1\n"
"v_mfma_f32_16x16x32_bf16 v[52:55], a[100:103], a[44:47], v[52:55]\n"
"v_mfma_f32_16x16x32_bf16 v[52:55], a[116:119], a[60:63], v[52:55]\n"
"ds_read_b64_tr_b16 v[104:105], v244 offset:0\n"
"v_mfma_f32_16x16x32_bf16 v[8:11], a[104:107], a[32:35], v[8:11]\n"
"ds_read_b64_tr_b16 v[106:107], v244 offset:2048\n"
"s_add_u32 s47, s47, 4096\n"
"v_mfma_f32_16x16x32_bf16 v[8:11], a[120:123], a[48:51], v[8:11]\n"
"ds_read_b64_tr_b16 v[120:121], v244 offset:4096\n"
"v_mfma_f32_16x16x32_bf16 v[24:27], a[104:107], a[36:39], v[24:27]\n"
"ds_read_b64_tr_b16 v[122:123], v244 offset:6144\n"
"buffer_load_dwordx4 a[200:203], v248, s[16:19], s47 offen offset:0 sc1\n"
"v_mfma_f32_16x16x32_bf16 v[24:27], a[120:123], a[52:55], v[24:27]\n"
"v_mfma_f32_16x16x32_bf16 v[40:43], a[104:107], a[40:43], v[40:43]\n"
"ds_read_b64_tr_b16 v[108:109], v245 offset:0\n"
"v_mfma_f32_16x16x32_bf16 v[40:43], a[120:123], a[56:59], v[40:43]\n"
"ds_read_b64_tr_b16 v[110:111], v245 offset:2048\n"
"buffer_load_dwordx4 a[216:219], v248, s[16:19], s47 offen offset:1024 sc1\n"
"v_mfma_f32_16x16x32_bf16 v[56:59], a[104:107], a[44:47], v[56:59]\n"
"ds_read_b64_tr_b16 v[124:125], v245 offset:4096\n"
"v_mfma_f32_16x16x32_bf16 v[56:59], a[120:123], a[60:63], v[56:59]\n"
"ds_read_b64_tr_b16 v[126:127], v245 offset:6144\n"
"v_mfma_f32_16x16x32_bf16 v[12:15], a[108:111], a[32:35], v[12:15]\n"
"s_add_u32 s48, s48, 4096\n"
"v_mfma_f32_16x16x32_bf16 v[12:15], a[124:127], a[48:51], v[12:15]\n"
"v_mfma_f32_16x16x32_bf16 v[28:31], a[108:111], a[36:39], v[28:31]\n"
"buffer_load_dwordx4 a[204:207], v248, s[16:19], s48 offen offset:0 sc1\n"
"v_mfma_f32_16x16x32_bf16 v[28:31], a[124:127], a[52:55], v[28:31]\n"
"v_mfma_f32_16x16x32_bf16 v[44:47], a[108:111], a[40:43], v[44:47]\n"
"v_mfma_f32_16x16x32_bf16 v[44:47], a[124:127], a[56:59], v[44:47]\n"
"buffer_load_dwordx4 a[220:223], v248, s[16:19], s48 offen offset:1024 sc1\n"
"v_mfma_f32_16x16x32_bf16 v[60:63], a[108:111], a[44:47], v[60:63]\n"
"v_mfma_f32_16x16x32_bf16 v[60:63], a[124:127], a[60:63], v[60:63]\n"
"s_waitcnt vmcnt(16) lgkmcnt(0)\n"
"v_mfma_f32_16x16x32_bf16 v[0:3], v[64:67], a[128:131], v[0:3]\n"
"buffer_load_dwordx4 a[224:227], v248, s[16:19], s45 offen offset:2048 sc1\n"
"v_mfma_f32_16x16x32_bf16 v[0:3], v[80:83], a[144:147], v[0:3]\n"
"v_mfma_f32_16x16x32_bf16 v[16:19], v[64:67], a[132:135], v[16:19]\n"
"v_mfma_f32_16x16x32_bf16 v[16:19], v[80:83], a[148:151], v[16:19]\n"
"v_mfma_f32_16x16x32_bf16 v[32:35], v[64:67], a[136:139], v[32:35]\n"
"buffer_load_dwordx4 a[240:243], v248, s[16:19], s45 offen offset:3072 sc1\n"
"v_mfma_f32_16x16x32_bf16 v[32:35], v[80:83], a[152:155], v[32:35]\n"
"v_mfma_f32_16x16x32_bf16 v[48:51], v[64:67], a[140:143], v[48:51]\n"
"v_mfma_f32_16x16x32_bf16 v[48:51], v[80:83], a[156:159], v[48:51]\n"
"v_mfma_f32_16x16x32_bf16 v[4:7], v[68:71], a[128:131], v[4:7]\n"
"buffer_load_dwordx4 a[228:231], v248, s[16:19], s46 offen offset:2048 sc1\n"
"v_mfma_f32_16x16x32_bf16 v[4:7], v[84:87], a[144:147], v[4:7]\n"
"v_mfma_f32_16x16x32_bf16 v[20:23], v[68:71], a[132:135], v[20:23]\n"
"v_mfma_f32_16x16x32_bf16 v[20:23], v[84:87], a[148:151], v[20:23]\n"
"v_mfma_f32_16x16x32_bf16 v[36:39], v[68:71], a[136:139], v[36:39]\n"
"buffer_load_dwordx4 a[244:247], v248, s[16:19], s46 offen offset:3072 sc1\n"
"v_mfma_f32_16x16x32_bf16 v[36:39], v[84:87], a[152:155], v[36:39]\n"
"v_mfma_f32_16x16x32_bf16 v[52:55], v[68:71], a[140:143], v[52:55]\n"
"v_mfma_f32_16x16x32_bf16 v[52:55], v[84:87], a[156:159], v[52:55]\n"
"v_mfma_f32_16x16x32_bf16 v[8:11], v[72:75], a[128:131], v[8:11]\n"
"buffer_load_dwordx4 a[232:235], v248, s[16:19], s47 offen offset:2048 sc1\n"
"v_mfma_f32_16x16x32_bf16 v[8:11], v[88:91], a[144:147], v[8:11]\n"
"v_mfma_f32_16x16x32_bf16 v[24:27], v[72:75], a[132:135], v[24:27]\n"
"v_mfma_f32_16x16x32_bf16 v[24:27], v[88:91], a[148:151], v[24:27]\n"
"v_mfma_f32_16x16x32_bf16 v[40:43], v[72:75], a[136:139], v[40:43]\n"
"buffer_load_dwordx4 a[248:251], v248, s[16:19], s47 offen offset:3072 sc1\n"
"v_mfma_f32_16x16x32_bf16 v[40:43], v[88:91], a[152:155], v[40:43]\n"
"v_mfma_f32_16x16x32_bf16 v[56:59], v[72:75], a[140:143], v[56:59]\n"
"v_mfma_f32_16x16x32_bf16 v[56:59], v[88:91], a[156:159], v[56:59]\n"
"v_mfma_f32_16x16x32_bf16 v[12:15], v[76:79], a[128:131], v[12:15]\n"
"buffer_load_dwordx4 a[236:239], v248, s[16:19], s48 offen offset:2048 sc1\n"
"v_mfma_f32_16x16x32_bf16 v[12:15], v[92:95], a[144:147], v[12:15]\n"
"v_mfma_f32_16x16x32_bf16 v[28:31], v[76:79], a[132:135], v[28:31]\n"
"v_mfma_f32_16x16x32_bf16 v[28:31], v[92:95], a[148:151], v[28:31]\n"
"v_mfma_f32_16x16x32_bf16 v[44:47], v[76:79], a[136:139], v[44:47]\n"
"buffer_load_dwordx4 a[252:255], v248, s[16:19], s48 offen offset:3072 sc1\n"
"v_mfma_f32_16x16x32_bf16 v[44:47], v[92:95], a[152:155], v[44:47]\n"
"v_mfma_f32_16x16x32_bf16 v[60:63], v[76:79], a[140:143], v[60:63]\n"
"v_mfma_f32_16x16x32_bf16 v[60:63], v[92:95], a[156:159], v[60:63]\n"
"s_waitcnt vmcnt(16) lgkmcnt(0)\n"
"v_mfma_f32_16x16x32_bf16 v[0:3], v[96:99], a[160:163], v[0:3]\n"
"ds_read_b64_tr_b16 v[64:65], v242 offset:32768\n"
"s_add_u32 s45, s45, 4096\n"
"v_mfma_f32_16x16x32_bf16 v[0:3], v[112:115], a[176:179], v[0:3]\n"
"ds_read_b64_tr_b16 v[66:67], v242 offset:34816\n"
"v_mfma_f32_16x16x32_bf16 v[16:19], v[96:99], a[164:167], v[16:19]\n"
"buffer_load_dwordx4 a[64:67], v248, s[16:19], s45 offen offset:0 sc1\n"
"v_mfma_f32_16x16x32_bf16 v[16:19], v[112:115], a[180:183], v[16:19]\n"
"ds_read_b64_tr_b16 v[80:81], v242 offset:36864\n"
"v_mfma_f32_16x16x32_bf16 v[32:35], v[96:99], a[168:171], v[32:35]\n"
"ds_read_b64_tr_b16 v[82:83], v242 offset:38912\n"
"v_mfma_f32_16x16x32_bf16 v[32:35], v[112:115], a[184:187], v[32:35]\n"
"buffer_load_dwordx4 a[80:83], v248, s[16:19], s45 offen offset:1024 sc1\n"
"v_mfma_f32_16x16x32_bf16 v[48:51], v[96:99], a[172:175], v[48:51]\n"
"ds_read_b64_tr_b16 v[68:69], v243 offset:32768\n"
"v_mfma_f32_16x16x32_bf16 v[48:51], v[112:115], a[188:191], v[48:51]\n"
"ds_read_b64_tr_b16 v[70:71], v243 offset:34816\n"
"v_mfma_f32_16x16x32_bf16 v[4:7], v[100:103], a[160:163], v[4:7]\n"
"s_add_u32 s46, s46, 4096\n"
"v_mfma_f32_16x16x32_bf16 v[4:7], v[116:119], a[176:179], v[4:7]\n"
"ds_read_b64_tr_b16 v[84:85], v243 offset:36864\n"
"v_mfma_f32_16x16x32_bf16 v[20:23], v[100:103], a[164:167], v[20:23]\n"
"ds_read_b64_tr_b16 v[86:87], v243 offset:38912\n"
"buffer_load_dwordx4 a[68:71], v248, s[16:19], s46 offen offset:0 sc1\n"
"v_mfma_f32_16x16x32_bf16 v[20:23], v[116:119], a[180:183], v[20:23]\n"
"v_mfma_f32_16x16x32_bf16 v[36:39], v[100:103], a[168:171], v[36:39]\n"
"ds_read_b64_tr_b16 v[72:73], v244 offset:32768\n"
"v_mfma_f32_16x16x32_bf16 v[36:39], v[116:119], a[184:187], v[36:39]\n"
"buffer_load_dwordx4 a[84:87], v248, s[16:19], s46 offen offset:1024 sc1\n"
"v_mfma_f32_16x16x32_bf16 v[52:55], v[100:103], a[172:175], v[52:55]\n"
"ds_read_b64_tr_b16 v[74:75], v244 offset:34816\n"
"v_mfma_f32_16x16x32_bf16 v[52:55], v[116:119], a[188:191], v[52:55]\n"
"ds_read_b64_tr_b16 v[88:89], v244 offset:36864\n"
"v_mfma_f32_16x16x32_bf16 v[8:11], v[104:107], a[160:163], v[8:11]\n"
"s_add_u32 s47, s47, 4096\n"
"v_mfma_f32_16x16x32_bf16 v[8:11], v[120:123], a[176:179], v[8:11]\n"
"ds_read_b64_tr_b16 v[90:91], v244 offset:38912\n"
"v_mfma_f32_16x16x32_bf16 v[24:27], v[104:107], a[164:167], v[24:27]\n"
"ds_read_b64_tr_b16 v[76:77], v245 offset:32768\n"
"buffer_load_dwordx4 a[72:75], v248, s[16:19], s47 offen offset:0 sc1\n"
"v_mfma_f32_16x16x32_bf16 v[24:27], v[120:123], a[180:183], v[24:27]\n"
"v_mfma_f32_16x16x32_bf16 v[40:43], v[104:107], a[168:171], v[40:43]\n"
"ds_read_b64_tr_b16 v[78:79], v245 offset:34816\n"
"v_mfma_f32_16x16x32_bf16 v[40:43], v[120:123], a[184:187], v[40:43]\n"
"ds_read_b64_tr_b16 v[92:93], v245 offset:36864\n"
"buffer_load_dwordx4 a[88:91], v248, s[16:19], s47 offen offset:1024 sc1\n"
"v_mfma_f32_16x16x32_bf16 v[56:59], v[104:107], a[172:175], v[56:59]\n"
"v_mfma_f32_16x16x32_bf16 v[56:59], v[120:123], a[188:191], v[56:59]\n"
"ds_read_b64_tr_b16 v[94:95], v245 offset:38912\n"
"v_mfma_f32_16x16x32_bf16 v[12:15], v[108:111], a[160:163], v[12:15]\n"
"s_add_u32 s48, s48, 4096\n"
"v_mfma_f32_16x16x32_bf16 v[12:15], v[124:127], a[176:179], v[12:15]\n"
"v_mfma_f32_16x16x32_bf16 v[28:31], v[108:111], a[164:167], v[28:31]\n"
"buffer_load_dwordx4 a[76:79], v248, s[16:19], s48 offen offset:0 sc1\n"
"v_mfma_f32_16x16x32_bf16 v[28:31], v[124:127], a[180:183], v[28:31]\n"
"v_mfma_f32_16x16x32_bf16 v[44:47], v[108:111], a[168:171], v[44:47]\n"
"v_mfma_f32_16x16x32_bf16 v[44:47], v[124:127], a[184:187], v[44:47]\n"
"buffer_load_dwordx4 a[92:95], v248, s[16:19], s48 offen offset:1024 sc1\n"
"v_mfma_f32_16x16x32_bf16 v[60:63], v[108:111], a[172:175], v[60:63]\n"
"v_mfma_f32_16x16x32_bf16 v[60:63], v[124:127], a[188:191], v[60:63]\n"
"s_waitcnt vmcnt(16) lgkmcnt(0)\n"
"v_mfma_f32_16x16x32_bf16 v[0:3], v[64:67], a[192:195], v[0:3]\n"
"v_add_u32 v242, 0x10000, v242\n"
"buffer_load_dwordx4 v[128:131], v248, s[40:43], s44 offen offset:0 sc1\n"
"v_mfma_f32_16x16x32_bf16 v[0:3], v[80:83], a[208:211], v[0:3]\n"
"v_add_u32 v243, 0x10000, v243\n"
"v_mfma_f32_16x16x32_bf16 v[16:19], v[64:67], a[196:199], v[16:19]\n"
"v_add_u32 v244, 0x10000, v244\n"
"v_mfma_f32_16x16x32_bf16 v[16:19], v[80:83], a[212:215], v[16:19]\n"
"v_add_u32 v245, 0x10000, v245\n"
"v_mfma_f32_16x16x32_bf16 v[32:35], v[64:67], a[200:203], v[32:35]\n"
"v_mfma_f32_16x16x32_bf16 v[32:35], v[80:83], a[216:219], v[32:35]\n"
"ds_read_b64_tr_b16 v[96:97], v242 offset:0\n"
"v_mfma_f32_16x16x32_bf16 v[48:51], v[64:67], a[204:207], v[48:51]\n"
"ds_read_b64_tr_b16 v[98:99], v242 offset:2048\n"
"v_mfma_f32_16x16x32_bf16 v[48:51], v[80:83], a[220:223], v[48:51]\n"
"ds_read_b64_tr_b16 v[112:113], v242 offset:4096\n"
"v_mfma_f32_16x16x32_bf16 v[4:7], v[68:71], a[192:195], v[4:7]\n"
"ds_read_b64_tr_b16 v[114:115], v242 offset:6144\n"
"buffer_load_dwordx4 v[132:135], v248, s[40:43], s44 offen offset:1024 sc1\n"
"v_mfma_f32_16x16x32_bf16 v[4:7], v[84:87], a[208:211], v[4:7]\n"
"v_mfma_f32_16x16x32_bf16 v[20:23], v[68:71], a[196:199], v[20:23]\n"
"ds_read_b64_tr_b16 v[100:101], v243 offset:0\n"
"v_mfma_f32_16x16x32_bf16 v[20:23], v[84:87], a[212:215], v[20:23]\n"
"ds_read_b64_tr_b16 v[102:103], v243 offset:2048\n"
"v_mfma_f32_16x16x32_bf16 v[36:39], v[68:71], a[200:203], v[36:39]\n"
"ds_read_b64_tr_b16 v[116:117], v243 offset:4096\n"
"v_mfma_f32_16x16x32_bf16 v[36:39], v[84:87], a[216:219], v[36:39]\n"
"ds_read_b64_tr_b16 v[118:119], v243 offset:6144\n"
"v_mfma_f32_16x16x32_bf16 v[52:55], v[68:71], a[204:207], v[52:55]\n"
"v_mfma_f32_16x16x32_bf16 v[52:55], v[84:87], a[220:223], v[52:55]\n"
"ds_read_b64_tr_b16 v[104:105], v244 offset:0\n"
"v_mfma_f32_16x16x32_bf16 v[8:11], v[72:75], a[192:195], v[8:11]\n"
"ds_read_b64_tr_b16 v[106:107], v244 offset:2048\n"
"buffer_load_dwordx4 v[136:139], v248, s[40:43], s44 offen offset:2048 sc1\n"
"v_mfma_f32_16x16x32_bf16 v[8:11], v[88:91], a[208:211], v[8:11]\n"
"ds_read_b64_tr_b16 v[120:121], v244 offset:4096\n"
"v_mfma_f32_16x16x32_bf16 v[24:27], v[72:75], a[196:199], v[24:27]\n"
"ds_read_b64_tr_b16 v[122:123], v244 offset:6144\n"
"v_mfma_f32_16x16x32_bf16 v[24:27], v[88:91], a[212:215], v[24:27]\n"
"v_mfma_f32_16x16x32_bf16 v[40:43], v[72:75], a[200:203], v[40:43]\n"
"ds_read_b64_tr_b16 v[108:109], v245 offset:0\n"
"v_mfma_f32_16x16x32_bf16 v[40:43], v[88:91], a[216:219], v[40:43]\n"
"ds_read_b64_tr_b16 v[110:111], v245 offset:2048\n"
"v_mfma_f32_16x16x32_bf16 v[56:59], v[72:75], a[204:207], v[56:59]\n"
"ds_read_b64_tr_b16 v[124:125], v245 offset:4096\n"
"v_mfma_f32_16x16x32_bf16 v[56:59], v[88:91], a[220:223], v[56:59]\n"
"ds_read_b64_tr_b16 v[126:127], v245 offset:6144\n"
"v_mfma_f32_16x16x32_bf16 v[12:15], v[76:79], a[192:195], v[12:15]\n"
"buffer_load_dwordx4 v[140:143], v248, s[40:43], s44 offen offset:3072 sc1\n"
"v_mfma_f32_16x16x32_bf16 v[12:15], v[92:95], a[208:211], v[12:15]\n"
"v_mfma_f32_16x16x32_bf16 v[28:31], v[76:79], a[196:199], v[28:31]\n"
"v_mfma_f32_16x16x32_bf16 v[28:31], v[92:95], a[212:215], v[28:31]\n"
"v_mfma_f32_16x16x32_bf16 v[44:47], v[76:79], a[200:203], v[44:47]\n"
"v_mfma_f32_16x16x32_bf16 v[44:47], v[92:95], a[216:219], v[44:47]\n"
"v_mfma_f32_16x16x32_bf16 v[60:63], v[76:79], a[204:207], v[60:63]\n"
"v_mfma_f32_16x16x32_bf16 v[60:63], v[92:95], a[220:223], v[60:63]\n"
"s_waitcnt vmcnt(12) lgkmcnt(0)\n"
"v_mfma_f32_16x16x32_bf16 v[0:3], v[96:99], a[224:227], v[0:3]\n"
"ds_read_b64_tr_b16 v[64:65], v242 offset:32768\n"
"v_mfma_f32_16x16x32_bf16 v[0:3], v[112:115], a[240:243], v[0:3]\n"
"ds_read_b64_tr_b16 v[66:67], v242 offset:34816\n"
"v_mfma_f32_16x16x32_bf16 v[16:19], v[96:99], a[228:231], v[16:19]\n"
"v_mfma_f32_16x16x32_bf16 v[16:19], v[112:115], a[244:247], v[16:19]\n"
"ds_read_b64_tr_b16 v[80:81], v242 offset:36864\n"
"v_mfma_f32_16x16x32_bf16 v[32:35], v[96:99], a[232:235], v[32:35]\n"
"ds_read_b64_tr_b16 v[82:83], v242 offset:38912\n"
"v_mfma_f32_16x16x32_bf16 v[32:35], v[112:115], a[248:251], v[32:35]\n"
"v_mfma_f32_16x16x32_bf16 v[48:51], v[96:99], a[236:239], v[48:51]\n"
"ds_read_b64_tr_b16 v[68:69], v243 offset:32768\n"
"v_mfma_f32_16x16x32_bf16 v[48:51], v[112:115], a[252:255], v[48:51]\n"
"ds_read_b64_tr_b16 v[70:71], v243 offset:34816\n"
"v_mfma_f32_16x16x32_bf16 v[4:7], v[100:103], a[224:227], v[4:7]\n"
"v_mfma_f32_16x16x32_bf16 v[4:7], v[116:119], a[240:243], v[4:7]\n"
"ds_read_b64_tr_b16 v[84:85], v243 offset:36864\n"
"v_mfma_f32_16x16x32_bf16 v[20:23], v[100:103], a[228:231], v[20:23]\n"
"ds_read_b64_tr_b16 v[86:87], v243 offset:38912\n"
"v_mfma_f32_16x16x32_bf16 v[20:23], v[116:119], a[244:247], v[20:23]\n"
"v_mfma_f32_16x16x32_bf16 v[36:39], v[100:103], a[232:235], v[36:39]\n"
"ds_read_b64_tr_b16 v[72:73], v244 offset:32768\n"
"v_mfma_f32_16x16x32_bf16 v[36:39], v[116:119], a[248:251], v[36:39]\n"
"v_mfma_f32_16x16x32_bf16 v[52:55], v[100:103], a[236:239], v[52:55]\n"
"ds_read_b64_tr_b16 v[74:75], v244 offset:34816\n"
"v_mfma_f32_16x16x32_bf16 v[52:55], v[116:119], a[252:255], v[52:55]\n"
"ds_read_b64_tr_b16 v[88:89], v244 offset:36864\n"
"v_mfma_f32_16x16x32_bf16 v[8:11], v[104:107], a[224:227], v[8:11]\n"
"v_mfma_f32_16x16x32_bf16 v[8:11], v[120:123], a[240:243], v[8:11]\n"
"ds_read_b64_tr_b16 v[90:91], v244 offset:38912\n"
"v_mfma_f32_16x16x32_bf16 v[24:27], v[104:107], a[228:231], v[24:27]\n"
"ds_read_b64_tr_b16 v[76:77], v245 offset:32768\n"
"v_mfma_f32_16x16x32_bf16 v[24:27], v[120:123], a[244:247], v[24:27]\n"
"v_mfma_f32_16x16x32_bf16 v[40:43], v[104:107], a[232:235], v[40:43]\n"
"ds_read_b64_tr_b16 v[78:79], v245 offset:34816\n"
"v_mfma_f32_16x16x32_bf16 v[40:43], v[120:123], a[248:251], v[40:43]\n"
"ds_read_b64_tr_b16 v[92:93], v245 offset:36864\n"
"v_mfma_f32_16x16x32_bf16 v[56:59], v[104:107], a[236:239], v[56:59]\n"
"v_mfma_f32_16x16x32_bf16 v[56:59], v[120:123], a[252:255], v[56:59]\n"
"ds_read_b64_tr_b16 v[94:95], v245 offset:38912\n"
"v_mfma_f32_16x16x32_bf16 v[12:15], v[108:111], a[224:227], v[12:15]\n"
"v_mfma_f32_16x16x32_bf16 v[12:15], v[124:127], a[240:243], v[12:15]\n"
"v_mfma_f32_16x16x32_bf16 v[28:31], v[108:111], a[228:231], v[28:31]\n"
"v_mfma_f32_16x16x32_bf16 v[28:31], v[124:127], a[244:247], v[28:31]\n"
"v_mfma_f32_16x16x32_bf16 v[44:47], v[108:111], a[232:235], v[44:47]\n"
"v_mfma_f32_16x16x32_bf16 v[44:47], v[124:127], a[248:251], v[44:47]\n"
"v_mfma_f32_16x16x32_bf16 v[60:63], v[108:111], a[236:239], v[60:63]\n"
"v_mfma_f32_16x16x32_bf16 v[60:63], v[124:127], a[252:255], v[60:63]\n"
"s_waitcnt vmcnt(4) lgkmcnt(0)\n"
"s_barrier\n"
"v_mfma_f32_16x16x32_bf16 v[0:3], v[64:67], a[64:67], v[0:3]\n"
"v_mfma_f32_16x16x32_bf16 v[0:3], v[80:83], a[80:83], v[0:3]\n"
"v_mfma_f32_16x16x32_bf16 v[4:7], v[68:71], a[64:67], v[4:7]\n"
"v_mfma_f32_16x16x32_bf16 v[4:7], v[84:87], a[80:83], v[4:7]\n"
"v_mfma_f32_16x16x32_bf16 v[8:11], v[72:75], a[64:67], v[8:11]\n"
"v_mfma_f32_16x16x32_bf16 v[8:11], v[88:91], a[80:83], v[8:11]\n"
"v_mfma_f32_16x16x32_bf16 v[12:15], v[76:79], a[64:67], v[12:15]\n"
"v_mfma_f32_16x16x32_bf16 v[12:15], v[92:95], a[80:83], v[12:15]\n"
"v_mfma_f32_16x16x32_bf16 v[16:19], v[64:67], a[68:71], v[16:19]\n"
"v_mfma_f32_16x16x32_bf16 v[16:19], v[80:83], a[84:87], v[16:19]\n"
"s_nop 0\n"
"ds_write_b128 v252, v[0:3] offset:0\n"
"v_mfma_f32_16x16x32_bf16 v[20:23], v[68:71], a[68:71], v[20:23]\n"
"v_mfma_f32_16x16x32_bf16 v[20:23], v[84:87], a[84:87], v[20:23]\n"
"s_nop 0\n"
"ds_write_b128 v252, v[4:7] offset:1024\n"
"v_mfma_f32_16x16x32_bf16 v[24:27], v[72:75], a[68:71], v[24:27]\n"
"v_mfma_f32_16x16x32_bf16 v[24:27], v[88:91], a[84:87], v[24:27]\n"
"s_nop 0\n"
"ds_write_b128 v252, v[8:11] offset:2048\n"
"v_mfma_f32_16x16x32_bf16 v[28:31], v[76:79], a[68:71], v[28:31]\n"
"v_mfma_f32_16x16x32_bf16 v[28:31], v[92:95], a[84:87], v[28:31]\n"
"s_nop 0\n"
"ds_write_b128 v252, v[12:15] offset:3072\n"
"v_mfma_f32_16x16x32_bf16 v[32:35], v[64:67], a[72:75], v[32:35]\n"
"v_mfma_f32_16x16x32_bf16 v[32:35], v[80:83], a[88:91], v[32:35]\n"
"s_nop 0\n"
"ds_write_b128 v252, v[16:19] offset:16384\n"
"v_mfma_f32_16x16x32_bf16 v[36:39], v[68:71], a[72:75], v[36:39]\n"
"v_mfma_f32_16x16x32_bf16 v[36:39], v[84:87], a[88:91], v[36:39]\n"
"s_nop 0\n"
"ds_write_b128 v252, v[20:23] offset:17408\n"
"v_mfma_f32_16x16x32_bf16 v[40:43], v[72:75], a[72:75], v[40:43]\n"
"v_mfma_f32_16x16x32_bf16 v[40:43], v[88:91], a[88:91], v[40:43]\n"
"s_nop 0\n"
"ds_write_b128 v252, v[24:27] offset:18432\n"
"v_mfma_f32_16x16x32_bf16 v[44:47], v[76:79], a[72:75], v[44:47]\n"
"v_mfma_f32_16x16x32_bf16 v[44:47], v[92:95], a[88:91], v[44:47]\n"
"s_nop 0\n"
"ds_write_b128 v252, v[28:31] offset:19456\n"
"v_mfma_f32_16x16x32_bf16 v[48:51], v[64:67], a[76:79], v[48:51]\n"
"v_mfma_f32_16x16x32_bf16 v[48:51], v[80:83], a[92:95], v[48:51]\n"
"s_nop 0\n"
"ds_write_b128 v252, v[32:35] offset:32768\n"
"v_mfma_f32_16x16x32_bf16 v[52:55], v[68:71], a[76:79], v[52:55]\n"
"v_mfma_f32_16x16x32_bf16 v[52:55], v[84:87], a[92:95], v[52:55]\n"
"s_nop 0\n"
"ds_write_b128 v252, v[36:39] offset:33792\n"
"v_mfma_f32_16x16x32_bf16 v[56:59], v[72:75], a[76:79], v[56:59]\n"
"v_mfma_f32_16x16x32_bf16 v[56:59], v[88:91], a[92:95], v[56:59]\n"
"s_nop 0\n"
"ds_write_b128 v252, v[40:43] offset:34816\n"
"v_mfma_f32_16x16x32_bf16 v[60:63], v[76:79], a[76:79], v[60:63]\n"
"v_mfma_f32_16x16x32_bf16 v[60:63], v[92:95], a[92:95], v[60:63]\n"
"s_nop 0\n"
"ds_write_b128 v252, v[44:47] offset:35840\n"
"s_nop 3\n"
"ds_write_b128 v252, v[48:51] offset:49152\n"
"ds_write_b128 v252, v[52:55] offset:50176\n"
"ds_write_b128 v252, v[56:59] offset:51200\n"
"ds_write_b128 v252, v[60:63] offset:52224\n"
"s_waitcnt lgkmcnt(0)\n"
"s_barrier\n"
"ds_read_b128 v[64:67], v253 offset:0\n"
"ds_read_b128 v[68:71], v253 offset:1024\n"
"ds_read_b128 v[72:75], v253 offset:2048\n"
"ds_read_b128 v[76:79], v253 offset:3072\n"
"ds_read_b128 v[80:83], v253 offset:4096\n"
"ds_read_b128 v[84:87], v253 offset:5120\n"
"ds_read_b128 v[88:91], v253 offset:6144\n"
"ds_read_b128 v[92:95], v253 offset:7168\n"
"ds_read_b128 v[96:99], v253 offset:8192\n"
"ds_read_b128 v[100:103], v253 offset:9216\n"
"ds_read_b128 v[104:107], v253 offset:10240\n"
"ds_read_b128 v[108:111], v253 offset:11264\n"
"ds_read_b128 v[112:115], v253 offset:12288\n"
"ds_read_b128 v[116:119], v253 offset:13312\n"
"ds_read_b128 v[120:123], v253 offset:14336\n"
"ds_read_b128 v[124:127], v253 offset:15360\n"
"s_waitcnt vmcnt(0) lgkmcnt(8)\n"
"v_add_f32 v64, v64, v80\n"
"v_add_f32 v65, v65, v81\n"
"v_add_f32 v66, v66, v82\n"
"v_add_f32 v67, v67, v83\n"
"v_add_f32 v68, v68, v84\n"
"v_add_f32 v69, v69, v85\n"
"v_add_f32 v70, v70, v86\n"
"v_add_f32 v71, v71, v87\n"
"v_add_f32 v72, v72, v88\n"
"v_add_f32 v73, v73, v89\n"
"v_add_f32 v74, v74, v90\n"
"v_add_f32 v75, v75, v91\n"
"v_add_f32 v76, v76, v92\n"
"v_add_f32 v77, v77, v93\n"
"v_add_f32 v78, v78, v94\n"
"v_add_f32 v79, v79, v95\n"
"s_waitcnt lgkmcnt(0)\n"
"v_add_f32 v96, v96, v112\n"
"v_add_f32 v97, v97, v113\n"
"v_add_f32 v98, v98, v114\n"
"v_add_f32 v99, v99, v115\n"
"v_add_f32 v100, v100, v116\n"
"v_add_f32 v101, v101, v117\n"
"v_add_f32 v102, v102, v118\n"
"v_add_f32 v103, v103, v119\n"
"v_add_f32 v104, v104, v120\n"
"v_add_f32 v105, v105, v121\n"
"v_add_f32 v106, v106, v122\n"
"v_add_f32 v107, v107, v123\n"
"v_add_f32 v108, v108, v124\n"
"v_add_f32 v109, v109, v125\n"
"v_add_f32 v110, v110, v126\n"
"v_add_f32 v111, v111, v127\n"
"v_add_f32 v64, v64, v96\n"
"v_add_f32 v65, v65, v97\n"
"v_add_f32 v66, v66, v98\n"
"v_add_f32 v67, v67, v99\n"
"v_add_f32 v68, v68, v100\n"
"v_add_f32 v69, v69, v101\n"
"v_add_f32 v70, v70, v102\n"
"v_add_f32 v71, v71, v103\n"
"v_add_f32 v72, v72, v104\n"
"v_add_f32 v73, v73, v105\n"
"v_add_f32 v74, v74, v106\n"
"v_add_f32 v75, v75, v107\n"
"v_add_f32 v76, v76, v108\n"
"v_add_f32 v77, v77, v109\n"
"v_add_f32 v78, v78, v110\n"
"v_add_f32 v79, v79, v111\n"
"v_fma_f32 v64, v64, v144, v128\n"
"v_fma_f32 v65, v65, v145, v129\n"
"v_fma_f32 v66, v66, v146, v130\n"
"v_fma_f32 v67, v67, v147, v131\n"
"v_fma_f32 v68, v68, v148, v132\n"
"v_fma_f32 v69, v69, v149, v133\n"
"v_fma_f32 v70, v70, v150, v134\n"
"v_fma_f32 v71, v71, v151, v135\n"
"v_fma_f32 v72, v72, v152, v136\n"
"v_fma_f32 v73, v73, v153, v137\n"
"v_fma_f32 v74, v74, v154, v138\n"
"v_fma_f32 v75, v75, v155, v139\n"
"v_fma_f32 v76, v76, v156, v140\n"
"v_fma_f32 v77, v77, v157, v141\n"
"v_fma_f32 v78, v78, v158, v142\n"
"v_fma_f32 v79, v79, v159, v143\n"
"v_cvt_pk_bf16_f32 v176, v64, v65\n"
"v_cvt_pk_bf16_f32 v177, v66, v67\n"
"v_cvt_pk_bf16_f32 v178, v68, v69\n"
"v_cvt_pk_bf16_f32 v179, v70, v71\n"
"s_nop 1\n"
"v_permlane16_swap_b32 v176, v178\n"
"v_permlane16_swap_b32 v177, v179\n"
"buffer_store_dwordx4 v[176:179], v239, s[36:39], s49 offen offset:0 sc1\n"
"v_cvt_pk_bf16_f32 v180, v72, v73\n"
"v_cvt_pk_bf16_f32 v181, v74, v75\n"
"v_cvt_pk_bf16_f32 v182, v76, v77\n"
"v_cvt_pk_bf16_f32 v183, v78, v79\n"
"s_nop 1\n"
"v_permlane16_swap_b32 v180, v182\n"
"v_permlane16_swap_b32 v181, v183\n"
"buffer_store_dwordx4 v[180:183], v239, s[36:39], s49 offen offset:64 sc1\n"
"s_cmp_lg_u32 s84, 0\n"
"s_cbranch_scc1 Leb104_%=\n"
"v_mov_b32 v253, s82\n"
"s_mov_b64 exec, 1\n"
"buffer_store_dword v253, off, s[88:91], 0 sc1\n"
"s_mov_b64 exec, -1\n"
"Leb104_%=:\n"
"s_branch LEND_%=\n"
"Lgs2_%=:\n"
"s_mov_b32 s96, 30000\n"
"Lgl3_%=:\n"
"s_sleep 1\n"
"buffer_load_dwordx4 v[192:195], v224, s[88:91], s55 offen offset:0 sc1\n"
"buffer_load_dwordx4 v[196:199], v224, s[88:91], s55 offen offset:512 sc1\n"
"buffer_load_dwordx4 v[200:203], v224, s[88:91], s55 offen offset:1024 sc1\n"
"buffer_load_dwordx4 v[204:207], v224, s[88:91], s55 offen offset:1536 sc1\n"
"s_waitcnt vmcnt(0)\n"
"v_xor_b32 v227, s82, v193\n"
"v_bitop3_b32 v227, v195, s82, v227 bitop3:0xbe\n"
"v_bitop3_b32 v227, v197, s82, v227 bitop3:0xbe\n"
"v_bitop3_b32 v227, v199, s82, v227 bitop3:0xbe\n"
"v_bitop3_b32 v227, v201, s82, v227 bitop3:0xbe\n"
"v_bitop3_b32 v227, v203, s82, v227 bitop3:0xbe\n"
"v_bitop3_b32 v227, v205, s82, v227 bitop3:0xbe\n"
"v_bitop3_b32 v227, v207, s82, v227 bitop3:0xbe\n"
"v_cmp_ne_u32 vcc, 0, v227\n"
"s_cbranch_vccz Lgb1_%=\n"
"s_sub_u32 s96, s96, 1\n"
"s_cmp_lg_u32 s96, 0\n"
"s_cbranch_scc1 Lgl3_%=\n"
"s_branch Lgb1_%=\n"
"Lgs5_%=:\n"
"s_mov_b32 s96, 30000\n"
"Lgl6_%=:\n"
"s_sleep 1\n"
"buffer_load_dwordx4 v[200:203], v247, s[88:91], s55 offen offset:0 sc1\n"
"buffer_load_dwordx4 v[204:207], v247, s[88:91], s55 offen offset:16 sc1\n"
"buffer_load_dwordx4 v[224:227], v247, s[88:91], s55 offen offset:2048 sc1\n"
"buffer_load_dwordx4 v[228:231], v247, s[88:91], s55 offen offset:2064 sc1\n"
"s_waitcnt vmcnt(0)\n"
"v_xor_b32 v232, s82, v201\n"
"v_bitop3_b32 v232, v203, s82, v232 bitop3:0xbe\n"
"v_bitop3_b32 v232, v205, s82, v232 bitop3:0xbe\n"
"v_bitop3_b32 v232, v207, s82, v232 bitop3:0xbe\n"
"v_bitop3_b32 v232, v225, s82, v232 bitop3:0xbe\n"
"v_bitop3_b32 v232, v227, s82, v232 bitop3:0xbe\n"
"v_bitop3_b32 v232, v229, s82, v232 bitop3:0xbe\n"
"v_bitop3_b32 v232, v231, s82, v232 bitop3:0xbe\n"
"v_cmp_ne_u32 vcc, 0, v232\n"
"s_cbranch_vccz Lgb4_%=\n"
"s_sub_u32 s96, s96, 1\n"
"s_cmp_lg_u32 s96, 0\n"
"s_cbranch_scc1 Lgl6_%=\n"
"s_branch Lgb4_%=\n"
"Lgs8_%=:\n"
"s_mov_b32 s96, 30000\n"
"Lgl9_%=:\n"
"s_sleep 1\n"
"buffer_load_dwordx4 v[200:203], v247, s[88:91], s55 offen offset:256 sc1\n"
"buffer_load_dwordx4 v[204:207], v247, s[88:91], s55 offen offset:272 sc1\n"
"buffer_load_dwordx4 v[224:227], v247, s[88:91], s55 offen offset:2304 sc1\n"
"buffer_load_dwordx4 v[228:231], v247, s[88:91], s55 offen offset:2320 sc1\n"
"s_waitcnt vmcnt(0)\n"
"v_xor_b32 v232, s82, v201\n"
"v_bitop3_b32 v232, v203, s82, v232 bitop3:0xbe\n"
"v_bitop3_b32 v232, v205, s82, v232 bitop3:0xbe\n"
"v_bitop3_b32 v232, v207, s82, v232 bitop3:0xbe\n"
"v_bitop3_b32 v232, v225, s82, v232 bitop3:0xbe\n"
"v_bitop3_b32 v232, v227, s82, v232 bitop3:0xbe\n"
"v_bitop3_b32 v232, v229, s82, v232 bitop3:0xbe\n"
"v_bitop3_b32 v232, v231, s82, v232 bitop3:0xbe\n"
"v_cmp_ne_u32 vcc, 0, v232\n"
"s_cbranch_vccz Lgb7_%=\n"
"s_sub_u32 s96, s96, 1\n"
"s_cmp_lg_u32 s96, 0\n"
"s_cbranch_scc1 Lgl9_%=\n"
"s_branch Lgb7_%=\n"
"Lps11_%=:\n"
"s_mov_b32 s96, 30000\n"
"Lpl12_%=:\n"
"s_sleep 8\n"
"buffer_load_dword v233, v232, s[88:91], s97 offen sc1\n"
"s_waitcnt vmcnt(0)\n"
"v_cmp_ne_u32 vcc, s82, v233\n"
"s_cbranch_vccz Lpd13_%=\n"
"s_sub_u32 s96, s96, 1\n"
"s_cmp_lg_u32 s96, 0\n"
"s_cbranch_scc1 Lpl12_%=\n"
"Lpd13_%=:\n"
"s_branch Lpb10_%=\n"
"Lps15_%=:\n"
"s_mov_b32 s96, 30000\n"
"Lpl16_%=:\n"
"s_sleep 8\n"
"buffer_load_dword v233, v232, s[88:91], s97 offen sc1\n"
"s_waitcnt vmcnt(0)\n"
"v_cmp_ne_u32 vcc, s82, v233\n"
"s_cbranch_vccz Lpd17_%=\n"
"s_sub_u32 s96, s96, 1\n"
"s_cmp_lg_u32 s96, 0\n"
"s_cbranch_scc1 Lpl16_%=\n"
"Lpd17_%=:\n"
"s_branch Lpb14_%=\n"
"Lmo19_%=:\n"
"s_cmp_eq_u32 s49, s47\n"
"s_cbranch_scc1 Lmd20_%=\n"
"v_mov_b32 v80, v246\n"
"v_mov_b32 v81, v246\n"
"v_mov_b32 v82, v246\n"
"v_mov_b32 v83, v246\n"
"v_mov_b32 v84, v246\n"
"v_mov_b32 v85, v246\n"
"v_mov_b32 v86, v246\n"
"v_mov_b32 v87, v246\n"
"v_mov_b32 v88, v246\n"
"v_mov_b32 v89, v246\n"
"v_mov_b32 v90, v246\n"
"v_mov_b32 v91, v246\n"
"v_mov_b32 v92, v246\n"
"v_mov_b32 v93, v246\n"
"v_mov_b32 v94, v246\n"
"v_mov_b32 v95, v246\n"
"s_branch Lmb18_%=\n"
"Lmd20_%=:\n"
"v_cmp_le_i32 vcc, 0, v249\n"
"s_nop 1\n"
"v_cndmask_b32 v80, v246, v80, vcc\n"
"v_cmp_le_i32 vcc, 1, v249\n"
"s_nop 1\n"
"v_cndmask_b32 v81, v246, v81, vcc\n"
"v_cmp_le_i32 vcc, 2, v249\n"
"s_nop 1\n"
"v_cndmask_b32 v82, v246, v82, vcc\n"
"v_cmp_le_i32 vcc, 3, v249\n"
"s_nop 1\n"
"v_cndmask_b32 v83, v246, v83, vcc\n"
"v_cmp_le_i32 vcc, 8, v249\n"
"s_nop 1\n"
"v_cndmask_b32 v84, v246, v84, vcc\n"
"v_cmp_le_i32 vcc, 9, v249\n"
"s_nop 1\n"
"v_cndmask_b32 v85, v246, v85, vcc\n"
"v_cmp_le_i32 vcc, 10, v249\n"
"s_nop 1\n"
"v_cndmask_b32 v86, v246, v86, vcc\n"
"v_cmp_le_i32 vcc, 11, v249\n"
"s_nop 1\n"
"v_cndmask_b32 v87, v246, v87, vcc\n"
"v_cmp_le_i32 vcc, 16, v249\n"
"s_nop 1\n"
"v_cndmask_b32 v88, v246, v88, vcc\n"
"v_cmp_le_i32 vcc, 17, v249\n"
"s_nop 1\n"
"v_cndmask_b32 v89, v246, v89, vcc\n"
"v_cmp_le_i32 vcc, 18, v249\n"
"s_nop 1\n"
"v_cndmask_b32 v90, v246, v90, vcc\n"
"v_cmp_le_i32 vcc, 19, v249\n"
"s_nop 1\n"
"v_cndmask_b32 v91, v246, v91, vcc\n"
"v_cmp_le_i32 vcc, 24, v249\n"
"s_nop 1\n"
"v_cndmask_b32 v92, v246, v92, vcc\n"
"v_cmp_le_i32 vcc, 25, v249\n"
"s_nop 1\n"
"v_cndmask_b32 v93, v246, v93, vcc\n"
"v_cmp_le_i32 vcc, 26, v249\n"
"s_nop 1\n"
"v_cndmask_b32 v94, v246, v94, vcc\n"
"v_cmp_le_i32 vcc, 27, v249\n"
"s_nop 1\n"
"v_cndmask_b32 v95, v246, v95, vcc\n"
"s_branch Lmb18_%=\n"
"Lmo22_%=:\n"
"s_cmp_eq_u32 s49, s47\n"
"s_cbranch_scc1 Lmd23_%=\n"
"v_mov_b32 v112, v246\n"
"v_mov_b32 v113, v246\n"
"v_mov_b32 v114, v246\n"
"v_mov_b32 v115, v246\n"
"v_mov_b32 v116, v246\n"
"v_mov_b32 v117, v246\n"
"v_mov_b32 v118, v246\n"
"v_mov_b32 v119, v246\n"
"v_mov_b32 v120, v246\n"
"v_mov_b32 v121, v246\n"
"v_mov_b32 v122, v246\n"
"v_mov_b32 v123, v246\n"
"v_mov_b32 v124, v246\n"
"v_mov_b32 v125, v246\n"
"v_mov_b32 v126, v246\n"
"v_mov_b32 v127, v246\n"
"s_branch Lmb21_%=\n"
"Lmd23_%=:\n"
"v_cmp_le_i32 vcc, 0, v249\n"
"s_nop 1\n"
"v_cndmask_b32 v112, v246, v112, vcc\n"
"v_cmp_le_i32 vcc, 1, v249\n"
"s_nop 1\n"
"v_cndmask_b32 v113, v246, v113, vcc\n"
"v_cmp_le_i32 vcc, 2, v249\n"
"s_nop 1\n"
"v_cndmask_b32 v114, v246, v114, vcc\n"
"v_cmp_le_i32 vcc, 3, v249\n"
"s_nop 1\n"
"v_cndmask_b32 v115, v246, v115, vcc\n"
"v_cmp_le_i32 vcc, 8, v249\n"
"s_nop 1\n"
"v_cndmask_b32 v116, v246, v116, vcc\n"
"v_cmp_le_i32 vcc, 9, v249\n"
"s_nop 1\n"
"v_cndmask_b32 v117, v246, v117, vcc\n"
"v_cmp_le_i32 vcc, 10, v249\n"
"s_nop 1\n"
"v_cndmask_b32 v118, v246, v118, vcc\n"
"v_cmp_le_i32 vcc, 11, v249\n"
"s_nop 1\n"
"v_cndmask_b32 v119, v246, v119, vcc\n"
"v_cmp_le_i32 vcc, 16, v249\n"
"s_nop 1\n"
"v_cndmask_b32 v120, v246, v120, vcc\n"
"v_cmp_le_i32 vcc, 17, v249\n"
"s_nop 1\n"
"v_cndmask_b32 v121, v246, v121, vcc\n"
"v_cmp_le_i32 vcc, 18, v249\n"
"s_nop 1\n"
"v_cndmask_b32 v122, v246, v122, vcc\n"
"v_cmp_le_i32 vcc, 19, v249\n"
"s_nop 1\n"
"v_cndmask_b32 v123, v246, v123, vcc\n"
"v_cmp_le_i32 vcc, 24, v249\n"
"s_nop 1\n"
"v_cndmask_b32 v124, v246, v124, vcc\n"
"v_cmp_le_i32 vcc, 25, v249\n"
"s_nop 1\n"
"v_cndmask_b32 v125, v246, v125, vcc\n"
"v_cmp_le_i32 vcc, 26, v249\n"
"s_nop 1\n"
"v_cndmask_b32 v126, v246, v126, vcc\n"
"v_cmp_le_i32 vcc, 27, v249\n"
"s_nop 1\n"
"v_cndmask_b32 v127, v246, v127, vcc\n"
"s_branch Lmb21_%=\n"
"Lmo27_%=:\n"
"s_cmp_eq_u32 s49, s47\n"
"s_cbranch_scc1 Lmd28_%=\n"
"v_mov_b32 v80, v246\n"
"v_mov_b32 v81, v246\n"
"v_mov_b32 v82, v246\n"
"v_mov_b32 v83, v246\n"
"v_mov_b32 v84, v246\n"
"v_mov_b32 v85, v246\n"
"v_mov_b32 v86, v246\n"
"v_mov_b32 v87, v246\n"
"v_mov_b32 v88, v246\n"
"v_mov_b32 v89, v246\n"
"v_mov_b32 v90, v246\n"
"v_mov_b32 v91, v246\n"
"v_mov_b32 v92, v246\n"
"v_mov_b32 v93, v246\n"
"v_mov_b32 v94, v246\n"
"v_mov_b32 v95, v246\n"
"s_branch Lmb26_%=\n"
"Lmd28_%=:\n"
"v_cmp_le_i32 vcc, 0, v249\n"
"s_nop 1\n"
"v_cndmask_b32 v80, v246, v80, vcc\n"
"v_cmp_le_i32 vcc, 1, v249\n"
"s_nop 1\n"
"v_cndmask_b32 v81, v246, v81, vcc\n"
"v_cmp_le_i32 vcc, 2, v249\n"
"s_nop 1\n"
"v_cndmask_b32 v82, v246, v82, vcc\n"
"v_cmp_le_i32 vcc, 3, v249\n"
"s_nop 1\n"
"v_cndmask_b32 v83, v246, v83, vcc\n"
"v_cmp_le_i32 vcc, 8, v249\n"
"s_nop 1\n"
"v_cndmask_b32 v84, v246, v84, vcc\n"
"v_cmp_le_i32 vcc, 9, v249\n"
"s_nop 1\n"
"v_cndmask_b32 v85, v246, v85, vcc\n"
"v_cmp_le_i32 vcc, 10, v249\n"
"s_nop 1\n"
"v_cndmask_b32 v86, v246, v86, vcc\n"
"v_cmp_le_i32 vcc, 11, v249\n"
"s_nop 1\n"
"v_cndmask_b32 v87, v246, v87, vcc\n"
"v_cmp_le_i32 vcc, 16, v249\n"
"s_nop 1\n"
"v_cndmask_b32 v88, v246, v88, vcc\n"
"v_cmp_le_i32 vcc, 17, v249\n"
"s_nop 1\n"
"v_cndmask_b32 v89, v246, v89, vcc\n"
"v_cmp_le_i32 vcc, 18, v249\n"
"s_nop 1\n"
"v_cndmask_b32 v90, v246, v90, vcc\n"
"v_cmp_le_i32 vcc, 19, v249\n"
"s_nop 1\n"
"v_cndmask_b32 v91, v246, v91, vcc\n"
"v_cmp_le_i32 vcc, 24, v249\n"
"s_nop 1\n"
"v_cndmask_b32 v92, v246, v92, vcc\n"
"v_cmp_le_i32 vcc, 25, v249\n"
"s_nop 1\n"
"v_cndmask_b32 v93, v246, v93, vcc\n"
"v_cmp_le_i32 vcc, 26, v249\n"
"s_nop 1\n"
"v_cndmask_b32 v94, v246, v94, vcc\n"
"v_cmp_le_i32 vcc, 27, v249\n"
"s_nop 1\n"
"v_cndmask_b32 v95, v246, v95, vcc\n"
"s_branch Lmb26_%=\n"
"Lmo32_%=:\n"
"s_cmp_eq_u32 s49, s47\n"
"s_cbranch_scc1 Lmd33_%=\n"
"v_mov_b32 v112, v246\n"
"v_mov_b32 v113, v246\n"
"v_mov_b32 v114, v246\n"
"v_mov_b32 v115, v246\n"
"v_mov_b32 v116, v246\n"
"v_mov_b32 v117, v246\n"
"v_mov_b32 v118, v246\n"
"v_mov_b32 v119, v246\n"
"v_mov_b32 v120, v246\n"
"v_mov_b32 v121, v246\n"
"v_mov_b32 v122, v246\n"
"v_mov_b32 v123, v246\n"
"v_mov_b32 v124, v246\n"
"v_mov_b32 v125, v246\n"
"v_mov_b32 v126, v246\n"
"v_mov_b32 v127, v246\n"
"s_branch Lmb31_%=\n"
"Lmd33_%=:\n"
"v_cmp_le_i32 vcc, 0, v249\n"
"s_nop 1\n"
"v_cndmask_b32 v112, v246, v112, vcc\n"
"v_cmp_le_i32 vcc, 1, v249\n"
"s_nop 1\n"
"v_cndmask_b32 v113, v246, v113, vcc\n"
"v_cmp_le_i32 vcc, 2, v249\n"
"s_nop 1\n"
"v_cndmask_b32 v114, v246, v114, vcc\n"
"v_cmp_le_i32 vcc, 3, v249\n"
"s_nop 1\n"
"v_cndmask_b32 v115, v246, v115, vcc\n"
"v_cmp_le_i32 vcc, 8, v249\n"
"s_nop 1\n"
"v_cndmask_b32 v116, v246, v116, vcc\n"
"v_cmp_le_i32 vcc, 9, v249\n"
"s_nop 1\n"
"v_cndmask_b32 v117, v246, v117, vcc\n"
"v_cmp_le_i32 vcc, 10, v249\n"
"s_nop 1\n"
"v_cndmask_b32 v118, v246, v118, vcc\n"
"v_cmp_le_i32 vcc, 11, v249\n"
"s_nop 1\n"
"v_cndmask_b32 v119, v246, v119, vcc\n"
"v_cmp_le_i32 vcc, 16, v249\n"
"s_nop 1\n"
"v_cndmask_b32 v120, v246, v120, vcc\n"
"v_cmp_le_i32 vcc, 17, v249\n"
"s_nop 1\n"
"v_cndmask_b32 v121, v246, v121, vcc\n"
"v_cmp_le_i32 vcc, 18, v249\n"
"s_nop 1\n"
"v_cndmask_b32 v122, v246, v122, vcc\n"
"v_cmp_le_i32 vcc, 19, v249\n"
"s_nop 1\n"
"v_cndmask_b32 v123, v246, v123, vcc\n"
"v_cmp_le_i32 vcc, 24, v249\n"
"s_nop 1\n"
"v_cndmask_b32 v124, v246, v124, vcc\n"
"v_cmp_le_i32 vcc, 25, v249\n"
"s_nop 1\n"
"v_cndmask_b32 v125, v246, v125, vcc\n"
"v_cmp_le_i32 vcc, 26, v249\n"
"s_nop 1\n"
"v_cndmask_b32 v126, v246, v126, vcc\n"
"v_cmp_le_i32 vcc, 27, v249\n"
"s_nop 1\n"
"v_cndmask_b32 v127, v246, v127, vcc\n"
"s_branch Lmb31_%=\n"
"Lmo37_%=:\n"
"s_cmp_eq_u32 s49, s48\n"
"s_cbranch_scc1 Lmd38_%=\n"
"v_mov_b32 v64, v246\n"
"v_mov_b32 v65, v246\n"
"v_mov_b32 v66, v246\n"
"v_mov_b32 v67, v246\n"
"v_mov_b32 v68, v246\n"
"v_mov_b32 v69, v246\n"
"v_mov_b32 v70, v246\n"
"v_mov_b32 v71, v246\n"
"v_mov_b32 v72, v246\n"
"v_mov_b32 v73, v246\n"
"v_mov_b32 v74, v246\n"
"v_mov_b32 v75, v246\n"
"v_mov_b32 v76, v246\n"
"v_mov_b32 v77, v246\n"
"v_mov_b32 v78, v246\n"
"v_mov_b32 v79, v246\n"
"s_branch Lmb36_%=\n"
"Lmd38_%=:\n"
"v_cmp_le_i32 vcc, 0, v249\n"
"s_nop 1\n"
"v_cndmask_b32 v64, v246, v64, vcc\n"
"v_cmp_le_i32 vcc, 1, v249\n"
"s_nop 1\n"
"v_cndmask_b32 v65, v246, v65, vcc\n"
"v_cmp_le_i32 vcc, 2, v249\n"
"s_nop 1\n"
"v_cndmask_b32 v66, v246, v66, vcc\n"
"v_cmp_le_i32 vcc, 3, v249\n"
"s_nop 1\n"
"v_cndmask_b32 v67, v246, v67, vcc\n"
"v_cmp_le_i32 vcc, 8, v249\n"
"s_nop 1\n"
"v_cndmask_b32 v68, v246, v68, vcc\n"
"v_cmp_le_i32 vcc, 9, v249\n"
"s_nop 1\n"
"v_cndmask_b32 v69, v246, v69, vcc\n"
"v_cmp_le_i32 vcc, 10, v249\n"
"s_nop 1\n"
"v_cndmask_b32 v70, v246, v70, vcc\n"
"v_cmp_le_i32 vcc, 11, v249\n"
"s_nop 1\n"
"v_cndmask_b32 v71, v246, v71, vcc\n"
"v_cmp_le_i32 vcc, 16, v249\n"
"s_nop 1\n"
"v_cndmask_b32 v72, v246, v72, vcc\n"
"v_cmp_le_i32 vcc, 17, v249\n"
"s_nop 1\n"
"v_cndmask_b32 v73, v246, v73, vcc\n"
"v_cmp_le_i32 vcc, 18, v249\n"
"s_nop 1\n"
"v_cndmask_b32 v74, v246, v74, vcc\n"
"v_cmp_le_i32 vcc, 19, v249\n"
"s_nop 1\n"
"v_cndmask_b32 v75, v246, v75, vcc\n"
"v_cmp_le_i32 vcc, 24, v249\n"
"s_nop 1\n"
"v_cndmask_b32 v76, v246, v76, vcc\n"
"v_cmp_le_i32 vcc, 25, v249\n"
"s_nop 1\n"
"v_cndmask_b32 v77, v246, v77, vcc\n"
"v_cmp_le_i32 vcc, 26, v249\n"
"s_nop 1\n"
"v_cndmask_b32 v78, v246, v78, vcc\n"
"v_cmp_le_i32 vcc, 27, v249\n"
"s_nop 1\n"
"v_cndmask_b32 v79, v246, v79, vcc\n"
"s_branch Lmb36_%=\n"
"Lmo40_%=:\n"
"s_cmp_eq_u32 s49, s48\n"
"s_cbranch_scc1 Lmd41_%=\n"
"v_mov_b32 v80, v246\n"
"v_mov_b32 v81, v246\n"
"v_mov_b32 v82, v246\n"
"v_mov_b32 v83, v246\n"
"v_mov_b32 v84, v246\n"
"v_mov_b32 v85, v246\n"
"v_mov_b32 v86, v246\n"
"v_mov_b32 v87, v246\n"
"v_mov_b32 v88, v246\n"
"v_mov_b32 v89, v246\n"
"v_mov_b32 v90, v246\n"
"v_mov_b32 v91, v246\n"
"v_mov_b32 v92, v246\n"
"v_mov_b32 v93, v246\n"
"v_mov_b32 v94, v246\n"
"v_mov_b32 v95, v246\n"
"s_branch Lmb39_%=\n"
"Lmd41_%=:\n"
"v_cmp_le_i32 vcc, 0, v249\n"
"s_nop 1\n"
"v_cndmask_b32 v80, v246, v80, vcc\n"
"v_cmp_le_i32 vcc, 1, v249\n"
"s_nop 1\n"
"v_cndmask_b32 v81, v246, v81, vcc\n"
"v_cmp_le_i32 vcc, 2, v249\n"
"s_nop 1\n"
"v_cndmask_b32 v82, v246, v82, vcc\n"
"v_cmp_le_i32 vcc, 3, v249\n"
"s_nop 1\n"
"v_cndmask_b32 v83, v246, v83, vcc\n"
"v_cmp_le_i32 vcc, 8, v249\n"
"s_nop 1\n"
"v_cndmask_b32 v84, v246, v84, vcc\n"
"v_cmp_le_i32 vcc, 9, v249\n"
"s_nop 1\n"
"v_cndmask_b32 v85, v246, v85, vcc\n"
"v_cmp_le_i32 vcc, 10, v249\n"
"s_nop 1\n"
"v_cndmask_b32 v86, v246, v86, vcc\n"
"v_cmp_le_i32 vcc, 11, v249\n"
"s_nop 1\n"
"v_cndmask_b32 v87, v246, v87, vcc\n"
"v_cmp_le_i32 vcc, 16, v249\n"
"s_nop 1\n"
"v_cndmask_b32 v88, v246, v88, vcc\n"
"v_cmp_le_i32 vcc, 17, v249\n"
"s_nop 1\n"
"v_cndmask_b32 v89, v246, v89, vcc\n"
"v_cmp_le_i32 vcc, 18, v249\n"
"s_nop 1\n"
"v_cndmask_b32 v90, v246, v90, vcc\n"
"v_cmp_le_i32 vcc, 19, v249\n"
"s_nop 1\n"
"v_cndmask_b32 v91, v246, v91, vcc\n"
"v_cmp_le_i32 vcc, 24, v249\n"
"s_nop 1\n"
"v_cndmask_b32 v92, v246, v92, vcc\n"
"v_cmp_le_i32 vcc, 25, v249\n"
"s_nop 1\n"
"v_cndmask_b32 v93, v246, v93, vcc\n"
"v_cmp_le_i32 vcc, 26, v249\n"
"s_nop 1\n"
"v_cndmask_b32 v94, v246, v94, vcc\n"
"v_cmp_le_i32 vcc, 27, v249\n"
"s_nop 1\n"
"v_cndmask_b32 v95, v246, v95, vcc\n"
"s_branch Lmb39_%=\n"
"Lmo45_%=:\n"
"s_cmp_eq_u32 s49, s48\n"
"s_cbranch_scc1 Lmd46_%=\n"
"v_mov_b32 v96, v246\n"
"v_mov_b32 v97, v246\n"
"v_mov_b32 v98, v246\n"
"v_mov_b32 v99, v246\n"
"v_mov_b32 v100, v246\n"
"v_mov_b32 v101, v246\n"
"v_mov_b32 v102, v246\n"
"v_mov_b32 v103, v246\n"
"v_mov_b32 v104, v246\n"
"v_mov_b32 v105, v246\n"
"v_mov_b32 v106, v246\n"
"v_mov_b32 v107, v246\n"
"v_mov_b32 v108, v246\n"
"v_mov_b32 v109, v246\n"
"v_mov_b32 v110, v246\n"
"v_mov_b32 v111, v246\n"
"s_branch Lmb44_%=\n"
"Lmd46_%=:\n"
"v_cmp_le_i32 vcc, 0, v249\n"
"s_nop 1\n"
"v_cndmask_b32 v96, v246, v96, vcc\n"
"v_cmp_le_i32 vcc, 1, v249\n"
"s_nop 1\n"
"v_cndmask_b32 v97, v246, v97, vcc\n"
"v_cmp_le_i32 vcc, 2, v249\n"
"s_nop 1\n"
"v_cndmask_b32 v98, v246, v98, vcc\n"
"v_cmp_le_i32 vcc, 3, v249\n"
"s_nop 1\n"
"v_cndmask_b32 v99, v246, v99, vcc\n"
"v_cmp_le_i32 vcc, 8, v249\n"
"s_nop 1\n"
"v_cndmask_b32 v100, v246, v100, vcc\n"
"v_cmp_le_i32 vcc, 9, v249\n"
"s_nop 1\n"
"v_cndmask_b32 v101, v246, v101, vcc\n"
"v_cmp_le_i32 vcc, 10, v249\n"
"s_nop 1\n"
"v_cndmask_b32 v102, v246, v102, vcc\n"
"v_cmp_le_i32 vcc, 11, v249\n"
"s_nop 1\n"
"v_cndmask_b32 v103, v246, v103, vcc\n"
"v_cmp_le_i32 vcc, 16, v249\n"
"s_nop 1\n"
"v_cndmask_b32 v104, v246, v104, vcc\n"
"v_cmp_le_i32 vcc, 17, v249\n"
"s_nop 1\n"
"v_cndmask_b32 v105, v246, v105, vcc\n"
"v_cmp_le_i32 vcc, 18, v249\n"
"s_nop 1\n"
"v_cndmask_b32 v106, v246, v106, vcc\n"
"v_cmp_le_i32 vcc, 19, v249\n"
"s_nop 1\n"
"v_cndmask_b32 v107, v246, v107, vcc\n"
"v_cmp_le_i32 vcc, 24, v249\n"
"s_nop 1\n"
"v_cndmask_b32 v108, v246, v108, vcc\n"
"v_cmp_le_i32 vcc, 25, v249\n"
"s_nop 1\n"
"v_cndmask_b32 v109, v246, v109, vcc\n"
"v_cmp_le_i32 vcc, 26, v249\n"
"s_nop 1\n"
"v_cndmask_b32 v110, v246, v110, vcc\n"
"v_cmp_le_i32 vcc, 27, v249\n"
"s_nop 1\n"
"v_cndmask_b32 v111, v246, v111, vcc\n"
"s_branch Lmb44_%=\n"
"Lmo48_%=:\n"
"s_cmp_eq_u32 s49, s48\n"
"s_cbranch_scc1 Lmd49_%=\n"
"v_mov_b32 v112, v246\n"
"v_mov_b32 v113, v246\n"
"v_mov_b32 v114, v246\n"
"v_mov_b32 v115, v246\n"
"v_mov_b32 v116, v246\n"
"v_mov_b32 v117, v246\n"
"v_mov_b32 v118, v246\n"
"v_mov_b32 v119, v246\n"
"v_mov_b32 v120, v246\n"
"v_mov_b32 v121, v246\n"
"v_mov_b32 v122, v246\n"
"v_mov_b32 v123, v246\n"
"v_mov_b32 v124, v246\n"
"v_mov_b32 v125, v246\n"
"v_mov_b32 v126, v246\n"
"v_mov_b32 v127, v246\n"
"s_branch Lmb47_%=\n"
"Lmd49_%=:\n"
"v_cmp_le_i32 vcc, 0, v249\n"
"s_nop 1\n"
"v_cndmask_b32 v112, v246, v112, vcc\n"
"v_cmp_le_i32 vcc, 1, v249\n"
"s_nop 1\n"
"v_cndmask_b32 v113, v246, v113, vcc\n"
"v_cmp_le_i32 vcc, 2, v249\n"
"s_nop 1\n"
"v_cndmask_b32 v114, v246, v114, vcc\n"
"v_cmp_le_i32 vcc, 3, v249\n"
"s_nop 1\n"
"v_cndmask_b32 v115, v246, v115, vcc\n"
"v_cmp_le_i32 vcc, 8, v249\n"
"s_nop 1\n"
"v_cndmask_b32 v116, v246, v116, vcc\n"
"v_cmp_le_i32 vcc, 9, v249\n"
"s_nop 1\n"
"v_cndmask_b32 v117, v246, v117, vcc\n"
"v_cmp_le_i32 vcc, 10, v249\n"
"s_nop 1\n"
"v_cndmask_b32 v118, v246, v118, vcc\n"
"v_cmp_le_i32 vcc, 11, v249\n"
"s_nop 1\n"
"v_cndmask_b32 v119, v246, v119, vcc\n"
"v_cmp_le_i32 vcc, 16, v249\n"
"s_nop 1\n"
"v_cndmask_b32 v120, v246, v120, vcc\n"
"v_cmp_le_i32 vcc, 17, v249\n"
"s_nop 1\n"
"v_cndmask_b32 v121, v246, v121, vcc\n"
"v_cmp_le_i32 vcc, 18, v249\n"
"s_nop 1\n"
"v_cndmask_b32 v122, v246, v122, vcc\n"
"v_cmp_le_i32 vcc, 19, v249\n"
"s_nop 1\n"
"v_cndmask_b32 v123, v246, v123, vcc\n"
"v_cmp_le_i32 vcc, 24, v249\n"
"s_nop 1\n"
"v_cndmask_b32 v124, v246, v124, vcc\n"
"v_cmp_le_i32 vcc, 25, v249\n"
"s_nop 1\n"
"v_cndmask_b32 v125, v246, v125, vcc\n"
"v_cmp_le_i32 vcc, 26, v249\n"
"s_nop 1\n"
"v_cndmask_b32 v126, v246, v126, vcc\n"
"v_cmp_le_i32 vcc, 27, v249\n"
"s_nop 1\n"
"v_cndmask_b32 v127, v246, v127, vcc\n"
"s_branch Lmb47_%=\n"
"Lmo53_%=:\n"
"s_cmp_eq_u32 s49, s48\n"
"s_cbranch_scc1 Lmd54_%=\n"
"v_mov_b32 v64, v246\n"
"v_mov_b32 v65, v246\n"
"v_mov_b32 v66, v246\n"
"v_mov_b32 v67, v246\n"
"v_mov_b32 v68, v246\n"
"v_mov_b32 v69, v246\n"
"v_mov_b32 v70, v246\n"
"v_mov_b32 v71, v246\n"
"v_mov_b32 v72, v246\n"
"v_mov_b32 v73, v246\n"
"v_mov_b32 v74, v246\n"
"v_mov_b32 v75, v246\n"
"v_mov_b32 v76, v246\n"
"v_mov_b32 v77, v246\n"
"v_mov_b32 v78, v246\n"
"v_mov_b32 v79, v246\n"
"s_branch Lmb52_%=\n"
"Lmd54_%=:\n"
"v_cmp_le_i32 vcc, 0, v249\n"
"s_nop 1\n"
"v_cndmask_b32 v64, v246, v64, vcc\n"
"v_cmp_le_i32 vcc, 1, v249\n"
"s_nop 1\n"
"v_cndmask_b32 v65, v246, v65, vcc\n"
"v_cmp_le_i32 vcc, 2, v249\n"
"s_nop 1\n"
"v_cndmask_b32 v66, v246, v66, vcc\n"
"v_cmp_le_i32 vcc, 3, v249\n"
"s_nop 1\n"
"v_cndmask_b32 v67, v246, v67, vcc\n"
"v_cmp_le_i32 vcc, 8, v249\n"
"s_nop 1\n"
"v_cndmask_b32 v68, v246, v68, vcc\n"
"v_cmp_le_i32 vcc, 9, v249\n"
"s_nop 1\n"
"v_cndmask_b32 v69, v246, v69, vcc\n"
"v_cmp_le_i32 vcc, 10, v249\n"
"s_nop 1\n"
"v_cndmask_b32 v70, v246, v70, vcc\n"
"v_cmp_le_i32 vcc, 11, v249\n"
"s_nop 1\n"
"v_cndmask_b32 v71, v246, v71, vcc\n"
"v_cmp_le_i32 vcc, 16, v249\n"
"s_nop 1\n"
"v_cndmask_b32 v72, v246, v72, vcc\n"
"v_cmp_le_i32 vcc, 17, v249\n"
"s_nop 1\n"
"v_cndmask_b32 v73, v246, v73, vcc\n"
"v_cmp_le_i32 vcc, 18, v249\n"
"s_nop 1\n"
"v_cndmask_b32 v74, v246, v74, vcc\n"
"v_cmp_le_i32 vcc, 19, v249\n"
"s_nop 1\n"
"v_cndmask_b32 v75, v246, v75, vcc\n"
"v_cmp_le_i32 vcc, 24, v249\n"
"s_nop 1\n"
"v_cndmask_b32 v76, v246, v76, vcc\n"
"v_cmp_le_i32 vcc, 25, v249\n"
"s_nop 1\n"
"v_cndmask_b32 v77, v246, v77, vcc\n"
"v_cmp_le_i32 vcc, 26, v249\n"
"s_nop 1\n"
"v_cndmask_b32 v78, v246, v78, vcc\n"
"v_cmp_le_i32 vcc, 27, v249\n"
"s_nop 1\n"
"v_cndmask_b32 v79, v246, v79, vcc\n"
"s_branch Lmb52_%=\n"
"Lmo56_%=:\n"
"s_cmp_eq_u32 s49, s48\n"
"s_cbranch_scc1 Lmd57_%=\n"
"v_mov_b32 v80, v246\n"
"v_mov_b32 v81, v246\n"
"v_mov_b32 v82, v246\n"
"v_mov_b32 v83, v246\n"
"v_mov_b32 v84, v246\n"
"v_mov_b32 v85, v246\n"
"v_mov_b32 v86, v246\n"
"v_mov_b32 v87, v246\n"
"v_mov_b32 v88, v246\n"
"v_mov_b32 v89, v246\n"
"v_mov_b32 v90, v246\n"
"v_mov_b32 v91, v246\n"
"v_mov_b32 v92, v246\n"
"v_mov_b32 v93, v246\n"
"v_mov_b32 v94, v246\n"
"v_mov_b32 v95, v246\n"
"s_branch Lmb55_%=\n"
"Lmd57_%=:\n"
"v_cmp_le_i32 vcc, 0, v249\n"
"s_nop 1\n"
"v_cndmask_b32 v80, v246, v80, vcc\n"
"v_cmp_le_i32 vcc, 1, v249\n"
"s_nop 1\n"
"v_cndmask_b32 v81, v246, v81, vcc\n"
"v_cmp_le_i32 vcc, 2, v249\n"
"s_nop 1\n"
"v_cndmask_b32 v82, v246, v82, vcc\n"
"v_cmp_le_i32 vcc, 3, v249\n"
"s_nop 1\n"
"v_cndmask_b32 v83, v246, v83, vcc\n"
"v_cmp_le_i32 vcc, 8, v249\n"
"s_nop 1\n"
"v_cndmask_b32 v84, v246, v84, vcc\n"
"v_cmp_le_i32 vcc, 9, v249\n"
"s_nop 1\n"
"v_cndmask_b32 v85, v246, v85, vcc\n"
"v_cmp_le_i32 vcc, 10, v249\n"
"s_nop 1\n"
"v_cndmask_b32 v86, v246, v86, vcc\n"
"v_cmp_le_i32 vcc, 11, v249\n"
"s_nop 1\n"
"v_cndmask_b32 v87, v246, v87, vcc\n"
"v_cmp_le_i32 vcc, 16, v249\n"
"s_nop 1\n"
"v_cndmask_b32 v88, v246, v88, vcc\n"
"v_cmp_le_i32 vcc, 17, v249\n"
"s_nop 1\n"
"v_cndmask_b32 v89, v246, v89, vcc\n"
"v_cmp_le_i32 vcc, 18, v249\n"
"s_nop 1\n"
"v_cndmask_b32 v90, v246, v90, vcc\n"
"v_cmp_le_i32 vcc, 19, v249\n"
"s_nop 1\n"
"v_cndmask_b32 v91, v246, v91, vcc\n"
"v_cmp_le_i32 vcc, 24, v249\n"
"s_nop 1\n"
"v_cndmask_b32 v92, v246, v92, vcc\n"
"v_cmp_le_i32 vcc, 25, v249\n"
"s_nop 1\n"
"v_cndmask_b32 v93, v246, v93, vcc\n"
"v_cmp_le_i32 vcc, 26, v249\n"
"s_nop 1\n"
"v_cndmask_b32 v94, v246, v94, vcc\n"
"v_cmp_le_i32 vcc, 27, v249\n"
"s_nop 1\n"
"v_cndmask_b32 v95, v246, v95, vcc\n"
"s_branch Lmb55_%=\n"
"Lmo61_%=:\n"
"s_cmp_eq_u32 s49, s48\n"
"s_cbranch_scc1 Lmd62_%=\n"
"v_mov_b32 v96, v246\n"
"v_mov_b32 v97, v246\n"
"v_mov_b32 v98, v246\n"
"v_mov_b32 v99, v246\n"
"v_mov_b32 v100, v246\n"
"v_mov_b32 v101, v246\n"
"v_mov_b32 v102, v246\n"
"v_mov_b32 v103, v246\n"
"v_mov_b32 v104, v246\n"
"v_mov_b32 v105, v246\n"
"v_mov_b32 v106, v246\n"
"v_mov_b32 v107, v246\n"
"v_mov_b32 v108, v246\n"
"v_mov_b32 v109, v246\n"
"v_mov_b32 v110, v246\n"
"v_mov_b32 v111, v246\n"
"s_branch Lmb60_%=\n"
"Lmd62_%=:\n"
"v_cmp_le_i32 vcc, 0, v249\n"
"s_nop 1\n"
"v_cndmask_b32 v96, v246, v96, vcc\n"
"v_cmp_le_i32 vcc, 1, v249\n"
"s_nop 1\n"
"v_cndmask_b32 v97, v246, v97, vcc\n"
"v_cmp_le_i32 vcc, 2, v249\n"
"s_nop 1\n"
"v_cndmask_b32 v98, v246, v98, vcc\n"
"v_cmp_le_i32 vcc, 3, v249\n"
"s_nop 1\n"
"v_cndmask_b32 v99, v246, v99, vcc\n"
"v_cmp_le_i32 vcc, 8, v249\n"
"s_nop 1\n"
"v_cndmask_b32 v100, v246, v100, vcc\n"
"v_cmp_le_i32 vcc, 9, v249\n"
"s_nop 1\n"
"v_cndmask_b32 v101, v246, v101, vcc\n"
"v_cmp_le_i32 vcc, 10, v249\n"
"s_nop 1\n"
"v_cndmask_b32 v102, v246, v102, vcc\n"
"v_cmp_le_i32 vcc, 11, v249\n"
"s_nop 1\n"
"v_cndmask_b32 v103, v246, v103, vcc\n"
"v_cmp_le_i32 vcc, 16, v249\n"
"s_nop 1\n"
"v_cndmask_b32 v104, v246, v104, vcc\n"
"v_cmp_le_i32 vcc, 17, v249\n"
"s_nop 1\n"
"v_cndmask_b32 v105, v246, v105, vcc\n"
"v_cmp_le_i32 vcc, 18, v249\n"
"s_nop 1\n"
"v_cndmask_b32 v106, v246, v106, vcc\n"
"v_cmp_le_i32 vcc, 19, v249\n"
"s_nop 1\n"
"v_cndmask_b32 v107, v246, v107, vcc\n"
"v_cmp_le_i32 vcc, 24, v249\n"
"s_nop 1\n"
"v_cndmask_b32 v108, v246, v108, vcc\n"
"v_cmp_le_i32 vcc, 25, v249\n"
"s_nop 1\n"
"v_cndmask_b32 v109, v246, v109, vcc\n"
"v_cmp_le_i32 vcc, 26, v249\n"
"s_nop 1\n"
"v_cndmask_b32 v110, v246, v110, vcc\n"
"v_cmp_le_i32 vcc, 27, v249\n"
"s_nop 1\n"
"v_cndmask_b32 v111, v246, v111, vcc\n"
"s_branch Lmb60_%=\n"
"Lmo64_%=:\n"
"s_cmp_eq_u32 s49, s48\n"
"s_cbranch_scc1 Lmd65_%=\n"
"v_mov_b32 v112, v246\n"
"v_mov_b32 v113, v246\n"
"v_mov_b32 v114, v246\n"
"v_mov_b32 v115, v246\n"
"v_mov_b32 v116, v246\n"
"v_mov_b32 v117, v246\n"
"v_mov_b32 v118, v246\n"
"v_mov_b32 v119, v246\n"
"v_mov_b32 v120, v246\n"
"v_mov_b32 v121, v246\n"
"v_mov_b32 v122, v246\n"
"v_mov_b32 v123, v246\n"
"v_mov_b32 v124, v246\n"
"v_mov_b32 v125, v246\n"
"v_mov_b32 v126, v246\n"
"v_mov_b32 v127, v246\n"
"s_branch Lmb63_%=\n"
"Lmd65_%=:\n"
"v_cmp_le_i32 vcc, 0, v249\n"
"s_nop 1\n"
"v_cndmask_b32 v112, v246, v112, vcc\n"
"v_cmp_le_i32 vcc, 1, v249\n"
"s_nop 1\n"
"v_cndmask_b32 v113, v246, v113, vcc\n"
"v_cmp_le_i32 vcc, 2, v249\n"
"s_nop 1\n"
"v_cndmask_b32 v114, v246, v114, vcc\n"
"v_cmp_le_i32 vcc, 3, v249\n"
"s_nop 1\n"
"v_cndmask_b32 v115, v246, v115, vcc\n"
"v_cmp_le_i32 vcc, 8, v249\n"
"s_nop 1\n"
"v_cndmask_b32 v116, v246, v116, vcc\n"
"v_cmp_le_i32 vcc, 9, v249\n"
"s_nop 1\n"
"v_cndmask_b32 v117, v246, v117, vcc\n"
"v_cmp_le_i32 vcc, 10, v249\n"
"s_nop 1\n"
"v_cndmask_b32 v118, v246, v118, vcc\n"
"v_cmp_le_i32 vcc, 11, v249\n"
"s_nop 1\n"
"v_cndmask_b32 v119, v246, v119, vcc\n"
"v_cmp_le_i32 vcc, 16, v249\n"
"s_nop 1\n"
"v_cndmask_b32 v120, v246, v120, vcc\n"
"v_cmp_le_i32 vcc, 17, v249\n"
"s_nop 1\n"
"v_cndmask_b32 v121, v246, v121, vcc\n"
"v_cmp_le_i32 vcc, 18, v249\n"
"s_nop 1\n"
"v_cndmask_b32 v122, v246, v122, vcc\n"
"v_cmp_le_i32 vcc, 19, v249\n"
"s_nop 1\n"
"v_cndmask_b32 v123, v246, v123, vcc\n"
"v_cmp_le_i32 vcc, 24, v249\n"
"s_nop 1\n"
"v_cndmask_b32 v124, v246, v124, vcc\n"
"v_cmp_le_i32 vcc, 25, v249\n"
"s_nop 1\n"
"v_cndmask_b32 v125, v246, v125, vcc\n"
"v_cmp_le_i32 vcc, 26, v249\n"
"s_nop 1\n"
"v_cndmask_b32 v126, v246, v126, vcc\n"
"v_cmp_le_i32 vcc, 27, v249\n"
"s_nop 1\n"
"v_cndmask_b32 v127, v246, v127, vcc\n"
"s_branch Lmb63_%=\n"
"Lps69_%=:\n"
"s_mov_b32 s96, 30000\n"
"Lpl70_%=:\n"
"s_sleep 8\n"
"buffer_load_dword v63, v62, s[88:91], s97 offen sc1\n"
"s_waitcnt vmcnt(0)\n"
"v_cmp_ne_u32 vcc, s82, v63\n"
"s_cbranch_vccz Lpd71_%=\n"
"s_sub_u32 s96, s96, 1\n"
"s_cmp_lg_u32 s96, 0\n"
"s_cbranch_scc1 Lpl70_%=\n"
"Lpd71_%=:\n"
"s_branch Lpb68_%=\n"
"Lps73_%=:\n"
"s_mov_b32 s96, 30000\n"
"Lpl74_%=:\n"
"s_sleep 8\n"
"buffer_load_dword v241, v240, s[88:91], s97 offen sc1\n"
"s_waitcnt vmcnt(0)\n"
"v_cmp_ne_u32 vcc, s82, v241\n"
"s_cbranch_vccz Lpd75_%=\n"
"s_sub_u32 s96, s96, 1\n"
"s_cmp_lg_u32 s96, 0\n"
"s_cbranch_scc1 Lpl74_%=\n"
"Lpd75_%=:\n"
"s_branch Lpb72_%=\n"
"Lps77_%=:\n"
"buffer_load_dword v241, v240, s[88:91], s97 offen sc1\n"
"s_add_u32 m0, s54, 16384\n"
"s_mov_b32 s92, 1146880\n"
"buffer_load_dwordx4 v184, s[24:27], s92 offen offset:0 lds\n"
"buffer_load_dwordx4 v185, s[28:31], s92 offen offset:1024 lds\n"
"buffer_load_dwordx4 v186, s[24:27], s92 offen offset:2048 lds\n"
"buffer_load_dwordx4 v187, s[28:31], s92 offen offset:3072 lds\n"
"s_mov_b32 s48, 1\n"
"s_waitcnt vmcnt(4)\n"
"v_cmp_ne_u32 vcc, s82, v241\n"
"s_cbranch_vccz Lpd79_%=\n"
"buffer_load_dword v241, v240, s[88:91], s97 offen sc1\n"
"s_add_u32 m0, s54, 32768\n"
"s_mov_b32 s92, 1376256\n"
"buffer_load_dwordx4 v184, s[24:27], s92 offen offset:0 lds\n"
"buffer_load_dwordx4 v185, s[28:31], s92 offen offset:1024 lds\n"
"buffer_load_dwordx4 v186, s[24:27], s92 offen offset:2048 lds\n"
"buffer_load_dwordx4 v187, s[28:31], s92 offen offset:3072 lds\n"
"s_mov_b32 s48, 2\n"
"s_waitcnt vmcnt(4)\n"
"v_cmp_ne_u32 vcc, s82, v241\n"
"s_cbranch_vccz Lpd79_%=\n"
"buffer_load_dword v241, v240, s[88:91], s97 offen sc1\n"
"s_add_u32 m0, s54, 49152\n"
"s_mov_b32 s92, 1605632\n"
"buffer_load_dwordx4 v184, s[24:27], s92 offen offset:0 lds\n"
"buffer_load_dwordx4 v185, s[28:31], s92 offen offset:1024 lds\n"
"buffer_load_dwordx4 v186, s[24:27], s92 offen offset:2048 lds\n"
"buffer_load_dwordx4 v187, s[28:31], s92 offen offset:3072 lds\n"
"s_mov_b32 s48, 3\n"
"s_waitcnt vmcnt(4)\n"
"v_cmp_ne_u32 vcc, s82, v241\n"
"s_cbranch_vccz Lpd79_%=\n"
"s_mov_b32 s96, 30000\n"
"Lpl78_%=:\n"
"s_sleep 8\n"
"buffer_load_dword v241, v240, s[88:91], s97 offen sc1\n"
"s_waitcnt vmcnt(0)\n"
"v_cmp_ne_u32 vcc, s82, v241\n"
"s_cbranch_vccz Lpd79_%=\n"
"s_sub_u32 s96, s96, 1\n"
"s_cmp_lg_u32 s96, 0\n"
"s_cbranch_scc1 Lpl78_%=\n"
"Lpd79_%=:\n"
"s_branch Lpb76_%=\n"
"Lgs81_%=:\n"
"s_mov_b32 s96, 30000\n"
"Lgl82_%=:\n"
"s_sleep 1\n"
"buffer_load_dwordx4 v[144:147], v235, s[88:91], s45 offen offset:0 sc1\n"
"buffer_load_dwordx4 v[148:151], v235, s[88:91], s45 offen offset:64 sc1\n"
"buffer_load_dwordx4 v[152:155], v235, s[88:91], s45 offen offset:256 sc1\n"
"buffer_load_dwordx4 v[156:159], v235, s[88:91], s45 offen offset:320 sc1\n"
"buffer_load_dwordx4 v[160:163], v235, s[88:91], s45 offen offset:2048 sc1\n"
"buffer_load_dwordx4 v[164:167], v235, s[88:91], s45 offen offset:2112 sc1\n"
"buffer_load_dwordx4 v[168:171], v235, s[88:91], s45 offen offset:2304 sc1\n"
"buffer_load_dwordx4 v[172:175], v235, s[88:91], s45 offen offset:2368 sc1\n"
"buffer_load_dwordx4 v[192:195], v235, s[88:91], s46 offen offset:0 sc1\n"
"buffer_load_dwordx4 v[196:199], v235, s[88:91], s46 offen offset:64 sc1\n"
"buffer_load_dwordx4 v[200:203], v235, s[88:91], s46 offen offset:256 sc1\n"
"buffer_load_dwordx4 v[204:207], v235, s[88:91], s46 offen offset:320 sc1\n"
"s_waitcnt vmcnt(0)\n"
"v_xor_b32 v228, s82, v145\n"
"v_bitop3_b32 v228, v147, s82, v228 bitop3:0xbe\n"
"v_bitop3_b32 v228, v149, s82, v228 bitop3:0xbe\n"
"v_bitop3_b32 v228, v151, s82, v228 bitop3:0xbe\n"
"v_bitop3_b32 v228, v153, s82, v228 bitop3:0xbe\n"
"v_bitop3_b32 v228, v155, s82, v228 bitop3:0xbe\n"
"v_bitop3_b32 v228, v157, s82, v228 bitop3:0xbe\n"
"v_bitop3_b32 v228, v159, s82, v228 bitop3:0xbe\n"
"v_bitop3_b32 v228, v161, s82, v228 bitop3:0xbe\n"
"v_bitop3_b32 v228, v163, s82, v228 bitop3:0xbe\n"
"v_bitop3_b32 v228, v165, s82, v228 bitop3:0xbe\n"
"v_bitop3_b32 v228, v167, s82, v228 bitop3:0xbe\n"
"v_bitop3_b32 v228, v169, s82, v228 bitop3:0xbe\n"
"v_bitop3_b32 v228, v171, s82, v228 bitop3:0xbe\n"
"v_bitop3_b32 v228, v173, s82, v228 bitop3:0xbe\n"
"v_bitop3_b32 v228, v175, s82, v228 bitop3:0xbe\n"
"v_bitop3_b32 v228, v193, s82, v228 bitop3:0xbe\n"
"v_bitop3_b32 v228, v195, s82, v228 bitop3:0xbe\n"
"v_bitop3_b32 v228, v197, s82, v228 bitop3:0xbe\n"
"v_bitop3_b32 v228, v199, s82, v228 bitop3:0xbe\n"
"v_bitop3_b32 v228, v201, s82, v228 bitop3:0xbe\n"
"v_bitop3_b32 v228, v203, s82, v228 bitop3:0xbe\n"
"v_bitop3_b32 v228, v205, s82, v228 bitop3:0xbe\n"
"v_bitop3_b32 v228, v207, s82, v228 bitop3:0xbe\n"
"v_cmp_ne_u32 vcc, 0, v228\n"
"s_cbranch_vccz Lgb80_%=\n"
"s_sub_u32 s96, s96, 1\n"
"s_cmp_lg_u32 s96, 0\n"
"s_cbranch_scc1 Lgl82_%=\n"
"s_branch Lgb80_%=\n"
"Lps87_%=:\n"
"buffer_store_dwordx4 v[72:75], v234, s[36:39], s42 offen offset:0 sc1\n"
"buffer_store_dwordx4 v[76:79], v234, s[36:39], s42 offen offset:2048 sc1\n"
"s_waitcnt vmcnt(0)\n"
"s_lshl_b32 s95, s41, 7\n"
"s_lshl_b32 s94, s83, 2\n"
"s_add_u32 s95, s95, s94\n"
"s_add_u32 s95, s95, 0x38000\n"
"v_mov_b32 v253, s82\n"
"s_mov_b64 exec, 1\n"
"buffer_store_dword v253, off, s[88:91], s95 sc1\n"
"s_mov_b64 exec, -1\n"
"s_cmp_gt_u32 s40, 0\n"
"s_cbranch_scc1 Lxs83_%=\n"
"s_add_u32 m0, s54, 16384\n"
"s_mov_b32 s92, 1146880\n"
"buffer_load_dwordx4 v184, s[24:27], s92 offen offset:0 lds\n"
"buffer_load_dwordx4 v185, s[28:31], s92 offen offset:1024 lds\n"
"buffer_load_dwordx4 v186, s[24:27], s92 offen offset:2048 lds\n"
"buffer_load_dwordx4 v187, s[28:31], s92 offen offset:3072 lds\n"
"Lxs83_%=:\n"
"s_cmp_gt_u32 s40, 1\n"
"s_cbranch_scc1 Lxs84_%=\n"
"s_add_u32 m0, s54, 32768\n"
"s_mov_b32 s92, 1376256\n"
"buffer_load_dwordx4 v184, s[24:27], s92 offen offset:0 lds\n"
"buffer_load_dwordx4 v185, s[28:31], s92 offen offset:1024 lds\n"
"buffer_load_dwordx4 v186, s[24:27], s92 offen offset:2048 lds\n"
"buffer_load_dwordx4 v187, s[28:31], s92 offen offset:3072 lds\n"
"Lxs84_%=:\n"
"s_cmp_gt_u32 s40, 2\n"
"s_cbranch_scc1 Lxs85_%=\n"
"s_add_u32 m0, s54, 49152\n"
"s_mov_b32 s92, 1605632\n"
"buffer_load_dwordx4 v184, s[24:27], s92 offen offset:0 lds\n"
"buffer_load_dwordx4 v185, s[28:31], s92 offen offset:1024 lds\n"
"buffer_load_dwordx4 v186, s[24:27], s92 offen offset:2048 lds\n"
"buffer_load_dwordx4 v187, s[28:31], s92 offen offset:3072 lds\n"
"Lxs85_%=:\n"
"s_mov_b32 s40, 3\n"
"s_mov_b32 s96, 30000\n"
"Lpl88_%=:\n"
"s_sleep 8\n"
"buffer_load_dword v241, v240, s[88:91], s97 offen sc1\n"
"s_waitcnt vmcnt(0)\n"
"v_cmp_ne_u32 vcc, s82, v241\n"
"s_cbranch_vccz Lpd89_%=\n"
"s_sub_u32 s96, s96, 1\n"
"s_cmp_lg_u32 s96, 0\n"
"s_cbranch_scc1 Lpl88_%=\n"
"Lpd89_%=:\n"
"buffer_load_dwordx4 a[64:67], v248, s[16:19], s45 offen offset:0 sc1\n"
"buffer_load_dwordx4 a[72:75], v248, s[16:19], s45 offen offset:1024 sc1\n"
"buffer_load_dwordx4 a[68:71], v248, s[16:19], s46 offen offset:0 sc1\n"
"buffer_load_dwordx4 a[76:79], v248, s[16:19], s46 offen offset:1024 sc1\n"
"buffer_load_dwordx4 a[80:83], v248, s[16:19], s45 offen offset:2048 sc1\n"
"buffer_load_dwordx4 a[88:91], v248, s[16:19], s45 offen offset:3072 sc1\n"
"buffer_load_dwordx4 a[84:87], v248, s[16:19], s46 offen offset:2048 sc1\n"
"buffer_load_dwordx4 a[92:95], v248, s[16:19], s46 offen offset:3072 sc1\n"
"s_add_u32 s45, s45, 4096\n"
"buffer_load_dwordx4 a[96:99], v248, s[16:19], s45 offen offset:0 sc1\n"
"buffer_load_dwordx4 a[104:107], v248, s[16:19], s45 offen offset:1024 sc1\n"
"s_add_u32 s46, s46, 4096\n"
"buffer_load_dwordx4 a[100:103], v248, s[16:19], s46 offen offset:0 sc1\n"
"buffer_load_dwordx4 a[108:111], v248, s[16:19], s46 offen offset:1024 sc1\n"
"s_branch Lpb86_%=\n"
"Lps94_%=:\n"
"s_mov_b32 s96, 30000\n"
"Lpl95_%=:\n"
"s_sleep 8\n"
"buffer_load_dword v241, v240, s[88:91], s97 offen sc1\n"
"s_waitcnt vmcnt(0)\n"
"v_cmp_ne_u32 vcc, s82, v241\n"
"s_cbranch_vccz Lpd96_%=\n"
"s_sub_u32 s96, s96, 1\n"
"s_cmp_lg_u32 s96, 0\n"
"s_cbranch_scc1 Lpl95_%=\n"
"Lpd96_%=:\n"
"s_branch Lpb93_%=\n"
"Lgs98_%=:\n"
"s_mov_b32 s96, 30000\n"
"Lgl99_%=:\n"
"s_sleep 1\n"
"buffer_load_dwordx4 v[160:163], v235, s[88:91], s44 offen offset:0 sc1\n"
"buffer_load_dwordx4 v[164:167], v235, s[88:91], s44 offen offset:64 sc1\n"
"buffer_load_dwordx4 v[168:171], v235, s[88:91], s44 offen offset:128 sc1\n"
"buffer_load_dwordx4 v[172:175], v235, s[88:91], s44 offen offset:192 sc1\n"
"s_waitcnt vmcnt(0)\n"
"v_xor_b32 v200, s82, v161\n"
"v_bitop3_b32 v200, v163, s82, v200 bitop3:0xbe\n"
"v_bitop3_b32 v200, v165, s82, v200 bitop3:0xbe\n"
"v_bitop3_b32 v200, v167, s82, v200 bitop3:0xbe\n"
"v_bitop3_b32 v200, v169, s82, v200 bitop3:0xbe\n"
"v_bitop3_b32 v200, v171, s82, v200 bitop3:0xbe\n"
"v_bitop3_b32 v200, v173, s82, v200 bitop3:0xbe\n"
"v_bitop3_b32 v200, v175, s82, v200 bitop3:0xbe\n"
"v_cmp_ne_u32 vcc, 0, v200\n"
"s_cbranch_vccz Lgb97_%=\n"
"s_sub_u32 s96, s96, 1\n"
"s_cmp_lg_u32 s96, 0\n"
"s_cbranch_scc1 Lgl99_%=\n"
"s_branch Lgb97_%=\n"
"Lps101_%=:\n"
"s_mov_b32 s96, 30000\n"
"Lpl102_%=:\n"
"s_sleep 8\n"
"buffer_load_dword v241, v240, s[88:91], s97 offen sc1\n"
"s_waitcnt vmcnt(0)\n"
"v_cmp_ne_u32 vcc, s82, v241\n"
"s_cbranch_vccz Lpd103_%=\n"
"s_sub_u32 s96, s96, 1\n"
"s_cmp_lg_u32 s96, 0\n"
"s_cbranch_scc1 Lpl102_%=\n"
"Lpd103_%=:\n"
"s_branch Lpb100_%=\n"
"LTE_%=:\n"
"s_waitcnt vmcnt(0)\n"
"v_mov_b32 v253, s82\n"
"s_mov_b64 exec, 1\n"
"buffer_store_dword v253, off, s[88:91], s87 sc1\n"
"s_mov_b64 exec, -1\n"
"s_lshr_b32 s95, s84, 3\n"
"s_lshl_b32 s95, s95, 2\n"
"s_add_u32 s24, s95, s83\n"
"s_and_b32 s95, s84, 7\n"
"s_lshl_b32 s95, s95, 19\n"
"s_lshl_b32 s25, s24, 12\n"
"s_add_u32 s95, s95, s25\n"
"v_and_b32 v60, 31, v255\n"
"v_lshlrev_b32 v60, 7, v60\n"
"v_add_u32 v60, s95, v60\n"
"global_load_dword v56, v60, s[66:67]\n"
"s_bfe_u32 s95, s84, 0x20001\n"
"s_mul_i32 s95, s95, 896\n"
"s_mul_i32 s25, s24, 14336\n"
"s_add_u32 s95, s95, s25\n"
"v_bfe_u32 v60, v255, 3, 2\n"
"v_mul_u32_u24 v60, 3584, v60\n"
"v_and_b32 v59, 7, v255\n"
"v_min_u32 v59, 6, v59\n"
"v_lshl_add_u32 v60, v59, 7, v60\n"
"v_add_u32 v60, s95, v60\n"
"global_load_dword v57, v60, s[68:69]\n"
"global_load_dword v58, v60, s[70:71]\n"
"s_branch LE_%=\n"
"LHOTEND_%=:\n"
"Lfb24_%=:\n"
"s_nop 7\n"
"s_nop 7\n"
"v_max3_f32 v240, v96, v97, v98\n"
"v_max3_f32 v240, v240, v99, v100\n"
"v_max3_f32 v240, v240, v101, v102\n"
"v_max3_f32 v240, v240, v103, v104\n"
"v_max3_f32 v240, v240, v105, v106\n"
"v_max3_f32 v240, v240, v107, v108\n"
"v_max3_f32 v240, v240, v109, v110\n"
"v_max_f32 v240, v240, v111\n"
"v_mov_b32 v244, v240\n"
"s_nop 1\n"
"v_permlane32_swap_b32 v240, v244\n"
"v_max_f32 v240, v240, v244\n"
"v_max_f32 v240, 0, v240\n"
"v_sub_f32 v244, 0, v240\n"
"v_exp_f32 v242, v244\n"
"s_nop 0\n"
"v_mul_f32 v234, v234, v242\n"
"v_sub_f32 v160, v160, v240\n"
"v_sub_f32 v161, v161, v240\n"
"v_sub_f32 v162, v162, v240\n"
"v_sub_f32 v163, v163, v240\n"
"v_sub_f32 v164, v164, v240\n"
"v_sub_f32 v165, v165, v240\n"
"v_sub_f32 v166, v166, v240\n"
"v_sub_f32 v167, v167, v240\n"
"v_sub_f32 v168, v168, v240\n"
"v_sub_f32 v169, v169, v240\n"
"v_sub_f32 v170, v170, v240\n"
"v_sub_f32 v171, v171, v240\n"
"v_sub_f32 v172, v172, v240\n"
"v_sub_f32 v173, v173, v240\n"
"v_sub_f32 v174, v174, v240\n"
"v_sub_f32 v175, v175, v240\n"
"v_accvgpr_read_b32 v244, a0\n"
"v_mul_f32 v244, v244, v242\n"
"v_accvgpr_write_b32 a0, v244\n"
"v_accvgpr_read_b32 v245, a1\n"
"v_mul_f32 v245, v245, v242\n"
"v_accvgpr_write_b32 a1, v245\n"
"v_accvgpr_read_b32 v244, a2\n"
"v_mul_f32 v244, v244, v242\n"
"v_accvgpr_write_b32 a2, v244\n"
"v_accvgpr_read_b32 v245, a3\n"
"v_mul_f32 v245, v245, v242\n"
"v_accvgpr_write_b32 a3, v245\n"
"v_accvgpr_read_b32 v244, a4\n"
"v_mul_f32 v244, v244, v242\n"
"v_accvgpr_write_b32 a4, v244\n"
"v_accvgpr_read_b32 v245, a5\n"
"v_mul_f32 v245, v245, v242\n"
"v_accvgpr_write_b32 a5, v245\n"
"v_accvgpr_read_b32 v244, a6\n"
"v_mul_f32 v244, v244, v242\n"
"v_accvgpr_write_b32 a6, v244\n"
"v_accvgpr_read_b32 v245, a7\n"
"v_mul_f32 v245, v245, v242\n"
"v_accvgpr_write_b32 a7, v245\n"
"v_accvgpr_read_b32 v244, a8\n"
"v_mul_f32 v244, v244, v242\n"
"v_accvgpr_write_b32 a8, v244\n"
"v_accvgpr_read_b32 v245, a9\n"
"v_mul_f32 v245, v245, v242\n"
"v_accvgpr_write_b32 a9, v245\n"
"v_accvgpr_read_b32 v244, a10\n"
"v_mul_f32 v244, v244, v242\n"
"v_accvgpr_write_b32 a10, v244\n"
"v_accvgpr_read_b32 v245, a11\n"
"v_mul_f32 v245, v245, v242\n"
"v_accvgpr_write_b32 a11, v245\n"
"v_accvgpr_read_b32 v244, a12\n"
"v_mul_f32 v244, v244, v242\n"
"v_accvgpr_write_b32 a12, v244\n"
"v_accvgpr_read_b32 v245, a13\n"
"v_mul_f32 v245, v245, v242\n"
"v_accvgpr_write_b32 a13, v245\n"
"v_accvgpr_read_b32 v244, a14\n"
"v_mul_f32 v244, v244, v242\n"
"v_accvgpr_write_b32 a14, v244\n"
"v_accvgpr_read_b32 v245, a15\n"
"v_mul_f32 v245, v245, v242\n"
"v_accvgpr_write_b32 a15, v245\n"
"v_accvgpr_read_b32 v244, a16\n"
"v_mul_f32 v244, v244, v242\n"
"v_accvgpr_write_b32 a16, v244\n"
"v_accvgpr_read_b32 v245, a17\n"
"v_mul_f32 v245, v245, v242\n"
"v_accvgpr_write_b32 a17, v245\n"
"v_accvgpr_read_b32 v244, a18\n"
"v_mul_f32 v244, v244, v242\n"
"v_accvgpr_write_b32 a18, v244\n"
"v_accvgpr_read_b32 v245, a19\n"
"v_mul_f32 v245, v245, v242\n"
"v_accvgpr_write_b32 a19, v245\n"
"v_accvgpr_read_b32 v244, a20\n"
"v_mul_f32 v244, v244, v242\n"
"v_accvgpr_write_b32 a20, v244\n"
"v_accvgpr_read_b32 v245, a21\n"
"v_mul_f32 v245, v245, v242\n"
"v_accvgpr_write_b32 a21, v245\n"
"v_accvgpr_read_b32 v244, a22\n"
"v_mul_f32 v244, v244, v242\n"
"v_accvgpr_write_b32 a22, v244\n"
"v_accvgpr_read_b32 v245, a23\n"
"v_mul_f32 v245, v245, v242\n"
"v_accvgpr_write_b32 a23, v245\n"
"v_accvgpr_read_b32 v244, a24\n"
"v_mul_f32 v244, v244, v242\n"
"v_accvgpr_write_b32 a24, v244\n"
"v_accvgpr_read_b32 v245, a25\n"
"v_mul_f32 v245, v245, v242\n"
"v_accvgpr_write_b32 a25, v245\n"
"v_accvgpr_read_b32 v244, a26\n"
"v_mul_f32 v244, v244, v242\n"
"v_accvgpr_write_b32 a26, v244\n"
"v_accvgpr_read_b32 v245, a27\n"
"v_mul_f32 v245, v245, v242\n"
"v_accvgpr_write_b32 a27, v245\n"
"v_accvgpr_read_b32 v244, a28\n"
"v_mul_f32 v244, v244, v242\n"
"v_accvgpr_write_b32 a28, v244\n"
"v_accvgpr_read_b32 v245, a29\n"
"v_mul_f32 v245, v245, v242\n"
"v_accvgpr_write_b32 a29, v245\n"
"v_accvgpr_read_b32 v244, a30\n"
"v_mul_f32 v244, v244, v242\n"
"v_accvgpr_write_b32 a30, v244\n"
"v_accvgpr_read_b32 v245, a31\n"
"v_mul_f32 v245, v245, v242\n"
"v_accvgpr_write_b32 a31, v245\n"
"v_accvgpr_read_b32 v244, a32\n"
"v_mul_f32 v244, v244, v242\n"
"v_accvgpr_write_b32 a32, v244\n"
"v_accvgpr_read_b32 v245, a33\n"
"v_mul_f32 v245, v245, v242\n"
"v_accvgpr_write_b32 a33, v245\n"
"v_accvgpr_read_b32 v244, a34\n"
"v_mul_f32 v244, v244, v242\n"
"v_accvgpr_write_b32 a34, v244\n"
"v_accvgpr_read_b32 v245, a35\n"
"v_mul_f32 v245, v245, v242\n"
"v_accvgpr_write_b32 a35, v245\n"
"v_accvgpr_read_b32 v244, a36\n"
"v_mul_f32 v244, v244, v242\n"
"v_accvgpr_write_b32 a36, v244\n"
"v_accvgpr_read_b32 v245, a37\n"
"v_mul_f32 v245, v245, v242\n"
"v_accvgpr_write_b32 a37, v245\n"
"v_accvgpr_read_b32 v244, a38\n"
"v_mul_f32 v244, v244, v242\n"
"v_accvgpr_write_b32 a38, v244\n"
"v_accvgpr_read_b32 v245, a39\n"
"v_mul_f32 v245, v245, v242\n"
"v_accvgpr_write_b32 a39, v245\n"
"v_accvgpr_read_b32 v244, a40\n"
"v_mul_f32 v244, v244, v242\n"
"v_accvgpr_write_b32 a40, v244\n"
"v_accvgpr_read_b32 v245, a41\n"
"v_mul_f32 v245, v245, v242\n"
"v_accvgpr_write_b32 a41, v245\n"
"v_accvgpr_read_b32 v244, a42\n"
"v_mul_f32 v244, v244, v242\n"
"v_accvgpr_write_b32 a42, v244\n"
"v_accvgpr_read_b32 v245, a43\n"
"v_mul_f32 v245, v245, v242\n"
"v_accvgpr_write_b32 a43, v245\n"
"v_accvgpr_read_b32 v244, a44\n"
"v_mul_f32 v244, v244, v242\n"
"v_accvgpr_write_b32 a44, v244\n"
"v_accvgpr_read_b32 v245, a45\n"
"v_mul_f32 v245, v245, v242\n"
"v_accvgpr_write_b32 a45, v245\n"
"v_accvgpr_read_b32 v244, a46\n"
"v_mul_f32 v244, v244, v242\n"
"v_accvgpr_write_b32 a46, v244\n"
"v_accvgpr_read_b32 v245, a47\n"
"v_mul_f32 v245, v245, v242\n"
"v_accvgpr_write_b32 a47, v245\n"
"v_accvgpr_read_b32 v244, a48\n"
"v_mul_f32 v244, v244, v242\n"
"v_accvgpr_write_b32 a48, v244\n"
"v_accvgpr_read_b32 v245, a49\n"
"v_mul_f32 v245, v245, v242\n"
"v_accvgpr_write_b32 a49, v245\n"
"v_accvgpr_read_b32 v244, a50\n"
"v_mul_f32 v244, v244, v242\n"
"v_accvgpr_write_b32 a50, v244\n"
"v_accvgpr_read_b32 v245, a51\n"
"v_mul_f32 v245, v245, v242\n"
"v_accvgpr_write_b32 a51, v245\n"
"v_accvgpr_read_b32 v244, a52\n"
"v_mul_f32 v244, v244, v242\n"
"v_accvgpr_write_b32 a52, v244\n"
"v_accvgpr_read_b32 v245, a53\n"
"v_mul_f32 v245, v245, v242\n"
"v_accvgpr_write_b32 a53, v245\n"
"v_accvgpr_read_b32 v244, a54\n"
"v_mul_f32 v244, v244, v242\n"
"v_accvgpr_write_b32 a54, v244\n"
"v_accvgpr_read_b32 v245, a55\n"
"v_mul_f32 v245, v245, v242\n"
"v_accvgpr_write_b32 a55, v245\n"
"v_accvgpr_read_b32 v244, a56\n"
"v_mul_f32 v244, v244, v242\n"
"v_accvgpr_write_b32 a56, v244\n"
"v_accvgpr_read_b32 v245, a57\n"
"v_mul_f32 v245, v245, v242\n"
"v_accvgpr_write_b32 a57, v245\n"
"v_accvgpr_read_b32 v244, a58\n"
"v_mul_f32 v244, v244, v242\n"
"v_accvgpr_write_b32 a58, v244\n"
"v_accvgpr_read_b32 v245, a59\n"
"v_mul_f32 v245, v245, v242\n"
"v_accvgpr_write_b32 a59, v245\n"
"v_accvgpr_read_b32 v244, a60\n"
"v_mul_f32 v244, v244, v242\n"
"v_accvgpr_write_b32 a60, v244\n"
"v_accvgpr_read_b32 v245, a61\n"
"v_mul_f32 v245, v245, v242\n"
"v_accvgpr_write_b32 a61, v245\n"
"v_accvgpr_read_b32 v244, a62\n"
"v_mul_f32 v244, v244, v242\n"
"v_accvgpr_write_b32 a62, v244\n"
"v_accvgpr_read_b32 v245, a63\n"
"v_mul_f32 v245, v245, v242\n"
"v_accvgpr_write_b32 a63, v245\n"
"v_max3_f32 v241, v112, v113, v114\n"
"v_max3_f32 v241, v241, v115, v116\n"
"v_max3_f32 v241, v241, v117, v118\n"
"v_max3_f32 v241, v241, v119, v120\n"
"v_max3_f32 v241, v241, v121, v122\n"
"v_max3_f32 v241, v241, v123, v124\n"
"v_max3_f32 v241, v241, v125, v126\n"
"v_max_f32 v241, v241, v127\n"
"v_mov_b32 v244, v241\n"
"s_nop 1\n"
"v_permlane32_swap_b32 v241, v244\n"
"v_max_f32 v241, v241, v244\n"
"v_max_f32 v241, 0, v241\n"
"v_sub_f32 v244, 0, v241\n"
"v_exp_f32 v243, v244\n"
"s_nop 0\n"
"v_mul_f32 v235, v235, v243\n"
"v_sub_f32 v176, v176, v241\n"
"v_sub_f32 v177, v177, v241\n"
"v_sub_f32 v178, v178, v241\n"
"v_sub_f32 v179, v179, v241\n"
"v_sub_f32 v180, v180, v241\n"
"v_sub_f32 v181, v181, v241\n"
"v_sub_f32 v182, v182, v241\n"
"v_sub_f32 v183, v183, v241\n"
"v_sub_f32 v184, v184, v241\n"
"v_sub_f32 v185, v185, v241\n"
"v_sub_f32 v186, v186, v241\n"
"v_sub_f32 v187, v187, v241\n"
"v_sub_f32 v188, v188, v241\n"
"v_sub_f32 v189, v189, v241\n"
"v_sub_f32 v190, v190, v241\n"
"v_sub_f32 v191, v191, v241\n"
"v_accvgpr_read_b32 v244, a64\n"
"v_mul_f32 v244, v244, v243\n"
"v_accvgpr_write_b32 a64, v244\n"
"v_accvgpr_read_b32 v245, a65\n"
"v_mul_f32 v245, v245, v243\n"
"v_accvgpr_write_b32 a65, v245\n"
"v_accvgpr_read_b32 v244, a66\n"
"v_mul_f32 v244, v244, v243\n"
"v_accvgpr_write_b32 a66, v244\n"
"v_accvgpr_read_b32 v245, a67\n"
"v_mul_f32 v245, v245, v243\n"
"v_accvgpr_write_b32 a67, v245\n"
"v_accvgpr_read_b32 v244, a68\n"
"v_mul_f32 v244, v244, v243\n"
"v_accvgpr_write_b32 a68, v244\n"
"v_accvgpr_read_b32 v245, a69\n"
"v_mul_f32 v245, v245, v243\n"
"v_accvgpr_write_b32 a69, v245\n"
"v_accvgpr_read_b32 v244, a70\n"
"v_mul_f32 v244, v244, v243\n"
"v_accvgpr_write_b32 a70, v244\n"
"v_accvgpr_read_b32 v245, a71\n"
"v_mul_f32 v245, v245, v243\n"
"v_accvgpr_write_b32 a71, v245\n"
"v_accvgpr_read_b32 v244, a72\n"
"v_mul_f32 v244, v244, v243\n"
"v_accvgpr_write_b32 a72, v244\n"
"v_accvgpr_read_b32 v245, a73\n"
"v_mul_f32 v245, v245, v243\n"
"v_accvgpr_write_b32 a73, v245\n"
"v_accvgpr_read_b32 v244, a74\n"
"v_mul_f32 v244, v244, v243\n"
"v_accvgpr_write_b32 a74, v244\n"
"v_accvgpr_read_b32 v245, a75\n"
"v_mul_f32 v245, v245, v243\n"
"v_accvgpr_write_b32 a75, v245\n"
"v_accvgpr_read_b32 v244, a76\n"
"v_mul_f32 v244, v244, v243\n"
"v_accvgpr_write_b32 a76, v244\n"
"v_accvgpr_read_b32 v245, a77\n"
"v_mul_f32 v245, v245, v243\n"
"v_accvgpr_write_b32 a77, v245\n"
"v_accvgpr_read_b32 v244, a78\n"
"v_mul_f32 v244, v244, v243\n"
"v_accvgpr_write_b32 a78, v244\n"
"v_accvgpr_read_b32 v245, a79\n"
"v_mul_f32 v245, v245, v243\n"
"v_accvgpr_write_b32 a79, v245\n"
"v_accvgpr_read_b32 v244, a80\n"
"v_mul_f32 v244, v244, v243\n"
"v_accvgpr_write_b32 a80, v244\n"
"v_accvgpr_read_b32 v245, a81\n"
"v_mul_f32 v245, v245, v243\n"
"v_accvgpr_write_b32 a81, v245\n"
"v_accvgpr_read_b32 v244, a82\n"
"v_mul_f32 v244, v244, v243\n"
"v_accvgpr_write_b32 a82, v244\n"
"v_accvgpr_read_b32 v245, a83\n"
"v_mul_f32 v245, v245, v243\n"
"v_accvgpr_write_b32 a83, v245\n"
"v_accvgpr_read_b32 v244, a84\n"
"v_mul_f32 v244, v244, v243\n"
"v_accvgpr_write_b32 a84, v244\n"
"v_accvgpr_read_b32 v245, a85\n"
"v_mul_f32 v245, v245, v243\n"
"v_accvgpr_write_b32 a85, v245\n"
"v_accvgpr_read_b32 v244, a86\n"
"v_mul_f32 v244, v244, v243\n"
"v_accvgpr_write_b32 a86, v244\n"
"v_accvgpr_read_b32 v245, a87\n"
"v_mul_f32 v245, v245, v243\n"
"v_accvgpr_write_b32 a87, v245\n"
"v_accvgpr_read_b32 v244, a88\n"
"v_mul_f32 v244, v244, v243\n"
"v_accvgpr_write_b32 a88, v244\n"
"v_accvgpr_read_b32 v245, a89\n"
"v_mul_f32 v245, v245, v243\n"
"v_accvgpr_write_b32 a89, v245\n"
"v_accvgpr_read_b32 v244, a90\n"
"v_mul_f32 v244, v244, v243\n"
"v_accvgpr_write_b32 a90, v244\n"
"v_accvgpr_read_b32 v245, a91\n"
"v_mul_f32 v245, v245, v243\n"
"v_accvgpr_write_b32 a91, v245\n"
"v_accvgpr_read_b32 v244, a92\n"
"v_mul_f32 v244, v244, v243\n"
"v_accvgpr_write_b32 a92, v244\n"
"v_accvgpr_read_b32 v245, a93\n"
"v_mul_f32 v245, v245, v243\n"
"v_accvgpr_write_b32 a93, v245\n"
"v_accvgpr_read_b32 v244, a94\n"
"v_mul_f32 v244, v244, v243\n"
"v_accvgpr_write_b32 a94, v244\n"
"v_accvgpr_read_b32 v245, a95\n"
"v_mul_f32 v245, v245, v243\n"
"v_accvgpr_write_b32 a95, v245\n"
"v_accvgpr_read_b32 v244, a96\n"
"v_mul_f32 v244, v244, v243\n"
"v_accvgpr_write_b32 a96, v244\n"
"v_accvgpr_read_b32 v245, a97\n"
"v_mul_f32 v245, v245, v243\n"
"v_accvgpr_write_b32 a97, v245\n"
"v_accvgpr_read_b32 v244, a98\n"
"v_mul_f32 v244, v244, v243\n"
"v_accvgpr_write_b32 a98, v244\n"
"v_accvgpr_read_b32 v245, a99\n"
"v_mul_f32 v245, v245, v243\n"
"v_accvgpr_write_b32 a99, v245\n"
"v_accvgpr_read_b32 v244, a100\n"
"v_mul_f32 v244, v244, v243\n"
"v_accvgpr_write_b32 a100, v244\n"
"v_accvgpr_read_b32 v245, a101\n"
"v_mul_f32 v245, v245, v243\n"
"v_accvgpr_write_b32 a101, v245\n"
"v_accvgpr_read_b32 v244, a102\n"
"v_mul_f32 v244, v244, v243\n"
"v_accvgpr_write_b32 a102, v244\n"
"v_accvgpr_read_b32 v245, a103\n"
"v_mul_f32 v245, v245, v243\n"
"v_accvgpr_write_b32 a103, v245\n"
"v_accvgpr_read_b32 v244, a104\n"
"v_mul_f32 v244, v244, v243\n"
"v_accvgpr_write_b32 a104, v244\n"
"v_accvgpr_read_b32 v245, a105\n"
"v_mul_f32 v245, v245, v243\n"
"v_accvgpr_write_b32 a105, v245\n"
"v_accvgpr_read_b32 v244, a106\n"
"v_mul_f32 v244, v244, v243\n"
"v_accvgpr_write_b32 a106, v244\n"
"v_accvgpr_read_b32 v245, a107\n"
"v_mul_f32 v245, v245, v243\n"
"v_accvgpr_write_b32 a107, v245\n"
"v_accvgpr_read_b32 v244, a108\n"
"v_mul_f32 v244, v244, v243\n"
"v_accvgpr_write_b32 a108, v244\n"
"v_accvgpr_read_b32 v245, a109\n"
"v_mul_f32 v245, v245, v243\n"
"v_accvgpr_write_b32 a109, v245\n"
"v_accvgpr_read_b32 v244, a110\n"
"v_mul_f32 v244, v244, v243\n"
"v_accvgpr_write_b32 a110, v244\n"
"v_accvgpr_read_b32 v245, a111\n"
"v_mul_f32 v245, v245, v243\n"
"v_accvgpr_write_b32 a111, v245\n"
"v_accvgpr_read_b32 v244, a112\n"
"v_mul_f32 v244, v244, v243\n"
"v_accvgpr_write_b32 a112, v244\n"
"v_accvgpr_read_b32 v245, a113\n"
"v_mul_f32 v245, v245, v243\n"
"v_accvgpr_write_b32 a113, v245\n"
"v_accvgpr_read_b32 v244, a114\n"
"v_mul_f32 v244, v244, v243\n"
"v_accvgpr_write_b32 a114, v244\n"
"v_accvgpr_read_b32 v245, a115\n"
"v_mul_f32 v245, v245, v243\n"
"v_accvgpr_write_b32 a115, v245\n"
"v_accvgpr_read_b32 v244, a116\n"
"v_mul_f32 v244, v244, v243\n"
"v_accvgpr_write_b32 a116, v244\n"
"v_accvgpr_read_b32 v245, a117\n"
"v_mul_f32 v245, v245, v243\n"
"v_accvgpr_write_b32 a117, v245\n"
"v_accvgpr_read_b32 v244, a118\n"
"v_mul_f32 v244, v244, v243\n"
"v_accvgpr_write_b32 a118, v244\n"
"v_accvgpr_read_b32 v245, a119\n"
"v_mul_f32 v245, v245, v243\n"
"v_accvgpr_write_b32 a119, v245\n"
"v_accvgpr_read_b32 v244, a120\n"
"v_mul_f32 v244, v244, v243\n"
"v_accvgpr_write_b32 a120, v244\n"
"v_accvgpr_read_b32 v245, a121\n"
"v_mul_f32 v245, v245, v243\n"
"v_accvgpr_write_b32 a121, v245\n"
"v_accvgpr_read_b32 v244, a122\n"
"v_mul_f32 v244, v244, v243\n"
"v_accvgpr_write_b32 a122, v244\n"
"v_accvgpr_read_b32 v245, a123\n"
"v_mul_f32 v245, v245, v243\n"
"v_accvgpr_write_b32 a123, v245\n"
"v_accvgpr_read_b32 v244, a124\n"
"v_mul_f32 v244, v244, v243\n"
"v_accvgpr_write_b32 a124, v244\n"
"v_accvgpr_read_b32 v245, a125\n"
"v_mul_f32 v245, v245, v243\n"
"v_accvgpr_write_b32 a125, v245\n"
"v_accvgpr_read_b32 v244, a126\n"
"v_mul_f32 v244, v244, v243\n"
"v_accvgpr_write_b32 a126, v244\n"
"v_accvgpr_read_b32 v245, a127\n"
"v_mul_f32 v245, v245, v243\n"
"v_accvgpr_write_b32 a127, v245\n"
"v_sub_f32 v96, v96, v240\n"
"v_sub_f32 v97, v97, v240\n"
"v_sub_f32 v98, v98, v240\n"
"v_sub_f32 v99, v99, v240\n"
"v_sub_f32 v100, v100, v240\n"
"v_sub_f32 v101, v101, v240\n"
"v_sub_f32 v102, v102, v240\n"
"v_sub_f32 v103, v103, v240\n"
"v_sub_f32 v104, v104, v240\n"
"v_sub_f32 v105, v105, v240\n"
"v_sub_f32 v106, v106, v240\n"
"v_sub_f32 v107, v107, v240\n"
"v_sub_f32 v108, v108, v240\n"
"v_sub_f32 v109, v109, v240\n"
"v_sub_f32 v110, v110, v240\n"
"v_sub_f32 v111, v111, v240\n"
"v_sub_f32 v112, v112, v241\n"
"v_sub_f32 v113, v113, v241\n"
"v_sub_f32 v114, v114, v241\n"
"v_sub_f32 v115, v115, v241\n"
"v_sub_f32 v116, v116, v241\n"
"v_sub_f32 v117, v117, v241\n"
"v_sub_f32 v118, v118, v241\n"
"v_sub_f32 v119, v119, v241\n"
"v_sub_f32 v120, v120, v241\n"
"v_sub_f32 v121, v121, v241\n"
"v_sub_f32 v122, v122, v241\n"
"v_sub_f32 v123, v123, v241\n"
"v_sub_f32 v124, v124, v241\n"
"v_sub_f32 v125, v125, v241\n"
"v_sub_f32 v126, v126, v241\n"
"v_sub_f32 v127, v127, v241\n"
"v_sub_f32 v64, v64, v240\n"
"v_sub_f32 v65, v65, v240\n"
"v_sub_f32 v66, v66, v240\n"
"v_sub_f32 v67, v67, v240\n"
"v_sub_f32 v68, v68, v240\n"
"v_sub_f32 v69, v69, v240\n"
"v_sub_f32 v70, v70, v240\n"
"v_sub_f32 v71, v71, v240\n"
"v_sub_f32 v72, v72, v240\n"
"v_sub_f32 v73, v73, v240\n"
"v_sub_f32 v74, v74, v240\n"
"v_sub_f32 v75, v75, v240\n"
"v_sub_f32 v76, v76, v240\n"
"v_sub_f32 v77, v77, v240\n"
"v_sub_f32 v78, v78, v240\n"
"v_sub_f32 v79, v79, v240\n"
"v_sub_f32 v80, v80, v241\n"
"v_sub_f32 v81, v81, v241\n"
"v_sub_f32 v82, v82, v241\n"
"v_sub_f32 v83, v83, v241\n"
"v_sub_f32 v84, v84, v241\n"
"v_sub_f32 v85, v85, v241\n"
"v_sub_f32 v86, v86, v241\n"
"v_sub_f32 v87, v87, v241\n"
"v_sub_f32 v88, v88, v241\n"
"v_sub_f32 v89, v89, v241\n"
"v_sub_f32 v90, v90, v241\n"
"v_sub_f32 v91, v91, v241\n"
"v_sub_f32 v92, v92, v241\n"
"v_sub_f32 v93, v93, v241\n"
"v_sub_f32 v94, v94, v241\n"
"v_sub_f32 v95, v95, v241\n"
"v_exp_f32 v224, v96\n"
"s_nop 0\n"
"v_exp_f32 v225, v97\n"
"s_nop 0\n"
"v_add_f32 v232, v224, v225\n"
"v_cvt_pk_bf16_f32 v144, v224, v225\n"
"v_exp_f32 v226, v98\n"
"s_nop 0\n"
"v_add_f32 v232, v232, v226\n"
"v_exp_f32 v227, v99\n"
"s_nop 0\n"
"v_add_f32 v232, v232, v227\n"
"v_cvt_pk_bf16_f32 v145, v226, v227\n"
"v_exp_f32 v228, v100\n"
"s_nop 0\n"
"v_add_f32 v232, v232, v228\n"
"v_exp_f32 v229, v101\n"
"s_nop 0\n"
"v_add_f32 v232, v232, v229\n"
"v_cvt_pk_bf16_f32 v146, v228, v229\n"
"v_exp_f32 v230, v102\n"
"s_nop 0\n"
"v_add_f32 v232, v232, v230\n"
"v_exp_f32 v231, v103\n"
"s_nop 0\n"
"v_add_f32 v232, v232, v231\n"
"v_cvt_pk_bf16_f32 v147, v230, v231\n"
"v_exp_f32 v224, v104\n"
"s_nop 0\n"
"v_add_f32 v232, v232, v224\n"
"v_exp_f32 v225, v105\n"
"s_nop 0\n"
"v_add_f32 v232, v232, v225\n"
"v_cvt_pk_bf16_f32 v148, v224, v225\n"
"v_exp_f32 v226, v106\n"
"s_nop 0\n"
"v_add_f32 v232, v232, v226\n"
"v_exp_f32 v227, v107\n"
"s_nop 0\n"
"v_add_f32 v232, v232, v227\n"
"v_cvt_pk_bf16_f32 v149, v226, v227\n"
"v_exp_f32 v228, v108\n"
"s_nop 0\n"
"v_add_f32 v232, v232, v228\n"
"v_exp_f32 v229, v109\n"
"s_nop 0\n"
"v_add_f32 v232, v232, v229\n"
"v_cvt_pk_bf16_f32 v150, v228, v229\n"
"v_exp_f32 v230, v110\n"
"s_nop 0\n"
"v_add_f32 v232, v232, v230\n"
"v_exp_f32 v231, v111\n"
"s_nop 0\n"
"v_add_f32 v232, v232, v231\n"
"v_cvt_pk_bf16_f32 v151, v230, v231\n"
"v_exp_f32 v224, v112\n"
"s_nop 0\n"
"v_exp_f32 v225, v113\n"
"s_nop 0\n"
"v_add_f32 v233, v224, v225\n"
"v_cvt_pk_bf16_f32 v152, v224, v225\n"
"v_exp_f32 v226, v114\n"
"s_nop 0\n"
"v_add_f32 v233, v233, v226\n"
"v_exp_f32 v227, v115\n"
"s_nop 0\n"
"v_add_f32 v233, v233, v227\n"
"v_cvt_pk_bf16_f32 v153, v226, v227\n"
"v_exp_f32 v228, v116\n"
"s_nop 0\n"
"v_add_f32 v233, v233, v228\n"
"v_exp_f32 v229, v117\n"
"s_nop 0\n"
"v_add_f32 v233, v233, v229\n"
"v_cvt_pk_bf16_f32 v154, v228, v229\n"
"v_exp_f32 v230, v118\n"
"s_nop 0\n"
"v_add_f32 v233, v233, v230\n"
"v_exp_f32 v231, v119\n"
"s_nop 0\n"
"v_add_f32 v233, v233, v231\n"
"v_cvt_pk_bf16_f32 v155, v230, v231\n"
"v_exp_f32 v224, v120\n"
"s_nop 0\n"
"v_add_f32 v233, v233, v224\n"
"v_exp_f32 v225, v121\n"
"s_nop 0\n"
"v_add_f32 v233, v233, v225\n"
"v_cvt_pk_bf16_f32 v156, v224, v225\n"
"v_exp_f32 v226, v122\n"
"s_nop 0\n"
"v_add_f32 v233, v233, v226\n"
"v_exp_f32 v227, v123\n"
"s_nop 0\n"
"v_add_f32 v233, v233, v227\n"
"v_cvt_pk_bf16_f32 v157, v226, v227\n"
"v_exp_f32 v228, v124\n"
"s_nop 0\n"
"v_add_f32 v233, v233, v228\n"
"v_exp_f32 v229, v125\n"
"s_nop 0\n"
"v_add_f32 v233, v233, v229\n"
"v_cvt_pk_bf16_f32 v158, v228, v229\n"
"v_exp_f32 v230, v126\n"
"s_nop 0\n"
"v_add_f32 v233, v233, v230\n"
"v_exp_f32 v231, v127\n"
"s_nop 0\n"
"v_add_f32 v233, v233, v231\n"
"v_cvt_pk_bf16_f32 v159, v230, v231\n"
"v_add_f32 v236, v232, v233\n"
"s_nop 1\n"
"s_branch Lfr25_%=\n"
"Lfb29_%=:\n"
"s_nop 7\n"
"s_nop 7\n"
"v_max3_f32 v240, v64, v65, v66\n"
"v_max3_f32 v240, v240, v67, v68\n"
"v_max3_f32 v240, v240, v69, v70\n"
"v_max3_f32 v240, v240, v71, v72\n"
"v_max3_f32 v240, v240, v73, v74\n"
"v_max3_f32 v240, v240, v75, v76\n"
"v_max3_f32 v240, v240, v77, v78\n"
"v_max_f32 v240, v240, v79\n"
"v_mov_b32 v244, v240\n"
"s_nop 1\n"
"v_permlane32_swap_b32 v240, v244\n"
"v_max_f32 v240, v240, v244\n"
"v_max_f32 v240, 0, v240\n"
"v_sub_f32 v244, 0, v240\n"
"v_exp_f32 v242, v244\n"
"s_nop 0\n"
"v_mul_f32 v234, v234, v242\n"
"v_sub_f32 v160, v160, v240\n"
"v_sub_f32 v161, v161, v240\n"
"v_sub_f32 v162, v162, v240\n"
"v_sub_f32 v163, v163, v240\n"
"v_sub_f32 v164, v164, v240\n"
"v_sub_f32 v165, v165, v240\n"
"v_sub_f32 v166, v166, v240\n"
"v_sub_f32 v167, v167, v240\n"
"v_sub_f32 v168, v168, v240\n"
"v_sub_f32 v169, v169, v240\n"
"v_sub_f32 v170, v170, v240\n"
"v_sub_f32 v171, v171, v240\n"
"v_sub_f32 v172, v172, v240\n"
"v_sub_f32 v173, v173, v240\n"
"v_sub_f32 v174, v174, v240\n"
"v_sub_f32 v175, v175, v240\n"
"v_accvgpr_read_b32 v244, a0\n"
"v_mul_f32 v244, v244, v242\n"
"v_accvgpr_write_b32 a0, v244\n"
"v_accvgpr_read_b32 v245, a1\n"
"v_mul_f32 v245, v245, v242\n"
"v_accvgpr_write_b32 a1, v245\n"
"v_accvgpr_read_b32 v244, a2\n"
"v_mul_f32 v244, v244, v242\n"
"v_accvgpr_write_b32 a2, v244\n"
"v_accvgpr_read_b32 v245, a3\n"
"v_mul_f32 v245, v245, v242\n"
"v_accvgpr_write_b32 a3, v245\n"
"v_accvgpr_read_b32 v244, a4\n"
"v_mul_f32 v244, v244, v242\n"
"v_accvgpr_write_b32 a4, v244\n"
"v_accvgpr_read_b32 v245, a5\n"
"v_mul_f32 v245, v245, v242\n"
"v_accvgpr_write_b32 a5, v245\n"
"v_accvgpr_read_b32 v244, a6\n"
"v_mul_f32 v244, v244, v242\n"
"v_accvgpr_write_b32 a6, v244\n"
"v_accvgpr_read_b32 v245, a7\n"
"v_mul_f32 v245, v245, v242\n"
"v_accvgpr_write_b32 a7, v245\n"
"v_accvgpr_read_b32 v244, a8\n"
"v_mul_f32 v244, v244, v242\n"
"v_accvgpr_write_b32 a8, v244\n"
"v_accvgpr_read_b32 v245, a9\n"
"v_mul_f32 v245, v245, v242\n"
"v_accvgpr_write_b32 a9, v245\n"
"v_accvgpr_read_b32 v244, a10\n"
"v_mul_f32 v244, v244, v242\n"
"v_accvgpr_write_b32 a10, v244\n"
"v_accvgpr_read_b32 v245, a11\n"
"v_mul_f32 v245, v245, v242\n"
"v_accvgpr_write_b32 a11, v245\n"
"v_accvgpr_read_b32 v244, a12\n"
"v_mul_f32 v244, v244, v242\n"
"v_accvgpr_write_b32 a12, v244\n"
"v_accvgpr_read_b32 v245, a13\n"
"v_mul_f32 v245, v245, v242\n"
"v_accvgpr_write_b32 a13, v245\n"
"v_accvgpr_read_b32 v244, a14\n"
"v_mul_f32 v244, v244, v242\n"
"v_accvgpr_write_b32 a14, v244\n"
"v_accvgpr_read_b32 v245, a15\n"
"v_mul_f32 v245, v245, v242\n"
"v_accvgpr_write_b32 a15, v245\n"
"v_accvgpr_read_b32 v244, a16\n"
"v_mul_f32 v244, v244, v242\n"
"v_accvgpr_write_b32 a16, v244\n"
"v_accvgpr_read_b32 v245, a17\n"
"v_mul_f32 v245, v245, v242\n"
"v_accvgpr_write_b32 a17, v245\n"
"v_accvgpr_read_b32 v244, a18\n"
"v_mul_f32 v244, v244, v242\n"
"v_accvgpr_write_b32 a18, v244\n"
"v_accvgpr_read_b32 v245, a19\n"
"v_mul_f32 v245, v245, v242\n"
"v_accvgpr_write_b32 a19, v245\n"
"v_accvgpr_read_b32 v244, a20\n"
"v_mul_f32 v244, v244, v242\n"
"v_accvgpr_write_b32 a20, v244\n"
"v_accvgpr_read_b32 v245, a21\n"
"v_mul_f32 v245, v245, v242\n"
"v_accvgpr_write_b32 a21, v245\n"
"v_accvgpr_read_b32 v244, a22\n"
"v_mul_f32 v244, v244, v242\n"
"v_accvgpr_write_b32 a22, v244\n"
"v_accvgpr_read_b32 v245, a23\n"
"v_mul_f32 v245, v245, v242\n"
"v_accvgpr_write_b32 a23, v245\n"
"v_accvgpr_read_b32 v244, a24\n"
"v_mul_f32 v244, v244, v242\n"
"v_accvgpr_write_b32 a24, v244\n"
"v_accvgpr_read_b32 v245, a25\n"
"v_mul_f32 v245, v245, v242\n"
"v_accvgpr_write_b32 a25, v245\n"
"v_accvgpr_read_b32 v244, a26\n"
"v_mul_f32 v244, v244, v242\n"
"v_accvgpr_write_b32 a26, v244\n"
"v_accvgpr_read_b32 v245, a27\n"
"v_mul_f32 v245, v245, v242\n"
"v_accvgpr_write_b32 a27, v245\n"
"v_accvgpr_read_b32 v244, a28\n"
"v_mul_f32 v244, v244, v242\n"
"v_accvgpr_write_b32 a28, v244\n"
"v_accvgpr_read_b32 v245, a29\n"
"v_mul_f32 v245, v245, v242\n"
"v_accvgpr_write_b32 a29, v245\n"
"v_accvgpr_read_b32 v244, a30\n"
"v_mul_f32 v244, v244, v242\n"
"v_accvgpr_write_b32 a30, v244\n"
"v_accvgpr_read_b32 v245, a31\n"
"v_mul_f32 v245, v245, v242\n"
"v_accvgpr_write_b32 a31, v245\n"
"v_accvgpr_read_b32 v244, a32\n"
"v_mul_f32 v244, v244, v242\n"
"v_accvgpr_write_b32 a32, v244\n"
"v_accvgpr_read_b32 v245, a33\n"
"v_mul_f32 v245, v245, v242\n"
"v_accvgpr_write_b32 a33, v245\n"
"v_accvgpr_read_b32 v244, a34\n"
"v_mul_f32 v244, v244, v242\n"
"v_accvgpr_write_b32 a34, v244\n"
"v_accvgpr_read_b32 v245, a35\n"
"v_mul_f32 v245, v245, v242\n"
"v_accvgpr_write_b32 a35, v245\n"
"v_accvgpr_read_b32 v244, a36\n"
"v_mul_f32 v244, v244, v242\n"
"v_accvgpr_write_b32 a36, v244\n"
"v_accvgpr_read_b32 v245, a37\n"
"v_mul_f32 v245, v245, v242\n"
"v_accvgpr_write_b32 a37, v245\n"
"v_accvgpr_read_b32 v244, a38\n"
"v_mul_f32 v244, v244, v242\n"
"v_accvgpr_write_b32 a38, v244\n"
"v_accvgpr_read_b32 v245, a39\n"
"v_mul_f32 v245, v245, v242\n"
"v_accvgpr_write_b32 a39, v245\n"
"v_accvgpr_read_b32 v244, a40\n"
"v_mul_f32 v244, v244, v242\n"
"v_accvgpr_write_b32 a40, v244\n"
"v_accvgpr_read_b32 v245, a41\n"
"v_mul_f32 v245, v245, v242\n"
"v_accvgpr_write_b32 a41, v245\n"
"v_accvgpr_read_b32 v244, a42\n"
"v_mul_f32 v244, v244, v242\n"
"v_accvgpr_write_b32 a42, v244\n"
"v_accvgpr_read_b32 v245, a43\n"
"v_mul_f32 v245, v245, v242\n"
"v_accvgpr_write_b32 a43, v245\n"
"v_accvgpr_read_b32 v244, a44\n"
"v_mul_f32 v244, v244, v242\n"
"v_accvgpr_write_b32 a44, v244\n"
"v_accvgpr_read_b32 v245, a45\n"
"v_mul_f32 v245, v245, v242\n"
"v_accvgpr_write_b32 a45, v245\n"
"v_accvgpr_read_b32 v244, a46\n"
"v_mul_f32 v244, v244, v242\n"
"v_accvgpr_write_b32 a46, v244\n"
"v_accvgpr_read_b32 v245, a47\n"
"v_mul_f32 v245, v245, v242\n"
"v_accvgpr_write_b32 a47, v245\n"
"v_accvgpr_read_b32 v244, a48\n"
"v_mul_f32 v244, v244, v242\n"
"v_accvgpr_write_b32 a48, v244\n"
"v_accvgpr_read_b32 v245, a49\n"
"v_mul_f32 v245, v245, v242\n"
"v_accvgpr_write_b32 a49, v245\n"
"v_accvgpr_read_b32 v244, a50\n"
"v_mul_f32 v244, v244, v242\n"
"v_accvgpr_write_b32 a50, v244\n"
"v_accvgpr_read_b32 v245, a51\n"
"v_mul_f32 v245, v245, v242\n"
"v_accvgpr_write_b32 a51, v245\n"
"v_accvgpr_read_b32 v244, a52\n"
"v_mul_f32 v244, v244, v242\n"
"v_accvgpr_write_b32 a52, v244\n"
"v_accvgpr_read_b32 v245, a53\n"
"v_mul_f32 v245, v245, v242\n"
"v_accvgpr_write_b32 a53, v245\n"
"v_accvgpr_read_b32 v244, a54\n"
"v_mul_f32 v244, v244, v242\n"
"v_accvgpr_write_b32 a54, v244\n"
"v_accvgpr_read_b32 v245, a55\n"
"v_mul_f32 v245, v245, v242\n"
"v_accvgpr_write_b32 a55, v245\n"
"v_accvgpr_read_b32 v244, a56\n"
"v_mul_f32 v244, v244, v242\n"
"v_accvgpr_write_b32 a56, v244\n"
"v_accvgpr_read_b32 v245, a57\n"
"v_mul_f32 v245, v245, v242\n"
"v_accvgpr_write_b32 a57, v245\n"
"v_accvgpr_read_b32 v244, a58\n"
"v_mul_f32 v244, v244, v242\n"
"v_accvgpr_write_b32 a58, v244\n"
"v_accvgpr_read_b32 v245, a59\n"
"v_mul_f32 v245, v245, v242\n"
"v_accvgpr_write_b32 a59, v245\n"
"v_accvgpr_read_b32 v244, a60\n"
"v_mul_f32 v244, v244, v242\n"
"v_accvgpr_write_b32 a60, v244\n"
"v_accvgpr_read_b32 v245, a61\n"
"v_mul_f32 v245, v245, v242\n"
"v_accvgpr_write_b32 a61, v245\n"
"v_accvgpr_read_b32 v244, a62\n"
"v_mul_f32 v244, v244, v242\n"
"v_accvgpr_write_b32 a62, v244\n"
"v_accvgpr_read_b32 v245, a63\n"
"v_mul_f32 v245, v245, v242\n"
"v_accvgpr_write_b32 a63, v245\n"
"v_max3_f32 v241, v80, v81, v82\n"
"v_max3_f32 v241, v241, v83, v84\n"
"v_max3_f32 v241, v241, v85, v86\n"
"v_max3_f32 v241, v241, v87, v88\n"
"v_max3_f32 v241, v241, v89, v90\n"
"v_max3_f32 v241, v241, v91, v92\n"
"v_max3_f32 v241, v241, v93, v94\n"
"v_max_f32 v241, v241, v95\n"
"v_mov_b32 v244, v241\n"
"s_nop 1\n"
"v_permlane32_swap_b32 v241, v244\n"
"v_max_f32 v241, v241, v244\n"
"v_max_f32 v241, 0, v241\n"
"v_sub_f32 v244, 0, v241\n"
"v_exp_f32 v243, v244\n"
"s_nop 0\n"
"v_mul_f32 v235, v235, v243\n"
"v_sub_f32 v176, v176, v241\n"
"v_sub_f32 v177, v177, v241\n"
"v_sub_f32 v178, v178, v241\n"
"v_sub_f32 v179, v179, v241\n"
"v_sub_f32 v180, v180, v241\n"
"v_sub_f32 v181, v181, v241\n"
"v_sub_f32 v182, v182, v241\n"
"v_sub_f32 v183, v183, v241\n"
"v_sub_f32 v184, v184, v241\n"
"v_sub_f32 v185, v185, v241\n"
"v_sub_f32 v186, v186, v241\n"
"v_sub_f32 v187, v187, v241\n"
"v_sub_f32 v188, v188, v241\n"
"v_sub_f32 v189, v189, v241\n"
"v_sub_f32 v190, v190, v241\n"
"v_sub_f32 v191, v191, v241\n"
"v_accvgpr_read_b32 v244, a64\n"
"v_mul_f32 v244, v244, v243\n"
"v_accvgpr_write_b32 a64, v244\n"
"v_accvgpr_read_b32 v245, a65\n"
"v_mul_f32 v245, v245, v243\n"
"v_accvgpr_write_b32 a65, v245\n"
"v_accvgpr_read_b32 v244, a66\n"
"v_mul_f32 v244, v244, v243\n"
"v_accvgpr_write_b32 a66, v244\n"
"v_accvgpr_read_b32 v245, a67\n"
"v_mul_f32 v245, v245, v243\n"
"v_accvgpr_write_b32 a67, v245\n"
"v_accvgpr_read_b32 v244, a68\n"
"v_mul_f32 v244, v244, v243\n"
"v_accvgpr_write_b32 a68, v244\n"
"v_accvgpr_read_b32 v245, a69\n"
"v_mul_f32 v245, v245, v243\n"
"v_accvgpr_write_b32 a69, v245\n"
"v_accvgpr_read_b32 v244, a70\n"
"v_mul_f32 v244, v244, v243\n"
"v_accvgpr_write_b32 a70, v244\n"
"v_accvgpr_read_b32 v245, a71\n"
"v_mul_f32 v245, v245, v243\n"
"v_accvgpr_write_b32 a71, v245\n"
"v_accvgpr_read_b32 v244, a72\n"
"v_mul_f32 v244, v244, v243\n"
"v_accvgpr_write_b32 a72, v244\n"
"v_accvgpr_read_b32 v245, a73\n"
"v_mul_f32 v245, v245, v243\n"
"v_accvgpr_write_b32 a73, v245\n"
"v_accvgpr_read_b32 v244, a74\n"
"v_mul_f32 v244, v244, v243\n"
"v_accvgpr_write_b32 a74, v244\n"
"v_accvgpr_read_b32 v245, a75\n"
"v_mul_f32 v245, v245, v243\n"
"v_accvgpr_write_b32 a75, v245\n"
"v_accvgpr_read_b32 v244, a76\n"
"v_mul_f32 v244, v244, v243\n"
"v_accvgpr_write_b32 a76, v244\n"
"v_accvgpr_read_b32 v245, a77\n"
"v_mul_f32 v245, v245, v243\n"
"v_accvgpr_write_b32 a77, v245\n"
"v_accvgpr_read_b32 v244, a78\n"
"v_mul_f32 v244, v244, v243\n"
"v_accvgpr_write_b32 a78, v244\n"
"v_accvgpr_read_b32 v245, a79\n"
"v_mul_f32 v245, v245, v243\n"
"v_accvgpr_write_b32 a79, v245\n"
"v_accvgpr_read_b32 v244, a80\n"
"v_mul_f32 v244, v244, v243\n"
"v_accvgpr_write_b32 a80, v244\n"
"v_accvgpr_read_b32 v245, a81\n"
"v_mul_f32 v245, v245, v243\n"
"v_accvgpr_write_b32 a81, v245\n"
"v_accvgpr_read_b32 v244, a82\n"
"v_mul_f32 v244, v244, v243\n"
"v_accvgpr_write_b32 a82, v244\n"
"v_accvgpr_read_b32 v245, a83\n"
"v_mul_f32 v245, v245, v243\n"
"v_accvgpr_write_b32 a83, v245\n"
"v_accvgpr_read_b32 v244, a84\n"
"v_mul_f32 v244, v244, v243\n"
"v_accvgpr_write_b32 a84, v244\n"
"v_accvgpr_read_b32 v245, a85\n"
"v_mul_f32 v245, v245, v243\n"
"v_accvgpr_write_b32 a85, v245\n"
"v_accvgpr_read_b32 v244, a86\n"
"v_mul_f32 v244, v244, v243\n"
"v_accvgpr_write_b32 a86, v244\n"
"v_accvgpr_read_b32 v245, a87\n"
"v_mul_f32 v245, v245, v243\n"
"v_accvgpr_write_b32 a87, v245\n"
"v_accvgpr_read_b32 v244, a88\n"
"v_mul_f32 v244, v244, v243\n"
"v_accvgpr_write_b32 a88, v244\n"
"v_accvgpr_read_b32 v245, a89\n"
"v_mul_f32 v245, v245, v243\n"
"v_accvgpr_write_b32 a89, v245\n"
"v_accvgpr_read_b32 v244, a90\n"
"v_mul_f32 v244, v244, v243\n"
"v_accvgpr_write_b32 a90, v244\n"
"v_accvgpr_read_b32 v245, a91\n"
"v_mul_f32 v245, v245, v243\n"
"v_accvgpr_write_b32 a91, v245\n"
"v_accvgpr_read_b32 v244, a92\n"
"v_mul_f32 v244, v244, v243\n"
"v_accvgpr_write_b32 a92, v244\n"
"v_accvgpr_read_b32 v245, a93\n"
"v_mul_f32 v245, v245, v243\n"
"v_accvgpr_write_b32 a93, v245\n"
"v_accvgpr_read_b32 v244, a94\n"
"v_mul_f32 v244, v244, v243\n"
"v_accvgpr_write_b32 a94, v244\n"
"v_accvgpr_read_b32 v245, a95\n"
"v_mul_f32 v245, v245, v243\n"
"v_accvgpr_write_b32 a95, v245\n"
"v_accvgpr_read_b32 v244, a96\n"
"v_mul_f32 v244, v244, v243\n"
"v_accvgpr_write_b32 a96, v244\n"
"v_accvgpr_read_b32 v245, a97\n"
"v_mul_f32 v245, v245, v243\n"
"v_accvgpr_write_b32 a97, v245\n"
"v_accvgpr_read_b32 v244, a98\n"
"v_mul_f32 v244, v244, v243\n"
"v_accvgpr_write_b32 a98, v244\n"
"v_accvgpr_read_b32 v245, a99\n"
"v_mul_f32 v245, v245, v243\n"
"v_accvgpr_write_b32 a99, v245\n"
"v_accvgpr_read_b32 v244, a100\n"
"v_mul_f32 v244, v244, v243\n"
"v_accvgpr_write_b32 a100, v244\n"
"v_accvgpr_read_b32 v245, a101\n"
"v_mul_f32 v245, v245, v243\n"
"v_accvgpr_write_b32 a101, v245\n"
"v_accvgpr_read_b32 v244, a102\n"
"v_mul_f32 v244, v244, v243\n"
"v_accvgpr_write_b32 a102, v244\n"
"v_accvgpr_read_b32 v245, a103\n"
"v_mul_f32 v245, v245, v243\n"
"v_accvgpr_write_b32 a103, v245\n"
"v_accvgpr_read_b32 v244, a104\n"
"v_mul_f32 v244, v244, v243\n"
"v_accvgpr_write_b32 a104, v244\n"
"v_accvgpr_read_b32 v245, a105\n"
"v_mul_f32 v245, v245, v243\n"
"v_accvgpr_write_b32 a105, v245\n"
"v_accvgpr_read_b32 v244, a106\n"
"v_mul_f32 v244, v244, v243\n"
"v_accvgpr_write_b32 a106, v244\n"
"v_accvgpr_read_b32 v245, a107\n"
"v_mul_f32 v245, v245, v243\n"
"v_accvgpr_write_b32 a107, v245\n"
"v_accvgpr_read_b32 v244, a108\n"
"v_mul_f32 v244, v244, v243\n"
"v_accvgpr_write_b32 a108, v244\n"
"v_accvgpr_read_b32 v245, a109\n"
"v_mul_f32 v245, v245, v243\n"
"v_accvgpr_write_b32 a109, v245\n"
"v_accvgpr_read_b32 v244, a110\n"
"v_mul_f32 v244, v244, v243\n"
"v_accvgpr_write_b32 a110, v244\n"
"v_accvgpr_read_b32 v245, a111\n"
"v_mul_f32 v245, v245, v243\n"
"v_accvgpr_write_b32 a111, v245\n"
"v_accvgpr_read_b32 v244, a112\n"
"v_mul_f32 v244, v244, v243\n"
"v_accvgpr_write_b32 a112, v244\n"
"v_accvgpr_read_b32 v245, a113\n"
"v_mul_f32 v245, v245, v243\n"
"v_accvgpr_write_b32 a113, v245\n"
"v_accvgpr_read_b32 v244, a114\n"
"v_mul_f32 v244, v244, v243\n"
"v_accvgpr_write_b32 a114, v244\n"
"v_accvgpr_read_b32 v245, a115\n"
"v_mul_f32 v245, v245, v243\n"
"v_accvgpr_write_b32 a115, v245\n"
"v_accvgpr_read_b32 v244, a116\n"
"v_mul_f32 v244, v244, v243\n"
"v_accvgpr_write_b32 a116, v244\n"
"v_accvgpr_read_b32 v245, a117\n"
"v_mul_f32 v245, v245, v243\n"
"v_accvgpr_write_b32 a117, v245\n"
"v_accvgpr_read_b32 v244, a118\n"
"v_mul_f32 v244, v244, v243\n"
"v_accvgpr_write_b32 a118, v244\n"
"v_accvgpr_read_b32 v245, a119\n"
"v_mul_f32 v245, v245, v243\n"
"v_accvgpr_write_b32 a119, v245\n"
"v_accvgpr_read_b32 v244, a120\n"
"v_mul_f32 v244, v244, v243\n"
"v_accvgpr_write_b32 a120, v244\n"
"v_accvgpr_read_b32 v245, a121\n"
"v_mul_f32 v245, v245, v243\n"
"v_accvgpr_write_b32 a121, v245\n"
"v_accvgpr_read_b32 v244, a122\n"
"v_mul_f32 v244, v244, v243\n"
"v_accvgpr_write_b32 a122, v244\n"
"v_accvgpr_read_b32 v245, a123\n"
"v_mul_f32 v245, v245, v243\n"
"v_accvgpr_write_b32 a123, v245\n"
"v_accvgpr_read_b32 v244, a124\n"
"v_mul_f32 v244, v244, v243\n"
"v_accvgpr_write_b32 a124, v244\n"
"v_accvgpr_read_b32 v245, a125\n"
"v_mul_f32 v245, v245, v243\n"
"v_accvgpr_write_b32 a125, v245\n"
"v_accvgpr_read_b32 v244, a126\n"
"v_mul_f32 v244, v244, v243\n"
"v_accvgpr_write_b32 a126, v244\n"
"v_accvgpr_read_b32 v245, a127\n"
"v_mul_f32 v245, v245, v243\n"
"v_accvgpr_write_b32 a127, v245\n"
"v_sub_f32 v64, v64, v240\n"
"v_sub_f32 v65, v65, v240\n"
"v_sub_f32 v66, v66, v240\n"
"v_sub_f32 v67, v67, v240\n"
"v_sub_f32 v68, v68, v240\n"
"v_sub_f32 v69, v69, v240\n"
"v_sub_f32 v70, v70, v240\n"
"v_sub_f32 v71, v71, v240\n"
"v_sub_f32 v72, v72, v240\n"
"v_sub_f32 v73, v73, v240\n"
"v_sub_f32 v74, v74, v240\n"
"v_sub_f32 v75, v75, v240\n"
"v_sub_f32 v76, v76, v240\n"
"v_sub_f32 v77, v77, v240\n"
"v_sub_f32 v78, v78, v240\n"
"v_sub_f32 v79, v79, v240\n"
"v_sub_f32 v80, v80, v241\n"
"v_sub_f32 v81, v81, v241\n"
"v_sub_f32 v82, v82, v241\n"
"v_sub_f32 v83, v83, v241\n"
"v_sub_f32 v84, v84, v241\n"
"v_sub_f32 v85, v85, v241\n"
"v_sub_f32 v86, v86, v241\n"
"v_sub_f32 v87, v87, v241\n"
"v_sub_f32 v88, v88, v241\n"
"v_sub_f32 v89, v89, v241\n"
"v_sub_f32 v90, v90, v241\n"
"v_sub_f32 v91, v91, v241\n"
"v_sub_f32 v92, v92, v241\n"
"v_sub_f32 v93, v93, v241\n"
"v_sub_f32 v94, v94, v241\n"
"v_sub_f32 v95, v95, v241\n"
"v_sub_f32 v96, v96, v240\n"
"v_sub_f32 v97, v97, v240\n"
"v_sub_f32 v98, v98, v240\n"
"v_sub_f32 v99, v99, v240\n"
"v_sub_f32 v100, v100, v240\n"
"v_sub_f32 v101, v101, v240\n"
"v_sub_f32 v102, v102, v240\n"
"v_sub_f32 v103, v103, v240\n"
"v_sub_f32 v104, v104, v240\n"
"v_sub_f32 v105, v105, v240\n"
"v_sub_f32 v106, v106, v240\n"
"v_sub_f32 v107, v107, v240\n"
"v_sub_f32 v108, v108, v240\n"
"v_sub_f32 v109, v109, v240\n"
"v_sub_f32 v110, v110, v240\n"
"v_sub_f32 v111, v111, v240\n"
"v_sub_f32 v112, v112, v241\n"
"v_sub_f32 v113, v113, v241\n"
"v_sub_f32 v114, v114, v241\n"
"v_sub_f32 v115, v115, v241\n"
"v_sub_f32 v116, v116, v241\n"
"v_sub_f32 v117, v117, v241\n"
"v_sub_f32 v118, v118, v241\n"
"v_sub_f32 v119, v119, v241\n"
"v_sub_f32 v120, v120, v241\n"
"v_sub_f32 v121, v121, v241\n"
"v_sub_f32 v122, v122, v241\n"
"v_sub_f32 v123, v123, v241\n"
"v_sub_f32 v124, v124, v241\n"
"v_sub_f32 v125, v125, v241\n"
"v_sub_f32 v126, v126, v241\n"
"v_sub_f32 v127, v127, v241\n"
"v_exp_f32 v224, v64\n"
"s_nop 0\n"
"v_exp_f32 v225, v65\n"
"s_nop 0\n"
"v_add_f32 v232, v224, v225\n"
"v_cvt_pk_bf16_f32 v128, v224, v225\n"
"v_exp_f32 v226, v66\n"
"s_nop 0\n"
"v_add_f32 v232, v232, v226\n"
"v_exp_f32 v227, v67\n"
"s_nop 0\n"
"v_add_f32 v232, v232, v227\n"
"v_cvt_pk_bf16_f32 v129, v226, v227\n"
"v_exp_f32 v228, v68\n"
"s_nop 0\n"
"v_add_f32 v232, v232, v228\n"
"v_exp_f32 v229, v69\n"
"s_nop 0\n"
"v_add_f32 v232, v232, v229\n"
"v_cvt_pk_bf16_f32 v130, v228, v229\n"
"v_exp_f32 v230, v70\n"
"s_nop 0\n"
"v_add_f32 v232, v232, v230\n"
"v_exp_f32 v231, v71\n"
"s_nop 0\n"
"v_add_f32 v232, v232, v231\n"
"v_cvt_pk_bf16_f32 v131, v230, v231\n"
"v_exp_f32 v224, v72\n"
"s_nop 0\n"
"v_add_f32 v232, v232, v224\n"
"v_exp_f32 v225, v73\n"
"s_nop 0\n"
"v_add_f32 v232, v232, v225\n"
"v_cvt_pk_bf16_f32 v132, v224, v225\n"
"v_exp_f32 v226, v74\n"
"s_nop 0\n"
"v_add_f32 v232, v232, v226\n"
"v_exp_f32 v227, v75\n"
"s_nop 0\n"
"v_add_f32 v232, v232, v227\n"
"v_cvt_pk_bf16_f32 v133, v226, v227\n"
"v_exp_f32 v228, v76\n"
"s_nop 0\n"
"v_add_f32 v232, v232, v228\n"
"v_exp_f32 v229, v77\n"
"s_nop 0\n"
"v_add_f32 v232, v232, v229\n"
"v_cvt_pk_bf16_f32 v134, v228, v229\n"
"v_exp_f32 v230, v78\n"
"s_nop 0\n"
"v_add_f32 v232, v232, v230\n"
"v_exp_f32 v231, v79\n"
"s_nop 0\n"
"v_add_f32 v232, v232, v231\n"
"v_cvt_pk_bf16_f32 v135, v230, v231\n"
"v_exp_f32 v224, v80\n"
"s_nop 0\n"
"v_exp_f32 v225, v81\n"
"s_nop 0\n"
"v_add_f32 v233, v224, v225\n"
"v_cvt_pk_bf16_f32 v136, v224, v225\n"
"v_exp_f32 v226, v82\n"
"s_nop 0\n"
"v_add_f32 v233, v233, v226\n"
"v_exp_f32 v227, v83\n"
"s_nop 0\n"
"v_add_f32 v233, v233, v227\n"
"v_cvt_pk_bf16_f32 v137, v226, v227\n"
"v_exp_f32 v228, v84\n"
"s_nop 0\n"
"v_add_f32 v233, v233, v228\n"
"v_exp_f32 v229, v85\n"
"s_nop 0\n"
"v_add_f32 v233, v233, v229\n"
"v_cvt_pk_bf16_f32 v138, v228, v229\n"
"v_exp_f32 v230, v86\n"
"s_nop 0\n"
"v_add_f32 v233, v233, v230\n"
"v_exp_f32 v231, v87\n"
"s_nop 0\n"
"v_add_f32 v233, v233, v231\n"
"v_cvt_pk_bf16_f32 v139, v230, v231\n"
"v_exp_f32 v224, v88\n"
"s_nop 0\n"
"v_add_f32 v233, v233, v224\n"
"v_exp_f32 v225, v89\n"
"s_nop 0\n"
"v_add_f32 v233, v233, v225\n"
"v_cvt_pk_bf16_f32 v140, v224, v225\n"
"v_exp_f32 v226, v90\n"
"s_nop 0\n"
"v_add_f32 v233, v233, v226\n"
"v_exp_f32 v227, v91\n"
"s_nop 0\n"
"v_add_f32 v233, v233, v227\n"
"v_cvt_pk_bf16_f32 v141, v226, v227\n"
"v_exp_f32 v228, v92\n"
"s_nop 0\n"
"v_add_f32 v233, v233, v228\n"
"v_exp_f32 v229, v93\n"
"s_nop 0\n"
"v_add_f32 v233, v233, v229\n"
"v_cvt_pk_bf16_f32 v142, v228, v229\n"
"v_exp_f32 v230, v94\n"
"s_nop 0\n"
"v_add_f32 v233, v233, v230\n"
"v_exp_f32 v231, v95\n"
"s_nop 0\n"
"v_add_f32 v233, v233, v231\n"
"v_cvt_pk_bf16_f32 v143, v230, v231\n"
"v_add_f32 v236, v232, v233\n"
"s_nop 1\n"
"s_branch Lfr30_%=\n"
"Lfb34_%=:\n"
"s_nop 7\n"
"s_nop 7\n"
"v_max3_f32 v240, v96, v97, v98\n"
"v_max3_f32 v240, v240, v99, v100\n"
"v_max3_f32 v240, v240, v101, v102\n"
"v_max3_f32 v240, v240, v103, v104\n"
"v_max3_f32 v240, v240, v105, v106\n"
"v_max3_f32 v240, v240, v107, v108\n"
"v_max3_f32 v240, v240, v109, v110\n"
"v_max_f32 v240, v240, v111\n"
"v_mov_b32 v244, v240\n"
"s_nop 1\n"
"v_permlane32_swap_b32 v240, v244\n"
"v_max_f32 v240, v240, v244\n"
"v_max_f32 v240, 0, v240\n"
"v_sub_f32 v244, 0, v240\n"
"v_exp_f32 v242, v244\n"
"s_nop 0\n"
"v_mul_f32 v234, v234, v242\n"
"v_sub_f32 v160, v160, v240\n"
"v_sub_f32 v161, v161, v240\n"
"v_sub_f32 v162, v162, v240\n"
"v_sub_f32 v163, v163, v240\n"
"v_sub_f32 v164, v164, v240\n"
"v_sub_f32 v165, v165, v240\n"
"v_sub_f32 v166, v166, v240\n"
"v_sub_f32 v167, v167, v240\n"
"v_sub_f32 v168, v168, v240\n"
"v_sub_f32 v169, v169, v240\n"
"v_sub_f32 v170, v170, v240\n"
"v_sub_f32 v171, v171, v240\n"
"v_sub_f32 v172, v172, v240\n"
"v_sub_f32 v173, v173, v240\n"
"v_sub_f32 v174, v174, v240\n"
"v_sub_f32 v175, v175, v240\n"
"v_accvgpr_read_b32 v244, a0\n"
"v_mul_f32 v244, v244, v242\n"
"v_accvgpr_write_b32 a0, v244\n"
"v_accvgpr_read_b32 v245, a1\n"
"v_mul_f32 v245, v245, v242\n"
"v_accvgpr_write_b32 a1, v245\n"
"v_accvgpr_read_b32 v244, a2\n"
"v_mul_f32 v244, v244, v242\n"
"v_accvgpr_write_b32 a2, v244\n"
"v_accvgpr_read_b32 v245, a3\n"
"v_mul_f32 v245, v245, v242\n"
"v_accvgpr_write_b32 a3, v245\n"
"v_accvgpr_read_b32 v244, a4\n"
"v_mul_f32 v244, v244, v242\n"
"v_accvgpr_write_b32 a4, v244\n"
"v_accvgpr_read_b32 v245, a5\n"
"v_mul_f32 v245, v245, v242\n"
"v_accvgpr_write_b32 a5, v245\n"
"v_accvgpr_read_b32 v244, a6\n"
"v_mul_f32 v244, v244, v242\n"
"v_accvgpr_write_b32 a6, v244\n"
"v_accvgpr_read_b32 v245, a7\n"
"v_mul_f32 v245, v245, v242\n"
"v_accvgpr_write_b32 a7, v245\n"
"v_accvgpr_read_b32 v244, a8\n"
"v_mul_f32 v244, v244, v242\n"
"v_accvgpr_write_b32 a8, v244\n"
"v_accvgpr_read_b32 v245, a9\n"
"v_mul_f32 v245, v245, v242\n"
"v_accvgpr_write_b32 a9, v245\n"
"v_accvgpr_read_b32 v244, a10\n"
"v_mul_f32 v244, v244, v242\n"
"v_accvgpr_write_b32 a10, v244\n"
"v_accvgpr_read_b32 v245, a11\n"
"v_mul_f32 v245, v245, v242\n"
"v_accvgpr_write_b32 a11, v245\n"
"v_accvgpr_read_b32 v244, a12\n"
"v_mul_f32 v244, v244, v242\n"
"v_accvgpr_write_b32 a12, v244\n"
"v_accvgpr_read_b32 v245, a13\n"
"v_mul_f32 v245, v245, v242\n"
"v_accvgpr_write_b32 a13, v245\n"
"v_accvgpr_read_b32 v244, a14\n"
"v_mul_f32 v244, v244, v242\n"
"v_accvgpr_write_b32 a14, v244\n"
"v_accvgpr_read_b32 v245, a15\n"
"v_mul_f32 v245, v245, v242\n"
"v_accvgpr_write_b32 a15, v245\n"
"v_accvgpr_read_b32 v244, a16\n"
"v_mul_f32 v244, v244, v242\n"
"v_accvgpr_write_b32 a16, v244\n"
"v_accvgpr_read_b32 v245, a17\n"
"v_mul_f32 v245, v245, v242\n"
"v_accvgpr_write_b32 a17, v245\n"
"v_accvgpr_read_b32 v244, a18\n"
"v_mul_f32 v244, v244, v242\n"
"v_accvgpr_write_b32 a18, v244\n"
"v_accvgpr_read_b32 v245, a19\n"
"v_mul_f32 v245, v245, v242\n"
"v_accvgpr_write_b32 a19, v245\n"
"v_accvgpr_read_b32 v244, a20\n"
"v_mul_f32 v244, v244, v242\n"
"v_accvgpr_write_b32 a20, v244\n"
"v_accvgpr_read_b32 v245, a21\n"
"v_mul_f32 v245, v245, v242\n"
"v_accvgpr_write_b32 a21, v245\n"
"v_accvgpr_read_b32 v244, a22\n"
"v_mul_f32 v244, v244, v242\n"
"v_accvgpr_write_b32 a22, v244\n"
"v_accvgpr_read_b32 v245, a23\n"
"v_mul_f32 v245, v245, v242\n"
"v_accvgpr_write_b32 a23, v245\n"
"v_accvgpr_read_b32 v244, a24\n"
"v_mul_f32 v244, v244, v242\n"
"v_accvgpr_write_b32 a24, v244\n"
"v_accvgpr_read_b32 v245, a25\n"
"v_mul_f32 v245, v245, v242\n"
"v_accvgpr_write_b32 a25, v245\n"
"v_accvgpr_read_b32 v244, a26\n"
"v_mul_f32 v244, v244, v242\n"
"v_accvgpr_write_b32 a26, v244\n"
"v_accvgpr_read_b32 v245, a27\n"
"v_mul_f32 v245, v245, v242\n"
"v_accvgpr_write_b32 a27, v245\n"
"v_accvgpr_read_b32 v244, a28\n"
"v_mul_f32 v244, v244, v242\n"
"v_accvgpr_write_b32 a28, v244\n"
"v_accvgpr_read_b32 v245, a29\n"
"v_mul_f32 v245, v245, v242\n"
"v_accvgpr_write_b32 a29, v245\n"
"v_accvgpr_read_b32 v244, a30\n"
"v_mul_f32 v244, v244, v242\n"
"v_accvgpr_write_b32 a30, v244\n"
"v_accvgpr_read_b32 v245, a31\n"
"v_mul_f32 v245, v245, v242\n"
"v_accvgpr_write_b32 a31, v245\n"
"v_accvgpr_read_b32 v244, a32\n"
"v_mul_f32 v244, v244, v242\n"
"v_accvgpr_write_b32 a32, v244\n"
"v_accvgpr_read_b32 v245, a33\n"
"v_mul_f32 v245, v245, v242\n"
"v_accvgpr_write_b32 a33, v245\n"
"v_accvgpr_read_b32 v244, a34\n"
"v_mul_f32 v244, v244, v242\n"
"v_accvgpr_write_b32 a34, v244\n"
"v_accvgpr_read_b32 v245, a35\n"
"v_mul_f32 v245, v245, v242\n"
"v_accvgpr_write_b32 a35, v245\n"
"v_accvgpr_read_b32 v244, a36\n"
"v_mul_f32 v244, v244, v242\n"
"v_accvgpr_write_b32 a36, v244\n"
"v_accvgpr_read_b32 v245, a37\n"
"v_mul_f32 v245, v245, v242\n"
"v_accvgpr_write_b32 a37, v245\n"
"v_accvgpr_read_b32 v244, a38\n"
"v_mul_f32 v244, v244, v242\n"
"v_accvgpr_write_b32 a38, v244\n"
"v_accvgpr_read_b32 v245, a39\n"
"v_mul_f32 v245, v245, v242\n"
"v_accvgpr_write_b32 a39, v245\n"
"v_accvgpr_read_b32 v244, a40\n"
"v_mul_f32 v244, v244, v242\n"
"v_accvgpr_write_b32 a40, v244\n"
"v_accvgpr_read_b32 v245, a41\n"
"v_mul_f32 v245, v245, v242\n"
"v_accvgpr_write_b32 a41, v245\n"
"v_accvgpr_read_b32 v244, a42\n"
"v_mul_f32 v244, v244, v242\n"
"v_accvgpr_write_b32 a42, v244\n"
"v_accvgpr_read_b32 v245, a43\n"
"v_mul_f32 v245, v245, v242\n"
"v_accvgpr_write_b32 a43, v245\n"
"v_accvgpr_read_b32 v244, a44\n"
"v_mul_f32 v244, v244, v242\n"
"v_accvgpr_write_b32 a44, v244\n"
"v_accvgpr_read_b32 v245, a45\n"
"v_mul_f32 v245, v245, v242\n"
"v_accvgpr_write_b32 a45, v245\n"
"v_accvgpr_read_b32 v244, a46\n"
"v_mul_f32 v244, v244, v242\n"
"v_accvgpr_write_b32 a46, v244\n"
"v_accvgpr_read_b32 v245, a47\n"
"v_mul_f32 v245, v245, v242\n"
"v_accvgpr_write_b32 a47, v245\n"
"v_accvgpr_read_b32 v244, a48\n"
"v_mul_f32 v244, v244, v242\n"
"v_accvgpr_write_b32 a48, v244\n"
"v_accvgpr_read_b32 v245, a49\n"
"v_mul_f32 v245, v245, v242\n"
"v_accvgpr_write_b32 a49, v245\n"
"v_accvgpr_read_b32 v244, a50\n"
"v_mul_f32 v244, v244, v242\n"
"v_accvgpr_write_b32 a50, v244\n"
"v_accvgpr_read_b32 v245, a51\n"
"v_mul_f32 v245, v245, v242\n"
"v_accvgpr_write_b32 a51, v245\n"
"v_accvgpr_read_b32 v244, a52\n"
"v_mul_f32 v244, v244, v242\n"
"v_accvgpr_write_b32 a52, v244\n"
"v_accvgpr_read_b32 v245, a53\n"
"v_mul_f32 v245, v245, v242\n"
"v_accvgpr_write_b32 a53, v245\n"
"v_accvgpr_read_b32 v244, a54\n"
"v_mul_f32 v244, v244, v242\n"
"v_accvgpr_write_b32 a54, v244\n"
"v_accvgpr_read_b32 v245, a55\n"
"v_mul_f32 v245, v245, v242\n"
"v_accvgpr_write_b32 a55, v245\n"
"v_accvgpr_read_b32 v244, a56\n"
"v_mul_f32 v244, v244, v242\n"
"v_accvgpr_write_b32 a56, v244\n"
"v_accvgpr_read_b32 v245, a57\n"
"v_mul_f32 v245, v245, v242\n"
"v_accvgpr_write_b32 a57, v245\n"
"v_accvgpr_read_b32 v244, a58\n"
"v_mul_f32 v244, v244, v242\n"
"v_accvgpr_write_b32 a58, v244\n"
"v_accvgpr_read_b32 v245, a59\n"
"v_mul_f32 v245, v245, v242\n"
"v_accvgpr_write_b32 a59, v245\n"
"v_accvgpr_read_b32 v244, a60\n"
"v_mul_f32 v244, v244, v242\n"
"v_accvgpr_write_b32 a60, v244\n"
"v_accvgpr_read_b32 v245, a61\n"
"v_mul_f32 v245, v245, v242\n"
"v_accvgpr_write_b32 a61, v245\n"
"v_accvgpr_read_b32 v244, a62\n"
"v_mul_f32 v244, v244, v242\n"
"v_accvgpr_write_b32 a62, v244\n"
"v_accvgpr_read_b32 v245, a63\n"
"v_mul_f32 v245, v245, v242\n"
"v_accvgpr_write_b32 a63, v245\n"
"v_max3_f32 v241, v112, v113, v114\n"
"v_max3_f32 v241, v241, v115, v116\n"
"v_max3_f32 v241, v241, v117, v118\n"
"v_max3_f32 v241, v241, v119, v120\n"
"v_max3_f32 v241, v241, v121, v122\n"
"v_max3_f32 v241, v241, v123, v124\n"
"v_max3_f32 v241, v241, v125, v126\n"
"v_max_f32 v241, v241, v127\n"
"v_mov_b32 v244, v241\n"
"s_nop 1\n"
"v_permlane32_swap_b32 v241, v244\n"
"v_max_f32 v241, v241, v244\n"
"v_max_f32 v241, 0, v241\n"
"v_sub_f32 v244, 0, v241\n"
"v_exp_f32 v243, v244\n"
"s_nop 0\n"
"v_mul_f32 v235, v235, v243\n"
"v_sub_f32 v176, v176, v241\n"
"v_sub_f32 v177, v177, v241\n"
"v_sub_f32 v178, v178, v241\n"
"v_sub_f32 v179, v179, v241\n"
"v_sub_f32 v180, v180, v241\n"
"v_sub_f32 v181, v181, v241\n"
"v_sub_f32 v182, v182, v241\n"
"v_sub_f32 v183, v183, v241\n"
"v_sub_f32 v184, v184, v241\n"
"v_sub_f32 v185, v185, v241\n"
"v_sub_f32 v186, v186, v241\n"
"v_sub_f32 v187, v187, v241\n"
"v_sub_f32 v188, v188, v241\n"
"v_sub_f32 v189, v189, v241\n"
"v_sub_f32 v190, v190, v241\n"
"v_sub_f32 v191, v191, v241\n"
"v_accvgpr_read_b32 v244, a64\n"
"v_mul_f32 v244, v244, v243\n"
"v_accvgpr_write_b32 a64, v244\n"
"v_accvgpr_read_b32 v245, a65\n"
"v_mul_f32 v245, v245, v243\n"
"v_accvgpr_write_b32 a65, v245\n"
"v_accvgpr_read_b32 v244, a66\n"
"v_mul_f32 v244, v244, v243\n"
"v_accvgpr_write_b32 a66, v244\n"
"v_accvgpr_read_b32 v245, a67\n"
"v_mul_f32 v245, v245, v243\n"
"v_accvgpr_write_b32 a67, v245\n"
"v_accvgpr_read_b32 v244, a68\n"
"v_mul_f32 v244, v244, v243\n"
"v_accvgpr_write_b32 a68, v244\n"
"v_accvgpr_read_b32 v245, a69\n"
"v_mul_f32 v245, v245, v243\n"
"v_accvgpr_write_b32 a69, v245\n"
"v_accvgpr_read_b32 v244, a70\n"
"v_mul_f32 v244, v244, v243\n"
"v_accvgpr_write_b32 a70, v244\n"
"v_accvgpr_read_b32 v245, a71\n"
"v_mul_f32 v245, v245, v243\n"
"v_accvgpr_write_b32 a71, v245\n"
"v_accvgpr_read_b32 v244, a72\n"
"v_mul_f32 v244, v244, v243\n"
"v_accvgpr_write_b32 a72, v244\n"
"v_accvgpr_read_b32 v245, a73\n"
"v_mul_f32 v245, v245, v243\n"
"v_accvgpr_write_b32 a73, v245\n"
"v_accvgpr_read_b32 v244, a74\n"
"v_mul_f32 v244, v244, v243\n"
"v_accvgpr_write_b32 a74, v244\n"
"v_accvgpr_read_b32 v245, a75\n"
"v_mul_f32 v245, v245, v243\n"
"v_accvgpr_write_b32 a75, v245\n"
"v_accvgpr_read_b32 v244, a76\n"
"v_mul_f32 v244, v244, v243\n"
"v_accvgpr_write_b32 a76, v244\n"
"v_accvgpr_read_b32 v245, a77\n"
"v_mul_f32 v245, v245, v243\n"
"v_accvgpr_write_b32 a77, v245\n"
"v_accvgpr_read_b32 v244, a78\n"
"v_mul_f32 v244, v244, v243\n"
"v_accvgpr_write_b32 a78, v244\n"
"v_accvgpr_read_b32 v245, a79\n"
"v_mul_f32 v245, v245, v243\n"
"v_accvgpr_write_b32 a79, v245\n"
"v_accvgpr_read_b32 v244, a80\n"
"v_mul_f32 v244, v244, v243\n"
"v_accvgpr_write_b32 a80, v244\n"
"v_accvgpr_read_b32 v245, a81\n"
"v_mul_f32 v245, v245, v243\n"
"v_accvgpr_write_b32 a81, v245\n"
"v_accvgpr_read_b32 v244, a82\n"
"v_mul_f32 v244, v244, v243\n"
"v_accvgpr_write_b32 a82, v244\n"
"v_accvgpr_read_b32 v245, a83\n"
"v_mul_f32 v245, v245, v243\n"
"v_accvgpr_write_b32 a83, v245\n"
"v_accvgpr_read_b32 v244, a84\n"
"v_mul_f32 v244, v244, v243\n"
"v_accvgpr_write_b32 a84, v244\n"
"v_accvgpr_read_b32 v245, a85\n"
"v_mul_f32 v245, v245, v243\n"
"v_accvgpr_write_b32 a85, v245\n"
"v_accvgpr_read_b32 v244, a86\n"
"v_mul_f32 v244, v244, v243\n"
"v_accvgpr_write_b32 a86, v244\n"
"v_accvgpr_read_b32 v245, a87\n"
"v_mul_f32 v245, v245, v243\n"
"v_accvgpr_write_b32 a87, v245\n"
"v_accvgpr_read_b32 v244, a88\n"
"v_mul_f32 v244, v244, v243\n"
"v_accvgpr_write_b32 a88, v244\n"
"v_accvgpr_read_b32 v245, a89\n"
"v_mul_f32 v245, v245, v243\n"
"v_accvgpr_write_b32 a89, v245\n"
"v_accvgpr_read_b32 v244, a90\n"
"v_mul_f32 v244, v244, v243\n"
"v_accvgpr_write_b32 a90, v244\n"
"v_accvgpr_read_b32 v245, a91\n"
"v_mul_f32 v245, v245, v243\n"
"v_accvgpr_write_b32 a91, v245\n"
"v_accvgpr_read_b32 v244, a92\n"
"v_mul_f32 v244, v244, v243\n"
"v_accvgpr_write_b32 a92, v244\n"
"v_accvgpr_read_b32 v245, a93\n"
"v_mul_f32 v245, v245, v243\n"
"v_accvgpr_write_b32 a93, v245\n"
"v_accvgpr_read_b32 v244, a94\n"
"v_mul_f32 v244, v244, v243\n"
"v_accvgpr_write_b32 a94, v244\n"
"v_accvgpr_read_b32 v245, a95\n"
"v_mul_f32 v245, v245, v243\n"
"v_accvgpr_write_b32 a95, v245\n"
"v_accvgpr_read_b32 v244, a96\n"
"v_mul_f32 v244, v244, v243\n"
"v_accvgpr_write_b32 a96, v244\n"
"v_accvgpr_read_b32 v245, a97\n"
"v_mul_f32 v245, v245, v243\n"
"v_accvgpr_write_b32 a97, v245\n"
"v_accvgpr_read_b32 v244, a98\n"
"v_mul_f32 v244, v244, v243\n"
"v_accvgpr_write_b32 a98, v244\n"
"v_accvgpr_read_b32 v245, a99\n"
"v_mul_f32 v245, v245, v243\n"
"v_accvgpr_write_b32 a99, v245\n"
"v_accvgpr_read_b32 v244, a100\n"
"v_mul_f32 v244, v244, v243\n"
"v_accvgpr_write_b32 a100, v244\n"
"v_accvgpr_read_b32 v245, a101\n"
"v_mul_f32 v245, v245, v243\n"
"v_accvgpr_write_b32 a101, v245\n"
"v_accvgpr_read_b32 v244, a102\n"
"v_mul_f32 v244, v244, v243\n"
"v_accvgpr_write_b32 a102, v244\n"
"v_accvgpr_read_b32 v245, a103\n"
"v_mul_f32 v245, v245, v243\n"
"v_accvgpr_write_b32 a103, v245\n"
"v_accvgpr_read_b32 v244, a104\n"
"v_mul_f32 v244, v244, v243\n"
"v_accvgpr_write_b32 a104, v244\n"
"v_accvgpr_read_b32 v245, a105\n"
"v_mul_f32 v245, v245, v243\n"
"v_accvgpr_write_b32 a105, v245\n"
"v_accvgpr_read_b32 v244, a106\n"
"v_mul_f32 v244, v244, v243\n"
"v_accvgpr_write_b32 a106, v244\n"
"v_accvgpr_read_b32 v245, a107\n"
"v_mul_f32 v245, v245, v243\n"
"v_accvgpr_write_b32 a107, v245\n"
"v_accvgpr_read_b32 v244, a108\n"
"v_mul_f32 v244, v244, v243\n"
"v_accvgpr_write_b32 a108, v244\n"
"v_accvgpr_read_b32 v245, a109\n"
"v_mul_f32 v245, v245, v243\n"
"v_accvgpr_write_b32 a109, v245\n"
"v_accvgpr_read_b32 v244, a110\n"
"v_mul_f32 v244, v244, v243\n"
"v_accvgpr_write_b32 a110, v244\n"
"v_accvgpr_read_b32 v245, a111\n"
"v_mul_f32 v245, v245, v243\n"
"v_accvgpr_write_b32 a111, v245\n"
"v_accvgpr_read_b32 v244, a112\n"
"v_mul_f32 v244, v244, v243\n"
"v_accvgpr_write_b32 a112, v244\n"
"v_accvgpr_read_b32 v245, a113\n"
"v_mul_f32 v245, v245, v243\n"
"v_accvgpr_write_b32 a113, v245\n"
"v_accvgpr_read_b32 v244, a114\n"
"v_mul_f32 v244, v244, v243\n"
"v_accvgpr_write_b32 a114, v244\n"
"v_accvgpr_read_b32 v245, a115\n"
"v_mul_f32 v245, v245, v243\n"
"v_accvgpr_write_b32 a115, v245\n"
"v_accvgpr_read_b32 v244, a116\n"
"v_mul_f32 v244, v244, v243\n"
"v_accvgpr_write_b32 a116, v244\n"
"v_accvgpr_read_b32 v245, a117\n"
"v_mul_f32 v245, v245, v243\n"
"v_accvgpr_write_b32 a117, v245\n"
"v_accvgpr_read_b32 v244, a118\n"
"v_mul_f32 v244, v244, v243\n"
"v_accvgpr_write_b32 a118, v244\n"
"v_accvgpr_read_b32 v245, a119\n"
"v_mul_f32 v245, v245, v243\n"
"v_accvgpr_write_b32 a119, v245\n"
"v_accvgpr_read_b32 v244, a120\n"
"v_mul_f32 v244, v244, v243\n"
"v_accvgpr_write_b32 a120, v244\n"
"v_accvgpr_read_b32 v245, a121\n"
"v_mul_f32 v245, v245, v243\n"
"v_accvgpr_write_b32 a121, v245\n"
"v_accvgpr_read_b32 v244, a122\n"
"v_mul_f32 v244, v244, v243\n"
"v_accvgpr_write_b32 a122, v244\n"
"v_accvgpr_read_b32 v245, a123\n"
"v_mul_f32 v245, v245, v243\n"
"v_accvgpr_write_b32 a123, v245\n"
"v_accvgpr_read_b32 v244, a124\n"
"v_mul_f32 v244, v244, v243\n"
"v_accvgpr_write_b32 a124, v244\n"
"v_accvgpr_read_b32 v245, a125\n"
"v_mul_f32 v245, v245, v243\n"
"v_accvgpr_write_b32 a125, v245\n"
"v_accvgpr_read_b32 v244, a126\n"
"v_mul_f32 v244, v244, v243\n"
"v_accvgpr_write_b32 a126, v244\n"
"v_accvgpr_read_b32 v245, a127\n"
"v_mul_f32 v245, v245, v243\n"
"v_accvgpr_write_b32 a127, v245\n"
"v_sub_f32 v96, v96, v240\n"
"v_sub_f32 v97, v97, v240\n"
"v_sub_f32 v98, v98, v240\n"
"v_sub_f32 v99, v99, v240\n"
"v_sub_f32 v100, v100, v240\n"
"v_sub_f32 v101, v101, v240\n"
"v_sub_f32 v102, v102, v240\n"
"v_sub_f32 v103, v103, v240\n"
"v_sub_f32 v104, v104, v240\n"
"v_sub_f32 v105, v105, v240\n"
"v_sub_f32 v106, v106, v240\n"
"v_sub_f32 v107, v107, v240\n"
"v_sub_f32 v108, v108, v240\n"
"v_sub_f32 v109, v109, v240\n"
"v_sub_f32 v110, v110, v240\n"
"v_sub_f32 v111, v111, v240\n"
"v_sub_f32 v112, v112, v241\n"
"v_sub_f32 v113, v113, v241\n"
"v_sub_f32 v114, v114, v241\n"
"v_sub_f32 v115, v115, v241\n"
"v_sub_f32 v116, v116, v241\n"
"v_sub_f32 v117, v117, v241\n"
"v_sub_f32 v118, v118, v241\n"
"v_sub_f32 v119, v119, v241\n"
"v_sub_f32 v120, v120, v241\n"
"v_sub_f32 v121, v121, v241\n"
"v_sub_f32 v122, v122, v241\n"
"v_sub_f32 v123, v123, v241\n"
"v_sub_f32 v124, v124, v241\n"
"v_sub_f32 v125, v125, v241\n"
"v_sub_f32 v126, v126, v241\n"
"v_sub_f32 v127, v127, v241\n"
"v_sub_f32 v64, v64, v240\n"
"v_sub_f32 v65, v65, v240\n"
"v_sub_f32 v66, v66, v240\n"
"v_sub_f32 v67, v67, v240\n"
"v_sub_f32 v68, v68, v240\n"
"v_sub_f32 v69, v69, v240\n"
"v_sub_f32 v70, v70, v240\n"
"v_sub_f32 v71, v71, v240\n"
"v_sub_f32 v72, v72, v240\n"
"v_sub_f32 v73, v73, v240\n"
"v_sub_f32 v74, v74, v240\n"
"v_sub_f32 v75, v75, v240\n"
"v_sub_f32 v76, v76, v240\n"
"v_sub_f32 v77, v77, v240\n"
"v_sub_f32 v78, v78, v240\n"
"v_sub_f32 v79, v79, v240\n"
"v_sub_f32 v80, v80, v240\n"
"v_sub_f32 v81, v81, v240\n"
"v_sub_f32 v82, v82, v240\n"
"v_sub_f32 v83, v83, v240\n"
"v_sub_f32 v84, v84, v240\n"
"v_sub_f32 v85, v85, v240\n"
"v_sub_f32 v86, v86, v240\n"
"v_sub_f32 v87, v87, v240\n"
"v_sub_f32 v88, v88, v240\n"
"v_sub_f32 v89, v89, v240\n"
"v_sub_f32 v90, v90, v240\n"
"v_sub_f32 v91, v91, v240\n"
"v_sub_f32 v92, v92, v240\n"
"v_sub_f32 v93, v93, v240\n"
"v_sub_f32 v94, v94, v240\n"
"v_sub_f32 v95, v95, v240\n"
"v_exp_f32 v224, v96\n"
"s_nop 0\n"
"v_exp_f32 v225, v97\n"
"s_nop 0\n"
"v_add_f32 v232, v224, v225\n"
"v_cvt_pk_bf16_f32 v144, v224, v225\n"
"v_exp_f32 v226, v98\n"
"s_nop 0\n"
"v_add_f32 v232, v232, v226\n"
"v_exp_f32 v227, v99\n"
"s_nop 0\n"
"v_add_f32 v232, v232, v227\n"
"v_cvt_pk_bf16_f32 v145, v226, v227\n"
"v_exp_f32 v228, v100\n"
"s_nop 0\n"
"v_add_f32 v232, v232, v228\n"
"v_exp_f32 v229, v101\n"
"s_nop 0\n"
"v_add_f32 v232, v232, v229\n"
"v_cvt_pk_bf16_f32 v146, v228, v229\n"
"v_exp_f32 v230, v102\n"
"s_nop 0\n"
"v_add_f32 v232, v232, v230\n"
"v_exp_f32 v231, v103\n"
"s_nop 0\n"
"v_add_f32 v232, v232, v231\n"
"v_cvt_pk_bf16_f32 v147, v230, v231\n"
"v_exp_f32 v224, v104\n"
"s_nop 0\n"
"v_add_f32 v232, v232, v224\n"
"v_exp_f32 v225, v105\n"
"s_nop 0\n"
"v_add_f32 v232, v232, v225\n"
"v_cvt_pk_bf16_f32 v148, v224, v225\n"
"v_exp_f32 v226, v106\n"
"s_nop 0\n"
"v_add_f32 v232, v232, v226\n"
"v_exp_f32 v227, v107\n"
"s_nop 0\n"
"v_add_f32 v232, v232, v227\n"
"v_cvt_pk_bf16_f32 v149, v226, v227\n"
"v_exp_f32 v228, v108\n"
"s_nop 0\n"
"v_add_f32 v232, v232, v228\n"
"v_exp_f32 v229, v109\n"
"s_nop 0\n"
"v_add_f32 v232, v232, v229\n"
"v_cvt_pk_bf16_f32 v150, v228, v229\n"
"v_exp_f32 v230, v110\n"
"s_nop 0\n"
"v_add_f32 v232, v232, v230\n"
"v_exp_f32 v231, v111\n"
"s_nop 0\n"
"v_add_f32 v232, v232, v231\n"
"v_cvt_pk_bf16_f32 v151, v230, v231\n"
"v_exp_f32 v224, v112\n"
"s_nop 0\n"
"v_exp_f32 v225, v113\n"
"s_nop 0\n"
"v_add_f32 v233, v224, v225\n"
"v_cvt_pk_bf16_f32 v152, v224, v225\n"
"v_exp_f32 v226, v114\n"
"s_nop 0\n"
"v_add_f32 v233, v233, v226\n"
"v_exp_f32 v227, v115\n"
"s_nop 0\n"
"v_add_f32 v233, v233, v227\n"
"v_cvt_pk_bf16_f32 v153, v226, v227\n"
"v_exp_f32 v228, v116\n"
"s_nop 0\n"
"v_add_f32 v233, v233, v228\n"
"v_exp_f32 v229, v117\n"
"s_nop 0\n"
"v_add_f32 v233, v233, v229\n"
"v_cvt_pk_bf16_f32 v154, v228, v229\n"
"v_exp_f32 v230, v118\n"
"s_nop 0\n"
"v_add_f32 v233, v233, v230\n"
"v_exp_f32 v231, v119\n"
"s_nop 0\n"
"v_add_f32 v233, v233, v231\n"
"v_cvt_pk_bf16_f32 v155, v230, v231\n"
"v_exp_f32 v224, v120\n"
"s_nop 0\n"
"v_add_f32 v233, v233, v224\n"
"v_exp_f32 v225, v121\n"
"s_nop 0\n"
"v_add_f32 v233, v233, v225\n"
"v_cvt_pk_bf16_f32 v156, v224, v225\n"
"v_exp_f32 v226, v122\n"
"s_nop 0\n"
"v_add_f32 v233, v233, v226\n"
"v_exp_f32 v227, v123\n"
"s_nop 0\n"
"v_add_f32 v233, v233, v227\n"
"v_cvt_pk_bf16_f32 v157, v226, v227\n"
"v_exp_f32 v228, v124\n"
"s_nop 0\n"
"v_add_f32 v233, v233, v228\n"
"v_exp_f32 v229, v125\n"
"s_nop 0\n"
"v_add_f32 v233, v233, v229\n"
"v_cvt_pk_bf16_f32 v158, v228, v229\n"
"v_exp_f32 v230, v126\n"
"s_nop 0\n"
"v_add_f32 v233, v233, v230\n"
"v_exp_f32 v231, v127\n"
"s_nop 0\n"
"v_add_f32 v233, v233, v231\n"
"v_cvt_pk_bf16_f32 v159, v230, v231\n"
"v_add_f32 v236, v232, v233\n"
"s_nop 1\n"
"s_branch Lfr35_%=\n"
"Lfb42_%=:\n"
"s_nop 7\n"
"s_nop 7\n"
"v_max3_f32 v240, v64, v65, v66\n"
"v_max3_f32 v240, v240, v67, v68\n"
"v_max3_f32 v240, v240, v69, v70\n"
"v_max3_f32 v240, v240, v71, v72\n"
"v_max3_f32 v240, v240, v73, v74\n"
"v_max3_f32 v240, v240, v75, v76\n"
"v_max3_f32 v240, v240, v77, v78\n"
"v_max3_f32 v240, v240, v79, v80\n"
"v_max3_f32 v240, v240, v81, v82\n"
"v_max3_f32 v240, v240, v83, v84\n"
"v_max3_f32 v240, v240, v85, v86\n"
"v_max3_f32 v240, v240, v87, v88\n"
"v_max3_f32 v240, v240, v89, v90\n"
"v_max3_f32 v240, v240, v91, v92\n"
"v_max3_f32 v240, v240, v93, v94\n"
"v_max_f32 v240, v240, v95\n"
"v_mov_b32 v244, v240\n"
"s_nop 1\n"
"v_permlane32_swap_b32 v240, v244\n"
"v_max_f32 v240, v240, v244\n"
"v_max_f32 v240, 0, v240\n"
"v_sub_f32 v244, 0, v240\n"
"v_exp_f32 v242, v244\n"
"s_nop 0\n"
"v_mul_f32 v234, v234, v242\n"
"v_sub_f32 v160, v160, v240\n"
"v_sub_f32 v161, v161, v240\n"
"v_sub_f32 v162, v162, v240\n"
"v_sub_f32 v163, v163, v240\n"
"v_sub_f32 v164, v164, v240\n"
"v_sub_f32 v165, v165, v240\n"
"v_sub_f32 v166, v166, v240\n"
"v_sub_f32 v167, v167, v240\n"
"v_sub_f32 v168, v168, v240\n"
"v_sub_f32 v169, v169, v240\n"
"v_sub_f32 v170, v170, v240\n"
"v_sub_f32 v171, v171, v240\n"
"v_sub_f32 v172, v172, v240\n"
"v_sub_f32 v173, v173, v240\n"
"v_sub_f32 v174, v174, v240\n"
"v_sub_f32 v175, v175, v240\n"
"v_accvgpr_read_b32 v244, a0\n"
"v_mul_f32 v244, v244, v242\n"
"v_accvgpr_write_b32 a0, v244\n"
"v_accvgpr_read_b32 v245, a1\n"
"v_mul_f32 v245, v245, v242\n"
"v_accvgpr_write_b32 a1, v245\n"
"v_accvgpr_read_b32 v244, a2\n"
"v_mul_f32 v244, v244, v242\n"
"v_accvgpr_write_b32 a2, v244\n"
"v_accvgpr_read_b32 v245, a3\n"
"v_mul_f32 v245, v245, v242\n"
"v_accvgpr_write_b32 a3, v245\n"
"v_accvgpr_read_b32 v244, a4\n"
"v_mul_f32 v244, v244, v242\n"
"v_accvgpr_write_b32 a4, v244\n"
"v_accvgpr_read_b32 v245, a5\n"
"v_mul_f32 v245, v245, v242\n"
"v_accvgpr_write_b32 a5, v245\n"
"v_accvgpr_read_b32 v244, a6\n"
"v_mul_f32 v244, v244, v242\n"
"v_accvgpr_write_b32 a6, v244\n"
"v_accvgpr_read_b32 v245, a7\n"
"v_mul_f32 v245, v245, v242\n"
"v_accvgpr_write_b32 a7, v245\n"
"v_accvgpr_read_b32 v244, a8\n"
"v_mul_f32 v244, v244, v242\n"
"v_accvgpr_write_b32 a8, v244\n"
"v_accvgpr_read_b32 v245, a9\n"
"v_mul_f32 v245, v245, v242\n"
"v_accvgpr_write_b32 a9, v245\n"
"v_accvgpr_read_b32 v244, a10\n"
"v_mul_f32 v244, v244, v242\n"
"v_accvgpr_write_b32 a10, v244\n"
"v_accvgpr_read_b32 v245, a11\n"
"v_mul_f32 v245, v245, v242\n"
"v_accvgpr_write_b32 a11, v245\n"
"v_accvgpr_read_b32 v244, a12\n"
"v_mul_f32 v244, v244, v242\n"
"v_accvgpr_write_b32 a12, v244\n"
"v_accvgpr_read_b32 v245, a13\n"
"v_mul_f32 v245, v245, v242\n"
"v_accvgpr_write_b32 a13, v245\n"
"v_accvgpr_read_b32 v244, a14\n"
"v_mul_f32 v244, v244, v242\n"
"v_accvgpr_write_b32 a14, v244\n"
"v_accvgpr_read_b32 v245, a15\n"
"v_mul_f32 v245, v245, v242\n"
"v_accvgpr_write_b32 a15, v245\n"
"v_accvgpr_read_b32 v244, a16\n"
"v_mul_f32 v244, v244, v242\n"
"v_accvgpr_write_b32 a16, v244\n"
"v_accvgpr_read_b32 v245, a17\n"
"v_mul_f32 v245, v245, v242\n"
"v_accvgpr_write_b32 a17, v245\n"
"v_accvgpr_read_b32 v244, a18\n"
"v_mul_f32 v244, v244, v242\n"
"v_accvgpr_write_b32 a18, v244\n"
"v_accvgpr_read_b32 v245, a19\n"
"v_mul_f32 v245, v245, v242\n"
"v_accvgpr_write_b32 a19, v245\n"
"v_accvgpr_read_b32 v244, a20\n"
"v_mul_f32 v244, v244, v242\n"
"v_accvgpr_write_b32 a20, v244\n"
"v_accvgpr_read_b32 v245, a21\n"
"v_mul_f32 v245, v245, v242\n"
"v_accvgpr_write_b32 a21, v245\n"
"v_accvgpr_read_b32 v244, a22\n"
"v_mul_f32 v244, v244, v242\n"
"v_accvgpr_write_b32 a22, v244\n"
"v_accvgpr_read_b32 v245, a23\n"
"v_mul_f32 v245, v245, v242\n"
"v_accvgpr_write_b32 a23, v245\n"
"v_accvgpr_read_b32 v244, a24\n"
"v_mul_f32 v244, v244, v242\n"
"v_accvgpr_write_b32 a24, v244\n"
"v_accvgpr_read_b32 v245, a25\n"
"v_mul_f32 v245, v245, v242\n"
"v_accvgpr_write_b32 a25, v245\n"
"v_accvgpr_read_b32 v244, a26\n"
"v_mul_f32 v244, v244, v242\n"
"v_accvgpr_write_b32 a26, v244\n"
"v_accvgpr_read_b32 v245, a27\n"
"v_mul_f32 v245, v245, v242\n"
"v_accvgpr_write_b32 a27, v245\n"
"v_accvgpr_read_b32 v244, a28\n"
"v_mul_f32 v244, v244, v242\n"
"v_accvgpr_write_b32 a28, v244\n"
"v_accvgpr_read_b32 v245, a29\n"
"v_mul_f32 v245, v245, v242\n"
"v_accvgpr_write_b32 a29, v245\n"
"v_accvgpr_read_b32 v244, a30\n"
"v_mul_f32 v244, v244, v242\n"
"v_accvgpr_write_b32 a30, v244\n"
"v_accvgpr_read_b32 v245, a31\n"
"v_mul_f32 v245, v245, v242\n"
"v_accvgpr_write_b32 a31, v245\n"
"v_accvgpr_read_b32 v244, a32\n"
"v_mul_f32 v244, v244, v242\n"
"v_accvgpr_write_b32 a32, v244\n"
"v_accvgpr_read_b32 v245, a33\n"
"v_mul_f32 v245, v245, v242\n"
"v_accvgpr_write_b32 a33, v245\n"
"v_accvgpr_read_b32 v244, a34\n"
"v_mul_f32 v244, v244, v242\n"
"v_accvgpr_write_b32 a34, v244\n"
"v_accvgpr_read_b32 v245, a35\n"
"v_mul_f32 v245, v245, v242\n"
"v_accvgpr_write_b32 a35, v245\n"
"v_accvgpr_read_b32 v244, a36\n"
"v_mul_f32 v244, v244, v242\n"
"v_accvgpr_write_b32 a36, v244\n"
"v_accvgpr_read_b32 v245, a37\n"
"v_mul_f32 v245, v245, v242\n"
"v_accvgpr_write_b32 a37, v245\n"
"v_accvgpr_read_b32 v244, a38\n"
"v_mul_f32 v244, v244, v242\n"
"v_accvgpr_write_b32 a38, v244\n"
"v_accvgpr_read_b32 v245, a39\n"
"v_mul_f32 v245, v245, v242\n"
"v_accvgpr_write_b32 a39, v245\n"
"v_accvgpr_read_b32 v244, a40\n"
"v_mul_f32 v244, v244, v242\n"
"v_accvgpr_write_b32 a40, v244\n"
"v_accvgpr_read_b32 v245, a41\n"
"v_mul_f32 v245, v245, v242\n"
"v_accvgpr_write_b32 a41, v245\n"
"v_accvgpr_read_b32 v244, a42\n"
"v_mul_f32 v244, v244, v242\n"
"v_accvgpr_write_b32 a42, v244\n"
"v_accvgpr_read_b32 v245, a43\n"
"v_mul_f32 v245, v245, v242\n"
"v_accvgpr_write_b32 a43, v245\n"
"v_accvgpr_read_b32 v244, a44\n"
"v_mul_f32 v244, v244, v242\n"
"v_accvgpr_write_b32 a44, v244\n"
"v_accvgpr_read_b32 v245, a45\n"
"v_mul_f32 v245, v245, v242\n"
"v_accvgpr_write_b32 a45, v245\n"
"v_accvgpr_read_b32 v244, a46\n"
"v_mul_f32 v244, v244, v242\n"
"v_accvgpr_write_b32 a46, v244\n"
"v_accvgpr_read_b32 v245, a47\n"
"v_mul_f32 v245, v245, v242\n"
"v_accvgpr_write_b32 a47, v245\n"
"v_accvgpr_read_b32 v244, a48\n"
"v_mul_f32 v244, v244, v242\n"
"v_accvgpr_write_b32 a48, v244\n"
"v_accvgpr_read_b32 v245, a49\n"
"v_mul_f32 v245, v245, v242\n"
"v_accvgpr_write_b32 a49, v245\n"
"v_accvgpr_read_b32 v244, a50\n"
"v_mul_f32 v244, v244, v242\n"
"v_accvgpr_write_b32 a50, v244\n"
"v_accvgpr_read_b32 v245, a51\n"
"v_mul_f32 v245, v245, v242\n"
"v_accvgpr_write_b32 a51, v245\n"
"v_accvgpr_read_b32 v244, a52\n"
"v_mul_f32 v244, v244, v242\n"
"v_accvgpr_write_b32 a52, v244\n"
"v_accvgpr_read_b32 v245, a53\n"
"v_mul_f32 v245, v245, v242\n"
"v_accvgpr_write_b32 a53, v245\n"
"v_accvgpr_read_b32 v244, a54\n"
"v_mul_f32 v244, v244, v242\n"
"v_accvgpr_write_b32 a54, v244\n"
"v_accvgpr_read_b32 v245, a55\n"
"v_mul_f32 v245, v245, v242\n"
"v_accvgpr_write_b32 a55, v245\n"
"v_accvgpr_read_b32 v244, a56\n"
"v_mul_f32 v244, v244, v242\n"
"v_accvgpr_write_b32 a56, v244\n"
"v_accvgpr_read_b32 v245, a57\n"
"v_mul_f32 v245, v245, v242\n"
"v_accvgpr_write_b32 a57, v245\n"
"v_accvgpr_read_b32 v244, a58\n"
"v_mul_f32 v244, v244, v242\n"
"v_accvgpr_write_b32 a58, v244\n"
"v_accvgpr_read_b32 v245, a59\n"
"v_mul_f32 v245, v245, v242\n"
"v_accvgpr_write_b32 a59, v245\n"
"v_accvgpr_read_b32 v244, a60\n"
"v_mul_f32 v244, v244, v242\n"
"v_accvgpr_write_b32 a60, v244\n"
"v_accvgpr_read_b32 v245, a61\n"
"v_mul_f32 v245, v245, v242\n"
"v_accvgpr_write_b32 a61, v245\n"
"v_accvgpr_read_b32 v244, a62\n"
"v_mul_f32 v244, v244, v242\n"
"v_accvgpr_write_b32 a62, v244\n"
"v_accvgpr_read_b32 v245, a63\n"
"v_mul_f32 v245, v245, v242\n"
"v_accvgpr_write_b32 a63, v245\n"
"v_sub_f32 v64, v64, v240\n"
"v_sub_f32 v65, v65, v240\n"
"v_sub_f32 v66, v66, v240\n"
"v_sub_f32 v67, v67, v240\n"
"v_sub_f32 v68, v68, v240\n"
"v_sub_f32 v69, v69, v240\n"
"v_sub_f32 v70, v70, v240\n"
"v_sub_f32 v71, v71, v240\n"
"v_sub_f32 v72, v72, v240\n"
"v_sub_f32 v73, v73, v240\n"
"v_sub_f32 v74, v74, v240\n"
"v_sub_f32 v75, v75, v240\n"
"v_sub_f32 v76, v76, v240\n"
"v_sub_f32 v77, v77, v240\n"
"v_sub_f32 v78, v78, v240\n"
"v_sub_f32 v79, v79, v240\n"
"v_sub_f32 v80, v80, v240\n"
"v_sub_f32 v81, v81, v240\n"
"v_sub_f32 v82, v82, v240\n"
"v_sub_f32 v83, v83, v240\n"
"v_sub_f32 v84, v84, v240\n"
"v_sub_f32 v85, v85, v240\n"
"v_sub_f32 v86, v86, v240\n"
"v_sub_f32 v87, v87, v240\n"
"v_sub_f32 v88, v88, v240\n"
"v_sub_f32 v89, v89, v240\n"
"v_sub_f32 v90, v90, v240\n"
"v_sub_f32 v91, v91, v240\n"
"v_sub_f32 v92, v92, v240\n"
"v_sub_f32 v93, v93, v240\n"
"v_sub_f32 v94, v94, v240\n"
"v_sub_f32 v95, v95, v240\n"
"v_sub_f32 v96, v96, v240\n"
"v_sub_f32 v97, v97, v240\n"
"v_sub_f32 v98, v98, v240\n"
"v_sub_f32 v99, v99, v240\n"
"v_sub_f32 v100, v100, v240\n"
"v_sub_f32 v101, v101, v240\n"
"v_sub_f32 v102, v102, v240\n"
"v_sub_f32 v103, v103, v240\n"
"v_sub_f32 v104, v104, v240\n"
"v_sub_f32 v105, v105, v240\n"
"v_sub_f32 v106, v106, v240\n"
"v_sub_f32 v107, v107, v240\n"
"v_sub_f32 v108, v108, v240\n"
"v_sub_f32 v109, v109, v240\n"
"v_sub_f32 v110, v110, v240\n"
"v_sub_f32 v111, v111, v240\n"
"v_sub_f32 v112, v112, v240\n"
"v_sub_f32 v113, v113, v240\n"
"v_sub_f32 v114, v114, v240\n"
"v_sub_f32 v115, v115, v240\n"
"v_sub_f32 v116, v116, v240\n"
"v_sub_f32 v117, v117, v240\n"
"v_sub_f32 v118, v118, v240\n"
"v_sub_f32 v119, v119, v240\n"
"v_sub_f32 v120, v120, v240\n"
"v_sub_f32 v121, v121, v240\n"
"v_sub_f32 v122, v122, v240\n"
"v_sub_f32 v123, v123, v240\n"
"v_sub_f32 v124, v124, v240\n"
"v_sub_f32 v125, v125, v240\n"
"v_sub_f32 v126, v126, v240\n"
"v_sub_f32 v127, v127, v240\n"
"v_exp_f32 v224, v64\n"
"s_nop 0\n"
"v_exp_f32 v225, v65\n"
"s_nop 0\n"
"v_add_f32 v232, v224, v225\n"
"v_cvt_pk_bf16_f32 v128, v224, v225\n"
"v_exp_f32 v226, v66\n"
"s_nop 0\n"
"v_add_f32 v232, v232, v226\n"
"v_exp_f32 v227, v67\n"
"s_nop 0\n"
"v_add_f32 v232, v232, v227\n"
"v_cvt_pk_bf16_f32 v129, v226, v227\n"
"v_exp_f32 v228, v68\n"
"s_nop 0\n"
"v_add_f32 v232, v232, v228\n"
"v_exp_f32 v229, v69\n"
"s_nop 0\n"
"v_add_f32 v232, v232, v229\n"
"v_cvt_pk_bf16_f32 v130, v228, v229\n"
"v_exp_f32 v230, v70\n"
"s_nop 0\n"
"v_add_f32 v232, v232, v230\n"
"v_exp_f32 v231, v71\n"
"s_nop 0\n"
"v_add_f32 v232, v232, v231\n"
"v_cvt_pk_bf16_f32 v131, v230, v231\n"
"v_exp_f32 v224, v72\n"
"s_nop 0\n"
"v_add_f32 v232, v232, v224\n"
"v_exp_f32 v225, v73\n"
"s_nop 0\n"
"v_add_f32 v232, v232, v225\n"
"v_cvt_pk_bf16_f32 v132, v224, v225\n"
"v_exp_f32 v226, v74\n"
"s_nop 0\n"
"v_add_f32 v232, v232, v226\n"
"v_exp_f32 v227, v75\n"
"s_nop 0\n"
"v_add_f32 v232, v232, v227\n"
"v_cvt_pk_bf16_f32 v133, v226, v227\n"
"v_exp_f32 v228, v76\n"
"s_nop 0\n"
"v_add_f32 v232, v232, v228\n"
"v_exp_f32 v229, v77\n"
"s_nop 0\n"
"v_add_f32 v232, v232, v229\n"
"v_cvt_pk_bf16_f32 v134, v228, v229\n"
"v_exp_f32 v230, v78\n"
"s_nop 0\n"
"v_add_f32 v232, v232, v230\n"
"v_exp_f32 v231, v79\n"
"s_nop 0\n"
"v_add_f32 v232, v232, v231\n"
"v_cvt_pk_bf16_f32 v135, v230, v231\n"
"v_exp_f32 v224, v80\n"
"s_nop 0\n"
"v_exp_f32 v225, v81\n"
"s_nop 0\n"
"v_add_f32 v233, v224, v225\n"
"v_cvt_pk_bf16_f32 v136, v224, v225\n"
"v_exp_f32 v226, v82\n"
"s_nop 0\n"
"v_add_f32 v233, v233, v226\n"
"v_exp_f32 v227, v83\n"
"s_nop 0\n"
"v_add_f32 v233, v233, v227\n"
"v_cvt_pk_bf16_f32 v137, v226, v227\n"
"v_exp_f32 v228, v84\n"
"s_nop 0\n"
"v_add_f32 v233, v233, v228\n"
"v_exp_f32 v229, v85\n"
"s_nop 0\n"
"v_add_f32 v233, v233, v229\n"
"v_cvt_pk_bf16_f32 v138, v228, v229\n"
"v_exp_f32 v230, v86\n"
"s_nop 0\n"
"v_add_f32 v233, v233, v230\n"
"v_exp_f32 v231, v87\n"
"s_nop 0\n"
"v_add_f32 v233, v233, v231\n"
"v_cvt_pk_bf16_f32 v139, v230, v231\n"
"v_exp_f32 v224, v88\n"
"s_nop 0\n"
"v_add_f32 v233, v233, v224\n"
"v_exp_f32 v225, v89\n"
"s_nop 0\n"
"v_add_f32 v233, v233, v225\n"
"v_cvt_pk_bf16_f32 v140, v224, v225\n"
"v_exp_f32 v226, v90\n"
"s_nop 0\n"
"v_add_f32 v233, v233, v226\n"
"v_exp_f32 v227, v91\n"
"s_nop 0\n"
"v_add_f32 v233, v233, v227\n"
"v_cvt_pk_bf16_f32 v141, v226, v227\n"
"v_exp_f32 v228, v92\n"
"s_nop 0\n"
"v_add_f32 v233, v233, v228\n"
"v_exp_f32 v229, v93\n"
"s_nop 0\n"
"v_add_f32 v233, v233, v229\n"
"v_cvt_pk_bf16_f32 v142, v228, v229\n"
"v_exp_f32 v230, v94\n"
"s_nop 0\n"
"v_add_f32 v233, v233, v230\n"
"v_exp_f32 v231, v95\n"
"s_nop 0\n"
"v_add_f32 v233, v233, v231\n"
"v_cvt_pk_bf16_f32 v143, v230, v231\n"
"v_add_f32 v236, v232, v233\n"
"s_nop 1\n"
"s_branch Lfr43_%=\n"
"Lfb50_%=:\n"
"s_nop 7\n"
"s_nop 7\n"
"v_max3_f32 v240, v96, v97, v98\n"
"v_max3_f32 v240, v240, v99, v100\n"
"v_max3_f32 v240, v240, v101, v102\n"
"v_max3_f32 v240, v240, v103, v104\n"
"v_max3_f32 v240, v240, v105, v106\n"
"v_max3_f32 v240, v240, v107, v108\n"
"v_max3_f32 v240, v240, v109, v110\n"
"v_max3_f32 v240, v240, v111, v112\n"
"v_max3_f32 v240, v240, v113, v114\n"
"v_max3_f32 v240, v240, v115, v116\n"
"v_max3_f32 v240, v240, v117, v118\n"
"v_max3_f32 v240, v240, v119, v120\n"
"v_max3_f32 v240, v240, v121, v122\n"
"v_max3_f32 v240, v240, v123, v124\n"
"v_max3_f32 v240, v240, v125, v126\n"
"v_max_f32 v240, v240, v127\n"
"v_mov_b32 v244, v240\n"
"s_nop 1\n"
"v_permlane32_swap_b32 v240, v244\n"
"v_max_f32 v240, v240, v244\n"
"v_max_f32 v240, 0, v240\n"
"v_sub_f32 v244, 0, v240\n"
"v_exp_f32 v242, v244\n"
"s_nop 0\n"
"v_mul_f32 v234, v234, v242\n"
"v_sub_f32 v160, v160, v240\n"
"v_sub_f32 v161, v161, v240\n"
"v_sub_f32 v162, v162, v240\n"
"v_sub_f32 v163, v163, v240\n"
"v_sub_f32 v164, v164, v240\n"
"v_sub_f32 v165, v165, v240\n"
"v_sub_f32 v166, v166, v240\n"
"v_sub_f32 v167, v167, v240\n"
"v_sub_f32 v168, v168, v240\n"
"v_sub_f32 v169, v169, v240\n"
"v_sub_f32 v170, v170, v240\n"
"v_sub_f32 v171, v171, v240\n"
"v_sub_f32 v172, v172, v240\n"
"v_sub_f32 v173, v173, v240\n"
"v_sub_f32 v174, v174, v240\n"
"v_sub_f32 v175, v175, v240\n"
"v_accvgpr_read_b32 v244, a0\n"
"v_mul_f32 v244, v244, v242\n"
"v_accvgpr_write_b32 a0, v244\n"
"v_accvgpr_read_b32 v245, a1\n"
"v_mul_f32 v245, v245, v242\n"
"v_accvgpr_write_b32 a1, v245\n"
"v_accvgpr_read_b32 v244, a2\n"
"v_mul_f32 v244, v244, v242\n"
"v_accvgpr_write_b32 a2, v244\n"
"v_accvgpr_read_b32 v245, a3\n"
"v_mul_f32 v245, v245, v242\n"
"v_accvgpr_write_b32 a3, v245\n"
"v_accvgpr_read_b32 v244, a4\n"
"v_mul_f32 v244, v244, v242\n"
"v_accvgpr_write_b32 a4, v244\n"
"v_accvgpr_read_b32 v245, a5\n"
"v_mul_f32 v245, v245, v242\n"
"v_accvgpr_write_b32 a5, v245\n"
"v_accvgpr_read_b32 v244, a6\n"
"v_mul_f32 v244, v244, v242\n"
"v_accvgpr_write_b32 a6, v244\n"
"v_accvgpr_read_b32 v245, a7\n"
"v_mul_f32 v245, v245, v242\n"
"v_accvgpr_write_b32 a7, v245\n"
"v_accvgpr_read_b32 v244, a8\n"
"v_mul_f32 v244, v244, v242\n"
"v_accvgpr_write_b32 a8, v244\n"
"v_accvgpr_read_b32 v245, a9\n"
"v_mul_f32 v245, v245, v242\n"
"v_accvgpr_write_b32 a9, v245\n"
"v_accvgpr_read_b32 v244, a10\n"
"v_mul_f32 v244, v244, v242\n"
"v_accvgpr_write_b32 a10, v244\n"
"v_accvgpr_read_b32 v245, a11\n"
"v_mul_f32 v245, v245, v242\n"
"v_accvgpr_write_b32 a11, v245\n"
"v_accvgpr_read_b32 v244, a12\n"
"v_mul_f32 v244, v244, v242\n"
"v_accvgpr_write_b32 a12, v244\n"
"v_accvgpr_read_b32 v245, a13\n"
"v_mul_f32 v245, v245, v242\n"
"v_accvgpr_write_b32 a13, v245\n"
"v_accvgpr_read_b32 v244, a14\n"
"v_mul_f32 v244, v244, v242\n"
"v_accvgpr_write_b32 a14, v244\n"
"v_accvgpr_read_b32 v245, a15\n"
"v_mul_f32 v245, v245, v242\n"
"v_accvgpr_write_b32 a15, v245\n"
"v_accvgpr_read_b32 v244, a16\n"
"v_mul_f32 v244, v244, v242\n"
"v_accvgpr_write_b32 a16, v244\n"
"v_accvgpr_read_b32 v245, a17\n"
"v_mul_f32 v245, v245, v242\n"
"v_accvgpr_write_b32 a17, v245\n"
"v_accvgpr_read_b32 v244, a18\n"
"v_mul_f32 v244, v244, v242\n"
"v_accvgpr_write_b32 a18, v244\n"
"v_accvgpr_read_b32 v245, a19\n"
"v_mul_f32 v245, v245, v242\n"
"v_accvgpr_write_b32 a19, v245\n"
"v_accvgpr_read_b32 v244, a20\n"
"v_mul_f32 v244, v244, v242\n"
"v_accvgpr_write_b32 a20, v244\n"
"v_accvgpr_read_b32 v245, a21\n"
"v_mul_f32 v245, v245, v242\n"
"v_accvgpr_write_b32 a21, v245\n"
"v_accvgpr_read_b32 v244, a22\n"
"v_mul_f32 v244, v244, v242\n"
"v_accvgpr_write_b32 a22, v244\n"
"v_accvgpr_read_b32 v245, a23\n"
"v_mul_f32 v245, v245, v242\n"
"v_accvgpr_write_b32 a23, v245\n"
"v_accvgpr_read_b32 v244, a24\n"
"v_mul_f32 v244, v244, v242\n"
"v_accvgpr_write_b32 a24, v244\n"
"v_accvgpr_read_b32 v245, a25\n"
"v_mul_f32 v245, v245, v242\n"
"v_accvgpr_write_b32 a25, v245\n"
"v_accvgpr_read_b32 v244, a26\n"
"v_mul_f32 v244, v244, v242\n"
"v_accvgpr_write_b32 a26, v244\n"
"v_accvgpr_read_b32 v245, a27\n"
"v_mul_f32 v245, v245, v242\n"
"v_accvgpr_write_b32 a27, v245\n"
"v_accvgpr_read_b32 v244, a28\n"
"v_mul_f32 v244, v244, v242\n"
"v_accvgpr_write_b32 a28, v244\n"
"v_accvgpr_read_b32 v245, a29\n"
"v_mul_f32 v245, v245, v242\n"
"v_accvgpr_write_b32 a29, v245\n"
"v_accvgpr_read_b32 v244, a30\n"
"v_mul_f32 v244, v244, v242\n"
"v_accvgpr_write_b32 a30, v244\n"
"v_accvgpr_read_b32 v245, a31\n"
"v_mul_f32 v245, v245, v242\n"
"v_accvgpr_write_b32 a31, v245\n"
"v_accvgpr_read_b32 v244, a32\n"
"v_mul_f32 v244, v244, v242\n"
"v_accvgpr_write_b32 a32, v244\n"
"v_accvgpr_read_b32 v245, a33\n"
"v_mul_f32 v245, v245, v242\n"
"v_accvgpr_write_b32 a33, v245\n"
"v_accvgpr_read_b32 v244, a34\n"
"v_mul_f32 v244, v244, v242\n"
"v_accvgpr_write_b32 a34, v244\n"
"v_accvgpr_read_b32 v245, a35\n"
"v_mul_f32 v245, v245, v242\n"
"v_accvgpr_write_b32 a35, v245\n"
"v_accvgpr_read_b32 v244, a36\n"
"v_mul_f32 v244, v244, v242\n"
"v_accvgpr_write_b32 a36, v244\n"
"v_accvgpr_read_b32 v245, a37\n"
"v_mul_f32 v245, v245, v242\n"
"v_accvgpr_write_b32 a37, v245\n"
"v_accvgpr_read_b32 v244, a38\n"
"v_mul_f32 v244, v244, v242\n"
"v_accvgpr_write_b32 a38, v244\n"
"v_accvgpr_read_b32 v245, a39\n"
"v_mul_f32 v245, v245, v242\n"
"v_accvgpr_write_b32 a39, v245\n"
"v_accvgpr_read_b32 v244, a40\n"
"v_mul_f32 v244, v244, v242\n"
"v_accvgpr_write_b32 a40, v244\n"
"v_accvgpr_read_b32 v245, a41\n"
"v_mul_f32 v245, v245, v242\n"
"v_accvgpr_write_b32 a41, v245\n"
"v_accvgpr_read_b32 v244, a42\n"
"v_mul_f32 v244, v244, v242\n"
"v_accvgpr_write_b32 a42, v244\n"
"v_accvgpr_read_b32 v245, a43\n"
"v_mul_f32 v245, v245, v242\n"
"v_accvgpr_write_b32 a43, v245\n"
"v_accvgpr_read_b32 v244, a44\n"
"v_mul_f32 v244, v244, v242\n"
"v_accvgpr_write_b32 a44, v244\n"
"v_accvgpr_read_b32 v245, a45\n"
"v_mul_f32 v245, v245, v242\n"
"v_accvgpr_write_b32 a45, v245\n"
"v_accvgpr_read_b32 v244, a46\n"
"v_mul_f32 v244, v244, v242\n"
"v_accvgpr_write_b32 a46, v244\n"
"v_accvgpr_read_b32 v245, a47\n"
"v_mul_f32 v245, v245, v242\n"
"v_accvgpr_write_b32 a47, v245\n"
"v_accvgpr_read_b32 v244, a48\n"
"v_mul_f32 v244, v244, v242\n"
"v_accvgpr_write_b32 a48, v244\n"
"v_accvgpr_read_b32 v245, a49\n"
"v_mul_f32 v245, v245, v242\n"
"v_accvgpr_write_b32 a49, v245\n"
"v_accvgpr_read_b32 v244, a50\n"
"v_mul_f32 v244, v244, v242\n"
"v_accvgpr_write_b32 a50, v244\n"
"v_accvgpr_read_b32 v245, a51\n"
"v_mul_f32 v245, v245, v242\n"
"v_accvgpr_write_b32 a51, v245\n"
"v_accvgpr_read_b32 v244, a52\n"
"v_mul_f32 v244, v244, v242\n"
"v_accvgpr_write_b32 a52, v244\n"
"v_accvgpr_read_b32 v245, a53\n"
"v_mul_f32 v245, v245, v242\n"
"v_accvgpr_write_b32 a53, v245\n"
"v_accvgpr_read_b32 v244, a54\n"
"v_mul_f32 v244, v244, v242\n"
"v_accvgpr_write_b32 a54, v244\n"
"v_accvgpr_read_b32 v245, a55\n"
"v_mul_f32 v245, v245, v242\n"
"v_accvgpr_write_b32 a55, v245\n"
"v_accvgpr_read_b32 v244, a56\n"
"v_mul_f32 v244, v244, v242\n"
"v_accvgpr_write_b32 a56, v244\n"
"v_accvgpr_read_b32 v245, a57\n"
"v_mul_f32 v245, v245, v242\n"
"v_accvgpr_write_b32 a57, v245\n"
"v_accvgpr_read_b32 v244, a58\n"
"v_mul_f32 v244, v244, v242\n"
"v_accvgpr_write_b32 a58, v244\n"
"v_accvgpr_read_b32 v245, a59\n"
"v_mul_f32 v245, v245, v242\n"
"v_accvgpr_write_b32 a59, v245\n"
"v_accvgpr_read_b32 v244, a60\n"
"v_mul_f32 v244, v244, v242\n"
"v_accvgpr_write_b32 a60, v244\n"
"v_accvgpr_read_b32 v245, a61\n"
"v_mul_f32 v245, v245, v242\n"
"v_accvgpr_write_b32 a61, v245\n"
"v_accvgpr_read_b32 v244, a62\n"
"v_mul_f32 v244, v244, v242\n"
"v_accvgpr_write_b32 a62, v244\n"
"v_accvgpr_read_b32 v245, a63\n"
"v_mul_f32 v245, v245, v242\n"
"v_accvgpr_write_b32 a63, v245\n"
"v_sub_f32 v96, v96, v240\n"
"v_sub_f32 v97, v97, v240\n"
"v_sub_f32 v98, v98, v240\n"
"v_sub_f32 v99, v99, v240\n"
"v_sub_f32 v100, v100, v240\n"
"v_sub_f32 v101, v101, v240\n"
"v_sub_f32 v102, v102, v240\n"
"v_sub_f32 v103, v103, v240\n"
"v_sub_f32 v104, v104, v240\n"
"v_sub_f32 v105, v105, v240\n"
"v_sub_f32 v106, v106, v240\n"
"v_sub_f32 v107, v107, v240\n"
"v_sub_f32 v108, v108, v240\n"
"v_sub_f32 v109, v109, v240\n"
"v_sub_f32 v110, v110, v240\n"
"v_sub_f32 v111, v111, v240\n"
"v_sub_f32 v112, v112, v240\n"
"v_sub_f32 v113, v113, v240\n"
"v_sub_f32 v114, v114, v240\n"
"v_sub_f32 v115, v115, v240\n"
"v_sub_f32 v116, v116, v240\n"
"v_sub_f32 v117, v117, v240\n"
"v_sub_f32 v118, v118, v240\n"
"v_sub_f32 v119, v119, v240\n"
"v_sub_f32 v120, v120, v240\n"
"v_sub_f32 v121, v121, v240\n"
"v_sub_f32 v122, v122, v240\n"
"v_sub_f32 v123, v123, v240\n"
"v_sub_f32 v124, v124, v240\n"
"v_sub_f32 v125, v125, v240\n"
"v_sub_f32 v126, v126, v240\n"
"v_sub_f32 v127, v127, v240\n"
"v_sub_f32 v64, v64, v240\n"
"v_sub_f32 v65, v65, v240\n"
"v_sub_f32 v66, v66, v240\n"
"v_sub_f32 v67, v67, v240\n"
"v_sub_f32 v68, v68, v240\n"
"v_sub_f32 v69, v69, v240\n"
"v_sub_f32 v70, v70, v240\n"
"v_sub_f32 v71, v71, v240\n"
"v_sub_f32 v72, v72, v240\n"
"v_sub_f32 v73, v73, v240\n"
"v_sub_f32 v74, v74, v240\n"
"v_sub_f32 v75, v75, v240\n"
"v_sub_f32 v76, v76, v240\n"
"v_sub_f32 v77, v77, v240\n"
"v_sub_f32 v78, v78, v240\n"
"v_sub_f32 v79, v79, v240\n"
"v_sub_f32 v80, v80, v240\n"
"v_sub_f32 v81, v81, v240\n"
"v_sub_f32 v82, v82, v240\n"
"v_sub_f32 v83, v83, v240\n"
"v_sub_f32 v84, v84, v240\n"
"v_sub_f32 v85, v85, v240\n"
"v_sub_f32 v86, v86, v240\n"
"v_sub_f32 v87, v87, v240\n"
"v_sub_f32 v88, v88, v240\n"
"v_sub_f32 v89, v89, v240\n"
"v_sub_f32 v90, v90, v240\n"
"v_sub_f32 v91, v91, v240\n"
"v_sub_f32 v92, v92, v240\n"
"v_sub_f32 v93, v93, v240\n"
"v_sub_f32 v94, v94, v240\n"
"v_sub_f32 v95, v95, v240\n"
"v_exp_f32 v224, v96\n"
"s_nop 0\n"
"v_exp_f32 v225, v97\n"
"s_nop 0\n"
"v_add_f32 v232, v224, v225\n"
"v_cvt_pk_bf16_f32 v144, v224, v225\n"
"v_exp_f32 v226, v98\n"
"s_nop 0\n"
"v_add_f32 v232, v232, v226\n"
"v_exp_f32 v227, v99\n"
"s_nop 0\n"
"v_add_f32 v232, v232, v227\n"
"v_cvt_pk_bf16_f32 v145, v226, v227\n"
"v_exp_f32 v228, v100\n"
"s_nop 0\n"
"v_add_f32 v232, v232, v228\n"
"v_exp_f32 v229, v101\n"
"s_nop 0\n"
"v_add_f32 v232, v232, v229\n"
"v_cvt_pk_bf16_f32 v146, v228, v229\n"
"v_exp_f32 v230, v102\n"
"s_nop 0\n"
"v_add_f32 v232, v232, v230\n"
"v_exp_f32 v231, v103\n"
"s_nop 0\n"
"v_add_f32 v232, v232, v231\n"
"v_cvt_pk_bf16_f32 v147, v230, v231\n"
"v_exp_f32 v224, v104\n"
"s_nop 0\n"
"v_add_f32 v232, v232, v224\n"
"v_exp_f32 v225, v105\n"
"s_nop 0\n"
"v_add_f32 v232, v232, v225\n"
"v_cvt_pk_bf16_f32 v148, v224, v225\n"
"v_exp_f32 v226, v106\n"
"s_nop 0\n"
"v_add_f32 v232, v232, v226\n"
"v_exp_f32 v227, v107\n"
"s_nop 0\n"
"v_add_f32 v232, v232, v227\n"
"v_cvt_pk_bf16_f32 v149, v226, v227\n"
"v_exp_f32 v228, v108\n"
"s_nop 0\n"
"v_add_f32 v232, v232, v228\n"
"v_exp_f32 v229, v109\n"
"s_nop 0\n"
"v_add_f32 v232, v232, v229\n"
"v_cvt_pk_bf16_f32 v150, v228, v229\n"
"v_exp_f32 v230, v110\n"
"s_nop 0\n"
"v_add_f32 v232, v232, v230\n"
"v_exp_f32 v231, v111\n"
"s_nop 0\n"
"v_add_f32 v232, v232, v231\n"
"v_cvt_pk_bf16_f32 v151, v230, v231\n"
"v_exp_f32 v224, v112\n"
"s_nop 0\n"
"v_exp_f32 v225, v113\n"
"s_nop 0\n"
"v_add_f32 v233, v224, v225\n"
"v_cvt_pk_bf16_f32 v152, v224, v225\n"
"v_exp_f32 v226, v114\n"
"s_nop 0\n"
"v_add_f32 v233, v233, v226\n"
"v_exp_f32 v227, v115\n"
"s_nop 0\n"
"v_add_f32 v233, v233, v227\n"
"v_cvt_pk_bf16_f32 v153, v226, v227\n"
"v_exp_f32 v228, v116\n"
"s_nop 0\n"
"v_add_f32 v233, v233, v228\n"
"v_exp_f32 v229, v117\n"
"s_nop 0\n"
"v_add_f32 v233, v233, v229\n"
"v_cvt_pk_bf16_f32 v154, v228, v229\n"
"v_exp_f32 v230, v118\n"
"s_nop 0\n"
"v_add_f32 v233, v233, v230\n"
"v_exp_f32 v231, v119\n"
"s_nop 0\n"
"v_add_f32 v233, v233, v231\n"
"v_cvt_pk_bf16_f32 v155, v230, v231\n"
"v_exp_f32 v224, v120\n"
"s_nop 0\n"
"v_add_f32 v233, v233, v224\n"
"v_exp_f32 v225, v121\n"
"s_nop 0\n"
"v_add_f32 v233, v233, v225\n"
"v_cvt_pk_bf16_f32 v156, v224, v225\n"
"v_exp_f32 v226, v122\n"
"s_nop 0\n"
"v_add_f32 v233, v233, v226\n"
"v_exp_f32 v227, v123\n"
"s_nop 0\n"
"v_add_f32 v233, v233, v227\n"
"v_cvt_pk_bf16_f32 v157, v226, v227\n"
"v_exp_f32 v228, v124\n"
"s_nop 0\n"
"v_add_f32 v233, v233, v228\n"
"v_exp_f32 v229, v125\n"
"s_nop 0\n"
"v_add_f32 v233, v233, v229\n"
"v_cvt_pk_bf16_f32 v158, v228, v229\n"
"v_exp_f32 v230, v126\n"
"s_nop 0\n"
"v_add_f32 v233, v233, v230\n"
"v_exp_f32 v231, v127\n"
"s_nop 0\n"
"v_add_f32 v233, v233, v231\n"
"v_cvt_pk_bf16_f32 v159, v230, v231\n"
"v_add_f32 v236, v232, v233\n"
"s_nop 1\n"
"s_branch Lfr51_%=\n"
"Lfb58_%=:\n"
"s_nop 7\n"
"s_nop 7\n"
"v_max3_f32 v240, v64, v65, v66\n"
"v_max3_f32 v240, v240, v67, v68\n"
"v_max3_f32 v240, v240, v69, v70\n"
"v_max3_f32 v240, v240, v71, v72\n"
"v_max3_f32 v240, v240, v73, v74\n"
"v_max3_f32 v240, v240, v75, v76\n"
"v_max3_f32 v240, v240, v77, v78\n"
"v_max3_f32 v240, v240, v79, v80\n"
"v_max3_f32 v240, v240, v81, v82\n"
"v_max3_f32 v240, v240, v83, v84\n"
"v_max3_f32 v240, v240, v85, v86\n"
"v_max3_f32 v240, v240, v87, v88\n"
"v_max3_f32 v240, v240, v89, v90\n"
"v_max3_f32 v240, v240, v91, v92\n"
"v_max3_f32 v240, v240, v93, v94\n"
"v_max_f32 v240, v240, v95\n"
"v_mov_b32 v244, v240\n"
"s_nop 1\n"
"v_permlane32_swap_b32 v240, v244\n"
"v_max_f32 v240, v240, v244\n"
"v_max_f32 v240, 0, v240\n"
"v_sub_f32 v244, 0, v240\n"
"v_exp_f32 v242, v244\n"
"s_nop 0\n"
"v_mul_f32 v234, v234, v242\n"
"v_sub_f32 v160, v160, v240\n"
"v_sub_f32 v161, v161, v240\n"
"v_sub_f32 v162, v162, v240\n"
"v_sub_f32 v163, v163, v240\n"
"v_sub_f32 v164, v164, v240\n"
"v_sub_f32 v165, v165, v240\n"
"v_sub_f32 v166, v166, v240\n"
"v_sub_f32 v167, v167, v240\n"
"v_sub_f32 v168, v168, v240\n"
"v_sub_f32 v169, v169, v240\n"
"v_sub_f32 v170, v170, v240\n"
"v_sub_f32 v171, v171, v240\n"
"v_sub_f32 v172, v172, v240\n"
"v_sub_f32 v173, v173, v240\n"
"v_sub_f32 v174, v174, v240\n"
"v_sub_f32 v175, v175, v240\n"
"v_accvgpr_read_b32 v244, a0\n"
"v_mul_f32 v244, v244, v242\n"
"v_accvgpr_write_b32 a0, v244\n"
"v_accvgpr_read_b32 v245, a1\n"
"v_mul_f32 v245, v245, v242\n"
"v_accvgpr_write_b32 a1, v245\n"
"v_accvgpr_read_b32 v244, a2\n"
"v_mul_f32 v244, v244, v242\n"
"v_accvgpr_write_b32 a2, v244\n"
"v_accvgpr_read_b32 v245, a3\n"
"v_mul_f32 v245, v245, v242\n"
"v_accvgpr_write_b32 a3, v245\n"
"v_accvgpr_read_b32 v244, a4\n"
"v_mul_f32 v244, v244, v242\n"
"v_accvgpr_write_b32 a4, v244\n"
"v_accvgpr_read_b32 v245, a5\n"
"v_mul_f32 v245, v245, v242\n"
"v_accvgpr_write_b32 a5, v245\n"
"v_accvgpr_read_b32 v244, a6\n"
"v_mul_f32 v244, v244, v242\n"
"v_accvgpr_write_b32 a6, v244\n"
"v_accvgpr_read_b32 v245, a7\n"
"v_mul_f32 v245, v245, v242\n"
"v_accvgpr_write_b32 a7, v245\n"
"v_accvgpr_read_b32 v244, a8\n"
"v_mul_f32 v244, v244, v242\n"
"v_accvgpr_write_b32 a8, v244\n"
"v_accvgpr_read_b32 v245, a9\n"
"v_mul_f32 v245, v245, v242\n"
"v_accvgpr_write_b32 a9, v245\n"
"v_accvgpr_read_b32 v244, a10\n"
"v_mul_f32 v244, v244, v242\n"
"v_accvgpr_write_b32 a10, v244\n"
"v_accvgpr_read_b32 v245, a11\n"
"v_mul_f32 v245, v245, v242\n"
"v_accvgpr_write_b32 a11, v245\n"
"v_accvgpr_read_b32 v244, a12\n"
"v_mul_f32 v244, v244, v242\n"
"v_accvgpr_write_b32 a12, v244\n"
"v_accvgpr_read_b32 v245, a13\n"
"v_mul_f32 v245, v245, v242\n"
"v_accvgpr_write_b32 a13, v245\n"
"v_accvgpr_read_b32 v244, a14\n"
"v_mul_f32 v244, v244, v242\n"
"v_accvgpr_write_b32 a14, v244\n"
"v_accvgpr_read_b32 v245, a15\n"
"v_mul_f32 v245, v245, v242\n"
"v_accvgpr_write_b32 a15, v245\n"
"v_accvgpr_read_b32 v244, a16\n"
"v_mul_f32 v244, v244, v242\n"
"v_accvgpr_write_b32 a16, v244\n"
"v_accvgpr_read_b32 v245, a17\n"
"v_mul_f32 v245, v245, v242\n"
"v_accvgpr_write_b32 a17, v245\n"
"v_accvgpr_read_b32 v244, a18\n"
"v_mul_f32 v244, v244, v242\n"
"v_accvgpr_write_b32 a18, v244\n"
"v_accvgpr_read_b32 v245, a19\n"
"v_mul_f32 v245, v245, v242\n"
"v_accvgpr_write_b32 a19, v245\n"
"v_accvgpr_read_b32 v244, a20\n"
"v_mul_f32 v244, v244, v242\n"
"v_accvgpr_write_b32 a20, v244\n"
"v_accvgpr_read_b32 v245, a21\n"
"v_mul_f32 v245, v245, v242\n"
"v_accvgpr_write_b32 a21, v245\n"
"v_accvgpr_read_b32 v244, a22\n"
"v_mul_f32 v244, v244, v242\n"
"v_accvgpr_write_b32 a22, v244\n"
"v_accvgpr_read_b32 v245, a23\n"
"v_mul_f32 v245, v245, v242\n"
"v_accvgpr_write_b32 a23, v245\n"
"v_accvgpr_read_b32 v244, a24\n"
"v_mul_f32 v244, v244, v242\n"
"v_accvgpr_write_b32 a24, v244\n"
"v_accvgpr_read_b32 v245, a25\n"
"v_mul_f32 v245, v245, v242\n"
"v_accvgpr_write_b32 a25, v245\n"
"v_accvgpr_read_b32 v244, a26\n"
"v_mul_f32 v244, v244, v242\n"
"v_accvgpr_write_b32 a26, v244\n"
"v_accvgpr_read_b32 v245, a27\n"
"v_mul_f32 v245, v245, v242\n"
"v_accvgpr_write_b32 a27, v245\n"
"v_accvgpr_read_b32 v244, a28\n"
"v_mul_f32 v244, v244, v242\n"
"v_accvgpr_write_b32 a28, v244\n"
"v_accvgpr_read_b32 v245, a29\n"
"v_mul_f32 v245, v245, v242\n"
"v_accvgpr_write_b32 a29, v245\n"
"v_accvgpr_read_b32 v244, a30\n"
"v_mul_f32 v244, v244, v242\n"
"v_accvgpr_write_b32 a30, v244\n"
"v_accvgpr_read_b32 v245, a31\n"
"v_mul_f32 v245, v245, v242\n"
"v_accvgpr_write_b32 a31, v245\n"
"v_accvgpr_read_b32 v244, a32\n"
"v_mul_f32 v244, v244, v242\n"
"v_accvgpr_write_b32 a32, v244\n"
"v_accvgpr_read_b32 v245, a33\n"
"v_mul_f32 v245, v245, v242\n"
"v_accvgpr_write_b32 a33, v245\n"
"v_accvgpr_read_b32 v244, a34\n"
"v_mul_f32 v244, v244, v242\n"
"v_accvgpr_write_b32 a34, v244\n"
"v_accvgpr_read_b32 v245, a35\n"
"v_mul_f32 v245, v245, v242\n"
"v_accvgpr_write_b32 a35, v245\n"
"v_accvgpr_read_b32 v244, a36\n"
"v_mul_f32 v244, v244, v242\n"
"v_accvgpr_write_b32 a36, v244\n"
"v_accvgpr_read_b32 v245, a37\n"
"v_mul_f32 v245, v245, v242\n"
"v_accvgpr_write_b32 a37, v245\n"
"v_accvgpr_read_b32 v244, a38\n"
"v_mul_f32 v244, v244, v242\n"
"v_accvgpr_write_b32 a38, v244\n"
"v_accvgpr_read_b32 v245, a39\n"
"v_mul_f32 v245, v245, v242\n"
"v_accvgpr_write_b32 a39, v245\n"
"v_accvgpr_read_b32 v244, a40\n"
"v_mul_f32 v244, v244, v242\n"
"v_accvgpr_write_b32 a40, v244\n"
"v_accvgpr_read_b32 v245, a41\n"
"v_mul_f32 v245, v245, v242\n"
"v_accvgpr_write_b32 a41, v245\n"
"v_accvgpr_read_b32 v244, a42\n"
"v_mul_f32 v244, v244, v242\n"
"v_accvgpr_write_b32 a42, v244\n"
"v_accvgpr_read_b32 v245, a43\n"
"v_mul_f32 v245, v245, v242\n"
"v_accvgpr_write_b32 a43, v245\n"
"v_accvgpr_read_b32 v244, a44\n"
"v_mul_f32 v244, v244, v242\n"
"v_accvgpr_write_b32 a44, v244\n"
"v_accvgpr_read_b32 v245, a45\n"
"v_mul_f32 v245, v245, v242\n"
"v_accvgpr_write_b32 a45, v245\n"
"v_accvgpr_read_b32 v244, a46\n"
"v_mul_f32 v244, v244, v242\n"
"v_accvgpr_write_b32 a46, v244\n"
"v_accvgpr_read_b32 v245, a47\n"
"v_mul_f32 v245, v245, v242\n"
"v_accvgpr_write_b32 a47, v245\n"
"v_accvgpr_read_b32 v244, a48\n"
"v_mul_f32 v244, v244, v242\n"
"v_accvgpr_write_b32 a48, v244\n"
"v_accvgpr_read_b32 v245, a49\n"
"v_mul_f32 v245, v245, v242\n"
"v_accvgpr_write_b32 a49, v245\n"
"v_accvgpr_read_b32 v244, a50\n"
"v_mul_f32 v244, v244, v242\n"
"v_accvgpr_write_b32 a50, v244\n"
"v_accvgpr_read_b32 v245, a51\n"
"v_mul_f32 v245, v245, v242\n"
"v_accvgpr_write_b32 a51, v245\n"
"v_accvgpr_read_b32 v244, a52\n"
"v_mul_f32 v244, v244, v242\n"
"v_accvgpr_write_b32 a52, v244\n"
"v_accvgpr_read_b32 v245, a53\n"
"v_mul_f32 v245, v245, v242\n"
"v_accvgpr_write_b32 a53, v245\n"
"v_accvgpr_read_b32 v244, a54\n"
"v_mul_f32 v244, v244, v242\n"
"v_accvgpr_write_b32 a54, v244\n"
"v_accvgpr_read_b32 v245, a55\n"
"v_mul_f32 v245, v245, v242\n"
"v_accvgpr_write_b32 a55, v245\n"
"v_accvgpr_read_b32 v244, a56\n"
"v_mul_f32 v244, v244, v242\n"
"v_accvgpr_write_b32 a56, v244\n"
"v_accvgpr_read_b32 v245, a57\n"
"v_mul_f32 v245, v245, v242\n"
"v_accvgpr_write_b32 a57, v245\n"
"v_accvgpr_read_b32 v244, a58\n"
"v_mul_f32 v244, v244, v242\n"
"v_accvgpr_write_b32 a58, v244\n"
"v_accvgpr_read_b32 v245, a59\n"
"v_mul_f32 v245, v245, v242\n"
"v_accvgpr_write_b32 a59, v245\n"
"v_accvgpr_read_b32 v244, a60\n"
"v_mul_f32 v244, v244, v242\n"
"v_accvgpr_write_b32 a60, v244\n"
"v_accvgpr_read_b32 v245, a61\n"
"v_mul_f32 v245, v245, v242\n"
"v_accvgpr_write_b32 a61, v245\n"
"v_accvgpr_read_b32 v244, a62\n"
"v_mul_f32 v244, v244, v242\n"
"v_accvgpr_write_b32 a62, v244\n"
"v_accvgpr_read_b32 v245, a63\n"
"v_mul_f32 v245, v245, v242\n"
"v_accvgpr_write_b32 a63, v245\n"
"v_sub_f32 v64, v64, v240\n"
"v_sub_f32 v65, v65, v240\n"
"v_sub_f32 v66, v66, v240\n"
"v_sub_f32 v67, v67, v240\n"
"v_sub_f32 v68, v68, v240\n"
"v_sub_f32 v69, v69, v240\n"
"v_sub_f32 v70, v70, v240\n"
"v_sub_f32 v71, v71, v240\n"
"v_sub_f32 v72, v72, v240\n"
"v_sub_f32 v73, v73, v240\n"
"v_sub_f32 v74, v74, v240\n"
"v_sub_f32 v75, v75, v240\n"
"v_sub_f32 v76, v76, v240\n"
"v_sub_f32 v77, v77, v240\n"
"v_sub_f32 v78, v78, v240\n"
"v_sub_f32 v79, v79, v240\n"
"v_sub_f32 v80, v80, v240\n"
"v_sub_f32 v81, v81, v240\n"
"v_sub_f32 v82, v82, v240\n"
"v_sub_f32 v83, v83, v240\n"
"v_sub_f32 v84, v84, v240\n"
"v_sub_f32 v85, v85, v240\n"
"v_sub_f32 v86, v86, v240\n"
"v_sub_f32 v87, v87, v240\n"
"v_sub_f32 v88, v88, v240\n"
"v_sub_f32 v89, v89, v240\n"
"v_sub_f32 v90, v90, v240\n"
"v_sub_f32 v91, v91, v240\n"
"v_sub_f32 v92, v92, v240\n"
"v_sub_f32 v93, v93, v240\n"
"v_sub_f32 v94, v94, v240\n"
"v_sub_f32 v95, v95, v240\n"
"v_sub_f32 v96, v96, v240\n"
"v_sub_f32 v97, v97, v240\n"
"v_sub_f32 v98, v98, v240\n"
"v_sub_f32 v99, v99, v240\n"
"v_sub_f32 v100, v100, v240\n"
"v_sub_f32 v101, v101, v240\n"
"v_sub_f32 v102, v102, v240\n"
"v_sub_f32 v103, v103, v240\n"
"v_sub_f32 v104, v104, v240\n"
"v_sub_f32 v105, v105, v240\n"
"v_sub_f32 v106, v106, v240\n"
"v_sub_f32 v107, v107, v240\n"
"v_sub_f32 v108, v108, v240\n"
"v_sub_f32 v109, v109, v240\n"
"v_sub_f32 v110, v110, v240\n"
"v_sub_f32 v111, v111, v240\n"
"v_sub_f32 v112, v112, v240\n"
"v_sub_f32 v113, v113, v240\n"
"v_sub_f32 v114, v114, v240\n"
"v_sub_f32 v115, v115, v240\n"
"v_sub_f32 v116, v116, v240\n"
"v_sub_f32 v117, v117, v240\n"
"v_sub_f32 v118, v118, v240\n"
"v_sub_f32 v119, v119, v240\n"
"v_sub_f32 v120, v120, v240\n"
"v_sub_f32 v121, v121, v240\n"
"v_sub_f32 v122, v122, v240\n"
"v_sub_f32 v123, v123, v240\n"
"v_sub_f32 v124, v124, v240\n"
"v_sub_f32 v125, v125, v240\n"
"v_sub_f32 v126, v126, v240\n"
"v_sub_f32 v127, v127, v240\n"
"v_exp_f32 v224, v64\n"
"s_nop 0\n"
"v_exp_f32 v225, v65\n"
"s_nop 0\n"
"v_add_f32 v232, v224, v225\n"
"v_cvt_pk_bf16_f32 v128, v224, v225\n"
"v_exp_f32 v226, v66\n"
"s_nop 0\n"
"v_add_f32 v232, v232, v226\n"
"v_exp_f32 v227, v67\n"
"s_nop 0\n"
"v_add_f32 v232, v232, v227\n"
"v_cvt_pk_bf16_f32 v129, v226, v227\n"
"v_exp_f32 v228, v68\n"
"s_nop 0\n"
"v_add_f32 v232, v232, v228\n"
"v_exp_f32 v229, v69\n"
"s_nop 0\n"
"v_add_f32 v232, v232, v229\n"
"v_cvt_pk_bf16_f32 v130, v228, v229\n"
"v_exp_f32 v230, v70\n"
"s_nop 0\n"
"v_add_f32 v232, v232, v230\n"
"v_exp_f32 v231, v71\n"
"s_nop 0\n"
"v_add_f32 v232, v232, v231\n"
"v_cvt_pk_bf16_f32 v131, v230, v231\n"
"v_exp_f32 v224, v72\n"
"s_nop 0\n"
"v_add_f32 v232, v232, v224\n"
"v_exp_f32 v225, v73\n"
"s_nop 0\n"
"v_add_f32 v232, v232, v225\n"
"v_cvt_pk_bf16_f32 v132, v224, v225\n"
"v_exp_f32 v226, v74\n"
"s_nop 0\n"
"v_add_f32 v232, v232, v226\n"
"v_exp_f32 v227, v75\n"
"s_nop 0\n"
"v_add_f32 v232, v232, v227\n"
"v_cvt_pk_bf16_f32 v133, v226, v227\n"
"v_exp_f32 v228, v76\n"
"s_nop 0\n"
"v_add_f32 v232, v232, v228\n"
"v_exp_f32 v229, v77\n"
"s_nop 0\n"
"v_add_f32 v232, v232, v229\n"
"v_cvt_pk_bf16_f32 v134, v228, v229\n"
"v_exp_f32 v230, v78\n"
"s_nop 0\n"
"v_add_f32 v232, v232, v230\n"
"v_exp_f32 v231, v79\n"
"s_nop 0\n"
"v_add_f32 v232, v232, v231\n"
"v_cvt_pk_bf16_f32 v135, v230, v231\n"
"v_exp_f32 v224, v80\n"
"s_nop 0\n"
"v_exp_f32 v225, v81\n"
"s_nop 0\n"
"v_add_f32 v233, v224, v225\n"
"v_cvt_pk_bf16_f32 v136, v224, v225\n"
"v_exp_f32 v226, v82\n"
"s_nop 0\n"
"v_add_f32 v233, v233, v226\n"
"v_exp_f32 v227, v83\n"
"s_nop 0\n"
"v_add_f32 v233, v233, v227\n"
"v_cvt_pk_bf16_f32 v137, v226, v227\n"
"v_exp_f32 v228, v84\n"
"s_nop 0\n"
"v_add_f32 v233, v233, v228\n"
"v_exp_f32 v229, v85\n"
"s_nop 0\n"
"v_add_f32 v233, v233, v229\n"
"v_cvt_pk_bf16_f32 v138, v228, v229\n"
"v_exp_f32 v230, v86\n"
"s_nop 0\n"
"v_add_f32 v233, v233, v230\n"
"v_exp_f32 v231, v87\n"
"s_nop 0\n"
"v_add_f32 v233, v233, v231\n"
"v_cvt_pk_bf16_f32 v139, v230, v231\n"
"v_exp_f32 v224, v88\n"
"s_nop 0\n"
"v_add_f32 v233, v233, v224\n"
"v_exp_f32 v225, v89\n"
"s_nop 0\n"
"v_add_f32 v233, v233, v225\n"
"v_cvt_pk_bf16_f32 v140, v224, v225\n"
"v_exp_f32 v226, v90\n"
"s_nop 0\n"
"v_add_f32 v233, v233, v226\n"
"v_exp_f32 v227, v91\n"
"s_nop 0\n"
"v_add_f32 v233, v233, v227\n"
"v_cvt_pk_bf16_f32 v141, v226, v227\n"
"v_exp_f32 v228, v92\n"
"s_nop 0\n"
"v_add_f32 v233, v233, v228\n"
"v_exp_f32 v229, v93\n"
"s_nop 0\n"
"v_add_f32 v233, v233, v229\n"
"v_cvt_pk_bf16_f32 v142, v228, v229\n"
"v_exp_f32 v230, v94\n"
"s_nop 0\n"
"v_add_f32 v233, v233, v230\n"
"v_exp_f32 v231, v95\n"
"s_nop 0\n"
"v_add_f32 v233, v233, v231\n"
"v_cvt_pk_bf16_f32 v143, v230, v231\n"
"v_add_f32 v236, v232, v233\n"
"s_nop 1\n"
"s_branch Lfr59_%=\n"
"Lfb66_%=:\n"
"s_nop 7\n"
"s_nop 7\n"
"v_max3_f32 v240, v96, v97, v98\n"
"v_max3_f32 v240, v240, v99, v100\n"
"v_max3_f32 v240, v240, v101, v102\n"
"v_max3_f32 v240, v240, v103, v104\n"
"v_max3_f32 v240, v240, v105, v106\n"
"v_max3_f32 v240, v240, v107, v108\n"
"v_max3_f32 v240, v240, v109, v110\n"
"v_max3_f32 v240, v240, v111, v112\n"
"v_max3_f32 v240, v240, v113, v114\n"
"v_max3_f32 v240, v240, v115, v116\n"
"v_max3_f32 v240, v240, v117, v118\n"
"v_max3_f32 v240, v240, v119, v120\n"
"v_max3_f32 v240, v240, v121, v122\n"
"v_max3_f32 v240, v240, v123, v124\n"
"v_max3_f32 v240, v240, v125, v126\n"
"v_max_f32 v240, v240, v127\n"
"v_mov_b32 v244, v240\n"
"s_nop 1\n"
"v_permlane32_swap_b32 v240, v244\n"
"v_max_f32 v240, v240, v244\n"
"v_max_f32 v240, 0, v240\n"
"v_sub_f32 v244, 0, v240\n"
"v_exp_f32 v242, v244\n"
"s_nop 0\n"
"v_mul_f32 v234, v234, v242\n"
"v_sub_f32 v160, v160, v240\n"
"v_sub_f32 v161, v161, v240\n"
"v_sub_f32 v162, v162, v240\n"
"v_sub_f32 v163, v163, v240\n"
"v_sub_f32 v164, v164, v240\n"
"v_sub_f32 v165, v165, v240\n"
"v_sub_f32 v166, v166, v240\n"
"v_sub_f32 v167, v167, v240\n"
"v_sub_f32 v168, v168, v240\n"
"v_sub_f32 v169, v169, v240\n"
"v_sub_f32 v170, v170, v240\n"
"v_sub_f32 v171, v171, v240\n"
"v_sub_f32 v172, v172, v240\n"
"v_sub_f32 v173, v173, v240\n"
"v_sub_f32 v174, v174, v240\n"
"v_sub_f32 v175, v175, v240\n"
"v_accvgpr_read_b32 v244, a0\n"
"v_mul_f32 v244, v244, v242\n"
"v_accvgpr_write_b32 a0, v244\n"
"v_accvgpr_read_b32 v245, a1\n"
"v_mul_f32 v245, v245, v242\n"
"v_accvgpr_write_b32 a1, v245\n"
"v_accvgpr_read_b32 v244, a2\n"
"v_mul_f32 v244, v244, v242\n"
"v_accvgpr_write_b32 a2, v244\n"
"v_accvgpr_read_b32 v245, a3\n"
"v_mul_f32 v245, v245, v242\n"
"v_accvgpr_write_b32 a3, v245\n"
"v_accvgpr_read_b32 v244, a4\n"
"v_mul_f32 v244, v244, v242\n"
"v_accvgpr_write_b32 a4, v244\n"
"v_accvgpr_read_b32 v245, a5\n"
"v_mul_f32 v245, v245, v242\n"
"v_accvgpr_write_b32 a5, v245\n"
"v_accvgpr_read_b32 v244, a6\n"
"v_mul_f32 v244, v244, v242\n"
"v_accvgpr_write_b32 a6, v244\n"
"v_accvgpr_read_b32 v245, a7\n"
"v_mul_f32 v245, v245, v242\n"
"v_accvgpr_write_b32 a7, v245\n"
"v_accvgpr_read_b32 v244, a8\n"
"v_mul_f32 v244, v244, v242\n"
"v_accvgpr_write_b32 a8, v244\n"
"v_accvgpr_read_b32 v245, a9\n"
"v_mul_f32 v245, v245, v242\n"
"v_accvgpr_write_b32 a9, v245\n"
"v_accvgpr_read_b32 v244, a10\n"
"v_mul_f32 v244, v244, v242\n"
"v_accvgpr_write_b32 a10, v244\n"
"v_accvgpr_read_b32 v245, a11\n"
"v_mul_f32 v245, v245, v242\n"
"v_accvgpr_write_b32 a11, v245\n"
"v_accvgpr_read_b32 v244, a12\n"
"v_mul_f32 v244, v244, v242\n"
"v_accvgpr_write_b32 a12, v244\n"
"v_accvgpr_read_b32 v245, a13\n"
"v_mul_f32 v245, v245, v242\n"
"v_accvgpr_write_b32 a13, v245\n"
"v_accvgpr_read_b32 v244, a14\n"
"v_mul_f32 v244, v244, v242\n"
"v_accvgpr_write_b32 a14, v244\n"
"v_accvgpr_read_b32 v245, a15\n"
"v_mul_f32 v245, v245, v242\n"
"v_accvgpr_write_b32 a15, v245\n"
"v_accvgpr_read_b32 v244, a16\n"
"v_mul_f32 v244, v244, v242\n"
"v_accvgpr_write_b32 a16, v244\n"
"v_accvgpr_read_b32 v245, a17\n"
"v_mul_f32 v245, v245, v242\n"
"v_accvgpr_write_b32 a17, v245\n"
"v_accvgpr_read_b32 v244, a18\n"
"v_mul_f32 v244, v244, v242\n"
"v_accvgpr_write_b32 a18, v244\n"
"v_accvgpr_read_b32 v245, a19\n"
"v_mul_f32 v245, v245, v242\n"
"v_accvgpr_write_b32 a19, v245\n"
"v_accvgpr_read_b32 v244, a20\n"
"v_mul_f32 v244, v244, v242\n"
"v_accvgpr_write_b32 a20, v244\n"
"v_accvgpr_read_b32 v245, a21\n"
"v_mul_f32 v245, v245, v242\n"
"v_accvgpr_write_b32 a21, v245\n"
"v_accvgpr_read_b32 v244, a22\n"
"v_mul_f32 v244, v244, v242\n"
"v_accvgpr_write_b32 a22, v244\n"
"v_accvgpr_read_b32 v245, a23\n"
"v_mul_f32 v245, v245, v242\n"
"v_accvgpr_write_b32 a23, v245\n"
"v_accvgpr_read_b32 v244, a24\n"
"v_mul_f32 v244, v244, v242\n"
"v_accvgpr_write_b32 a24, v244\n"
"v_accvgpr_read_b32 v245, a25\n"
"v_mul_f32 v245, v245, v242\n"
"v_accvgpr_write_b32 a25, v245\n"
"v_accvgpr_read_b32 v244, a26\n"
"v_mul_f32 v244, v244, v242\n"
"v_accvgpr_write_b32 a26, v244\n"
"v_accvgpr_read_b32 v245, a27\n"
"v_mul_f32 v245, v245, v242\n"
"v_accvgpr_write_b32 a27, v245\n"
"v_accvgpr_read_b32 v244, a28\n"
"v_mul_f32 v244, v244, v242\n"
"v_accvgpr_write_b32 a28, v244\n"
"v_accvgpr_read_b32 v245, a29\n"
"v_mul_f32 v245, v245, v242\n"
"v_accvgpr_write_b32 a29, v245\n"
"v_accvgpr_read_b32 v244, a30\n"
"v_mul_f32 v244, v244, v242\n"
"v_accvgpr_write_b32 a30, v244\n"
"v_accvgpr_read_b32 v245, a31\n"
"v_mul_f32 v245, v245, v242\n"
"v_accvgpr_write_b32 a31, v245\n"
"v_accvgpr_read_b32 v244, a32\n"
"v_mul_f32 v244, v244, v242\n"
"v_accvgpr_write_b32 a32, v244\n"
"v_accvgpr_read_b32 v245, a33\n"
"v_mul_f32 v245, v245, v242\n"
"v_accvgpr_write_b32 a33, v245\n"
"v_accvgpr_read_b32 v244, a34\n"
"v_mul_f32 v244, v244, v242\n"
"v_accvgpr_write_b32 a34, v244\n"
"v_accvgpr_read_b32 v245, a35\n"
"v_mul_f32 v245, v245, v242\n"
"v_accvgpr_write_b32 a35, v245\n"
"v_accvgpr_read_b32 v244, a36\n"
"v_mul_f32 v244, v244, v242\n"
"v_accvgpr_write_b32 a36, v244\n"
"v_accvgpr_read_b32 v245, a37\n"
"v_mul_f32 v245, v245, v242\n"
"v_accvgpr_write_b32 a37, v245\n"
"v_accvgpr_read_b32 v244, a38\n"
"v_mul_f32 v244, v244, v242\n"
"v_accvgpr_write_b32 a38, v244\n"
"v_accvgpr_read_b32 v245, a39\n"
"v_mul_f32 v245, v245, v242\n"
"v_accvgpr_write_b32 a39, v245\n"
"v_accvgpr_read_b32 v244, a40\n"
"v_mul_f32 v244, v244, v242\n"
"v_accvgpr_write_b32 a40, v244\n"
"v_accvgpr_read_b32 v245, a41\n"
"v_mul_f32 v245, v245, v242\n"
"v_accvgpr_write_b32 a41, v245\n"
"v_accvgpr_read_b32 v244, a42\n"
"v_mul_f32 v244, v244, v242\n"
"v_accvgpr_write_b32 a42, v244\n"
"v_accvgpr_read_b32 v245, a43\n"
"v_mul_f32 v245, v245, v242\n"
"v_accvgpr_write_b32 a43, v245\n"
"v_accvgpr_read_b32 v244, a44\n"
"v_mul_f32 v244, v244, v242\n"
"v_accvgpr_write_b32 a44, v244\n"
"v_accvgpr_read_b32 v245, a45\n"
"v_mul_f32 v245, v245, v242\n"
"v_accvgpr_write_b32 a45, v245\n"
"v_accvgpr_read_b32 v244, a46\n"
"v_mul_f32 v244, v244, v242\n"
"v_accvgpr_write_b32 a46, v244\n"
"v_accvgpr_read_b32 v245, a47\n"
"v_mul_f32 v245, v245, v242\n"
"v_accvgpr_write_b32 a47, v245\n"
"v_accvgpr_read_b32 v244, a48\n"
"v_mul_f32 v244, v244, v242\n"
"v_accvgpr_write_b32 a48, v244\n"
"v_accvgpr_read_b32 v245, a49\n"
"v_mul_f32 v245, v245, v242\n"
"v_accvgpr_write_b32 a49, v245\n"
"v_accvgpr_read_b32 v244, a50\n"
"v_mul_f32 v244, v244, v242\n"
"v_accvgpr_write_b32 a50, v244\n"
"v_accvgpr_read_b32 v245, a51\n"
"v_mul_f32 v245, v245, v242\n"
"v_accvgpr_write_b32 a51, v245\n"
"v_accvgpr_read_b32 v244, a52\n"
"v_mul_f32 v244, v244, v242\n"
"v_accvgpr_write_b32 a52, v244\n"
"v_accvgpr_read_b32 v245, a53\n"
"v_mul_f32 v245, v245, v242\n"
"v_accvgpr_write_b32 a53, v245\n"
"v_accvgpr_read_b32 v244, a54\n"
"v_mul_f32 v244, v244, v242\n"
"v_accvgpr_write_b32 a54, v244\n"
"v_accvgpr_read_b32 v245, a55\n"
"v_mul_f32 v245, v245, v242\n"
"v_accvgpr_write_b32 a55, v245\n"
"v_accvgpr_read_b32 v244, a56\n"
"v_mul_f32 v244, v244, v242\n"
"v_accvgpr_write_b32 a56, v244\n"
"v_accvgpr_read_b32 v245, a57\n"
"v_mul_f32 v245, v245, v242\n"
"v_accvgpr_write_b32 a57, v245\n"
"v_accvgpr_read_b32 v244, a58\n"
"v_mul_f32 v244, v244, v242\n"
"v_accvgpr_write_b32 a58, v244\n"
"v_accvgpr_read_b32 v245, a59\n"
"v_mul_f32 v245, v245, v242\n"
"v_accvgpr_write_b32 a59, v245\n"
"v_accvgpr_read_b32 v244, a60\n"
"v_mul_f32 v244, v244, v242\n"
"v_accvgpr_write_b32 a60, v244\n"
"v_accvgpr_read_b32 v245, a61\n"
"v_mul_f32 v245, v245, v242\n"
"v_accvgpr_write_b32 a61, v245\n"
"v_accvgpr_read_b32 v244, a62\n"
"v_mul_f32 v244, v244, v242\n"
"v_accvgpr_write_b32 a62, v244\n"
"v_accvgpr_read_b32 v245, a63\n"
"v_mul_f32 v245, v245, v242\n"
"v_accvgpr_write_b32 a63, v245\n"
"v_sub_f32 v96, v96, v240\n"
"v_sub_f32 v97, v97, v240\n"
"v_sub_f32 v98, v98, v240\n"
"v_sub_f32 v99, v99, v240\n"
"v_sub_f32 v100, v100, v240\n"
"v_sub_f32 v101, v101, v240\n"
"v_sub_f32 v102, v102, v240\n"
"v_sub_f32 v103, v103, v240\n"
"v_sub_f32 v104, v104, v240\n"
"v_sub_f32 v105, v105, v240\n"
"v_sub_f32 v106, v106, v240\n"
"v_sub_f32 v107, v107, v240\n"
"v_sub_f32 v108, v108, v240\n"
"v_sub_f32 v109, v109, v240\n"
"v_sub_f32 v110, v110, v240\n"
"v_sub_f32 v111, v111, v240\n"
"v_sub_f32 v112, v112, v240\n"
"v_sub_f32 v113, v113, v240\n"
"v_sub_f32 v114, v114, v240\n"
"v_sub_f32 v115, v115, v240\n"
"v_sub_f32 v116, v116, v240\n"
"v_sub_f32 v117, v117, v240\n"
"v_sub_f32 v118, v118, v240\n"
"v_sub_f32 v119, v119, v240\n"
"v_sub_f32 v120, v120, v240\n"
"v_sub_f32 v121, v121, v240\n"
"v_sub_f32 v122, v122, v240\n"
"v_sub_f32 v123, v123, v240\n"
"v_sub_f32 v124, v124, v240\n"
"v_sub_f32 v125, v125, v240\n"
"v_sub_f32 v126, v126, v240\n"
"v_sub_f32 v127, v127, v240\n"
"v_exp_f32 v224, v96\n"
"s_nop 0\n"
"v_exp_f32 v225, v97\n"
"s_nop 0\n"
"v_add_f32 v232, v224, v225\n"
"v_cvt_pk_bf16_f32 v144, v224, v225\n"
"v_exp_f32 v226, v98\n"
"s_nop 0\n"
"v_add_f32 v232, v232, v226\n"
"v_exp_f32 v227, v99\n"
"s_nop 0\n"
"v_add_f32 v232, v232, v227\n"
"v_cvt_pk_bf16_f32 v145, v226, v227\n"
"v_exp_f32 v228, v100\n"
"s_nop 0\n"
"v_add_f32 v232, v232, v228\n"
"v_exp_f32 v229, v101\n"
"s_nop 0\n"
"v_add_f32 v232, v232, v229\n"
"v_cvt_pk_bf16_f32 v146, v228, v229\n"
"v_exp_f32 v230, v102\n"
"s_nop 0\n"
"v_add_f32 v232, v232, v230\n"
"v_exp_f32 v231, v103\n"
"s_nop 0\n"
"v_add_f32 v232, v232, v231\n"
"v_cvt_pk_bf16_f32 v147, v230, v231\n"
"v_exp_f32 v224, v104\n"
"s_nop 0\n"
"v_add_f32 v232, v232, v224\n"
"v_exp_f32 v225, v105\n"
"s_nop 0\n"
"v_add_f32 v232, v232, v225\n"
"v_cvt_pk_bf16_f32 v148, v224, v225\n"
"v_exp_f32 v226, v106\n"
"s_nop 0\n"
"v_add_f32 v232, v232, v226\n"
"v_exp_f32 v227, v107\n"
"s_nop 0\n"
"v_add_f32 v232, v232, v227\n"
"v_cvt_pk_bf16_f32 v149, v226, v227\n"
"v_exp_f32 v228, v108\n"
"s_nop 0\n"
"v_add_f32 v232, v232, v228\n"
"v_exp_f32 v229, v109\n"
"s_nop 0\n"
"v_add_f32 v232, v232, v229\n"
"v_cvt_pk_bf16_f32 v150, v228, v229\n"
"v_exp_f32 v230, v110\n"
"s_nop 0\n"
"v_add_f32 v232, v232, v230\n"
"v_exp_f32 v231, v111\n"
"s_nop 0\n"
"v_add_f32 v232, v232, v231\n"
"v_cvt_pk_bf16_f32 v151, v230, v231\n"
"v_exp_f32 v224, v112\n"
"s_nop 0\n"
"v_exp_f32 v225, v113\n"
"s_nop 0\n"
"v_add_f32 v233, v224, v225\n"
"v_cvt_pk_bf16_f32 v152, v224, v225\n"
"v_exp_f32 v226, v114\n"
"s_nop 0\n"
"v_add_f32 v233, v233, v226\n"
"v_exp_f32 v227, v115\n"
"s_nop 0\n"
"v_add_f32 v233, v233, v227\n"
"v_cvt_pk_bf16_f32 v153, v226, v227\n"
"v_exp_f32 v228, v116\n"
"s_nop 0\n"
"v_add_f32 v233, v233, v228\n"
"v_exp_f32 v229, v117\n"
"s_nop 0\n"
"v_add_f32 v233, v233, v229\n"
"v_cvt_pk_bf16_f32 v154, v228, v229\n"
"v_exp_f32 v230, v118\n"
"s_nop 0\n"
"v_add_f32 v233, v233, v230\n"
"v_exp_f32 v231, v119\n"
"s_nop 0\n"
"v_add_f32 v233, v233, v231\n"
"v_cvt_pk_bf16_f32 v155, v230, v231\n"
"v_exp_f32 v224, v120\n"
"s_nop 0\n"
"v_add_f32 v233, v233, v224\n"
"v_exp_f32 v225, v121\n"
"s_nop 0\n"
"v_add_f32 v233, v233, v225\n"
"v_cvt_pk_bf16_f32 v156, v224, v225\n"
"v_exp_f32 v226, v122\n"
"s_nop 0\n"
"v_add_f32 v233, v233, v226\n"
"v_exp_f32 v227, v123\n"
"s_nop 0\n"
"v_add_f32 v233, v233, v227\n"
"v_cvt_pk_bf16_f32 v157, v226, v227\n"
"v_exp_f32 v228, v124\n"
"s_nop 0\n"
"v_add_f32 v233, v233, v228\n"
"v_exp_f32 v229, v125\n"
"s_nop 0\n"
"v_add_f32 v233, v233, v229\n"
"v_cvt_pk_bf16_f32 v158, v228, v229\n"
"v_exp_f32 v230, v126\n"
"s_nop 0\n"
"v_add_f32 v233, v233, v230\n"
"v_exp_f32 v231, v127\n"
"s_nop 0\n"
"v_add_f32 v233, v233, v231\n"
"v_cvt_pk_bf16_f32 v159, v230, v231\n"
"v_add_f32 v236, v232, v233\n"
"s_nop 1\n"
"s_branch Lfr67_%=\n"
"Lfb105_%=:\n"
"s_nop 7\n"
"s_nop 7\n"
"v_max3_f32 v240, v96, v97, v98\n"
"v_max3_f32 v240, v240, v99, v100\n"
"v_max3_f32 v240, v240, v101, v102\n"
"v_max3_f32 v240, v240, v103, v104\n"
"v_max3_f32 v240, v240, v105, v106\n"
"v_max3_f32 v240, v240, v107, v108\n"
"v_max3_f32 v240, v240, v109, v110\n"
"v_max_f32 v240, v240, v111\n"
"v_mov_b32 v244, v240\n"
"s_nop 1\n"
"v_permlane32_swap_b32 v240, v244\n"
"v_max_f32 v240, v240, v244\n"
"v_max_f32 v240, 0, v240\n"
"v_sub_f32 v244, 0, v240\n"
"v_exp_f32 v242, v244\n"
"s_nop 0\n"
"v_mul_f32 v234, v234, v242\n"
"v_sub_f32 v160, v160, v240\n"
"v_sub_f32 v161, v161, v240\n"
"v_sub_f32 v162, v162, v240\n"
"v_sub_f32 v163, v163, v240\n"
"v_sub_f32 v164, v164, v240\n"
"v_sub_f32 v165, v165, v240\n"
"v_sub_f32 v166, v166, v240\n"
"v_sub_f32 v167, v167, v240\n"
"v_sub_f32 v168, v168, v240\n"
"v_sub_f32 v169, v169, v240\n"
"v_sub_f32 v170, v170, v240\n"
"v_sub_f32 v171, v171, v240\n"
"v_sub_f32 v172, v172, v240\n"
"v_sub_f32 v173, v173, v240\n"
"v_sub_f32 v174, v174, v240\n"
"v_sub_f32 v175, v175, v240\n"
"v_accvgpr_read_b32 v244, a0\n"
"v_mul_f32 v244, v244, v242\n"
"v_accvgpr_write_b32 a0, v244\n"
"v_accvgpr_read_b32 v245, a1\n"
"v_mul_f32 v245, v245, v242\n"
"v_accvgpr_write_b32 a1, v245\n"
"v_accvgpr_read_b32 v244, a2\n"
"v_mul_f32 v244, v244, v242\n"
"v_accvgpr_write_b32 a2, v244\n"
"v_accvgpr_read_b32 v245, a3\n"
"v_mul_f32 v245, v245, v242\n"
"v_accvgpr_write_b32 a3, v245\n"
"v_accvgpr_read_b32 v244, a4\n"
"v_mul_f32 v244, v244, v242\n"
"v_accvgpr_write_b32 a4, v244\n"
"v_accvgpr_read_b32 v245, a5\n"
"v_mul_f32 v245, v245, v242\n"
"v_accvgpr_write_b32 a5, v245\n"
"v_accvgpr_read_b32 v244, a6\n"
"v_mul_f32 v244, v244, v242\n"
"v_accvgpr_write_b32 a6, v244\n"
"v_accvgpr_read_b32 v245, a7\n"
"v_mul_f32 v245, v245, v242\n"
"v_accvgpr_write_b32 a7, v245\n"
"v_accvgpr_read_b32 v244, a8\n"
"v_mul_f32 v244, v244, v242\n"
"v_accvgpr_write_b32 a8, v244\n"
"v_accvgpr_read_b32 v245, a9\n"
"v_mul_f32 v245, v245, v242\n"
"v_accvgpr_write_b32 a9, v245\n"
"v_accvgpr_read_b32 v244, a10\n"
"v_mul_f32 v244, v244, v242\n"
"v_accvgpr_write_b32 a10, v244\n"
"v_accvgpr_read_b32 v245, a11\n"
"v_mul_f32 v245, v245, v242\n"
"v_accvgpr_write_b32 a11, v245\n"
"v_accvgpr_read_b32 v244, a12\n"
"v_mul_f32 v244, v244, v242\n"
"v_accvgpr_write_b32 a12, v244\n"
"v_accvgpr_read_b32 v245, a13\n"
"v_mul_f32 v245, v245, v242\n"
"v_accvgpr_write_b32 a13, v245\n"
"v_accvgpr_read_b32 v244, a14\n"
"v_mul_f32 v244, v244, v242\n"
"v_accvgpr_write_b32 a14, v244\n"
"v_accvgpr_read_b32 v245, a15\n"
"v_mul_f32 v245, v245, v242\n"
"v_accvgpr_write_b32 a15, v245\n"
"v_accvgpr_read_b32 v244, a16\n"
"v_mul_f32 v244, v244, v242\n"
"v_accvgpr_write_b32 a16, v244\n"
"v_accvgpr_read_b32 v245, a17\n"
"v_mul_f32 v245, v245, v242\n"
"v_accvgpr_write_b32 a17, v245\n"
"v_accvgpr_read_b32 v244, a18\n"
"v_mul_f32 v244, v244, v242\n"
"v_accvgpr_write_b32 a18, v244\n"
"v_accvgpr_read_b32 v245, a19\n"
"v_mul_f32 v245, v245, v242\n"
"v_accvgpr_write_b32 a19, v245\n"
"v_accvgpr_read_b32 v244, a20\n"
"v_mul_f32 v244, v244, v242\n"
"v_accvgpr_write_b32 a20, v244\n"
"v_accvgpr_read_b32 v245, a21\n"
"v_mul_f32 v245, v245, v242\n"
"v_accvgpr_write_b32 a21, v245\n"
"v_accvgpr_read_b32 v244, a22\n"
"v_mul_f32 v244, v244, v242\n"
"v_accvgpr_write_b32 a22, v244\n"
"v_accvgpr_read_b32 v245, a23\n"
"v_mul_f32 v245, v245, v242\n"
"v_accvgpr_write_b32 a23, v245\n"
"v_accvgpr_read_b32 v244, a24\n"
"v_mul_f32 v244, v244, v242\n"
"v_accvgpr_write_b32 a24, v244\n"
"v_accvgpr_read_b32 v245, a25\n"
"v_mul_f32 v245, v245, v242\n"
"v_accvgpr_write_b32 a25, v245\n"
"v_accvgpr_read_b32 v244, a26\n"
"v_mul_f32 v244, v244, v242\n"
"v_accvgpr_write_b32 a26, v244\n"
"v_accvgpr_read_b32 v245, a27\n"
"v_mul_f32 v245, v245, v242\n"
"v_accvgpr_write_b32 a27, v245\n"
"v_accvgpr_read_b32 v244, a28\n"
"v_mul_f32 v244, v244, v242\n"
"v_accvgpr_write_b32 a28, v244\n"
"v_accvgpr_read_b32 v245, a29\n"
"v_mul_f32 v245, v245, v242\n"
"v_accvgpr_write_b32 a29, v245\n"
"v_accvgpr_read_b32 v244, a30\n"
"v_mul_f32 v244, v244, v242\n"
"v_accvgpr_write_b32 a30, v244\n"
"v_accvgpr_read_b32 v245, a31\n"
"v_mul_f32 v245, v245, v242\n"
"v_accvgpr_write_b32 a31, v245\n"
"v_accvgpr_read_b32 v244, a32\n"
"v_mul_f32 v244, v244, v242\n"
"v_accvgpr_write_b32 a32, v244\n"
"v_accvgpr_read_b32 v245, a33\n"
"v_mul_f32 v245, v245, v242\n"
"v_accvgpr_write_b32 a33, v245\n"
"v_accvgpr_read_b32 v244, a34\n"
"v_mul_f32 v244, v244, v242\n"
"v_accvgpr_write_b32 a34, v244\n"
"v_accvgpr_read_b32 v245, a35\n"
"v_mul_f32 v245, v245, v242\n"
"v_accvgpr_write_b32 a35, v245\n"
"v_accvgpr_read_b32 v244, a36\n"
"v_mul_f32 v244, v244, v242\n"
"v_accvgpr_write_b32 a36, v244\n"
"v_accvgpr_read_b32 v245, a37\n"
"v_mul_f32 v245, v245, v242\n"
"v_accvgpr_write_b32 a37, v245\n"
"v_accvgpr_read_b32 v244, a38\n"
"v_mul_f32 v244, v244, v242\n"
"v_accvgpr_write_b32 a38, v244\n"
"v_accvgpr_read_b32 v245, a39\n"
"v_mul_f32 v245, v245, v242\n"
"v_accvgpr_write_b32 a39, v245\n"
"v_accvgpr_read_b32 v244, a40\n"
"v_mul_f32 v244, v244, v242\n"
"v_accvgpr_write_b32 a40, v244\n"
"v_accvgpr_read_b32 v245, a41\n"
"v_mul_f32 v245, v245, v242\n"
"v_accvgpr_write_b32 a41, v245\n"
"v_accvgpr_read_b32 v244, a42\n"
"v_mul_f32 v244, v244, v242\n"
"v_accvgpr_write_b32 a42, v244\n"
"v_accvgpr_read_b32 v245, a43\n"
"v_mul_f32 v245, v245, v242\n"
"v_accvgpr_write_b32 a43, v245\n"
"v_accvgpr_read_b32 v244, a44\n"
"v_mul_f32 v244, v244, v242\n"
"v_accvgpr_write_b32 a44, v244\n"
"v_accvgpr_read_b32 v245, a45\n"
"v_mul_f32 v245, v245, v242\n"
"v_accvgpr_write_b32 a45, v245\n"
"v_accvgpr_read_b32 v244, a46\n"
"v_mul_f32 v244, v244, v242\n"
"v_accvgpr_write_b32 a46, v244\n"
"v_accvgpr_read_b32 v245, a47\n"
"v_mul_f32 v245, v245, v242\n"
"v_accvgpr_write_b32 a47, v245\n"
"v_accvgpr_read_b32 v244, a48\n"
"v_mul_f32 v244, v244, v242\n"
"v_accvgpr_write_b32 a48, v244\n"
"v_accvgpr_read_b32 v245, a49\n"
"v_mul_f32 v245, v245, v242\n"
"v_accvgpr_write_b32 a49, v245\n"
"v_accvgpr_read_b32 v244, a50\n"
"v_mul_f32 v244, v244, v242\n"
"v_accvgpr_write_b32 a50, v244\n"
"v_accvgpr_read_b32 v245, a51\n"
"v_mul_f32 v245, v245, v242\n"
"v_accvgpr_write_b32 a51, v245\n"
"v_accvgpr_read_b32 v244, a52\n"
"v_mul_f32 v244, v244, v242\n"
"v_accvgpr_write_b32 a52, v244\n"
"v_accvgpr_read_b32 v245, a53\n"
"v_mul_f32 v245, v245, v242\n"
"v_accvgpr_write_b32 a53, v245\n"
"v_accvgpr_read_b32 v244, a54\n"
"v_mul_f32 v244, v244, v242\n"
"v_accvgpr_write_b32 a54, v244\n"
"v_accvgpr_read_b32 v245, a55\n"
"v_mul_f32 v245, v245, v242\n"
"v_accvgpr_write_b32 a55, v245\n"
"v_accvgpr_read_b32 v244, a56\n"
"v_mul_f32 v244, v244, v242\n"
"v_accvgpr_write_b32 a56, v244\n"
"v_accvgpr_read_b32 v245, a57\n"
"v_mul_f32 v245, v245, v242\n"
"v_accvgpr_write_b32 a57, v245\n"
"v_accvgpr_read_b32 v244, a58\n"
"v_mul_f32 v244, v244, v242\n"
"v_accvgpr_write_b32 a58, v244\n"
"v_accvgpr_read_b32 v245, a59\n"
"v_mul_f32 v245, v245, v242\n"
"v_accvgpr_write_b32 a59, v245\n"
"v_accvgpr_read_b32 v244, a60\n"
"v_mul_f32 v244, v244, v242\n"
"v_accvgpr_write_b32 a60, v244\n"
"v_accvgpr_read_b32 v245, a61\n"
"v_mul_f32 v245, v245, v242\n"
"v_accvgpr_write_b32 a61, v245\n"
"v_accvgpr_read_b32 v244, a62\n"
"v_mul_f32 v244, v244, v242\n"
"v_accvgpr_write_b32 a62, v244\n"
"v_accvgpr_read_b32 v245, a63\n"
"v_mul_f32 v245, v245, v242\n"
"v_accvgpr_write_b32 a63, v245\n"
"v_max3_f32 v241, v112, v113, v114\n"
"v_max3_f32 v241, v241, v115, v116\n"
"v_max3_f32 v241, v241, v117, v118\n"
"v_max3_f32 v241, v241, v119, v120\n"
"v_max3_f32 v241, v241, v121, v122\n"
"v_max3_f32 v241, v241, v123, v124\n"
"v_max3_f32 v241, v241, v125, v126\n"
"v_max_f32 v241, v241, v127\n"
"v_mov_b32 v244, v241\n"
"s_nop 1\n"
"v_permlane32_swap_b32 v241, v244\n"
"v_max_f32 v241, v241, v244\n"
"v_max_f32 v241, 0, v241\n"
"v_sub_f32 v244, 0, v241\n"
"v_exp_f32 v243, v244\n"
"s_nop 0\n"
"v_mul_f32 v235, v235, v243\n"
"v_sub_f32 v176, v176, v241\n"
"v_sub_f32 v177, v177, v241\n"
"v_sub_f32 v178, v178, v241\n"
"v_sub_f32 v179, v179, v241\n"
"v_sub_f32 v180, v180, v241\n"
"v_sub_f32 v181, v181, v241\n"
"v_sub_f32 v182, v182, v241\n"
"v_sub_f32 v183, v183, v241\n"
"v_sub_f32 v184, v184, v241\n"
"v_sub_f32 v185, v185, v241\n"
"v_sub_f32 v186, v186, v241\n"
"v_sub_f32 v187, v187, v241\n"
"v_sub_f32 v188, v188, v241\n"
"v_sub_f32 v189, v189, v241\n"
"v_sub_f32 v190, v190, v241\n"
"v_sub_f32 v191, v191, v241\n"
"v_accvgpr_read_b32 v244, a64\n"
"v_mul_f32 v244, v244, v243\n"
"v_accvgpr_write_b32 a64, v244\n"
"v_accvgpr_read_b32 v245, a65\n"
"v_mul_f32 v245, v245, v243\n"
"v_accvgpr_write_b32 a65, v245\n"
"v_accvgpr_read_b32 v244, a66\n"
"v_mul_f32 v244, v244, v243\n"
"v_accvgpr_write_b32 a66, v244\n"
"v_accvgpr_read_b32 v245, a67\n"
"v_mul_f32 v245, v245, v243\n"
"v_accvgpr_write_b32 a67, v245\n"
"v_accvgpr_read_b32 v244, a68\n"
"v_mul_f32 v244, v244, v243\n"
"v_accvgpr_write_b32 a68, v244\n"
"v_accvgpr_read_b32 v245, a69\n"
"v_mul_f32 v245, v245, v243\n"
"v_accvgpr_write_b32 a69, v245\n"
"v_accvgpr_read_b32 v244, a70\n"
"v_mul_f32 v244, v244, v243\n"
"v_accvgpr_write_b32 a70, v244\n"
"v_accvgpr_read_b32 v245, a71\n"
"v_mul_f32 v245, v245, v243\n"
"v_accvgpr_write_b32 a71, v245\n"
"v_accvgpr_read_b32 v244, a72\n"
"v_mul_f32 v244, v244, v243\n"
"v_accvgpr_write_b32 a72, v244\n"
"v_accvgpr_read_b32 v245, a73\n"
"v_mul_f32 v245, v245, v243\n"
"v_accvgpr_write_b32 a73, v245\n"
"v_accvgpr_read_b32 v244, a74\n"
"v_mul_f32 v244, v244, v243\n"
"v_accvgpr_write_b32 a74, v244\n"
"v_accvgpr_read_b32 v245, a75\n"
"v_mul_f32 v245, v245, v243\n"
"v_accvgpr_write_b32 a75, v245\n"
"v_accvgpr_read_b32 v244, a76\n"
"v_mul_f32 v244, v244, v243\n"
"v_accvgpr_write_b32 a76, v244\n"
"v_accvgpr_read_b32 v245, a77\n"
"v_mul_f32 v245, v245, v243\n"
"v_accvgpr_write_b32 a77, v245\n"
"v_accvgpr_read_b32 v244, a78\n"
"v_mul_f32 v244, v244, v243\n"
"v_accvgpr_write_b32 a78, v244\n"
"v_accvgpr_read_b32 v245, a79\n"
"v_mul_f32 v245, v245, v243\n"
"v_accvgpr_write_b32 a79, v245\n"
"v_accvgpr_read_b32 v244, a80\n"
"v_mul_f32 v244, v244, v243\n"
"v_accvgpr_write_b32 a80, v244\n"
"v_accvgpr_read_b32 v245, a81\n"
"v_mul_f32 v245, v245, v243\n"
"v_accvgpr_write_b32 a81, v245\n"
"v_accvgpr_read_b32 v244, a82\n"
"v_mul_f32 v244, v244, v243\n"
"v_accvgpr_write_b32 a82, v244\n"
"v_accvgpr_read_b32 v245, a83\n"
"v_mul_f32 v245, v245, v243\n"
"v_accvgpr_write_b32 a83, v245\n"
"v_accvgpr_read_b32 v244, a84\n"
"v_mul_f32 v244, v244, v243\n"
"v_accvgpr_write_b32 a84, v244\n"
"v_accvgpr_read_b32 v245, a85\n"
"v_mul_f32 v245, v245, v243\n"
"v_accvgpr_write_b32 a85, v245\n"
"v_accvgpr_read_b32 v244, a86\n"
"v_mul_f32 v244, v244, v243\n"
"v_accvgpr_write_b32 a86, v244\n"
"v_accvgpr_read_b32 v245, a87\n"
"v_mul_f32 v245, v245, v243\n"
"v_accvgpr_write_b32 a87, v245\n"
"v_accvgpr_read_b32 v244, a88\n"
"v_mul_f32 v244, v244, v243\n"
"v_accvgpr_write_b32 a88, v244\n"
"v_accvgpr_read_b32 v245, a89\n"
"v_mul_f32 v245, v245, v243\n"
"v_accvgpr_write_b32 a89, v245\n"
"v_accvgpr_read_b32 v244, a90\n"
"v_mul_f32 v244, v244, v243\n"
"v_accvgpr_write_b32 a90, v244\n"
"v_accvgpr_read_b32 v245, a91\n"
"v_mul_f32 v245, v245, v243\n"
"v_accvgpr_write_b32 a91, v245\n"
"v_accvgpr_read_b32 v244, a92\n"
"v_mul_f32 v244, v244, v243\n"
"v_accvgpr_write_b32 a92, v244\n"
"v_accvgpr_read_b32 v245, a93\n"
"v_mul_f32 v245, v245, v243\n"
"v_accvgpr_write_b32 a93, v245\n"
"v_accvgpr_read_b32 v244, a94\n"
"v_mul_f32 v244, v244, v243\n"
"v_accvgpr_write_b32 a94, v244\n"
"v_accvgpr_read_b32 v245, a95\n"
"v_mul_f32 v245, v245, v243\n"
"v_accvgpr_write_b32 a95, v245\n"
"v_accvgpr_read_b32 v244, a96\n"
"v_mul_f32 v244, v244, v243\n"
"v_accvgpr_write_b32 a96, v244\n"
"v_accvgpr_read_b32 v245, a97\n"
"v_mul_f32 v245, v245, v243\n"
"v_accvgpr_write_b32 a97, v245\n"
"v_accvgpr_read_b32 v244, a98\n"
"v_mul_f32 v244, v244, v243\n"
"v_accvgpr_write_b32 a98, v244\n"
"v_accvgpr_read_b32 v245, a99\n"
"v_mul_f32 v245, v245, v243\n"
"v_accvgpr_write_b32 a99, v245\n"
"v_accvgpr_read_b32 v244, a100\n"
"v_mul_f32 v244, v244, v243\n"
"v_accvgpr_write_b32 a100, v244\n"
"v_accvgpr_read_b32 v245, a101\n"
"v_mul_f32 v245, v245, v243\n"
"v_accvgpr_write_b32 a101, v245\n"
"v_accvgpr_read_b32 v244, a102\n"
"v_mul_f32 v244, v244, v243\n"
"v_accvgpr_write_b32 a102, v244\n"
"v_accvgpr_read_b32 v245, a103\n"
"v_mul_f32 v245, v245, v243\n"
"v_accvgpr_write_b32 a103, v245\n"
"v_accvgpr_read_b32 v244, a104\n"
"v_mul_f32 v244, v244, v243\n"
"v_accvgpr_write_b32 a104, v244\n"
"v_accvgpr_read_b32 v245, a105\n"
"v_mul_f32 v245, v245, v243\n"
"v_accvgpr_write_b32 a105, v245\n"
"v_accvgpr_read_b32 v244, a106\n"
"v_mul_f32 v244, v244, v243\n"
"v_accvgpr_write_b32 a106, v244\n"
"v_accvgpr_read_b32 v245, a107\n"
"v_mul_f32 v245, v245, v243\n"
"v_accvgpr_write_b32 a107, v245\n"
"v_accvgpr_read_b32 v244, a108\n"
"v_mul_f32 v244, v244, v243\n"
"v_accvgpr_write_b32 a108, v244\n"
"v_accvgpr_read_b32 v245, a109\n"
"v_mul_f32 v245, v245, v243\n"
"v_accvgpr_write_b32 a109, v245\n"
"v_accvgpr_read_b32 v244, a110\n"
"v_mul_f32 v244, v244, v243\n"
"v_accvgpr_write_b32 a110, v244\n"
"v_accvgpr_read_b32 v245, a111\n"
"v_mul_f32 v245, v245, v243\n"
"v_accvgpr_write_b32 a111, v245\n"
"v_accvgpr_read_b32 v244, a112\n"
"v_mul_f32 v244, v244, v243\n"
"v_accvgpr_write_b32 a112, v244\n"
"v_accvgpr_read_b32 v245, a113\n"
"v_mul_f32 v245, v245, v243\n"
"v_accvgpr_write_b32 a113, v245\n"
"v_accvgpr_read_b32 v244, a114\n"
"v_mul_f32 v244, v244, v243\n"
"v_accvgpr_write_b32 a114, v244\n"
"v_accvgpr_read_b32 v245, a115\n"
"v_mul_f32 v245, v245, v243\n"
"v_accvgpr_write_b32 a115, v245\n"
"v_accvgpr_read_b32 v244, a116\n"
"v_mul_f32 v244, v244, v243\n"
"v_accvgpr_write_b32 a116, v244\n"
"v_accvgpr_read_b32 v245, a117\n"
"v_mul_f32 v245, v245, v243\n"
"v_accvgpr_write_b32 a117, v245\n"
"v_accvgpr_read_b32 v244, a118\n"
"v_mul_f32 v244, v244, v243\n"
"v_accvgpr_write_b32 a118, v244\n"
"v_accvgpr_read_b32 v245, a119\n"
"v_mul_f32 v245, v245, v243\n"
"v_accvgpr_write_b32 a119, v245\n"
"v_accvgpr_read_b32 v244, a120\n"
"v_mul_f32 v244, v244, v243\n"
"v_accvgpr_write_b32 a120, v244\n"
"v_accvgpr_read_b32 v245, a121\n"
"v_mul_f32 v245, v245, v243\n"
"v_accvgpr_write_b32 a121, v245\n"
"v_accvgpr_read_b32 v244, a122\n"
"v_mul_f32 v244, v244, v243\n"
"v_accvgpr_write_b32 a122, v244\n"
"v_accvgpr_read_b32 v245, a123\n"
"v_mul_f32 v245, v245, v243\n"
"v_accvgpr_write_b32 a123, v245\n"
"v_accvgpr_read_b32 v244, a124\n"
"v_mul_f32 v244, v244, v243\n"
"v_accvgpr_write_b32 a124, v244\n"
"v_accvgpr_read_b32 v245, a125\n"
"v_mul_f32 v245, v245, v243\n"
"v_accvgpr_write_b32 a125, v245\n"
"v_accvgpr_read_b32 v244, a126\n"
"v_mul_f32 v244, v244, v243\n"
"v_accvgpr_write_b32 a126, v244\n"
"v_accvgpr_read_b32 v245, a127\n"
"v_mul_f32 v245, v245, v243\n"
"v_accvgpr_write_b32 a127, v245\n"
"v_sub_f32 v96, v96, v240\n"
"v_sub_f32 v97, v97, v240\n"
"v_sub_f32 v98, v98, v240\n"
"v_sub_f32 v99, v99, v240\n"
"v_sub_f32 v100, v100, v240\n"
"v_sub_f32 v101, v101, v240\n"
"v_sub_f32 v102, v102, v240\n"
"v_sub_f32 v103, v103, v240\n"
"v_sub_f32 v104, v104, v240\n"
"v_sub_f32 v105, v105, v240\n"
"v_sub_f32 v106, v106, v240\n"
"v_sub_f32 v107, v107, v240\n"
"v_sub_f32 v108, v108, v240\n"
"v_sub_f32 v109, v109, v240\n"
"v_sub_f32 v110, v110, v240\n"
"v_sub_f32 v111, v111, v240\n"
"v_sub_f32 v112, v112, v241\n"
"v_sub_f32 v113, v113, v241\n"
"v_sub_f32 v114, v114, v241\n"
"v_sub_f32 v115, v115, v241\n"
"v_sub_f32 v116, v116, v241\n"
"v_sub_f32 v117, v117, v241\n"
"v_sub_f32 v118, v118, v241\n"
"v_sub_f32 v119, v119, v241\n"
"v_sub_f32 v120, v120, v241\n"
"v_sub_f32 v121, v121, v241\n"
"v_sub_f32 v122, v122, v241\n"
"v_sub_f32 v123, v123, v241\n"
"v_sub_f32 v124, v124, v241\n"
"v_sub_f32 v125, v125, v241\n"
"v_sub_f32 v126, v126, v241\n"
"v_sub_f32 v127, v127, v241\n"
"v_sub_f32 v64, v64, v240\n"
"v_sub_f32 v65, v65, v240\n"
"v_sub_f32 v66, v66, v240\n"
"v_sub_f32 v67, v67, v240\n"
"v_sub_f32 v68, v68, v240\n"
"v_sub_f32 v69, v69, v240\n"
"v_sub_f32 v70, v70, v240\n"
"v_sub_f32 v71, v71, v240\n"
"v_sub_f32 v72, v72, v240\n"
"v_sub_f32 v73, v73, v240\n"
"v_sub_f32 v74, v74, v240\n"
"v_sub_f32 v75, v75, v240\n"
"v_sub_f32 v76, v76, v240\n"
"v_sub_f32 v77, v77, v240\n"
"v_sub_f32 v78, v78, v240\n"
"v_sub_f32 v79, v79, v240\n"
"v_sub_f32 v80, v80, v241\n"
"v_sub_f32 v81, v81, v241\n"
"v_sub_f32 v82, v82, v241\n"
"v_sub_f32 v83, v83, v241\n"
"v_sub_f32 v84, v84, v241\n"
"v_sub_f32 v85, v85, v241\n"
"v_sub_f32 v86, v86, v241\n"
"v_sub_f32 v87, v87, v241\n"
"v_sub_f32 v88, v88, v241\n"
"v_sub_f32 v89, v89, v241\n"
"v_sub_f32 v90, v90, v241\n"
"v_sub_f32 v91, v91, v241\n"
"v_sub_f32 v92, v92, v241\n"
"v_sub_f32 v93, v93, v241\n"
"v_sub_f32 v94, v94, v241\n"
"v_sub_f32 v95, v95, v241\n"
"v_exp_f32 v224, v96\n"
"s_nop 0\n"
"v_exp_f32 v225, v97\n"
"s_nop 0\n"
"v_add_f32 v232, v224, v225\n"
"v_cvt_pk_bf16_f32 v144, v224, v225\n"
"v_exp_f32 v226, v98\n"
"s_nop 0\n"
"v_add_f32 v232, v232, v226\n"
"v_exp_f32 v227, v99\n"
"s_nop 0\n"
"v_add_f32 v232, v232, v227\n"
"v_cvt_pk_bf16_f32 v145, v226, v227\n"
"v_exp_f32 v228, v100\n"
"s_nop 0\n"
"v_add_f32 v232, v232, v228\n"
"v_exp_f32 v229, v101\n"
"s_nop 0\n"
"v_add_f32 v232, v232, v229\n"
"v_cvt_pk_bf16_f32 v146, v228, v229\n"
"v_exp_f32 v230, v102\n"
"s_nop 0\n"
"v_add_f32 v232, v232, v230\n"
"v_exp_f32 v231, v103\n"
"s_nop 0\n"
"v_add_f32 v232, v232, v231\n"
"v_cvt_pk_bf16_f32 v147, v230, v231\n"
"v_exp_f32 v224, v104\n"
"s_nop 0\n"
"v_add_f32 v232, v232, v224\n"
"v_exp_f32 v225, v105\n"
"s_nop 0\n"
"v_add_f32 v232, v232, v225\n"
"v_cvt_pk_bf16_f32 v148, v224, v225\n"
"v_exp_f32 v226, v106\n"
"s_nop 0\n"
"v_add_f32 v232, v232, v226\n"
"v_exp_f32 v227, v107\n"
"s_nop 0\n"
"v_add_f32 v232, v232, v227\n"
"v_cvt_pk_bf16_f32 v149, v226, v227\n"
"v_exp_f32 v228, v108\n"
"s_nop 0\n"
"v_add_f32 v232, v232, v228\n"
"v_exp_f32 v229, v109\n"
"s_nop 0\n"
"v_add_f32 v232, v232, v229\n"
"v_cvt_pk_bf16_f32 v150, v228, v229\n"
"v_exp_f32 v230, v110\n"
"s_nop 0\n"
"v_add_f32 v232, v232, v230\n"
"v_exp_f32 v231, v111\n"
"s_nop 0\n"
"v_add_f32 v232, v232, v231\n"
"v_cvt_pk_bf16_f32 v151, v230, v231\n"
"v_exp_f32 v224, v112\n"
"s_nop 0\n"
"v_exp_f32 v225, v113\n"
"s_nop 0\n"
"v_add_f32 v233, v224, v225\n"
"v_cvt_pk_bf16_f32 v152, v224, v225\n"
"v_exp_f32 v226, v114\n"
"s_nop 0\n"
"v_add_f32 v233, v233, v226\n"
"v_exp_f32 v227, v115\n"
"s_nop 0\n"
"v_add_f32 v233, v233, v227\n"
"v_cvt_pk_bf16_f32 v153, v226, v227\n"
"v_exp_f32 v228, v116\n"
"s_nop 0\n"
"v_add_f32 v233, v233, v228\n"
"v_exp_f32 v229, v117\n"
"s_nop 0\n"
"v_add_f32 v233, v233, v229\n"
"v_cvt_pk_bf16_f32 v154, v228, v229\n"
"v_exp_f32 v230, v118\n"
"s_nop 0\n"
"v_add_f32 v233, v233, v230\n"
"v_exp_f32 v231, v119\n"
"s_nop 0\n"
"v_add_f32 v233, v233, v231\n"
"v_cvt_pk_bf16_f32 v155, v230, v231\n"
"v_exp_f32 v224, v120\n"
"s_nop 0\n"
"v_add_f32 v233, v233, v224\n"
"v_exp_f32 v225, v121\n"
"s_nop 0\n"
"v_add_f32 v233, v233, v225\n"
"v_cvt_pk_bf16_f32 v156, v224, v225\n"
"v_exp_f32 v226, v122\n"
"s_nop 0\n"
"v_add_f32 v233, v233, v226\n"
"v_exp_f32 v227, v123\n"
"s_nop 0\n"
"v_add_f32 v233, v233, v227\n"
"v_cvt_pk_bf16_f32 v157, v226, v227\n"
"v_exp_f32 v228, v124\n"
"s_nop 0\n"
"v_add_f32 v233, v233, v228\n"
"v_exp_f32 v229, v125\n"
"s_nop 0\n"
"v_add_f32 v233, v233, v229\n"
"v_cvt_pk_bf16_f32 v158, v228, v229\n"
"v_exp_f32 v230, v126\n"
"s_nop 0\n"
"v_add_f32 v233, v233, v230\n"
"v_exp_f32 v231, v127\n"
"s_nop 0\n"
"v_add_f32 v233, v233, v231\n"
"v_cvt_pk_bf16_f32 v159, v230, v231\n"
"v_add_f32 v236, v232, v233\n"
"s_nop 1\n"
"s_branch Lfr106_%=\n"
"Lfb107_%=:\n"
"s_nop 7\n"
"s_nop 7\n"
"v_max3_f32 v240, v64, v65, v66\n"
"v_max3_f32 v240, v240, v67, v68\n"
"v_max3_f32 v240, v240, v69, v70\n"
"v_max3_f32 v240, v240, v71, v72\n"
"v_max3_f32 v240, v240, v73, v74\n"
"v_max3_f32 v240, v240, v75, v76\n"
"v_max3_f32 v240, v240, v77, v78\n"
"v_max_f32 v240, v240, v79\n"
"v_mov_b32 v244, v240\n"
"s_nop 1\n"
"v_permlane32_swap_b32 v240, v244\n"
"v_max_f32 v240, v240, v244\n"
"v_max_f32 v240, 0, v240\n"
"v_sub_f32 v244, 0, v240\n"
"v_exp_f32 v242, v244\n"
"s_nop 0\n"
"v_mul_f32 v234, v234, v242\n"
"v_sub_f32 v160, v160, v240\n"
"v_sub_f32 v161, v161, v240\n"
"v_sub_f32 v162, v162, v240\n"
"v_sub_f32 v163, v163, v240\n"
"v_sub_f32 v164, v164, v240\n"
"v_sub_f32 v165, v165, v240\n"
"v_sub_f32 v166, v166, v240\n"
"v_sub_f32 v167, v167, v240\n"
"v_sub_f32 v168, v168, v240\n"
"v_sub_f32 v169, v169, v240\n"
"v_sub_f32 v170, v170, v240\n"
"v_sub_f32 v171, v171, v240\n"
"v_sub_f32 v172, v172, v240\n"
"v_sub_f32 v173, v173, v240\n"
"v_sub_f32 v174, v174, v240\n"
"v_sub_f32 v175, v175, v240\n"
"v_accvgpr_read_b32 v244, a0\n"
"v_mul_f32 v244, v244, v242\n"
"v_accvgpr_write_b32 a0, v244\n"
"v_accvgpr_read_b32 v245, a1\n"
"v_mul_f32 v245, v245, v242\n"
"v_accvgpr_write_b32 a1, v245\n"
"v_accvgpr_read_b32 v244, a2\n"
"v_mul_f32 v244, v244, v242\n"
"v_accvgpr_write_b32 a2, v244\n"
"v_accvgpr_read_b32 v245, a3\n"
"v_mul_f32 v245, v245, v242\n"
"v_accvgpr_write_b32 a3, v245\n"
"v_accvgpr_read_b32 v244, a4\n"
"v_mul_f32 v244, v244, v242\n"
"v_accvgpr_write_b32 a4, v244\n"
"v_accvgpr_read_b32 v245, a5\n"
"v_mul_f32 v245, v245, v242\n"
"v_accvgpr_write_b32 a5, v245\n"
"v_accvgpr_read_b32 v244, a6\n"
"v_mul_f32 v244, v244, v242\n"
"v_accvgpr_write_b32 a6, v244\n"
"v_accvgpr_read_b32 v245, a7\n"
"v_mul_f32 v245, v245, v242\n"
"v_accvgpr_write_b32 a7, v245\n"
"v_accvgpr_read_b32 v244, a8\n"
"v_mul_f32 v244, v244, v242\n"
"v_accvgpr_write_b32 a8, v244\n"
"v_accvgpr_read_b32 v245, a9\n"
"v_mul_f32 v245, v245, v242\n"
"v_accvgpr_write_b32 a9, v245\n"
"v_accvgpr_read_b32 v244, a10\n"
"v_mul_f32 v244, v244, v242\n"
"v_accvgpr_write_b32 a10, v244\n"
"v_accvgpr_read_b32 v245, a11\n"
"v_mul_f32 v245, v245, v242\n"
"v_accvgpr_write_b32 a11, v245\n"
"v_accvgpr_read_b32 v244, a12\n"
"v_mul_f32 v244, v244, v242\n"
"v_accvgpr_write_b32 a12, v244\n"
"v_accvgpr_read_b32 v245, a13\n"
"v_mul_f32 v245, v245, v242\n"
"v_accvgpr_write_b32 a13, v245\n"
"v_accvgpr_read_b32 v244, a14\n"
"v_mul_f32 v244, v244, v242\n"
"v_accvgpr_write_b32 a14, v244\n"
"v_accvgpr_read_b32 v245, a15\n"
"v_mul_f32 v245, v245, v242\n"
"v_accvgpr_write_b32 a15, v245\n"
"v_accvgpr_read_b32 v244, a16\n"
"v_mul_f32 v244, v244, v242\n"
"v_accvgpr_write_b32 a16, v244\n"
"v_accvgpr_read_b32 v245, a17\n"
"v_mul_f32 v245, v245, v242\n"
"v_accvgpr_write_b32 a17, v245\n"
"v_accvgpr_read_b32 v244, a18\n"
"v_mul_f32 v244, v244, v242\n"
"v_accvgpr_write_b32 a18, v244\n"
"v_accvgpr_read_b32 v245, a19\n"
"v_mul_f32 v245, v245, v242\n"
"v_accvgpr_write_b32 a19, v245\n"
"v_accvgpr_read_b32 v244, a20\n"
"v_mul_f32 v244, v244, v242\n"
"v_accvgpr_write_b32 a20, v244\n"
"v_accvgpr_read_b32 v245, a21\n"
"v_mul_f32 v245, v245, v242\n"
"v_accvgpr_write_b32 a21, v245\n"
"v_accvgpr_read_b32 v244, a22\n"
"v_mul_f32 v244, v244, v242\n"
"v_accvgpr_write_b32 a22, v244\n"
"v_accvgpr_read_b32 v245, a23\n"
"v_mul_f32 v245, v245, v242\n"
"v_accvgpr_write_b32 a23, v245\n"
"v_accvgpr_read_b32 v244, a24\n"
"v_mul_f32 v244, v244, v242\n"
"v_accvgpr_write_b32 a24, v244\n"
"v_accvgpr_read_b32 v245, a25\n"
"v_mul_f32 v245, v245, v242\n"
"v_accvgpr_write_b32 a25, v245\n"
"v_accvgpr_read_b32 v244, a26\n"
"v_mul_f32 v244, v244, v242\n"
"v_accvgpr_write_b32 a26, v244\n"
"v_accvgpr_read_b32 v245, a27\n"
"v_mul_f32 v245, v245, v242\n"
"v_accvgpr_write_b32 a27, v245\n"
"v_accvgpr_read_b32 v244, a28\n"
"v_mul_f32 v244, v244, v242\n"
"v_accvgpr_write_b32 a28, v244\n"
"v_accvgpr_read_b32 v245, a29\n"
"v_mul_f32 v245, v245, v242\n"
"v_accvgpr_write_b32 a29, v245\n"
"v_accvgpr_read_b32 v244, a30\n"
"v_mul_f32 v244, v244, v242\n"
"v_accvgpr_write_b32 a30, v244\n"
"v_accvgpr_read_b32 v245, a31\n"
"v_mul_f32 v245, v245, v242\n"
"v_accvgpr_write_b32 a31, v245\n"
"v_accvgpr_read_b32 v244, a32\n"
"v_mul_f32 v244, v244, v242\n"
"v_accvgpr_write_b32 a32, v244\n"
"v_accvgpr_read_b32 v245, a33\n"
"v_mul_f32 v245, v245, v242\n"
"v_accvgpr_write_b32 a33, v245\n"
"v_accvgpr_read_b32 v244, a34\n"
"v_mul_f32 v244, v244, v242\n"
"v_accvgpr_write_b32 a34, v244\n"
"v_accvgpr_read_b32 v245, a35\n"
"v_mul_f32 v245, v245, v242\n"
"v_accvgpr_write_b32 a35, v245\n"
"v_accvgpr_read_b32 v244, a36\n"
"v_mul_f32 v244, v244, v242\n"
"v_accvgpr_write_b32 a36, v244\n"
"v_accvgpr_read_b32 v245, a37\n"
"v_mul_f32 v245, v245, v242\n"
"v_accvgpr_write_b32 a37, v245\n"
"v_accvgpr_read_b32 v244, a38\n"
"v_mul_f32 v244, v244, v242\n"
"v_accvgpr_write_b32 a38, v244\n"
"v_accvgpr_read_b32 v245, a39\n"
"v_mul_f32 v245, v245, v242\n"
"v_accvgpr_write_b32 a39, v245\n"
"v_accvgpr_read_b32 v244, a40\n"
"v_mul_f32 v244, v244, v242\n"
"v_accvgpr_write_b32 a40, v244\n"
"v_accvgpr_read_b32 v245, a41\n"
"v_mul_f32 v245, v245, v242\n"
"v_accvgpr_write_b32 a41, v245\n"
"v_accvgpr_read_b32 v244, a42\n"
"v_mul_f32 v244, v244, v242\n"
"v_accvgpr_write_b32 a42, v244\n"
"v_accvgpr_read_b32 v245, a43\n"
"v_mul_f32 v245, v245, v242\n"
"v_accvgpr_write_b32 a43, v245\n"
"v_accvgpr_read_b32 v244, a44\n"
"v_mul_f32 v244, v244, v242\n"
"v_accvgpr_write_b32 a44, v244\n"
"v_accvgpr_read_b32 v245, a45\n"
"v_mul_f32 v245, v245, v242\n"
"v_accvgpr_write_b32 a45, v245\n"
"v_accvgpr_read_b32 v244, a46\n"
"v_mul_f32 v244, v244, v242\n"
"v_accvgpr_write_b32 a46, v244\n"
"v_accvgpr_read_b32 v245, a47\n"
"v_mul_f32 v245, v245, v242\n"
"v_accvgpr_write_b32 a47, v245\n"
"v_accvgpr_read_b32 v244, a48\n"
"v_mul_f32 v244, v244, v242\n"
"v_accvgpr_write_b32 a48, v244\n"
"v_accvgpr_read_b32 v245, a49\n"
"v_mul_f32 v245, v245, v242\n"
"v_accvgpr_write_b32 a49, v245\n"
"v_accvgpr_read_b32 v244, a50\n"
"v_mul_f32 v244, v244, v242\n"
"v_accvgpr_write_b32 a50, v244\n"
"v_accvgpr_read_b32 v245, a51\n"
"v_mul_f32 v245, v245, v242\n"
"v_accvgpr_write_b32 a51, v245\n"
"v_accvgpr_read_b32 v244, a52\n"
"v_mul_f32 v244, v244, v242\n"
"v_accvgpr_write_b32 a52, v244\n"
"v_accvgpr_read_b32 v245, a53\n"
"v_mul_f32 v245, v245, v242\n"
"v_accvgpr_write_b32 a53, v245\n"
"v_accvgpr_read_b32 v244, a54\n"
"v_mul_f32 v244, v244, v242\n"
"v_accvgpr_write_b32 a54, v244\n"
"v_accvgpr_read_b32 v245, a55\n"
"v_mul_f32 v245, v245, v242\n"
"v_accvgpr_write_b32 a55, v245\n"
"v_accvgpr_read_b32 v244, a56\n"
"v_mul_f32 v244, v244, v242\n"
"v_accvgpr_write_b32 a56, v244\n"
"v_accvgpr_read_b32 v245, a57\n"
"v_mul_f32 v245, v245, v242\n"
"v_accvgpr_write_b32 a57, v245\n"
"v_accvgpr_read_b32 v244, a58\n"
"v_mul_f32 v244, v244, v242\n"
"v_accvgpr_write_b32 a58, v244\n"
"v_accvgpr_read_b32 v245, a59\n"
"v_mul_f32 v245, v245, v242\n"
"v_accvgpr_write_b32 a59, v245\n"
"v_accvgpr_read_b32 v244, a60\n"
"v_mul_f32 v244, v244, v242\n"
"v_accvgpr_write_b32 a60, v244\n"
"v_accvgpr_read_b32 v245, a61\n"
"v_mul_f32 v245, v245, v242\n"
"v_accvgpr_write_b32 a61, v245\n"
"v_accvgpr_read_b32 v244, a62\n"
"v_mul_f32 v244, v244, v242\n"
"v_accvgpr_write_b32 a62, v244\n"
"v_accvgpr_read_b32 v245, a63\n"
"v_mul_f32 v245, v245, v242\n"
"v_accvgpr_write_b32 a63, v245\n"
"v_max3_f32 v241, v80, v81, v82\n"
"v_max3_f32 v241, v241, v83, v84\n"
"v_max3_f32 v241, v241, v85, v86\n"
"v_max3_f32 v241, v241, v87, v88\n"
"v_max3_f32 v241, v241, v89, v90\n"
"v_max3_f32 v241, v241, v91, v92\n"
"v_max3_f32 v241, v241, v93, v94\n"
"v_max_f32 v241, v241, v95\n"
"v_mov_b32 v244, v241\n"
"s_nop 1\n"
"v_permlane32_swap_b32 v241, v244\n"
"v_max_f32 v241, v241, v244\n"
"v_max_f32 v241, 0, v241\n"
"v_sub_f32 v244, 0, v241\n"
"v_exp_f32 v243, v244\n"
"s_nop 0\n"
"v_mul_f32 v235, v235, v243\n"
"v_sub_f32 v176, v176, v241\n"
"v_sub_f32 v177, v177, v241\n"
"v_sub_f32 v178, v178, v241\n"
"v_sub_f32 v179, v179, v241\n"
"v_sub_f32 v180, v180, v241\n"
"v_sub_f32 v181, v181, v241\n"
"v_sub_f32 v182, v182, v241\n"
"v_sub_f32 v183, v183, v241\n"
"v_sub_f32 v184, v184, v241\n"
"v_sub_f32 v185, v185, v241\n"
"v_sub_f32 v186, v186, v241\n"
"v_sub_f32 v187, v187, v241\n"
"v_sub_f32 v188, v188, v241\n"
"v_sub_f32 v189, v189, v241\n"
"v_sub_f32 v190, v190, v241\n"
"v_sub_f32 v191, v191, v241\n"
"v_accvgpr_read_b32 v244, a64\n"
"v_mul_f32 v244, v244, v243\n"
"v_accvgpr_write_b32 a64, v244\n"
"v_accvgpr_read_b32 v245, a65\n"
"v_mul_f32 v245, v245, v243\n"
"v_accvgpr_write_b32 a65, v245\n"
"v_accvgpr_read_b32 v244, a66\n"
"v_mul_f32 v244, v244, v243\n"
"v_accvgpr_write_b32 a66, v244\n"
"v_accvgpr_read_b32 v245, a67\n"
"v_mul_f32 v245, v245, v243\n"
"v_accvgpr_write_b32 a67, v245\n"
"v_accvgpr_read_b32 v244, a68\n"
"v_mul_f32 v244, v244, v243\n"
"v_accvgpr_write_b32 a68, v244\n"
"v_accvgpr_read_b32 v245, a69\n"
"v_mul_f32 v245, v245, v243\n"
"v_accvgpr_write_b32 a69, v245\n"
"v_accvgpr_read_b32 v244, a70\n"
"v_mul_f32 v244, v244, v243\n"
"v_accvgpr_write_b32 a70, v244\n"
"v_accvgpr_read_b32 v245, a71\n"
"v_mul_f32 v245, v245, v243\n"
"v_accvgpr_write_b32 a71, v245\n"
"v_accvgpr_read_b32 v244, a72\n"
"v_mul_f32 v244, v244, v243\n"
"v_accvgpr_write_b32 a72, v244\n"
"v_accvgpr_read_b32 v245, a73\n"
"v_mul_f32 v245, v245, v243\n"
"v_accvgpr_write_b32 a73, v245\n"
"v_accvgpr_read_b32 v244, a74\n"
"v_mul_f32 v244, v244, v243\n"
"v_accvgpr_write_b32 a74, v244\n"
"v_accvgpr_read_b32 v245, a75\n"
"v_mul_f32 v245, v245, v243\n"
"v_accvgpr_write_b32 a75, v245\n"
"v_accvgpr_read_b32 v244, a76\n"
"v_mul_f32 v244, v244, v243\n"
"v_accvgpr_write_b32 a76, v244\n"
"v_accvgpr_read_b32 v245, a77\n"
"v_mul_f32 v245, v245, v243\n"
"v_accvgpr_write_b32 a77, v245\n"
"v_accvgpr_read_b32 v244, a78\n"
"v_mul_f32 v244, v244, v243\n"
"v_accvgpr_write_b32 a78, v244\n"
"v_accvgpr_read_b32 v245, a79\n"
"v_mul_f32 v245, v245, v243\n"
"v_accvgpr_write_b32 a79, v245\n"
"v_accvgpr_read_b32 v244, a80\n"
"v_mul_f32 v244, v244, v243\n"
"v_accvgpr_write_b32 a80, v244\n"
"v_accvgpr_read_b32 v245, a81\n"
"v_mul_f32 v245, v245, v243\n"
"v_accvgpr_write_b32 a81, v245\n"
"v_accvgpr_read_b32 v244, a82\n"
"v_mul_f32 v244, v244, v243\n"
"v_accvgpr_write_b32 a82, v244\n"
"v_accvgpr_read_b32 v245, a83\n"
"v_mul_f32 v245, v245, v243\n"
"v_accvgpr_write_b32 a83, v245\n"
"v_accvgpr_read_b32 v244, a84\n"
"v_mul_f32 v244, v244, v243\n"
"v_accvgpr_write_b32 a84, v244\n"
"v_accvgpr_read_b32 v245, a85\n"
"v_mul_f32 v245, v245, v243\n"
"v_accvgpr_write_b32 a85, v245\n"
"v_accvgpr_read_b32 v244, a86\n"
"v_mul_f32 v244, v244, v243\n"
"v_accvgpr_write_b32 a86, v244\n"
"v_accvgpr_read_b32 v245, a87\n"
"v_mul_f32 v245, v245, v243\n"
"v_accvgpr_write_b32 a87, v245\n"
"v_accvgpr_read_b32 v244, a88\n"
"v_mul_f32 v244, v244, v243\n"
"v_accvgpr_write_b32 a88, v244\n"
"v_accvgpr_read_b32 v245, a89\n"
"v_mul_f32 v245, v245, v243\n"
"v_accvgpr_write_b32 a89, v245\n"
"v_accvgpr_read_b32 v244, a90\n"
"v_mul_f32 v244, v244, v243\n"
"v_accvgpr_write_b32 a90, v244\n"
"v_accvgpr_read_b32 v245, a91\n"
"v_mul_f32 v245, v245, v243\n"
"v_accvgpr_write_b32 a91, v245\n"
"v_accvgpr_read_b32 v244, a92\n"
"v_mul_f32 v244, v244, v243\n"
"v_accvgpr_write_b32 a92, v244\n"
"v_accvgpr_read_b32 v245, a93\n"
"v_mul_f32 v245, v245, v243\n"
"v_accvgpr_write_b32 a93, v245\n"
"v_accvgpr_read_b32 v244, a94\n"
"v_mul_f32 v244, v244, v243\n"
"v_accvgpr_write_b32 a94, v244\n"
"v_accvgpr_read_b32 v245, a95\n"
"v_mul_f32 v245, v245, v243\n"
"v_accvgpr_write_b32 a95, v245\n"
"v_accvgpr_read_b32 v244, a96\n"
"v_mul_f32 v244, v244, v243\n"
"v_accvgpr_write_b32 a96, v244\n"
"v_accvgpr_read_b32 v245, a97\n"
"v_mul_f32 v245, v245, v243\n"
"v_accvgpr_write_b32 a97, v245\n"
"v_accvgpr_read_b32 v244, a98\n"
"v_mul_f32 v244, v244, v243\n"
"v_accvgpr_write_b32 a98, v244\n"
"v_accvgpr_read_b32 v245, a99\n"
"v_mul_f32 v245, v245, v243\n"
"v_accvgpr_write_b32 a99, v245\n"
"v_accvgpr_read_b32 v244, a100\n"
"v_mul_f32 v244, v244, v243\n"
"v_accvgpr_write_b32 a100, v244\n"
"v_accvgpr_read_b32 v245, a101\n"
"v_mul_f32 v245, v245, v243\n"
"v_accvgpr_write_b32 a101, v245\n"
"v_accvgpr_read_b32 v244, a102\n"
"v_mul_f32 v244, v244, v243\n"
"v_accvgpr_write_b32 a102, v244\n"
"v_accvgpr_read_b32 v245, a103\n"
"v_mul_f32 v245, v245, v243\n"
"v_accvgpr_write_b32 a103, v245\n"
"v_accvgpr_read_b32 v244, a104\n"
"v_mul_f32 v244, v244, v243\n"
"v_accvgpr_write_b32 a104, v244\n"
"v_accvgpr_read_b32 v245, a105\n"
"v_mul_f32 v245, v245, v243\n"
"v_accvgpr_write_b32 a105, v245\n"
"v_accvgpr_read_b32 v244, a106\n"
"v_mul_f32 v244, v244, v243\n"
"v_accvgpr_write_b32 a106, v244\n"
"v_accvgpr_read_b32 v245, a107\n"
"v_mul_f32 v245, v245, v243\n"
"v_accvgpr_write_b32 a107, v245\n"
"v_accvgpr_read_b32 v244, a108\n"
"v_mul_f32 v244, v244, v243\n"
"v_accvgpr_write_b32 a108, v244\n"
"v_accvgpr_read_b32 v245, a109\n"
"v_mul_f32 v245, v245, v243\n"
"v_accvgpr_write_b32 a109, v245\n"
"v_accvgpr_read_b32 v244, a110\n"
"v_mul_f32 v244, v244, v243\n"
"v_accvgpr_write_b32 a110, v244\n"
"v_accvgpr_read_b32 v245, a111\n"
"v_mul_f32 v245, v245, v243\n"
"v_accvgpr_write_b32 a111, v245\n"
"v_accvgpr_read_b32 v244, a112\n"
"v_mul_f32 v244, v244, v243\n"
"v_accvgpr_write_b32 a112, v244\n"
"v_accvgpr_read_b32 v245, a113\n"
"v_mul_f32 v245, v245, v243\n"
"v_accvgpr_write_b32 a113, v245\n"
"v_accvgpr_read_b32 v244, a114\n"
"v_mul_f32 v244, v244, v243\n"
"v_accvgpr_write_b32 a114, v244\n"
"v_accvgpr_read_b32 v245, a115\n"
"v_mul_f32 v245, v245, v243\n"
"v_accvgpr_write_b32 a115, v245\n"
"v_accvgpr_read_b32 v244, a116\n"
"v_mul_f32 v244, v244, v243\n"
"v_accvgpr_write_b32 a116, v244\n"
"v_accvgpr_read_b32 v245, a117\n"
"v_mul_f32 v245, v245, v243\n"
"v_accvgpr_write_b32 a117, v245\n"
"v_accvgpr_read_b32 v244, a118\n"
"v_mul_f32 v244, v244, v243\n"
"v_accvgpr_write_b32 a118, v244\n"
"v_accvgpr_read_b32 v245, a119\n"
"v_mul_f32 v245, v245, v243\n"
"v_accvgpr_write_b32 a119, v245\n"
"v_accvgpr_read_b32 v244, a120\n"
"v_mul_f32 v244, v244, v243\n"
"v_accvgpr_write_b32 a120, v244\n"
"v_accvgpr_read_b32 v245, a121\n"
"v_mul_f32 v245, v245, v243\n"
"v_accvgpr_write_b32 a121, v245\n"
"v_accvgpr_read_b32 v244, a122\n"
"v_mul_f32 v244, v244, v243\n"
"v_accvgpr_write_b32 a122, v244\n"
"v_accvgpr_read_b32 v245, a123\n"
"v_mul_f32 v245, v245, v243\n"
"v_accvgpr_write_b32 a123, v245\n"
"v_accvgpr_read_b32 v244, a124\n"
"v_mul_f32 v244, v244, v243\n"
"v_accvgpr_write_b32 a124, v244\n"
"v_accvgpr_read_b32 v245, a125\n"
"v_mul_f32 v245, v245, v243\n"
"v_accvgpr_write_b32 a125, v245\n"
"v_accvgpr_read_b32 v244, a126\n"
"v_mul_f32 v244, v244, v243\n"
"v_accvgpr_write_b32 a126, v244\n"
"v_accvgpr_read_b32 v245, a127\n"
"v_mul_f32 v245, v245, v243\n"
"v_accvgpr_write_b32 a127, v245\n"
"v_sub_f32 v64, v64, v240\n"
"v_sub_f32 v65, v65, v240\n"
"v_sub_f32 v66, v66, v240\n"
"v_sub_f32 v67, v67, v240\n"
"v_sub_f32 v68, v68, v240\n"
"v_sub_f32 v69, v69, v240\n"
"v_sub_f32 v70, v70, v240\n"
"v_sub_f32 v71, v71, v240\n"
"v_sub_f32 v72, v72, v240\n"
"v_sub_f32 v73, v73, v240\n"
"v_sub_f32 v74, v74, v240\n"
"v_sub_f32 v75, v75, v240\n"
"v_sub_f32 v76, v76, v240\n"
"v_sub_f32 v77, v77, v240\n"
"v_sub_f32 v78, v78, v240\n"
"v_sub_f32 v79, v79, v240\n"
"v_sub_f32 v80, v80, v241\n"
"v_sub_f32 v81, v81, v241\n"
"v_sub_f32 v82, v82, v241\n"
"v_sub_f32 v83, v83, v241\n"
"v_sub_f32 v84, v84, v241\n"
"v_sub_f32 v85, v85, v241\n"
"v_sub_f32 v86, v86, v241\n"
"v_sub_f32 v87, v87, v241\n"
"v_sub_f32 v88, v88, v241\n"
"v_sub_f32 v89, v89, v241\n"
"v_sub_f32 v90, v90, v241\n"
"v_sub_f32 v91, v91, v241\n"
"v_sub_f32 v92, v92, v241\n"
"v_sub_f32 v93, v93, v241\n"
"v_sub_f32 v94, v94, v241\n"
"v_sub_f32 v95, v95, v241\n"
"v_sub_f32 v96, v96, v240\n"
"v_sub_f32 v97, v97, v240\n"
"v_sub_f32 v98, v98, v240\n"
"v_sub_f32 v99, v99, v240\n"
"v_sub_f32 v100, v100, v240\n"
"v_sub_f32 v101, v101, v240\n"
"v_sub_f32 v102, v102, v240\n"
"v_sub_f32 v103, v103, v240\n"
"v_sub_f32 v104, v104, v240\n"
"v_sub_f32 v105, v105, v240\n"
"v_sub_f32 v106, v106, v240\n"
"v_sub_f32 v107, v107, v240\n"
"v_sub_f32 v108, v108, v240\n"
"v_sub_f32 v109, v109, v240\n"
"v_sub_f32 v110, v110, v240\n"
"v_sub_f32 v111, v111, v240\n"
"v_sub_f32 v112, v112, v241\n"
"v_sub_f32 v113, v113, v241\n"
"v_sub_f32 v114, v114, v241\n"
"v_sub_f32 v115, v115, v241\n"
"v_sub_f32 v116, v116, v241\n"
"v_sub_f32 v117, v117, v241\n"
"v_sub_f32 v118, v118, v241\n"
"v_sub_f32 v119, v119, v241\n"
"v_sub_f32 v120, v120, v241\n"
"v_sub_f32 v121, v121, v241\n"
"v_sub_f32 v122, v122, v241\n"
"v_sub_f32 v123, v123, v241\n"
"v_sub_f32 v124, v124, v241\n"
"v_sub_f32 v125, v125, v241\n"
"v_sub_f32 v126, v126, v241\n"
"v_sub_f32 v127, v127, v241\n"
"v_exp_f32 v224, v64\n"
"s_nop 0\n"
"v_exp_f32 v225, v65\n"
"s_nop 0\n"
"v_add_f32 v232, v224, v225\n"
"v_cvt_pk_bf16_f32 v128, v224, v225\n"
"v_exp_f32 v226, v66\n"
"s_nop 0\n"
"v_add_f32 v232, v232, v226\n"
"v_exp_f32 v227, v67\n"
"s_nop 0\n"
"v_add_f32 v232, v232, v227\n"
"v_cvt_pk_bf16_f32 v129, v226, v227\n"
"v_exp_f32 v228, v68\n"
"s_nop 0\n"
"v_add_f32 v232, v232, v228\n"
"v_exp_f32 v229, v69\n"
"s_nop 0\n"
"v_add_f32 v232, v232, v229\n"
"v_cvt_pk_bf16_f32 v130, v228, v229\n"
"v_exp_f32 v230, v70\n"
"s_nop 0\n"
"v_add_f32 v232, v232, v230\n"
"v_exp_f32 v231, v71\n"
"s_nop 0\n"
"v_add_f32 v232, v232, v231\n"
"v_cvt_pk_bf16_f32 v131, v230, v231\n"
"v_exp_f32 v224, v72\n"
"s_nop 0\n"
"v_add_f32 v232, v232, v224\n"
"v_exp_f32 v225, v73\n"
"s_nop 0\n"
"v_add_f32 v232, v232, v225\n"
"v_cvt_pk_bf16_f32 v132, v224, v225\n"
"v_exp_f32 v226, v74\n"
"s_nop 0\n"
"v_add_f32 v232, v232, v226\n"
"v_exp_f32 v227, v75\n"
"s_nop 0\n"
"v_add_f32 v232, v232, v227\n"
"v_cvt_pk_bf16_f32 v133, v226, v227\n"
"v_exp_f32 v228, v76\n"
"s_nop 0\n"
"v_add_f32 v232, v232, v228\n"
"v_exp_f32 v229, v77\n"
"s_nop 0\n"
"v_add_f32 v232, v232, v229\n"
"v_cvt_pk_bf16_f32 v134, v228, v229\n"
"v_exp_f32 v230, v78\n"
"s_nop 0\n"
"v_add_f32 v232, v232, v230\n"
"v_exp_f32 v231, v79\n"
"s_nop 0\n"
"v_add_f32 v232, v232, v231\n"
"v_cvt_pk_bf16_f32 v135, v230, v231\n"
"v_exp_f32 v224, v80\n"
"s_nop 0\n"
"v_exp_f32 v225, v81\n"
"s_nop 0\n"
"v_add_f32 v233, v224, v225\n"
"v_cvt_pk_bf16_f32 v136, v224, v225\n"
"v_exp_f32 v226, v82\n"
"s_nop 0\n"
"v_add_f32 v233, v233, v226\n"
"v_exp_f32 v227, v83\n"
"s_nop 0\n"
"v_add_f32 v233, v233, v227\n"
"v_cvt_pk_bf16_f32 v137, v226, v227\n"
"v_exp_f32 v228, v84\n"
"s_nop 0\n"
"v_add_f32 v233, v233, v228\n"
"v_exp_f32 v229, v85\n"
"s_nop 0\n"
"v_add_f32 v233, v233, v229\n"
"v_cvt_pk_bf16_f32 v138, v228, v229\n"
"v_exp_f32 v230, v86\n"
"s_nop 0\n"
"v_add_f32 v233, v233, v230\n"
"v_exp_f32 v231, v87\n"
"s_nop 0\n"
"v_add_f32 v233, v233, v231\n"
"v_cvt_pk_bf16_f32 v139, v230, v231\n"
"v_exp_f32 v224, v88\n"
"s_nop 0\n"
"v_add_f32 v233, v233, v224\n"
"v_exp_f32 v225, v89\n"
"s_nop 0\n"
"v_add_f32 v233, v233, v225\n"
"v_cvt_pk_bf16_f32 v140, v224, v225\n"
"v_exp_f32 v226, v90\n"
"s_nop 0\n"
"v_add_f32 v233, v233, v226\n"
"v_exp_f32 v227, v91\n"
"s_nop 0\n"
"v_add_f32 v233, v233, v227\n"
"v_cvt_pk_bf16_f32 v141, v226, v227\n"
"v_exp_f32 v228, v92\n"
"s_nop 0\n"
"v_add_f32 v233, v233, v228\n"
"v_exp_f32 v229, v93\n"
"s_nop 0\n"
"v_add_f32 v233, v233, v229\n"
"v_cvt_pk_bf16_f32 v142, v228, v229\n"
"v_exp_f32 v230, v94\n"
"s_nop 0\n"
"v_add_f32 v233, v233, v230\n"
"v_exp_f32 v231, v95\n"
"s_nop 0\n"
"v_add_f32 v233, v233, v231\n"
"v_cvt_pk_bf16_f32 v143, v230, v231\n"
"v_add_f32 v236, v232, v233\n"
"s_nop 1\n"
"s_branch Lfr108_%=\n"
"Lfb109_%=:\n"
"s_nop 7\n"
"s_nop 7\n"
"v_max3_f32 v240, v96, v97, v98\n"
"v_max3_f32 v240, v240, v99, v100\n"
"v_max3_f32 v240, v240, v101, v102\n"
"v_max3_f32 v240, v240, v103, v104\n"
"v_max3_f32 v240, v240, v105, v106\n"
"v_max3_f32 v240, v240, v107, v108\n"
"v_max3_f32 v240, v240, v109, v110\n"
"v_max3_f32 v240, v240, v111, v112\n"
"v_max3_f32 v240, v240, v113, v114\n"
"v_max3_f32 v240, v240, v115, v116\n"
"v_max3_f32 v240, v240, v117, v118\n"
"v_max3_f32 v240, v240, v119, v120\n"
"v_max3_f32 v240, v240, v121, v122\n"
"v_max3_f32 v240, v240, v123, v124\n"
"v_max3_f32 v240, v240, v125, v126\n"
"v_max_f32 v240, v240, v127\n"
"v_mov_b32 v244, v240\n"
"s_nop 1\n"
"v_permlane32_swap_b32 v240, v244\n"
"v_max_f32 v240, v240, v244\n"
"v_max_f32 v240, 0, v240\n"
"v_sub_f32 v244, 0, v240\n"
"v_exp_f32 v242, v244\n"
"s_nop 0\n"
"v_mul_f32 v234, v234, v242\n"
"v_sub_f32 v160, v160, v240\n"
"v_sub_f32 v161, v161, v240\n"
"v_sub_f32 v162, v162, v240\n"
"v_sub_f32 v163, v163, v240\n"
"v_sub_f32 v164, v164, v240\n"
"v_sub_f32 v165, v165, v240\n"
"v_sub_f32 v166, v166, v240\n"
"v_sub_f32 v167, v167, v240\n"
"v_sub_f32 v168, v168, v240\n"
"v_sub_f32 v169, v169, v240\n"
"v_sub_f32 v170, v170, v240\n"
"v_sub_f32 v171, v171, v240\n"
"v_sub_f32 v172, v172, v240\n"
"v_sub_f32 v173, v173, v240\n"
"v_sub_f32 v174, v174, v240\n"
"v_sub_f32 v175, v175, v240\n"
"v_accvgpr_read_b32 v244, a0\n"
"v_mul_f32 v244, v244, v242\n"
"v_accvgpr_write_b32 a0, v244\n"
"v_accvgpr_read_b32 v245, a1\n"
"v_mul_f32 v245, v245, v242\n"
"v_accvgpr_write_b32 a1, v245\n"
"v_accvgpr_read_b32 v244, a2\n"
"v_mul_f32 v244, v244, v242\n"
"v_accvgpr_write_b32 a2, v244\n"
"v_accvgpr_read_b32 v245, a3\n"
"v_mul_f32 v245, v245, v242\n"
"v_accvgpr_write_b32 a3, v245\n"
"v_accvgpr_read_b32 v244, a4\n"
"v_mul_f32 v244, v244, v242\n"
"v_accvgpr_write_b32 a4, v244\n"
"v_accvgpr_read_b32 v245, a5\n"
"v_mul_f32 v245, v245, v242\n"
"v_accvgpr_write_b32 a5, v245\n"
"v_accvgpr_read_b32 v244, a6\n"
"v_mul_f32 v244, v244, v242\n"
"v_accvgpr_write_b32 a6, v244\n"
"v_accvgpr_read_b32 v245, a7\n"
"v_mul_f32 v245, v245, v242\n"
"v_accvgpr_write_b32 a7, v245\n"
"v_accvgpr_read_b32 v244, a8\n"
"v_mul_f32 v244, v244, v242\n"
"v_accvgpr_write_b32 a8, v244\n"
"v_accvgpr_read_b32 v245, a9\n"
"v_mul_f32 v245, v245, v242\n"
"v_accvgpr_write_b32 a9, v245\n"
"v_accvgpr_read_b32 v244, a10\n"
"v_mul_f32 v244, v244, v242\n"
"v_accvgpr_write_b32 a10, v244\n"
"v_accvgpr_read_b32 v245, a11\n"
"v_mul_f32 v245, v245, v242\n"
"v_accvgpr_write_b32 a11, v245\n"
"v_accvgpr_read_b32 v244, a12\n"
"v_mul_f32 v244, v244, v242\n"
"v_accvgpr_write_b32 a12, v244\n"
"v_accvgpr_read_b32 v245, a13\n"
"v_mul_f32 v245, v245, v242\n"
"v_accvgpr_write_b32 a13, v245\n"
"v_accvgpr_read_b32 v244, a14\n"
"v_mul_f32 v244, v244, v242\n"
"v_accvgpr_write_b32 a14, v244\n"
"v_accvgpr_read_b32 v245, a15\n"
"v_mul_f32 v245, v245, v242\n"
"v_accvgpr_write_b32 a15, v245\n"
"v_accvgpr_read_b32 v244, a16\n"
"v_mul_f32 v244, v244, v242\n"
"v_accvgpr_write_b32 a16, v244\n"
"v_accvgpr_read_b32 v245, a17\n"
"v_mul_f32 v245, v245, v242\n"
"v_accvgpr_write_b32 a17, v245\n"
"v_accvgpr_read_b32 v244, a18\n"
"v_mul_f32 v244, v244, v242\n"
"v_accvgpr_write_b32 a18, v244\n"
"v_accvgpr_read_b32 v245, a19\n"
"v_mul_f32 v245, v245, v242\n"
"v_accvgpr_write_b32 a19, v245\n"
"v_accvgpr_read_b32 v244, a20\n"
"v_mul_f32 v244, v244, v242\n"
"v_accvgpr_write_b32 a20, v244\n"
"v_accvgpr_read_b32 v245, a21\n"
"v_mul_f32 v245, v245, v242\n"
"v_accvgpr_write_b32 a21, v245\n"
"v_accvgpr_read_b32 v244, a22\n"
"v_mul_f32 v244, v244, v242\n"
"v_accvgpr_write_b32 a22, v244\n"
"v_accvgpr_read_b32 v245, a23\n"
"v_mul_f32 v245, v245, v242\n"
"v_accvgpr_write_b32 a23, v245\n"
"v_accvgpr_read_b32 v244, a24\n"
"v_mul_f32 v244, v244, v242\n"
"v_accvgpr_write_b32 a24, v244\n"
"v_accvgpr_read_b32 v245, a25\n"
"v_mul_f32 v245, v245, v242\n"
"v_accvgpr_write_b32 a25, v245\n"
"v_accvgpr_read_b32 v244, a26\n"
"v_mul_f32 v244, v244, v242\n"
"v_accvgpr_write_b32 a26, v244\n"
"v_accvgpr_read_b32 v245, a27\n"
"v_mul_f32 v245, v245, v242\n"
"v_accvgpr_write_b32 a27, v245\n"
"v_accvgpr_read_b32 v244, a28\n"
"v_mul_f32 v244, v244, v242\n"
"v_accvgpr_write_b32 a28, v244\n"
"v_accvgpr_read_b32 v245, a29\n"
"v_mul_f32 v245, v245, v242\n"
"v_accvgpr_write_b32 a29, v245\n"
"v_accvgpr_read_b32 v244, a30\n"
"v_mul_f32 v244, v244, v242\n"
"v_accvgpr_write_b32 a30, v244\n"
"v_accvgpr_read_b32 v245, a31\n"
"v_mul_f32 v245, v245, v242\n"
"v_accvgpr_write_b32 a31, v245\n"
"v_accvgpr_read_b32 v244, a32\n"
"v_mul_f32 v244, v244, v242\n"
"v_accvgpr_write_b32 a32, v244\n"
"v_accvgpr_read_b32 v245, a33\n"
"v_mul_f32 v245, v245, v242\n"
"v_accvgpr_write_b32 a33, v245\n"
"v_accvgpr_read_b32 v244, a34\n"
"v_mul_f32 v244, v244, v242\n"
"v_accvgpr_write_b32 a34, v244\n"
"v_accvgpr_read_b32 v245, a35\n"
"v_mul_f32 v245, v245, v242\n"
"v_accvgpr_write_b32 a35, v245\n"
"v_accvgpr_read_b32 v244, a36\n"
"v_mul_f32 v244, v244, v242\n"
"v_accvgpr_write_b32 a36, v244\n"
"v_accvgpr_read_b32 v245, a37\n"
"v_mul_f32 v245, v245, v242\n"
"v_accvgpr_write_b32 a37, v245\n"
"v_accvgpr_read_b32 v244, a38\n"
"v_mul_f32 v244, v244, v242\n"
"v_accvgpr_write_b32 a38, v244\n"
"v_accvgpr_read_b32 v245, a39\n"
"v_mul_f32 v245, v245, v242\n"
"v_accvgpr_write_b32 a39, v245\n"
"v_accvgpr_read_b32 v244, a40\n"
"v_mul_f32 v244, v244, v242\n"
"v_accvgpr_write_b32 a40, v244\n"
"v_accvgpr_read_b32 v245, a41\n"
"v_mul_f32 v245, v245, v242\n"
"v_accvgpr_write_b32 a41, v245\n"
"v_accvgpr_read_b32 v244, a42\n"
"v_mul_f32 v244, v244, v242\n"
"v_accvgpr_write_b32 a42, v244\n"
"v_accvgpr_read_b32 v245, a43\n"
"v_mul_f32 v245, v245, v242\n"
"v_accvgpr_write_b32 a43, v245\n"
"v_accvgpr_read_b32 v244, a44\n"
"v_mul_f32 v244, v244, v242\n"
"v_accvgpr_write_b32 a44, v244\n"
"v_accvgpr_read_b32 v245, a45\n"
"v_mul_f32 v245, v245, v242\n"
"v_accvgpr_write_b32 a45, v245\n"
"v_accvgpr_read_b32 v244, a46\n"
"v_mul_f32 v244, v244, v242\n"
"v_accvgpr_write_b32 a46, v244\n"
"v_accvgpr_read_b32 v245, a47\n"
"v_mul_f32 v245, v245, v242\n"
"v_accvgpr_write_b32 a47, v245\n"
"v_accvgpr_read_b32 v244, a48\n"
"v_mul_f32 v244, v244, v242\n"
"v_accvgpr_write_b32 a48, v244\n"
"v_accvgpr_read_b32 v245, a49\n"
"v_mul_f32 v245, v245, v242\n"
"v_accvgpr_write_b32 a49, v245\n"
"v_accvgpr_read_b32 v244, a50\n"
"v_mul_f32 v244, v244, v242\n"
"v_accvgpr_write_b32 a50, v244\n"
"v_accvgpr_read_b32 v245, a51\n"
"v_mul_f32 v245, v245, v242\n"
"v_accvgpr_write_b32 a51, v245\n"
"v_accvgpr_read_b32 v244, a52\n"
"v_mul_f32 v244, v244, v242\n"
"v_accvgpr_write_b32 a52, v244\n"
"v_accvgpr_read_b32 v245, a53\n"
"v_mul_f32 v245, v245, v242\n"
"v_accvgpr_write_b32 a53, v245\n"
"v_accvgpr_read_b32 v244, a54\n"
"v_mul_f32 v244, v244, v242\n"
"v_accvgpr_write_b32 a54, v244\n"
"v_accvgpr_read_b32 v245, a55\n"
"v_mul_f32 v245, v245, v242\n"
"v_accvgpr_write_b32 a55, v245\n"
"v_accvgpr_read_b32 v244, a56\n"
"v_mul_f32 v244, v244, v242\n"
"v_accvgpr_write_b32 a56, v244\n"
"v_accvgpr_read_b32 v245, a57\n"
"v_mul_f32 v245, v245, v242\n"
"v_accvgpr_write_b32 a57, v245\n"
"v_accvgpr_read_b32 v244, a58\n"
"v_mul_f32 v244, v244, v242\n"
"v_accvgpr_write_b32 a58, v244\n"
"v_accvgpr_read_b32 v245, a59\n"
"v_mul_f32 v245, v245, v242\n"
"v_accvgpr_write_b32 a59, v245\n"
"v_accvgpr_read_b32 v244, a60\n"
"v_mul_f32 v244, v244, v242\n"
"v_accvgpr_write_b32 a60, v244\n"
"v_accvgpr_read_b32 v245, a61\n"
"v_mul_f32 v245, v245, v242\n"
"v_accvgpr_write_b32 a61, v245\n"
"v_accvgpr_read_b32 v244, a62\n"
"v_mul_f32 v244, v244, v242\n"
"v_accvgpr_write_b32 a62, v244\n"
"v_accvgpr_read_b32 v245, a63\n"
"v_mul_f32 v245, v245, v242\n"
"v_accvgpr_write_b32 a63, v245\n"
"v_sub_f32 v96, v96, v240\n"
"v_sub_f32 v97, v97, v240\n"
"v_sub_f32 v98, v98, v240\n"
"v_sub_f32 v99, v99, v240\n"
"v_sub_f32 v100, v100, v240\n"
"v_sub_f32 v101, v101, v240\n"
"v_sub_f32 v102, v102, v240\n"
"v_sub_f32 v103, v103, v240\n"
"v_sub_f32 v104, v104, v240\n"
"v_sub_f32 v105, v105, v240\n"
"v_sub_f32 v106, v106, v240\n"
"v_sub_f32 v107, v107, v240\n"
"v_sub_f32 v108, v108, v240\n"
"v_sub_f32 v109, v109, v240\n"
"v_sub_f32 v110, v110, v240\n"
"v_sub_f32 v111, v111, v240\n"
"v_sub_f32 v112, v112, v240\n"
"v_sub_f32 v113, v113, v240\n"
"v_sub_f32 v114, v114, v240\n"
"v_sub_f32 v115, v115, v240\n"
"v_sub_f32 v116, v116, v240\n"
"v_sub_f32 v117, v117, v240\n"
"v_sub_f32 v118, v118, v240\n"
"v_sub_f32 v119, v119, v240\n"
"v_sub_f32 v120, v120, v240\n"
"v_sub_f32 v121, v121, v240\n"
"v_sub_f32 v122, v122, v240\n"
"v_sub_f32 v123, v123, v240\n"
"v_sub_f32 v124, v124, v240\n"
"v_sub_f32 v125, v125, v240\n"
"v_sub_f32 v126, v126, v240\n"
"v_sub_f32 v127, v127, v240\n"
"v_sub_f32 v64, v64, v240\n"
"v_sub_f32 v65, v65, v240\n"
"v_sub_f32 v66, v66, v240\n"
"v_sub_f32 v67, v67, v240\n"
"v_sub_f32 v68, v68, v240\n"
"v_sub_f32 v69, v69, v240\n"
"v_sub_f32 v70, v70, v240\n"
"v_sub_f32 v71, v71, v240\n"
"v_sub_f32 v72, v72, v240\n"
"v_sub_f32 v73, v73, v240\n"
"v_sub_f32 v74, v74, v240\n"
"v_sub_f32 v75, v75, v240\n"
"v_sub_f32 v76, v76, v240\n"
"v_sub_f32 v77, v77, v240\n"
"v_sub_f32 v78, v78, v240\n"
"v_sub_f32 v79, v79, v240\n"
"v_sub_f32 v80, v80, v240\n"
"v_sub_f32 v81, v81, v240\n"
"v_sub_f32 v82, v82, v240\n"
"v_sub_f32 v83, v83, v240\n"
"v_sub_f32 v84, v84, v240\n"
"v_sub_f32 v85, v85, v240\n"
"v_sub_f32 v86, v86, v240\n"
"v_sub_f32 v87, v87, v240\n"
"v_sub_f32 v88, v88, v240\n"
"v_sub_f32 v89, v89, v240\n"
"v_sub_f32 v90, v90, v240\n"
"v_sub_f32 v91, v91, v240\n"
"v_sub_f32 v92, v92, v240\n"
"v_sub_f32 v93, v93, v240\n"
"v_sub_f32 v94, v94, v240\n"
"v_sub_f32 v95, v95, v240\n"
"v_exp_f32 v224, v96\n"
"s_nop 0\n"
"v_exp_f32 v225, v97\n"
"s_nop 0\n"
"v_add_f32 v232, v224, v225\n"
"v_cvt_pk_bf16_f32 v144, v224, v225\n"
"v_exp_f32 v226, v98\n"
"s_nop 0\n"
"v_add_f32 v232, v232, v226\n"
"v_exp_f32 v227, v99\n"
"s_nop 0\n"
"v_add_f32 v232, v232, v227\n"
"v_cvt_pk_bf16_f32 v145, v226, v227\n"
"v_exp_f32 v228, v100\n"
"s_nop 0\n"
"v_add_f32 v232, v232, v228\n"
"v_exp_f32 v229, v101\n"
"s_nop 0\n"
"v_add_f32 v232, v232, v229\n"
"v_cvt_pk_bf16_f32 v146, v228, v229\n"
"v_exp_f32 v230, v102\n"
"s_nop 0\n"
"v_add_f32 v232, v232, v230\n"
"v_exp_f32 v231, v103\n"
"s_nop 0\n"
"v_add_f32 v232, v232, v231\n"
"v_cvt_pk_bf16_f32 v147, v230, v231\n"
"v_exp_f32 v224, v104\n"
"s_nop 0\n"
"v_add_f32 v232, v232, v224\n"
"v_exp_f32 v225, v105\n"
"s_nop 0\n"
"v_add_f32 v232, v232, v225\n"
"v_cvt_pk_bf16_f32 v148, v224, v225\n"
"v_exp_f32 v226, v106\n"
"s_nop 0\n"
"v_add_f32 v232, v232, v226\n"
"v_exp_f32 v227, v107\n"
"s_nop 0\n"
"v_add_f32 v232, v232, v227\n"
"v_cvt_pk_bf16_f32 v149, v226, v227\n"
"v_exp_f32 v228, v108\n"
"s_nop 0\n"
"v_add_f32 v232, v232, v228\n"
"v_exp_f32 v229, v109\n"
"s_nop 0\n"
"v_add_f32 v232, v232, v229\n"
"v_cvt_pk_bf16_f32 v150, v228, v229\n"
"v_exp_f32 v230, v110\n"
"s_nop 0\n"
"v_add_f32 v232, v232, v230\n"
"v_exp_f32 v231, v111\n"
"s_nop 0\n"
"v_add_f32 v232, v232, v231\n"
"v_cvt_pk_bf16_f32 v151, v230, v231\n"
"v_exp_f32 v224, v112\n"
"s_nop 0\n"
"v_exp_f32 v225, v113\n"
"s_nop 0\n"
"v_add_f32 v233, v224, v225\n"
"v_cvt_pk_bf16_f32 v152, v224, v225\n"
"v_exp_f32 v226, v114\n"
"s_nop 0\n"
"v_add_f32 v233, v233, v226\n"
"v_exp_f32 v227, v115\n"
"s_nop 0\n"
"v_add_f32 v233, v233, v227\n"
"v_cvt_pk_bf16_f32 v153, v226, v227\n"
"v_exp_f32 v228, v116\n"
"s_nop 0\n"
"v_add_f32 v233, v233, v228\n"
"v_exp_f32 v229, v117\n"
"s_nop 0\n"
"v_add_f32 v233, v233, v229\n"
"v_cvt_pk_bf16_f32 v154, v228, v229\n"
"v_exp_f32 v230, v118\n"
"s_nop 0\n"
"v_add_f32 v233, v233, v230\n"
"v_exp_f32 v231, v119\n"
"s_nop 0\n"
"v_add_f32 v233, v233, v231\n"
"v_cvt_pk_bf16_f32 v155, v230, v231\n"
"v_exp_f32 v224, v120\n"
"s_nop 0\n"
"v_add_f32 v233, v233, v224\n"
"v_exp_f32 v225, v121\n"
"s_nop 0\n"
"v_add_f32 v233, v233, v225\n"
"v_cvt_pk_bf16_f32 v156, v224, v225\n"
"v_exp_f32 v226, v122\n"
"s_nop 0\n"
"v_add_f32 v233, v233, v226\n"
"v_exp_f32 v227, v123\n"
"s_nop 0\n"
"v_add_f32 v233, v233, v227\n"
"v_cvt_pk_bf16_f32 v157, v226, v227\n"
"v_exp_f32 v228, v124\n"
"s_nop 0\n"
"v_add_f32 v233, v233, v228\n"
"v_exp_f32 v229, v125\n"
"s_nop 0\n"
"v_add_f32 v233, v233, v229\n"
"v_cvt_pk_bf16_f32 v158, v228, v229\n"
"v_exp_f32 v230, v126\n"
"s_nop 0\n"
"v_add_f32 v233, v233, v230\n"
"v_exp_f32 v231, v127\n"
"s_nop 0\n"
"v_add_f32 v233, v233, v231\n"
"v_cvt_pk_bf16_f32 v159, v230, v231\n"
"v_add_f32 v236, v232, v233\n"
"s_nop 1\n"
"s_branch Lfr110_%=\n"
"Lfb111_%=:\n"
"s_nop 7\n"
"s_nop 7\n"
"v_max3_f32 v240, v64, v65, v66\n"
"v_max3_f32 v240, v240, v67, v68\n"
"v_max3_f32 v240, v240, v69, v70\n"
"v_max3_f32 v240, v240, v71, v72\n"
"v_max3_f32 v240, v240, v73, v74\n"
"v_max3_f32 v240, v240, v75, v76\n"
"v_max3_f32 v240, v240, v77, v78\n"
"v_max3_f32 v240, v240, v79, v80\n"
"v_max3_f32 v240, v240, v81, v82\n"
"v_max3_f32 v240, v240, v83, v84\n"
"v_max3_f32 v240, v240, v85, v86\n"
"v_max3_f32 v240, v240, v87, v88\n"
"v_max3_f32 v240, v240, v89, v90\n"
"v_max3_f32 v240, v240, v91, v92\n"
"v_max3_f32 v240, v240, v93, v94\n"
"v_max_f32 v240, v240, v95\n"
"v_mov_b32 v244, v240\n"
"s_nop 1\n"
"v_permlane32_swap_b32 v240, v244\n"
"v_max_f32 v240, v240, v244\n"
"v_max_f32 v240, 0, v240\n"
"v_sub_f32 v244, 0, v240\n"
"v_exp_f32 v242, v244\n"
"s_nop 0\n"
"v_mul_f32 v234, v234, v242\n"
"v_sub_f32 v160, v160, v240\n"
"v_sub_f32 v161, v161, v240\n"
"v_sub_f32 v162, v162, v240\n"
"v_sub_f32 v163, v163, v240\n"
"v_sub_f32 v164, v164, v240\n"
"v_sub_f32 v165, v165, v240\n"
"v_sub_f32 v166, v166, v240\n"
"v_sub_f32 v167, v167, v240\n"
"v_sub_f32 v168, v168, v240\n"
"v_sub_f32 v169, v169, v240\n"
"v_sub_f32 v170, v170, v240\n"
"v_sub_f32 v171, v171, v240\n"
"v_sub_f32 v172, v172, v240\n"
"v_sub_f32 v173, v173, v240\n"
"v_sub_f32 v174, v174, v240\n"
"v_sub_f32 v175, v175, v240\n"
"v_accvgpr_read_b32 v244, a0\n"
"v_mul_f32 v244, v244, v242\n"
"v_accvgpr_write_b32 a0, v244\n"
"v_accvgpr_read_b32 v245, a1\n"
"v_mul_f32 v245, v245, v242\n"
"v_accvgpr_write_b32 a1, v245\n"
"v_accvgpr_read_b32 v244, a2\n"
"v_mul_f32 v244, v244, v242\n"
"v_accvgpr_write_b32 a2, v244\n"
"v_accvgpr_read_b32 v245, a3\n"
"v_mul_f32 v245, v245, v242\n"
"v_accvgpr_write_b32 a3, v245\n"
"v_accvgpr_read_b32 v244, a4\n"
"v_mul_f32 v244, v244, v242\n"
"v_accvgpr_write_b32 a4, v244\n"
"v_accvgpr_read_b32 v245, a5\n"
"v_mul_f32 v245, v245, v242\n"
"v_accvgpr_write_b32 a5, v245\n"
"v_accvgpr_read_b32 v244, a6\n"
"v_mul_f32 v244, v244, v242\n"
"v_accvgpr_write_b32 a6, v244\n"
"v_accvgpr_read_b32 v245, a7\n"
"v_mul_f32 v245, v245, v242\n"
"v_accvgpr_write_b32 a7, v245\n"
"v_accvgpr_read_b32 v244, a8\n"
"v_mul_f32 v244, v244, v242\n"
"v_accvgpr_write_b32 a8, v244\n"
"v_accvgpr_read_b32 v245, a9\n"
"v_mul_f32 v245, v245, v242\n"
"v_accvgpr_write_b32 a9, v245\n"
"v_accvgpr_read_b32 v244, a10\n"
"v_mul_f32 v244, v244, v242\n"
"v_accvgpr_write_b32 a10, v244\n"
"v_accvgpr_read_b32 v245, a11\n"
"v_mul_f32 v245, v245, v242\n"
"v_accvgpr_write_b32 a11, v245\n"
"v_accvgpr_read_b32 v244, a12\n"
"v_mul_f32 v244, v244, v242\n"
"v_accvgpr_write_b32 a12, v244\n"
"v_accvgpr_read_b32 v245, a13\n"
"v_mul_f32 v245, v245, v242\n"
"v_accvgpr_write_b32 a13, v245\n"
"v_accvgpr_read_b32 v244, a14\n"
"v_mul_f32 v244, v244, v242\n"
"v_accvgpr_write_b32 a14, v244\n"
"v_accvgpr_read_b32 v245, a15\n"
"v_mul_f32 v245, v245, v242\n"
"v_accvgpr_write_b32 a15, v245\n"
"v_accvgpr_read_b32 v244, a16\n"
"v_mul_f32 v244, v244, v242\n"
"v_accvgpr_write_b32 a16, v244\n"
"v_accvgpr_read_b32 v245, a17\n"
"v_mul_f32 v245, v245, v242\n"
"v_accvgpr_write_b32 a17, v245\n"
"v_accvgpr_read_b32 v244, a18\n"
"v_mul_f32 v244, v244, v242\n"
"v_accvgpr_write_b32 a18, v244\n"
"v_accvgpr_read_b32 v245, a19\n"
"v_mul_f32 v245, v245, v242\n"
"v_accvgpr_write_b32 a19, v245\n"
"v_accvgpr_read_b32 v244, a20\n"
"v_mul_f32 v244, v244, v242\n"
"v_accvgpr_write_b32 a20, v244\n"
"v_accvgpr_read_b32 v245, a21\n"
"v_mul_f32 v245, v245, v242\n"
"v_accvgpr_write_b32 a21, v245\n"
"v_accvgpr_read_b32 v244, a22\n"
"v_mul_f32 v244, v244, v242\n"
"v_accvgpr_write_b32 a22, v244\n"
"v_accvgpr_read_b32 v245, a23\n"
"v_mul_f32 v245, v245, v242\n"
"v_accvgpr_write_b32 a23, v245\n"
"v_accvgpr_read_b32 v244, a24\n"
"v_mul_f32 v244, v244, v242\n"
"v_accvgpr_write_b32 a24, v244\n"
"v_accvgpr_read_b32 v245, a25\n"
"v_mul_f32 v245, v245, v242\n"
"v_accvgpr_write_b32 a25, v245\n"
"v_accvgpr_read_b32 v244, a26\n"
"v_mul_f32 v244, v244, v242\n"
"v_accvgpr_write_b32 a26, v244\n"
"v_accvgpr_read_b32 v245, a27\n"
"v_mul_f32 v245, v245, v242\n"
"v_accvgpr_write_b32 a27, v245\n"
"v_accvgpr_read_b32 v244, a28\n"
"v_mul_f32 v244, v244, v242\n"
"v_accvgpr_write_b32 a28, v244\n"
"v_accvgpr_read_b32 v245, a29\n"
"v_mul_f32 v245, v245, v242\n"
"v_accvgpr_write_b32 a29, v245\n"
"v_accvgpr_read_b32 v244, a30\n"
"v_mul_f32 v244, v244, v242\n"
"v_accvgpr_write_b32 a30, v244\n"
"v_accvgpr_read_b32 v245, a31\n"
"v_mul_f32 v245, v245, v242\n"
"v_accvgpr_write_b32 a31, v245\n"
"v_accvgpr_read_b32 v244, a32\n"
"v_mul_f32 v244, v244, v242\n"
"v_accvgpr_write_b32 a32, v244\n"
"v_accvgpr_read_b32 v245, a33\n"
"v_mul_f32 v245, v245, v242\n"
"v_accvgpr_write_b32 a33, v245\n"
"v_accvgpr_read_b32 v244, a34\n"
"v_mul_f32 v244, v244, v242\n"
"v_accvgpr_write_b32 a34, v244\n"
"v_accvgpr_read_b32 v245, a35\n"
"v_mul_f32 v245, v245, v242\n"
"v_accvgpr_write_b32 a35, v245\n"
"v_accvgpr_read_b32 v244, a36\n"
"v_mul_f32 v244, v244, v242\n"
"v_accvgpr_write_b32 a36, v244\n"
"v_accvgpr_read_b32 v245, a37\n"
"v_mul_f32 v245, v245, v242\n"
"v_accvgpr_write_b32 a37, v245\n"
"v_accvgpr_read_b32 v244, a38\n"
"v_mul_f32 v244, v244, v242\n"
"v_accvgpr_write_b32 a38, v244\n"
"v_accvgpr_read_b32 v245, a39\n"
"v_mul_f32 v245, v245, v242\n"
"v_accvgpr_write_b32 a39, v245\n"
"v_accvgpr_read_b32 v244, a40\n"
"v_mul_f32 v244, v244, v242\n"
"v_accvgpr_write_b32 a40, v244\n"
"v_accvgpr_read_b32 v245, a41\n"
"v_mul_f32 v245, v245, v242\n"
"v_accvgpr_write_b32 a41, v245\n"
"v_accvgpr_read_b32 v244, a42\n"
"v_mul_f32 v244, v244, v242\n"
"v_accvgpr_write_b32 a42, v244\n"
"v_accvgpr_read_b32 v245, a43\n"
"v_mul_f32 v245, v245, v242\n"
"v_accvgpr_write_b32 a43, v245\n"
"v_accvgpr_read_b32 v244, a44\n"
"v_mul_f32 v244, v244, v242\n"
"v_accvgpr_write_b32 a44, v244\n"
"v_accvgpr_read_b32 v245, a45\n"
"v_mul_f32 v245, v245, v242\n"
"v_accvgpr_write_b32 a45, v245\n"
"v_accvgpr_read_b32 v244, a46\n"
"v_mul_f32 v244, v244, v242\n"
"v_accvgpr_write_b32 a46, v244\n"
"v_accvgpr_read_b32 v245, a47\n"
"v_mul_f32 v245, v245, v242\n"
"v_accvgpr_write_b32 a47, v245\n"
"v_accvgpr_read_b32 v244, a48\n"
"v_mul_f32 v244, v244, v242\n"
"v_accvgpr_write_b32 a48, v244\n"
"v_accvgpr_read_b32 v245, a49\n"
"v_mul_f32 v245, v245, v242\n"
"v_accvgpr_write_b32 a49, v245\n"
"v_accvgpr_read_b32 v244, a50\n"
"v_mul_f32 v244, v244, v242\n"
"v_accvgpr_write_b32 a50, v244\n"
"v_accvgpr_read_b32 v245, a51\n"
"v_mul_f32 v245, v245, v242\n"
"v_accvgpr_write_b32 a51, v245\n"
"v_accvgpr_read_b32 v244, a52\n"
"v_mul_f32 v244, v244, v242\n"
"v_accvgpr_write_b32 a52, v244\n"
"v_accvgpr_read_b32 v245, a53\n"
"v_mul_f32 v245, v245, v242\n"
"v_accvgpr_write_b32 a53, v245\n"
"v_accvgpr_read_b32 v244, a54\n"
"v_mul_f32 v244, v244, v242\n"
"v_accvgpr_write_b32 a54, v244\n"
"v_accvgpr_read_b32 v245, a55\n"
"v_mul_f32 v245, v245, v242\n"
"v_accvgpr_write_b32 a55, v245\n"
"v_accvgpr_read_b32 v244, a56\n"
"v_mul_f32 v244, v244, v242\n"
"v_accvgpr_write_b32 a56, v244\n"
"v_accvgpr_read_b32 v245, a57\n"
"v_mul_f32 v245, v245, v242\n"
"v_accvgpr_write_b32 a57, v245\n"
"v_accvgpr_read_b32 v244, a58\n"
"v_mul_f32 v244, v244, v242\n"
"v_accvgpr_write_b32 a58, v244\n"
"v_accvgpr_read_b32 v245, a59\n"
"v_mul_f32 v245, v245, v242\n"
"v_accvgpr_write_b32 a59, v245\n"
"v_accvgpr_read_b32 v244, a60\n"
"v_mul_f32 v244, v244, v242\n"
"v_accvgpr_write_b32 a60, v244\n"
"v_accvgpr_read_b32 v245, a61\n"
"v_mul_f32 v245, v245, v242\n"
"v_accvgpr_write_b32 a61, v245\n"
"v_accvgpr_read_b32 v244, a62\n"
"v_mul_f32 v244, v244, v242\n"
"v_accvgpr_write_b32 a62, v244\n"
"v_accvgpr_read_b32 v245, a63\n"
"v_mul_f32 v245, v245, v242\n"
"v_accvgpr_write_b32 a63, v245\n"
"v_sub_f32 v64, v64, v240\n"
"v_sub_f32 v65, v65, v240\n"
"v_sub_f32 v66, v66, v240\n"
"v_sub_f32 v67, v67, v240\n"
"v_sub_f32 v68, v68, v240\n"
"v_sub_f32 v69, v69, v240\n"
"v_sub_f32 v70, v70, v240\n"
"v_sub_f32 v71, v71, v240\n"
"v_sub_f32 v72, v72, v240\n"
"v_sub_f32 v73, v73, v240\n"
"v_sub_f32 v74, v74, v240\n"
"v_sub_f32 v75, v75, v240\n"
"v_sub_f32 v76, v76, v240\n"
"v_sub_f32 v77, v77, v240\n"
"v_sub_f32 v78, v78, v240\n"
"v_sub_f32 v79, v79, v240\n"
"v_sub_f32 v80, v80, v240\n"
"v_sub_f32 v81, v81, v240\n"
"v_sub_f32 v82, v82, v240\n"
"v_sub_f32 v83, v83, v240\n"
"v_sub_f32 v84, v84, v240\n"
"v_sub_f32 v85, v85, v240\n"
"v_sub_f32 v86, v86, v240\n"
"v_sub_f32 v87, v87, v240\n"
"v_sub_f32 v88, v88, v240\n"
"v_sub_f32 v89, v89, v240\n"
"v_sub_f32 v90, v90, v240\n"
"v_sub_f32 v91, v91, v240\n"
"v_sub_f32 v92, v92, v240\n"
"v_sub_f32 v93, v93, v240\n"
"v_sub_f32 v94, v94, v240\n"
"v_sub_f32 v95, v95, v240\n"
"v_sub_f32 v96, v96, v240\n"
"v_sub_f32 v97, v97, v240\n"
"v_sub_f32 v98, v98, v240\n"
"v_sub_f32 v99, v99, v240\n"
"v_sub_f32 v100, v100, v240\n"
"v_sub_f32 v101, v101, v240\n"
"v_sub_f32 v102, v102, v240\n"
"v_sub_f32 v103, v103, v240\n"
"v_sub_f32 v104, v104, v240\n"
"v_sub_f32 v105, v105, v240\n"
"v_sub_f32 v106, v106, v240\n"
"v_sub_f32 v107, v107, v240\n"
"v_sub_f32 v108, v108, v240\n"
"v_sub_f32 v109, v109, v240\n"
"v_sub_f32 v110, v110, v240\n"
"v_sub_f32 v111, v111, v240\n"
"v_sub_f32 v112, v112, v240\n"
"v_sub_f32 v113, v113, v240\n"
"v_sub_f32 v114, v114, v240\n"
"v_sub_f32 v115, v115, v240\n"
"v_sub_f32 v116, v116, v240\n"
"v_sub_f32 v117, v117, v240\n"
"v_sub_f32 v118, v118, v240\n"
"v_sub_f32 v119, v119, v240\n"
"v_sub_f32 v120, v120, v240\n"
"v_sub_f32 v121, v121, v240\n"
"v_sub_f32 v122, v122, v240\n"
"v_sub_f32 v123, v123, v240\n"
"v_sub_f32 v124, v124, v240\n"
"v_sub_f32 v125, v125, v240\n"
"v_sub_f32 v126, v126, v240\n"
"v_sub_f32 v127, v127, v240\n"
"v_exp_f32 v224, v64\n"
"s_nop 0\n"
"v_exp_f32 v225, v65\n"
"s_nop 0\n"
"v_add_f32 v232, v224, v225\n"
"v_cvt_pk_bf16_f32 v128, v224, v225\n"
"v_exp_f32 v226, v66\n"
"s_nop 0\n"
"v_add_f32 v232, v232, v226\n"
"v_exp_f32 v227, v67\n"
"s_nop 0\n"
"v_add_f32 v232, v232, v227\n"
"v_cvt_pk_bf16_f32 v129, v226, v227\n"
"v_exp_f32 v228, v68\n"
"s_nop 0\n"
"v_add_f32 v232, v232, v228\n"
"v_exp_f32 v229, v69\n"
"s_nop 0\n"
"v_add_f32 v232, v232, v229\n"
"v_cvt_pk_bf16_f32 v130, v228, v229\n"
"v_exp_f32 v230, v70\n"
"s_nop 0\n"
"v_add_f32 v232, v232, v230\n"
"v_exp_f32 v231, v71\n"
"s_nop 0\n"
"v_add_f32 v232, v232, v231\n"
"v_cvt_pk_bf16_f32 v131, v230, v231\n"
"v_exp_f32 v224, v72\n"
"s_nop 0\n"
"v_add_f32 v232, v232, v224\n"
"v_exp_f32 v225, v73\n"
"s_nop 0\n"
"v_add_f32 v232, v232, v225\n"
"v_cvt_pk_bf16_f32 v132, v224, v225\n"
"v_exp_f32 v226, v74\n"
"s_nop 0\n"
"v_add_f32 v232, v232, v226\n"
"v_exp_f32 v227, v75\n"
"s_nop 0\n"
"v_add_f32 v232, v232, v227\n"
"v_cvt_pk_bf16_f32 v133, v226, v227\n"
"v_exp_f32 v228, v76\n"
"s_nop 0\n"
"v_add_f32 v232, v232, v228\n"
"v_exp_f32 v229, v77\n"
"s_nop 0\n"
"v_add_f32 v232, v232, v229\n"
"v_cvt_pk_bf16_f32 v134, v228, v229\n"
"v_exp_f32 v230, v78\n"
"s_nop 0\n"
"v_add_f32 v232, v232, v230\n"
"v_exp_f32 v231, v79\n"
"s_nop 0\n"
"v_add_f32 v232, v232, v231\n"
"v_cvt_pk_bf16_f32 v135, v230, v231\n"
"v_exp_f32 v224, v80\n"
"s_nop 0\n"
"v_exp_f32 v225, v81\n"
"s_nop 0\n"
"v_add_f32 v233, v224, v225\n"
"v_cvt_pk_bf16_f32 v136, v224, v225\n"
"v_exp_f32 v226, v82\n"
"s_nop 0\n"
"v_add_f32 v233, v233, v226\n"
"v_exp_f32 v227, v83\n"
"s_nop 0\n"
"v_add_f32 v233, v233, v227\n"
"v_cvt_pk_bf16_f32 v137, v226, v227\n"
"v_exp_f32 v228, v84\n"
"s_nop 0\n"
"v_add_f32 v233, v233, v228\n"
"v_exp_f32 v229, v85\n"
"s_nop 0\n"
"v_add_f32 v233, v233, v229\n"
"v_cvt_pk_bf16_f32 v138, v228, v229\n"
"v_exp_f32 v230, v86\n"
"s_nop 0\n"
"v_add_f32 v233, v233, v230\n"
"v_exp_f32 v231, v87\n"
"s_nop 0\n"
"v_add_f32 v233, v233, v231\n"
"v_cvt_pk_bf16_f32 v139, v230, v231\n"
"v_exp_f32 v224, v88\n"
"s_nop 0\n"
"v_add_f32 v233, v233, v224\n"
"v_exp_f32 v225, v89\n"
"s_nop 0\n"
"v_add_f32 v233, v233, v225\n"
"v_cvt_pk_bf16_f32 v140, v224, v225\n"
"v_exp_f32 v226, v90\n"
"s_nop 0\n"
"v_add_f32 v233, v233, v226\n"
"v_exp_f32 v227, v91\n"
"s_nop 0\n"
"v_add_f32 v233, v233, v227\n"
"v_cvt_pk_bf16_f32 v141, v226, v227\n"
"v_exp_f32 v228, v92\n"
"s_nop 0\n"
"v_add_f32 v233, v233, v228\n"
"v_exp_f32 v229, v93\n"
"s_nop 0\n"
"v_add_f32 v233, v233, v229\n"
"v_cvt_pk_bf16_f32 v142, v228, v229\n"
"v_exp_f32 v230, v94\n"
"s_nop 0\n"
"v_add_f32 v233, v233, v230\n"
"v_exp_f32 v231, v95\n"
"s_nop 0\n"
"v_add_f32 v233, v233, v231\n"
"v_cvt_pk_bf16_f32 v143, v230, v231\n"
"v_add_f32 v236, v232, v233\n"
"s_nop 1\n"
"s_branch Lfr112_%=\n"
"LEND_%=:\n"
    :: [karg] "s"(karg), [wave] "s"(wave), [bid] "s"(b)
    : "v0","v1","v2","v3","v4","v5","v6","v7","v8","v9","v10","v11","v12","v13","v14","v15","v16","v17","v18","v19","v20","v21","v22","v23","v24","v25","v26","v27","v28","v29","v30","v31","v32","v33","v34","v35","v36","v37","v38","v39","v40","v41","v42","v43","v44","v45","v46","v47","v48","v49","v50","v51","v52","v53","v54","v55","v56","v57","v58","v59","v60","v61","v62","v63","v64","v65","v66","v67","v68","v69","v70","v71","v72","v73","v74","v75","v76","v77","v78","v79","v80","v81","v82","v83","v84","v85","v86","v87","v88","v89","v90","v91","v92","v93","v94","v95","v96","v97","v98","v99","v100","v101","v102","v103","v104","v105","v106","v107","v108","v109","v110","v111","v112","v113","v114","v115","v116","v117","v118","v119","v120","v121","v122","v123","v124","v125","v126","v127","v128","v129","v130","v131","v132","v133","v134","v135","v136","v137","v138","v139","v140","v141","v142","v143","v144","v145","v146","v147","v148","v149","v150","v151","v152","v153","v154","v155","v156","v157","v158","v159","v160","v161","v162","v163","v164","v165","v166","v167","v168","v169","v170","v171","v172","v173","v174","v175","v176","v177","v178","v179","v180","v181","v182","v183","v184","v185","v186","v187","v188","v189","v190","v191","v192","v193","v194","v195","v196","v197","v198","v199","v200","v201","v202","v203","v204","v205","v206","v207","v208","v209","v210","v211","v212","v213","v214","v215","v216","v217","v218","v219","v220","v221","v222","v223","v224","v225","v226","v227","v228","v229","v230","v231","v232","v233","v234","v235","v236","v237","v238","v239","v240","v241","v242","v243","v244","v245","v246","v247","v248","v249","v250","v251","v252","v253","v254","v255","a0","a1","a2","a3","a4","a5","a6","a7","a8","a9","a10","a11","a12","a13","a14","a15","a16","a17","a18","a19","a20","a21","a22","a23","a24","a25","a26","a27","a28","a29","a30","a31","a32","a33","a34","a35","a36","a37","a38","a39","a40","a41","a42","a43","a44","a45","a46","a47","a48","a49","a50","a51","a52","a53","a54","a55","a56","a57","a58","a59","a60","a61","a62","a63","a64","a65","a66","a67","a68","a69","a70","a71","a72","a73","a74","a75","a76","a77","a78","a79","a80","a81","a82","a83","a84","a85","a86","a87","a88","a89","a90","a91","a92","a93","a94","a95","a96","a97","a98","a99","a100","a101","a102","a103","a104","a105","a106","a107","a108","a109","a110","a111","a112","a113","a114","a115","a116","a117","a118","a119","a120","a121","a122","a123","a124","a125","a126","a127","a128","a129","a130","a131","a132","a133","a134","a135","a136","a137","a138","a139","a140","a141","a142","a143","a144","a145","a146","a147","a148","a149","a150","a151","a152","a153","a154","a155","a156","a157","a158","a159","a160","a161","a162","a163","a164","a165","a166","a167","a168","a169","a170","a171","a172","a173","a174","a175","a176","a177","a178","a179","a180","a181","a182","a183","a184","a185","a186","a187","a188","a189","a190","a191","a192","a193","a194","a195","a196","a197","a198","a199","a200","a201","a202","a203","a204","a205","a206","a207","a208","a209","a210","a211","a212","a213","a214","a215","a216","a217","a218","a219","a220","a221","a222","a223","a224","a225","a226","a227","a228","a229","a230","a231","a232","a233","a234","a235","a236","a237","a238","a239","a240","a241","a242","a243","a244","a245","a246","a247","a248","a249","a250","a251","a252","a253","a254","a255","s16","s17","s18","s19","s20","s21","s22","s23","s24","s25","s26","s27","s28","s29","s30","s31","s36","s37","s38","s39","s40","s41","s42","s43","s44","s45","s46","s47","s48","s49","s50","s51","s52","s53","s54","s55","s56","s57","s58","s59","s60","s61","s62","s63","s64","s65","s66","s67","s68","s69","s70","s71","s72","s73","s74","s75","s76","s77","s78","s79","s80","s81","s82","s83","s84","s85","s86","s87","s88","s89","s90","s91","s92","s93","s94","s95","s96","s97","s98","s99","vcc","scc","memory");
}
extern "C" void kernel_launch(void* const* d_in, const int* in_sizes, int n_in, void* d_out, int out_size, void* d_ws, size_t ws_size, hipStream_t stream) {
  (void)in_sizes; (void)n_in; (void)out_size; (void)ws_size;
  (void)hipFuncSetAttribute((const void*)k_all, hipFuncAttributeMaxDynamicSharedMemorySize, 163840);
  k_all<<<256, 256, 163840, stream>>>((const u16*)d_in[0], (const u16*)d_in[1], (const u16*)d_in[2], (const u16*)d_in[3], (const u16*)d_in[4], (const u16*)d_in[5],
                                      (const u16*)d_in[7], (const u16*)d_in[8], (const u16*)d_in[9], (const u16*)d_in[10], (const u16*)d_in[11], (u16*)d_out, (char*)d_ws);
}
```
